```python
import torch

def _softmax(x, axis=-1):
    return torch.softmax(x, dim=axis)
def _gelu(x):
    return torch.nn.functional.gelu(x, approximate="tanh")
def _silu(x):
    return torch.nn.functional.silu(x)
def _sigmoid(x):
    return torch.sigmoid(x)
def _layernorm(x, axis=-1, eps=1e-5):
    m = x.mean(dim=axis, keepdim=True)
    v = x.var(dim=axis, keepdim=True, unbiased=False)
    return (x - m) / torch.sqrt(v + eps)
def _scatter_add(tgt, idx, src, axis=0):
    return tgt.clone().index_add_(axis, idx, src)
def _segment_sum(src, seg, n):
    out = torch.zeros((n,) + src.shape[1:], dtype=src.dtype)
    return out.index_add_(0, seg, src)
def _pad(x, pw):
    flat = tuple(v for pair in reversed(pw) for v in pair)
    return torch.nn.functional.pad(x, flat)

kernel_name = 'standalone__cheap_equivariant_egnn__graph'

def setup_inputs():

    _w = (lambda a: (lambda t: t.to(torch.bfloat16) if t.is_floating_point() else t)(torch.as_tensor(a)))
    N, deg, D, De = 12500, 32, 128, max(4, 1)
    E = N * deg
    return {
        "h": _w((lambda *s: torch.randn(*s, dtype=torch.bfloat16) * 0.1)(N, D)),
        "pos": _w((lambda *s: torch.randn(*s, dtype=torch.bfloat16) * 0.8)(N, 3)),
        "e_attr": _w((lambda *s: torch.randn(*s, dtype=torch.bfloat16) * 0.8)(E, De)),
        "src": _w((lambda lo, hi, *s: torch.randint(lo, hi, s))(0, N, E).to(torch.int64)),
        "dst": _w((lambda lo, hi, *s: torch.randint(lo, hi, s))(0, N, E).to(torch.int64)),
        "w_e": _w(0.1 * (lambda *s: torch.randn(*s, dtype=torch.bfloat16) * 0.8)(2 * D + 1 + De, D)),
        "w_x": _w(0.1 * (lambda *s: torch.randn(*s, dtype=torch.bfloat16) * 0.1)(D, 1)),
        "w_h": _w(0.1 * (lambda *s: torch.randn(*s, dtype=torch.bfloat16) * 0.1)(2 * D, D)),
    }

def reference(h, pos, e_attr, src, dst, w_e, w_x, w_h):
    N, D = h.shape[0], h.shape[1]
    diff = pos[dst] - pos[src]
    dist2 = torch.sum(diff * diff, axis=-1, keepdims=True)
    e_in = torch.concatenate([h[dst], h[src], dist2, e_attr], axis=-1)
    m_ij = _silu(e_in @ w_e)
    coord_scale = m_ij @ w_x
    pos_new = pos + _segment_sum(diff * coord_scale, dst, N)
    m_i = _segment_sum(m_ij, dst, N)
    h_new = h + _silu(torch.concatenate([h, m_i], axis=-1) @ w_h)
    return h_new, pos_new
```

```cpp
#include <hip/hip_runtime.h>
#include <hip/hip_bf16.h>
#include <stdint.h>


typedef unsigned short u16;

__device__ __forceinline__ float bf2f(u16 v) { return __uint_as_float(((unsigned)v) << 16); }
__device__ __forceinline__ u16 f2bf(float f) {
    __hip_bfloat16 b = __float2bfloat16(f);
    return *reinterpret_cast<u16*>(&b);
}
__device__ __forceinline__ float silu_f(float x) { return x / (1.0f + __expf(-x)); }

#define D 128
#define CAP 64

struct __align__(16) EdgeRec { int src; u16 ea[4]; int pad; };

__global__ void k_node_gemm(const u16* __restrict__ h, const u16* __restrict__ we,
                            float* __restrict__ AB, int N) {
    __shared__ float hs[8][D];
    int n0 = blockIdx.x * 8;
    int t = threadIdx.x;
    for (int i = t; i < 8 * D; i += 256) {
        int r = i / D, c = i % D;
        int n = n0 + r;
        hs[r][c] = (n < N) ? bf2f(h[(size_t)n * D + c]) : 0.f;
    }
    __syncthreads();
    int half = t >> 7, col = t & 127;
    float acc[8];
#pragma unroll
    for (int r = 0; r < 8; ++r) acc[r] = 0.f;
    for (int k = 0; k < D; ++k) {
        float w = bf2f(we[(size_t)(half * D + k) * D + col]);
#pragma unroll
        for (int r = 0; r < 8; ++r) acc[r] += hs[r][k] * w;
    }
#pragma unroll
    for (int r = 0; r < 8; ++r) {
        int n = n0 + r;
        if (n < N) AB[(size_t)n * 256 + t] = acc[r];
    }
}

__global__ void k_scatter(const int* __restrict__ src, const int* __restrict__ dst,
                          const u16* __restrict__ ea, int* __restrict__ cnt,
                          EdgeRec* __restrict__ rec, int* __restrict__ ovf_cnt,
                          int* __restrict__ ovf_list, int E) {
    int e = blockIdx.x * blockDim.x + threadIdx.x;
    if (e >= E) return;
    int d = dst[e];
    int slot = atomicAdd(&cnt[d], 1);
    if (slot < CAP) {
        EdgeRec r;
        r.src = src[e];
        for (int k = 0; k < 4; ++k) r.ea[k] = ea[(size_t)e * 4 + k];
        r.pad = 0;
        rec[(size_t)d * CAP + slot] = r;
    } else {
        int o = atomicAdd(ovf_cnt, 1);
        ovf_list[o] = e;
    }
}

__global__ void k_agg(const u16* __restrict__ h, const u16* __restrict__ pos,
                      const int* __restrict__ src, const int* __restrict__ dst,
                      const u16* __restrict__ ea_all,
                      const u16* __restrict__ we, const u16* __restrict__ wx,
                      const u16* __restrict__ wh, const float* __restrict__ AB,
                      const int* __restrict__ cnt, const EdgeRec* __restrict__ rec,
                      const int* __restrict__ ovf_cnt, const int* __restrict__ ovf_list,
                      u16* __restrict__ out_h, u16* __restrict__ out_pos, int N) {
    __shared__ float mi_s[4][D];
    __shared__ float hs[4][D];
    int wave = threadIdx.x >> 6, lane = threadIdx.x & 63;
    int n = blockIdx.x * 4 + wave;
    bool valid = n < N;
    int c0 = lane * 2;
    float wd[2], wa[4][2], wxv[2];
    for (int j = 0; j < 2; ++j) {
        wd[j] = bf2f(we[(size_t)256 * D + c0 + j]);
        for (int k = 0; k < 4; ++k) wa[k][j] = bf2f(we[(size_t)(257 + k) * D + c0 + j]);
        wxv[j] = bf2f(wx[c0 + j]);
    }
    float m0 = 0.f, m1 = 0.f, px = 0.f, py = 0.f, pz = 0.f;
    if (valid) {
        float a0 = AB[(size_t)n * 256 + c0], a1 = AB[(size_t)n * 256 + c0 + 1];
        float pdx = bf2f(pos[n * 3 + 0]), pdy = bf2f(pos[n * 3 + 1]), pdz = bf2f(pos[n * 3 + 2]);
        int deg = cnt[n];
        int nov = (deg > CAP) ? *ovf_cnt : 0;
        int dmain = deg < CAP ? deg : CAP;
        for (int i = 0; i < dmain + nov; ++i) {
            int s; float e0, e1, e2, e3;
            if (i < dmain) {
                EdgeRec r = rec[(size_t)n * CAP + i];
                s = r.src; e0 = bf2f(r.ea[0]); e1 = bf2f(r.ea[1]); e2 = bf2f(r.ea[2]); e3 = bf2f(r.ea[3]);
            } else {
                int e = ovf_list[i - dmain];
                if (dst[e] != n) continue;
                s = src[e];
                e0 = bf2f(ea_all[(size_t)e * 4 + 0]); e1 = bf2f(ea_all[(size_t)e * 4 + 1]);
                e2 = bf2f(ea_all[(size_t)e * 4 + 2]); e3 = bf2f(ea_all[(size_t)e * 4 + 3]);
            }
            float dx = pdx - bf2f(pos[s * 3 + 0]);
            float dy = pdy - bf2f(pos[s * 3 + 1]);
            float dz = pdz - bf2f(pos[s * 3 + 2]);
            float d2 = dx * dx + dy * dy + dz * dz;
            float b0 = AB[(size_t)s * 256 + 128 + c0], b1 = AB[(size_t)s * 256 + 128 + c0 + 1];
            float p0 = a0 + b0 + d2 * wd[0] + e0 * wa[0][0] + e1 * wa[1][0] + e2 * wa[2][0] + e3 * wa[3][0];
            float p1 = a1 + b1 + d2 * wd[1] + e0 * wa[0][1] + e1 * wa[1][1] + e2 * wa[2][1] + e3 * wa[3][1];
            float s0 = silu_f(p0), s1 = silu_f(p1);
            m0 += s0; m1 += s1;
            float cs = s0 * wxv[0] + s1 * wxv[1];
            for (int o = 32; o > 0; o >>= 1) cs += __shfl_xor(cs, o);
            px += dx * cs; py += dy * cs; pz += dz * cs;
        }
        if (lane == 0) {
            out_pos[n * 3 + 0] = f2bf(pdx + px);
            out_pos[n * 3 + 1] = f2bf(pdy + py);
            out_pos[n * 3 + 2] = f2bf(pdz + pz);
        }
        mi_s[wave][c0] = m0; mi_s[wave][c0 + 1] = m1;
        hs[wave][c0] = bf2f(h[(size_t)n * D + c0]); hs[wave][c0 + 1] = bf2f(h[(size_t)n * D + c0 + 1]);
    }
    __syncthreads();
    if (valid) {
        float acc0 = 0.f, acc1 = 0.f;
        for (int k = 0; k < D; ++k) {
            float hv = hs[wave][k];
            acc0 += hv * bf2f(wh[(size_t)k * D + c0]);
            acc1 += hv * bf2f(wh[(size_t)k * D + c0 + 1]);
        }
        for (int k = 0; k < D; ++k) {
            float mv = mi_s[wave][k];
            acc0 += mv * bf2f(wh[(size_t)(D + k) * D + c0]);
            acc1 += mv * bf2f(wh[(size_t)(D + k) * D + c0 + 1]);
        }
        out_h[(size_t)n * D + c0] = f2bf(hs[wave][c0] + silu_f(acc0));
        out_h[(size_t)n * D + c0 + 1] = f2bf(hs[wave][c0 + 1] + silu_f(acc1));
    }
}

extern "C" void kernel_launch(void* const* d_in, const int* in_sizes, int n_in,
                              void* d_out, int out_size, void* d_ws, size_t ws_size,
                              hipStream_t stream) {
    const u16* h   = (const u16*)d_in[0];
    const u16* pos = (const u16*)d_in[1];
    const u16* ea  = (const u16*)d_in[2];
    const int* src = (const int*)d_in[3];
    const int* dst = (const int*)d_in[4];
    const u16* we  = (const u16*)d_in[5];
    const u16* wx  = (const u16*)d_in[6];
    const u16* wh  = (const u16*)d_in[7];
    int N = in_sizes[0] / D;
    int E = in_sizes[3];
    u16* out_h = (u16*)d_out;
    u16* out_pos = out_h + (size_t)N * D;

    char* ws = (char*)d_ws;
    size_t off = 0;
    int* cnt = (int*)(ws + off); off += ((size_t)(N + 1) * 4 + 255) & ~(size_t)255;
    int* ovf_cnt = cnt + N;
    size_t zero_bytes = (size_t)(N + 1) * 4;
    float* AB = (float*)(ws + off); off += ((size_t)N * 256 * 4 + 255) & ~(size_t)255;
    EdgeRec* rec = (EdgeRec*)(ws + off); off += ((size_t)N * CAP * 16 + 255) & ~(size_t)255;
    int* ovf_list = (int*)(ws + off); off += ((size_t)E * 4 + 255) & ~(size_t)255;

    hipMemsetAsync(cnt, 0, zero_bytes, stream);
    k_node_gemm<<<(N + 7) / 8, 256, 0, stream>>>(h, we, AB, N);
    k_scatter<<<(E + 255) / 256, 256, 0, stream>>>(src, dst, ea, cnt, rec, ovf_cnt, ovf_list, E);
    k_agg<<<(N + 3) / 4, 256, 0, stream>>>(h, pos, src, dst, ea, we, wx, wh, AB, cnt, rec, ovf_cnt,
                                          ovf_list, out_h, out_pos, N);
}
```

```cpp
#include <hip/hip_runtime.h>
#include <hip/hip_bf16.h>
#include <stdint.h>


typedef unsigned short u16;
typedef unsigned int u32;
typedef __attribute__((ext_vector_type(8))) short bf16x8;
typedef __attribute__((ext_vector_type(4))) short bf16x4;
typedef __attribute__((ext_vector_type(4))) float f32x4;

#define D 128
#define NB 512
#define NPBMAX 32
#define KA_THREADS 512
#define KA_ITERS 8
#define KB_THREADS 512
#define CAP 1024

__device__ __forceinline__ float bf2f(u16 v) { return __uint_as_float(((u32)v) << 16); }
__device__ __forceinline__ u16 f2bf(float f) {
    __hip_bfloat16 b = __float2bfloat16(f);
    return *reinterpret_cast<u16*>(&b);
}
__device__ __forceinline__ float silu_f(float x) {
    float e = __builtin_amdgcn_exp2f(x * -1.4426950408889634f);
    return x * __builtin_amdgcn_rcpf(1.0f + e);
}
__device__ __forceinline__ u32 pack2(float a, float b) {
    return (u32)f2bf(a) | ((u32)f2bf(b) << 16);
}

__global__ void __launch_bounds__(KA_THREADS)
k_part(const u16* __restrict__ h, const int* __restrict__ src, const int* __restrict__ dst,
       const u16* __restrict__ ea, const u16* __restrict__ we, const u16* __restrict__ wh,
       uint4* __restrict__ rec_g, u32* __restrict__ tab, u16* __restrict__ Bg,
       u16* __restrict__ weAT, u16* __restrict__ whT,
       int N, int E, int G, int CH, int NPB, int nGemm) {
    __shared__ __align__(16) u16 smem[D * (D + 8)];
    int t = threadIdx.x;
    int bid = blockIdx.x;
    if (bid < G) {
        int* hist = (int*)smem;
        hist[t] = 0;
        __syncthreads();
        int e0 = bid * CH;
        int e1 = min(E, e0 + CH);
        int sv[KA_ITERS], bv[KA_ITERS], rk[KA_ITERS], lo[KA_ITERS];
        uint2 eav[KA_ITERS];
#pragma unroll
        for (int i = 0; i < KA_ITERS; ++i) {
            int e = e0 + t + i * KA_THREADS;
            bv[i] = -1;
            if (e < e1) {
                int d = dst[e];
                sv[i] = src[e];
                eav[i] = *reinterpret_cast<const uint2*>(ea + (size_t)e * 4);
                int b = d / NPB;
                bv[i] = b;
                lo[i] = d - b * NPB;
            }
        }
#pragma unroll
        for (int i = 0; i < KA_ITERS; ++i)
            if (bv[i] >= 0) rk[i] = atomicAdd(&hist[bv[i]], 1);
        __syncthreads();
        int cnt = hist[t];
        int lane = t & 63, wv = t >> 6;
        int x = cnt;
#pragma unroll
        for (int o = 1; o < 64; o <<= 1) {
            int y = __shfl_up(x, o);
            if (lane >= o) x += y;
        }
        __shared__ int wsum[KA_THREADS / 64];
        if (lane == 63) wsum[wv] = x;
        __syncthreads();
        int base = 0;
        for (int w = 0; w < wv; ++w) base += wsum[w];
        int off = base + x - cnt;
        __syncthreads();
        hist[t] = off;
        tab[(size_t)t * G + bid] = ((u32)off << 16) | (u32)cnt;
        __syncthreads();
#pragma unroll
        for (int i = 0; i < KA_ITERS; ++i) {
            if (bv[i] >= 0) {
                int e = e0 + t + i * KA_THREADS;
                int p = hist[bv[i]] + rk[i];
                uint4 r;
                r.x = (u32)sv[i]; r.y = eav[i].x; r.z = eav[i].y;
                r.w = ((u32)e << 8) | (u32)lo[i];
                rec_g[(size_t)bid * CH + p] = r;
            }
        }
        return;
    }
    bid -= G;
    if (bid < nGemm) {
        for (int i = t; i < D * D; i += KA_THREADS) {
            int k = i >> 7, c = i & 127;
            smem[c * (D + 8) + k] = we[(size_t)(D + k) * D + c];
        }
        __syncthreads();
        int lane = t & 63, wv = t >> 6;
        int e16 = lane & 15, g = lane >> 4;
        int rt = wv & 3;
        int ch = wv >> 2;
        int n = bid * 64 + rt * 16 + e16;
        int nn = n < N ? n : N - 1;
        bf16x8 af[4];
#pragma unroll
        for (int ks = 0; ks < 4; ++ks)
            af[ks] = *reinterpret_cast<const bf16x8*>(h + (size_t)nn * D + ks * 32 + g * 8);
#pragma unroll
        for (int ct = 0; ct < 4; ++ct) {
            int c0 = ch * 64 + ct * 16;
            f32x4 acc = {0.f, 0.f, 0.f, 0.f};
#pragma unroll
            for (int ks = 0; ks < 4; ++ks) {
                bf16x8 bfr = *reinterpret_cast<const bf16x8*>(&smem[(c0 + e16) * (D + 8) + ks * 32 + g * 8]);
                acc = __builtin_amdgcn_mfma_f32_16x16x32_bf16(af[ks], bfr, acc, 0, 0, 0);
            }
#pragma unroll
            for (int r = 0; r < 4; ++r) {
                int row = bid * 64 + rt * 16 + g * 4 + r;
                if (row < N) Bg[(size_t)row * D + c0 + e16] = f2bf(acc[r]);
            }
        }
        return;
    }
    bid -= nGemm;
    if (bid == 0) {
        for (int i = t; i < D * D; i += KA_THREADS) {
            int c = i >> 7, k = i & 127;
            weAT[i] = we[(size_t)k * D + c];
        }
    } else {
        for (int i = t; i < D * 2 * D; i += KA_THREADS) {
            int c = i >> 8, k = i & 255;
            whT[i] = wh[(size_t)k * D + c];
        }
    }
}

struct KbSmem {
    uint4 bufA[CAP];
    uint4 bufB[CAP];
    float Al[NPBMAX][D + 4];
    float macc[NPBMAX][D + 4];
    float head[KB_THREADS / 64][D + 4];
    float pacc[NPBMAX][4];
    int segoff[512 + 1];
    int ncnt[NPBMAX + 1];
    int nstart[NPBMAX + 1];
    int headnode[KB_THREADS / 64];
    int wsum[KB_THREADS / 64];
};

__global__ void __launch_bounds__(KB_THREADS)
k_main(const u16* __restrict__ h, const u16* __restrict__ pos,
       const u16* __restrict__ we, const u16* __restrict__ wx,
       const uint4* __restrict__ rec_g, const u32* __restrict__ tab, const u16* __restrict__ Bg,
       const u16* __restrict__ weAT, const u16* __restrict__ whT,
       u16* __restrict__ out_h, u16* __restrict__ out_pos,
       int N, int G, int CH, int NPB) {
    extern __shared__ __align__(16) char smem_raw[];
    KbSmem& S = *reinterpret_cast<KbSmem*>(smem_raw);
    const int t = threadIdx.x, lane = t & 63, wv = t >> 6;
    const int e16 = lane & 15, g = lane >> 4;
    const int b = blockIdx.x;
    const int nb0 = b * NPB;
    const int nnodes = min(NPB, N - nb0);
    if (nnodes <= 0) return;

    u32 tv = (t < G) ? tab[(size_t)b * G + t] : 0u;
    int scnt = (int)(tv & 0xffffu), sstart = (int)(tv >> 16);
    {
        int x = scnt;
#pragma unroll
        for (int o = 1; o < 64; o <<= 1) {
            int y = __shfl_up(x, o);
            if (lane >= o) x += y;
        }
        if (lane == 63) S.wsum[wv] = x;
        __syncthreads();
        int base = 0;
        for (int w = 0; w < wv; ++w) base += S.wsum[w];
        S.segoff[t] = base + x - scnt;
        if (t == KB_THREADS - 1) S.segoff[KB_THREADS] = base + x;
    }
    for (int i = t; i < NPBMAX * (D + 4); i += KB_THREADS) (&S.macc[0][0])[i] = 0.f;
    if (t < NPBMAX * 4) (&S.pacc[0][0])[t] = 0.f;

    {
        int rt = wv & 1;
        int n = nb0 + rt * 16 + e16;
        int nn = n < N ? n : N - 1;
        bf16x8 af[4];
#pragma unroll
        for (int ks = 0; ks < 4; ++ks)
            af[ks] = *reinterpret_cast<const bf16x8*>(h + (size_t)nn * D + ks * 32 + g * 8);
#pragma unroll
        for (int q = 0; q < 2; ++q) {
            int ct = (wv >> 1) * 2 + q;
            f32x4 acc = {0.f, 0.f, 0.f, 0.f};
#pragma unroll
            for (int ks = 0; ks < 4; ++ks) {
                bf16x8 bfr = *reinterpret_cast<const bf16x8*>(weAT + (size_t)(ct * 16 + e16) * D + ks * 32 + g * 8);
                acc = __builtin_amdgcn_mfma_f32_16x16x32_bf16(af[ks], bfr, acc, 0, 0, 0);
            }
#pragma unroll
            for (int r = 0; r < 4; ++r) S.Al[rt * 16 + g * 4 + r][ct * 16 + e16] = acc[r];
        }
    }
    __syncthreads();
    const int T = S.segoff[KB_THREADS];

    bf16x8 selLo, selHi;
#pragma unroll
    for (int j = 0; j < 8; ++j) {
        int k = 8 * g + j;
        selLo[j] = (k == e16) ? (short)0x3F80 : (short)0;
        selHi[j] = (k == e16 + 16) ? (short)0x3F80 : (short)0;
    }
    bf16x8 w5[8];
    float wxv[8];
#pragma unroll
    for (int tt = 0; tt < 8; ++tt) {
        int c = tt * 16 + e16;
#pragma unroll
        for (int j = 0; j < 8; ++j) {
            int k = 8 * g + j;
            int kr = (k == 5) ? 0 : k;
            w5[tt][j] = (k < 6) ? (short)we[(size_t)(256 + kr) * D + c] : (short)0;
        }
        wxv[tt] = bf2f(wx[c]);
    }

    for (int base = 0; base < T; base += CAP) {
        const int Tr = min(CAP, T - base);
        {
            int c = t >> 2, sub = t & 3;
            if (c < G) {
                int so = S.segoff[c], sc = S.segoff[c + 1] - so;
                u32 tvc = tab[(size_t)b * G + c];
                int st = (int)(tvc >> 16);
                for (int i = sub; i < sc; i += 4) {
                    int fp = so + i - base;
                    if (fp >= 0 && fp < CAP) S.bufA[fp] = rec_g[(size_t)c * CH + st + i];
                }
            }
        }
        if (t <= NPBMAX) S.ncnt[t] = 0;
        __syncthreads();
        int myrank[2], mynode[2];
#pragma unroll
        for (int i = 0; i < 2; ++i) {
            int p = t + i * KB_THREADS;
            mynode[i] = -1;
            if (p < Tr) {
                mynode[i] = (int)(S.bufA[p].w & 0xffu);
                myrank[i] = atomicAdd(&S.ncnt[mynode[i]], 1);
            }
        }
        __syncthreads();
        if (t == 0) {
            int a = 0;
            for (int n = 0; n < NPBMAX; ++n) { S.nstart[n] = a; a += S.ncnt[n]; }
            S.nstart[NPBMAX] = a;
        }
        __syncthreads();
#pragma unroll
        for (int i = 0; i < 2; ++i) {
            int p = t + i * KB_THREADS;
            if (mynode[i] >= 0) S.bufB[S.nstart[mynode[i]] + myrank[i]] = S.bufA[p];
        }
        __syncthreads();
        uint2 ddv[2];
        int ddp[2];
#pragma unroll
        for (int i = 0; i < 2; ++i) {
            int p = t + i * KB_THREADS;
            ddp[i] = -1;
            if (p < Tr) {
                uint4 r = S.bufB[p];
                int node = (int)(r.w & 0xffu);
                u32 key = r.w >> 8;
                int s0 = S.nstart[node], s1 = S.nstart[node + 1];
                int rank = 0;
                for (int q = s0; q < s1; ++q) rank += ((S.bufB[q].w >> 8) < key) ? 1 : 0;
                int sidx = (int)r.x;
                int nd = nb0 + node;
                float dx = bf2f(pos[nd * 3 + 0]) - bf2f(pos[sidx * 3 + 0]);
                float dy = bf2f(pos[nd * 3 + 1]) - bf2f(pos[sidx * 3 + 1]);
                float dz = bf2f(pos[nd * 3 + 2]) - bf2f(pos[sidx * 3 + 2]);
                u16 bx = f2bf(dx), by = f2bf(dy), bz = f2bf(dz);
                float d2 = dx * dx + dy * dy + dz * dz;
                u16 d2h = f2bf(d2);
                u16 d2l = f2bf(d2 - bf2f(d2h));
                uint4 o;
                o.x = r.x;
                o.y = r.y; o.z = r.z;
                o.w = (u32)d2h | ((u32)d2l << 16);
                S.bufA[s0 + rank] = o;
                ddp[i] = s0 + rank;
                ddv[i].x = (u32)bx | ((u32)by << 16);
                ddv[i].y = (u32)bz | ((u32)node << 16);
            }
        }
        __syncthreads();
        {
            uint2* ddw = reinterpret_cast<uint2*>(S.bufB);
#pragma unroll
            for (int i = 0; i < 2; ++i)
                if (ddp[i] >= 0) ddw[ddp[i]] = ddv[i];
        }
        __syncthreads();

        const uint2* dd = reinterpret_cast<const uint2*>(S.bufB);
        const int nst = (Tr + 31) >> 5;
        const int st0 = (wv * nst) / (KB_THREADS / 64);
        const int st1 = ((wv + 1) * nst) / (KB_THREADS / 64);
        int headnode = -1;
        if (st0 < st1) headnode = (int)(dd[st0 * 32].y >> 16);
        if (lane == 0) S.headnode[wv] = headnode;
        for (int i = lane; i < D; i += 64) S.head[wv][i] = 0.f;
        float phead[3] = {0.f, 0.f, 0.f};

        for (int st = st0; st < st1; ++st) {
            const int p0 = st * 32;
            bf16x8 gat[2][4];
            bf16x8 x5[2];
            int rownode[2][4];
#pragma unroll
            for (int T2 = 0; T2 < 2; ++T2) {
                int p = p0 + T2 * 16 + e16;
                bool valid = p < Tr;
                uint4 r = S.bufA[valid ? p : 0];
                int sidx = valid ? (int)r.x : 0;
                const u16* brow = Bg + (size_t)sidx * D + g * 8;
#pragma unroll
                for (int tp = 0; tp < 4; ++tp)
                    gat[T2][tp] = *reinterpret_cast<const bf16x8*>(brow + tp * 32);
                u32 x01 = 0u, x23 = 0u, x45 = 0u;
                if (valid && g == 0) {
                    x01 = (r.w & 0xffffu) | (r.y << 16);
                    x23 = (r.y >> 16) | (r.z << 16);
                    x45 = (r.z >> 16) | (r.w & 0xffff0000u);
                }
                uint4 xu = make_uint4(x01, x23, x45, 0u);
                bf16x8 x = *reinterpret_cast<bf16x8*>(&xu);
                x5[T2] = x;
#pragma unroll
                for (int r4 = 0; r4 < 4; ++r4) {
                    int pe = p0 + T2 * 16 + g * 4 + r4;
                    rownode[T2][r4] = (int)(dd[pe < Tr ? pe : 0].y >> 16);
                }
            }
            const int node0 = (int)(dd[p0].y >> 16);
            const int lastp = min(p0 + 31, Tr - 1);
            const int nodeL = (int)(dd[lastp].y >> 16);

            u32 mop[8][4];
#pragma unroll
            for (int tp = 0; tp < 4; ++tp) {
#pragma unroll
                for (int u = 0; u < 2; ++u) {
                    const int tt = tp * 2 + u;
                    const int c = tt * 16 + e16;
                    float m[2][4];
#pragma unroll
                    for (int T2 = 0; T2 < 2; ++T2) {
                        f32x4 acc;
#pragma unroll
                        for (int r4 = 0; r4 < 4; ++r4) acc[r4] = S.Al[rownode[T2][r4]][c];
                        acc = __builtin_amdgcn_mfma_f32_16x16x32_bf16(gat[T2][tp], u ? selHi : selLo, acc, 0, 0, 0);
                        acc = __builtin_amdgcn_mfma_f32_16x16x32_bf16(x5[T2], w5[tt], acc, 0, 0, 0);
#pragma unroll
                        for (int r4 = 0; r4 < 4; ++r4) m[T2][r4] = silu_f(acc[r4]);
                    }
                    mop[tt][0] = pack2(m[0][0], m[0][1]);
                    mop[tt][1] = pack2(m[0][2], m[0][3]);
                    mop[tt][2] = pack2(m[1][0], m[1][1]);
                    mop[tt][3] = pack2(m[1][2], m[1][3]);
                }
            }
            for (int nb = node0; nb <= nodeL; nb += 4) {
                const int slot = e16 >> 2, comp = e16 & 3;
                bf16x8 sop;
#pragma unroll
                for (int j = 0; j < 8; ++j) {
                    int q = p0 + 16 * (j >> 2) + 4 * g + (j & 3);
                    short v = 0;
                    if (q < Tr) {
                        uint2 dq = dd[q];
                        int nq = (int)(dq.y >> 16);
                        if (nq == nb + slot) {
                            u32 w = comp == 0 ? 0x3F80u : comp == 1 ? (dq.x & 0xffffu)
                                  : comp == 2 ? (dq.x >> 16) : (dq.y & 0xffffu);
                            v = (short)w;
                        }
                    }
                    sop[j] = v;
                }
                const int mynd = nb + g;
                const bool ndok = mynd <= nodeL;
                float pv0 = 0.f, pv1 = 0.f, pv2 = 0.f;
#pragma unroll
                for (int tt = 0; tt < 8; ++tt) {
                    bf16x8 mb;
                    mb = *reinterpret_cast<const bf16x8*>(&mop[tt][0]);
                    f32x4 z = {0.f, 0.f, 0.f, 0.f};
                    f32x4 d2 = __builtin_amdgcn_mfma_f32_16x16x32_bf16(sop, mb, z, 0, 0, 0);
                    if (ndok) {
                        float* ap = (mynd == headnode) ? &S.head[wv][tt * 16 + e16] : &S.macc[mynd][tt * 16 + e16];
                        *ap += d2[0];
                    }
                    pv0 += d2[1] * wxv[tt];
                    pv1 += d2[2] * wxv[tt];
                    pv2 += d2[3] * wxv[tt];
                }
#pragma unroll
                for (int o = 8; o > 0; o >>= 1) {
                    pv0 += __shfl_xor(pv0, o);
                    pv1 += __shfl_xor(pv1, o);
                    pv2 += __shfl_xor(pv2, o);
                }
                if (ndok && e16 == 0) {
                    if (mynd == headnode) { phead[0] += pv0; phead[1] += pv1; phead[2] += pv2; }
                    else { S.pacc[mynd][0] += pv0; S.pacc[mynd][1] += pv1; S.pacc[mynd][2] += pv2; }
                }
            }
        }
        if (lane == 0 && headnode >= 0) {
            S.head[wv][D + 0] = phead[0]; S.head[wv][D + 1] = phead[1]; S.head[wv][D + 2] = phead[2];
        }
        __syncthreads();
        if (t < D + 3) {
            for (int w = 0; w < KB_THREADS / 64; ++w) {
                int hn = S.headnode[w];
                if (hn >= 0) {
                    if (t < D) S.macc[hn][t] += S.head[w][t];
                    else S.pacc[hn][t - D] += S.head[w][t];
                }
            }
        }
        __syncthreads();
    }

    if (t < nnodes * 3) {
        int n = t / 3, cpt = t - n * 3;
        int nd = nb0 + n;
        out_pos[(size_t)nd * 3 + cpt] = f2bf(bf2f(pos[nd * 3 + cpt]) + S.pacc[n][cpt]);
    }
    {
        int rt = wv & 1;
        int n = nb0 + rt * 16 + e16;
        int nn = n < N ? n : N - 1;
        bf16x8 af[8];
#pragma unroll
        for (int ks = 0; ks < 4; ++ks)
            af[ks] = *reinterpret_cast<const bf16x8*>(h + (size_t)nn * D + ks * 32 + g * 8);
#pragma unroll
        for (int ks = 0; ks < 4; ++ks) {
            const float* mp = &S.macc[rt * 16 + e16][ks * 32 + g * 8];
            u32 w0 = pack2(mp[0], mp[1]), w1 = pack2(mp[2], mp[3]);
            u32 w2 = pack2(mp[4], mp[5]), w3 = pack2(mp[6], mp[7]);
            uint4 wv4 = make_uint4(w0, w1, w2, w3);
            af[4 + ks] = *reinterpret_cast<bf16x8*>(&wv4);
        }
#pragma unroll
        for (int q = 0; q < 2; ++q) {
            int ct = (wv >> 1) * 2 + q;
            f32x4 acc = {0.f, 0.f, 0.f, 0.f};
#pragma unroll
            for (int ks = 0; ks < 8; ++ks) {
                bf16x8 bfr = *reinterpret_cast<const bf16x8*>(whT + (size_t)(ct * 16 + e16) * 2 * D + ks * 32 + g * 8);
                acc = __builtin_amdgcn_mfma_f32_16x16x32_bf16(af[ks], bfr, acc, 0, 0, 0);
            }
#pragma unroll
            for (int r = 0; r < 4; ++r) {
                int ln = rt * 16 + g * 4 + r;
                int nd = nb0 + ln;
                if (ln < nnodes) {
                    int c = ct * 16 + e16;
                    float hv = bf2f(h[(size_t)nd * D + c]);
                    out_h[(size_t)nd * D + c] = f2bf(hv + silu_f(acc[r]));
                }
            }
        }
    }
}

extern "C" void kernel_launch(void* const* d_in, const int* in_sizes, int n_in,
                              void* d_out, int out_size, void* d_ws, size_t ws_size,
                              hipStream_t stream) {
    const u16* h   = (const u16*)d_in[0];
    const u16* pos = (const u16*)d_in[1];
    const u16* ea  = (const u16*)d_in[2];
    const int* src = (const int*)d_in[3];
    const int* dst = (const int*)d_in[4];
    const u16* we  = (const u16*)d_in[5];
    const u16* wx  = (const u16*)d_in[6];
    const u16* wh  = (const u16*)d_in[7];
    int N = in_sizes[0] / D;
    int E = in_sizes[3];
    u16* out_h = (u16*)d_out;
    u16* out_pos = out_h + (size_t)N * D;

    int G = 128;
    int CH = (E + G - 1) / G;
    int NPB = (N + NB - 1) / NB;
    int nGemm = (N + 63) / 64;

    char* ws = (char*)d_ws;
    size_t off = 0;
    auto take = [&](size_t bytes) { char* p = ws + off; off += (bytes + 255) & ~(size_t)255; return p; };
    uint4* rec_g = (uint4*)take((size_t)G * CH * 16);
    u32* tab = (u32*)take((size_t)NB * G * 4);
    u16* Bg = (u16*)take((size_t)N * D * 2);
    u16* weAT = (u16*)take((size_t)D * D * 2);
    u16* whT = (u16*)take((size_t)2 * D * D * 2);

    k_part<<<G + nGemm + 2, KA_THREADS, 0, stream>>>(h, src, dst, ea, we, wh, rec_g, tab, Bg, weAT, whT,
                                                     N, E, G, CH, NPB, nGemm);
    k_main<<<NB, KB_THREADS, sizeof(KbSmem), stream>>>(h, pos, we, wx, rec_g, tab, Bg, weAT, whT,
                                                      out_h, out_pos, N, G, CH, NPB);
}
```

```cpp
#include <hip/hip_runtime.h>
#include <hip/hip_bf16.h>
#include <stdint.h>


typedef unsigned short u16;
typedef unsigned int u32;
typedef __attribute__((ext_vector_type(8))) short bf16x8;
typedef __attribute__((ext_vector_type(4))) float f32x4;

#define D 128
#define NB 1024
#define NPBMAX 16
#define KA_THREADS 512
#define KA_ITERS 8
#define KB_THREADS 256
#define KB_WAVES (KB_THREADS / 64)
#define CAP 512
#define AST (D + 4)

__device__ __forceinline__ float bf2f(u16 v) { return __uint_as_float(((u32)v) << 16); }
__device__ __forceinline__ u16 f2bf(float f) {
    __hip_bfloat16 b = __float2bfloat16(f);
    return *reinterpret_cast<u16*>(&b);
}
__device__ __forceinline__ float silu_f(float x) {
    float e = __builtin_amdgcn_exp2f(x * -1.4426950408889634f);
    return x * __builtin_amdgcn_rcpf(1.0f + e);
}
__device__ __forceinline__ u32 pack2(float a, float b) {
    return (u32)f2bf(a) | ((u32)f2bf(b) << 16);
}
__device__ __forceinline__ bf16x8 as_bf16x8(uint4 v) { return *reinterpret_cast<bf16x8*>(&v); }

__global__ void __launch_bounds__(KA_THREADS)
k_part(const u16* __restrict__ h, const int* __restrict__ src, const int* __restrict__ dst,
       const u16* __restrict__ ea, const u16* __restrict__ we, const u16* __restrict__ wh,
       uint4* __restrict__ rec_g, u32* __restrict__ tab, float* __restrict__ Ag, u16* __restrict__ Bg,
       u16* __restrict__ whT,
       int N, int E, int G, int CH, int NPB, int nGemm) {
    __shared__ __align__(16) u16 smem[2 * D * (D + 8)];
    __shared__ int wsum[KA_THREADS / 64];
    int t = threadIdx.x;
    int bid = blockIdx.x;
    if (bid < G) {
        int* hist = (int*)smem;
        hist[t] = 0; hist[t + KA_THREADS] = 0;
        __syncthreads();
        int e0 = bid * CH;
        int e1 = min(E, e0 + CH);
        int sv[KA_ITERS], bv[KA_ITERS], rk[KA_ITERS], lo[KA_ITERS];
        uint2 eav[KA_ITERS];
#pragma unroll
        for (int i = 0; i < KA_ITERS; ++i) {
            int e = e0 + t + i * KA_THREADS;
            bv[i] = -1;
            if (e < e1) {
                int d = dst[e];
                sv[i] = src[e];
                eav[i] = *reinterpret_cast<const uint2*>(ea + (size_t)e * 4);
                int b = d / NPB;
                bv[i] = b;
                lo[i] = d - b * NPB;
            }
        }
#pragma unroll
        for (int i = 0; i < KA_ITERS; ++i)
            if (bv[i] >= 0) rk[i] = atomicAdd(&hist[bv[i]], 1);
        __syncthreads();
        int cnt0 = hist[2 * t], cnt1 = hist[2 * t + 1];
        int cnt = cnt0 + cnt1;
        int lane = t & 63, wv = t >> 6;
        int x = cnt;
#pragma unroll
        for (int o = 1; o < 64; o <<= 1) {
            int y = __shfl_up(x, o);
            if (lane >= o) x += y;
        }
        if (lane == 63) wsum[wv] = x;
        __syncthreads();
        int base = 0;
        for (int w = 0; w < wv; ++w) base += wsum[w];
        int off = base + x - cnt;
        hist[2 * t] = off; hist[2 * t + 1] = off + cnt0;
        tab[(size_t)(2 * t) * G + bid] = ((u32)off << 16) | (u32)cnt0;
        tab[(size_t)(2 * t + 1) * G + bid] = ((u32)(off + cnt0) << 16) | (u32)cnt1;
        __syncthreads();
#pragma unroll
        for (int i = 0; i < KA_ITERS; ++i) {
            if (bv[i] >= 0) {
                int e = e0 + t + i * KA_THREADS;
                int p = hist[bv[i]] + rk[i];
                uint4 r;
                r.x = (u32)sv[i]; r.y = eav[i].x; r.z = eav[i].y;
                r.w = ((u32)e << 8) | (u32)lo[i];
                rec_g[(size_t)bid * CH + p] = r;
            }
        }
        return;
    }
    bid -= G;
    if (bid < nGemm) {
        {
            for (int i = t; i < 2 * D * D; i += KA_THREADS) {
                int kk = i >> 7, c = i & 127;
                int half = kk >> 7, k = kk & 127;
                smem[(half * D + c) * (D + 8) + k] = we[(size_t)kk * D + c];
            }
        }
        __syncthreads();
        int lane = t & 63, wv = t >> 6;
        int e16 = lane & 15, g = lane >> 4;
        int rt = wv & 3;
        int half = wv >> 2;
        int n = bid * 64 + rt * 16 + e16;
        int nn = n < N ? n : N - 1;
        bf16x8 af[4];
#pragma unroll
        for (int ks = 0; ks < 4; ++ks)
            af[ks] = *reinterpret_cast<const bf16x8*>(h + (size_t)nn * D + ks * 32 + g * 8);
#pragma unroll
        for (int ct = 0; ct < 8; ++ct) {
            int c0 = ct * 16;
            f32x4 acc = {0.f, 0.f, 0.f, 0.f};
#pragma unroll
            for (int ks = 0; ks < 4; ++ks) {
                bf16x8 bfr = *reinterpret_cast<const bf16x8*>(&smem[(half * D + c0 + e16) * (D + 8) + ks * 32 + g * 8]);
                acc = __builtin_amdgcn_mfma_f32_16x16x32_bf16(af[ks], bfr, acc, 0, 0, 0);
            }
#pragma unroll
            for (int r = 0; r < 4; ++r) {
                int row = bid * 64 + rt * 16 + g * 4 + r;
                if (row < N) {
                    if (half == 0) Ag[(size_t)row * D + c0 + e16] = acc[r];
                    else Bg[(size_t)row * D + c0 + e16] = f2bf(acc[r]);
                }
            }
        }
        return;
    }
    bid -= nGemm;
    {
        int i = bid * KA_THREADS + t;
        if (i < 2 * D * D) {
            int k = i >> 7, c = i & 127;
            whT[(size_t)c * 2 * D + k] = wh[i];
        }
    }
}

struct KbSmem {
    uint4 bufA[CAP];
    uint4 bufB[CAP];
    float Al[NPBMAX][AST];
    float macc[NPBMAX][AST];
    float head[KB_WAVES][AST];
    float pacc[NPBMAX][4];
    uint4 w5s[8][16];
    float wxs[D];
    int segoff[512 + 1];
    int ncnt[NPBMAX + 1];
    int nstart[NPBMAX + 1];
    int headnode[KB_WAVES];
    int wsum[KB_WAVES];
};

__global__ void __launch_bounds__(KB_THREADS, 3)
k_main(const u16* __restrict__ h, const u16* __restrict__ pos,
       const u16* __restrict__ we, const u16* __restrict__ wx,
       const uint4* __restrict__ rec_g, const u32* __restrict__ tab,
       const float* __restrict__ Ag, const u16* __restrict__ Bg,
       const u16* __restrict__ whT,
       u16* __restrict__ out_h, u16* __restrict__ out_pos,
       int N, int G, int CH, int NPB) {
    extern __shared__ __align__(16) char smem_raw[];
    KbSmem& S = *reinterpret_cast<KbSmem*>(smem_raw);
    const int t = threadIdx.x, lane = t & 63, wv = t >> 6;
    const int e16 = lane & 15, g = lane >> 4;
    const int b = blockIdx.x;
    const int nb0 = b * NPB;
    const int nnodes = min(NPB, N - nb0);
    if (nnodes <= 0) return;

    u32 tv = (t < G) ? tab[(size_t)b * G + t] : 0u;
    for (int i = t; i < NPBMAX * D; i += KB_THREADS) {
        int n = i >> 7, c = i & 127;
        S.Al[n][c] = (n < nnodes) ? Ag[(size_t)(nb0 + n) * D + c] : 0.f;
    }
    if (t < 8 * 16) {
        int tt = t >> 4, c16 = t & 15, c = tt * 16 + c16;
        u32 v[8];
#pragma unroll
        for (int k = 0; k < 8; ++k) {
            int kr = (k == 5) ? 0 : k;
            v[k] = (k < 6) ? (u32)we[(size_t)(256 + kr) * D + c] : 0u;
        }
        S.w5s[tt][c16] = make_uint4(v[0] | (v[1] << 16), v[2] | (v[3] << 16), v[4] | (v[5] << 16), 0u);
    }
    if (t < D) S.wxs[t] = bf2f(wx[t]);
    for (int i = t; i < NPBMAX * AST; i += KB_THREADS) (&S.macc[0][0])[i] = 0.f;
    if (t < NPBMAX * 4) (&S.pacc[0][0])[t] = 0.f;
    {
        int scnt = (int)(tv & 0xffffu);
        int x = scnt;
#pragma unroll
        for (int o = 1; o < 64; o <<= 1) {
            int y = __shfl_up(x, o);
            if (lane >= o) x += y;
        }
        if (lane == 63) S.wsum[wv] = x;
        __syncthreads();
        int base = 0;
        for (int w = 0; w < wv; ++w) base += S.wsum[w];
        S.segoff[t] = base + x - scnt;
        if (t == KB_THREADS - 1) S.segoff[KB_THREADS] = base + x;
    }
    __syncthreads();
    const int T = S.segoff[KB_THREADS];

    for (int base = 0; base < T; base += CAP) {
        const int Tr = min(CAP, T - base);
        {
            int c = t >> 1, sub = t & 1;
            if (c < G) {
                int so = S.segoff[c], sc = S.segoff[c + 1] - so;
                u32 tvc = tab[(size_t)b * G + c];
                int st = (int)(tvc >> 16);
                for (int i = sub; i < sc; i += 2) {
                    int fp = so + i - base;
                    if (fp >= 0 && fp < CAP) S.bufA[fp] = rec_g[(size_t)c * CH + st + i];
                }
            }
        }
        if (t <= NPBMAX) S.ncnt[t] = 0;
        __syncthreads();
        int myrank[2], mynode[2];
#pragma unroll
        for (int i = 0; i < 2; ++i) {
            int p = t + i * KB_THREADS;
            mynode[i] = -1;
            if (p < Tr) {
                mynode[i] = (int)(S.bufA[p].w & 0xffu);
                myrank[i] = atomicAdd(&S.ncnt[mynode[i]], 1);
            }
        }
        __syncthreads();
        if (wv == 0) {
            int c = (lane < NPBMAX) ? S.ncnt[lane] : 0;
            int x = c;
#pragma unroll
            for (int o = 1; o < 64; o <<= 1) {
                int y = __shfl_up(x, o);
                if (lane >= o) x += y;
            }
            if (lane < NPBMAX) S.nstart[lane] = x - c;
            if (lane == NPBMAX - 1) S.nstart[NPBMAX] = x;
        }
        __syncthreads();
#pragma unroll
        for (int i = 0; i < 2; ++i) {
            int p = t + i * KB_THREADS;
            if (mynode[i] >= 0) S.bufB[S.nstart[mynode[i]] + myrank[i]] = S.bufA[p];
        }
        __syncthreads();
        uint2 ddv[2];
        int ddp[2];
#pragma unroll
        for (int i = 0; i < 2; ++i) {
            int p = t + i * KB_THREADS;
            ddp[i] = -1;
            if (p < Tr) {
                uint4 r = S.bufB[p];
                int node = (int)(r.w & 0xffu);
                u32 key = r.w >> 8;
                int sidx = (int)r.x;
                int nd = nb0 + node;
                float dx = bf2f(pos[nd * 3 + 0]) - bf2f(pos[sidx * 3 + 0]);
                float dy = bf2f(pos[nd * 3 + 1]) - bf2f(pos[sidx * 3 + 1]);
                float dz = bf2f(pos[nd * 3 + 2]) - bf2f(pos[sidx * 3 + 2]);
                int s0 = S.nstart[node], s1 = S.nstart[node + 1];
                int rank = 0;
                for (int q = s0; q < s1; ++q) rank += ((S.bufB[q].w >> 8) < key) ? 1 : 0;
                u16 bx = f2bf(dx), by = f2bf(dy), bz = f2bf(dz);
                float d2 = dx * dx + dy * dy + dz * dz;
                u16 d2h = f2bf(d2);
                u16 d2l = f2bf(d2 - bf2f(d2h));
                uint4 o;
                o.x = r.x;
                o.y = r.y; o.z = r.z;
                o.w = (u32)d2h | ((u32)d2l << 16);
                S.bufA[s0 + rank] = o;
                ddp[i] = s0 + rank;
                ddv[i].x = (u32)bx | ((u32)by << 16);
                ddv[i].y = (u32)bz | ((u32)node << 16);
            }
        }
        __syncthreads();
        {
            uint2* ddw = reinterpret_cast<uint2*>(S.bufB);
#pragma unroll
            for (int i = 0; i < 2; ++i)
                if (ddp[i] >= 0) ddw[ddp[i]] = ddv[i];
        }
        __syncthreads();

        int lq = lane;
        asm volatile("" : "+v"(lq));
        bf16x8 selLo, selHi;
#pragma unroll
        for (int j = 0; j < 8; ++j) {
            int k = 8 * (lq >> 4) + j;
            selLo[j] = (k == (lq & 15)) ? (short)0x3F80 : (short)0;
            selHi[j] = (k == (lq & 15) + 16) ? (short)0x3F80 : (short)0;
        }
        const uint2* dd = reinterpret_cast<const uint2*>(S.bufB);
        const int nst = (Tr + 31) >> 5;
        const int st0 = (wv * nst) / KB_WAVES;
        const int st1 = ((wv + 1) * nst) / KB_WAVES;
        int headnode = -1;
        if (st0 < st1) headnode = (int)(dd[st0 * 32].y >> 16);
        if (lane == 0) S.headnode[wv] = headnode;
        for (int i = lane; i < D; i += 64) S.head[wv][i] = 0.f;
        float phead0 = 0.f, phead1 = 0.f, phead2 = 0.f;

        auto issue = [&](int st, bf16x8 (&gat)[2][4]) {
#pragma unroll
            for (int T2 = 0; T2 < 2; ++T2) {
                int p = st * 32 + T2 * 16 + e16;
                int sidx = (p < Tr) ? (int)S.bufA[p].x : 0;
                const u16* brow = Bg + (size_t)sidx * D + g * 8;
#pragma unroll
                for (int tp = 0; tp < 4; ++tp)
                    gat[T2][tp] = *reinterpret_cast<const bf16x8*>(brow + tp * 32);
            }
        };
        bf16x8 gcur[2][4];
        for (int st = st0; st < st1; ++st) {
            issue(st, gcur);
            const int p0 = st * 32;
            bf16x8 x5[2];
            int arow[2][4];
#pragma unroll
            for (int T2 = 0; T2 < 2; ++T2) {
                int p = p0 + T2 * 16 + e16;
                uint4 xu = make_uint4(0u, 0u, 0u, 0u);
                if (p < Tr && g == 0) {
                    uint4 r = S.bufA[p];
                    xu.x = (r.w & 0xffffu) | (r.y << 16);
                    xu.y = (r.y >> 16) | (r.z << 16);
                    xu.z = (r.z >> 16) | (r.w & 0xffff0000u);
                }
                x5[T2] = as_bf16x8(xu);
#pragma unroll
                for (int r4 = 0; r4 < 4; ++r4) {
                    int pe = p0 + T2 * 16 + g * 4 + r4;
                    arow[T2][r4] = (int)(dd[pe < Tr ? pe : 0].y >> 16) * AST + e16;
                }
            }
            const int node0 = (int)(dd[p0].y >> 16);
            const int nodeL = (int)(dd[min(p0 + 31, Tr - 1)].y >> 16);
            const int slot = e16 >> 2, comp = e16 & 3;
            auto build_s = [&](int nb) {
                bf16x8 sop;
#pragma unroll
                for (int j = 0; j < 8; ++j) {
                    int q = p0 + 16 * (j >> 2) + 4 * g + (j & 3);
                    u32 w = 0u;
                    if (q < Tr) {
                        uint2 dq = dd[q];
                        if ((int)(dq.y >> 16) == nb + slot)
                            w = comp == 0 ? 0x3F80u : comp == 1 ? (dq.x & 0xffffu)
                              : comp == 2 ? (dq.x >> 16) : (dq.y & 0xffffu);
                    }
                    sop[j] = (short)w;
                }
                return sop;
            };
            for (int nb = node0; nb <= nodeL; nb += 4) {
            bf16x8 sop = build_s(nb);
            const int mynd = nb + g;
            const bool ndok = mynd <= nodeL;
            float* mbase = (mynd == headnode) ? &S.head[wv][e16] : &S.macc[ndok ? mynd : 0][e16];
            float pv0 = 0.f, pv1 = 0.f, pv2 = 0.f;
#pragma unroll
            for (int tp = 0; tp < 4; ++tp) {
#pragma unroll
                for (int u = 0; u < 2; ++u) {
                    const int tt = tp * 2 + u;
                    __builtin_amdgcn_sched_barrier(0);
                    bf16x8 w5 = as_bf16x8(g == 0 ? S.w5s[tt][e16] : make_uint4(0u, 0u, 0u, 0u));
                    float m[2][4];
#pragma unroll
                    for (int T2 = 0; T2 < 2; ++T2) {
                        f32x4 acc;
#pragma unroll
                        for (int r4 = 0; r4 < 4; ++r4) acc[r4] = (&S.Al[0][0])[arow[T2][r4] + tt * 16];
                        acc = __builtin_amdgcn_mfma_f32_16x16x32_bf16(gcur[T2][tp], u ? selHi : selLo, acc, 0, 0, 0);
                        acc = __builtin_amdgcn_mfma_f32_16x16x32_bf16(x5[T2], w5, acc, 0, 0, 0);
#pragma unroll
                        for (int r4 = 0; r4 < 4; ++r4) m[T2][r4] = silu_f(acc[r4]);
                    }
                    uint4 mu = make_uint4(pack2(m[0][0], m[0][1]), pack2(m[0][2], m[0][3]),
                                          pack2(m[1][0], m[1][1]), pack2(m[1][2], m[1][3]));
                    bf16x8 mb = as_bf16x8(mu);
                    f32x4 z = {0.f, 0.f, 0.f, 0.f};
                    f32x4 d2 = __builtin_amdgcn_mfma_f32_16x16x32_bf16(sop, mb, z, 0, 0, 0);
                    float wxc = S.wxs[tt * 16 + e16];
                    if (ndok) mbase[tt * 16] += d2[0];
                    pv0 += d2[1] * wxc;
                    pv1 += d2[2] * wxc;
                    pv2 += d2[3] * wxc;
                }
            }
#pragma unroll
            for (int o = 8; o > 0; o >>= 1) {
                pv0 += __shfl_xor(pv0, o);
                pv1 += __shfl_xor(pv1, o);
                pv2 += __shfl_xor(pv2, o);
            }
            if (ndok && e16 == 0) {
                if (mynd == headnode) { phead0 += pv0; phead1 += pv1; phead2 += pv2; }
                else { S.pacc[mynd][0] += pv0; S.pacc[mynd][1] += pv1; S.pacc[mynd][2] += pv2; }
            }
            }
        }
        if (lane == 0 && headnode >= 0) {
            S.head[wv][D + 0] = phead0; S.head[wv][D + 1] = phead1; S.head[wv][D + 2] = phead2;
        }
        __syncthreads();
        if (t < D + 3) {
            for (int w = 0; w < KB_WAVES; ++w) {
                int hn = S.headnode[w];
                if (hn >= 0) {
                    if (t < D) S.macc[hn][t] += S.head[w][t];
                    else S.pacc[hn][t - D] += S.head[w][t];
                }
            }
        }
        __syncthreads();
    }

    int tq = threadIdx.x;
    asm volatile("" : "+v"(tq));
    {
    const int t = tq, lane = tq & 63, wv = tq >> 6, e16 = lane & 15, g = lane >> 4;
    if (t < nnodes * 3) {
        int n = t / 3, cpt = t - n * 3;
        int nd = nb0 + n;
        out_pos[(size_t)nd * 3 + cpt] = f2bf(bf2f(pos[nd * 3 + cpt]) + S.pacc[n][cpt]);
    }
    {
        int rt = 0;
        int n = nb0 + e16;
        int nn = n < N ? n : N - 1;
        bf16x8 af[8];
#pragma unroll
        for (int ks = 0; ks < 4; ++ks)
            af[ks] = *reinterpret_cast<const bf16x8*>(h + (size_t)nn * D + ks * 32 + g * 8);
#pragma unroll
        for (int ks = 0; ks < 4; ++ks) {
            const float* mp = &S.macc[rt * 16 + e16][ks * 32 + g * 8];
            af[4 + ks] = as_bf16x8(make_uint4(pack2(mp[0], mp[1]), pack2(mp[2], mp[3]),
                                              pack2(mp[4], mp[5]), pack2(mp[6], mp[7])));
        }
#pragma unroll
        for (int q = 0; q < 2; ++q) {
            int ct = wv * 2 + q;
            f32x4 acc = {0.f, 0.f, 0.f, 0.f};
#pragma unroll
            for (int ks = 0; ks < 8; ++ks) {
                bf16x8 bfr = *reinterpret_cast<const bf16x8*>(whT + (size_t)(ct * 16 + e16) * 2 * D + ks * 32 + g * 8);
                acc = __builtin_amdgcn_mfma_f32_16x16x32_bf16(af[ks], bfr, acc, 0, 0, 0);
            }
#pragma unroll
            for (int r = 0; r < 4; ++r) {
                int ln = rt * 16 + g * 4 + r;
                int nd = nb0 + ln;
                if (ln < nnodes) {
                    int c = ct * 16 + e16;
                    float hv = bf2f(h[(size_t)nd * D + c]);
                    out_h[(size_t)nd * D + c] = f2bf(hv + silu_f(acc[r]));
                }
            }
        }
    }
}
}

extern "C" void kernel_launch(void* const* d_in, const int* in_sizes, int n_in,
                              void* d_out, int out_size, void* d_ws, size_t ws_size,
                              hipStream_t stream) {
    const u16* h   = (const u16*)d_in[0];
    const u16* pos = (const u16*)d_in[1];
    const u16* ea  = (const u16*)d_in[2];
    const int* src = (const int*)d_in[3];
    const int* dst = (const int*)d_in[4];
    const u16* we  = (const u16*)d_in[5];
    const u16* wx  = (const u16*)d_in[6];
    const u16* wh  = (const u16*)d_in[7];
    int N = in_sizes[0] / D;
    int E = in_sizes[3];
    u16* out_h = (u16*)d_out;
    u16* out_pos = out_h + (size_t)N * D;

    int G = 128;
    int CH = (E + G - 1) / G;
    int NPB = (N + NB - 1) / NB;
    int nGemm = (N + 63) / 64;
    int nT = (2 * D * D + KA_THREADS - 1) / KA_THREADS;

    char* ws = (char*)d_ws;
    size_t off = 0;
    auto take = [&](size_t bytes) { char* p = ws + off; off += (bytes + 255) & ~(size_t)255; return p; };
    uint4* rec_g = (uint4*)take((size_t)G * CH * 16);
    u32* tab = (u32*)take((size_t)NB * G * 4);
    float* Ag = (float*)take((size_t)N * D * 4);
    u16* Bg = (u16*)take((size_t)N * D * 2);
    u16* whT = (u16*)take((size_t)2 * D * D * 2);

    k_part<<<G + nGemm + nT, KA_THREADS, 0, stream>>>(h, src, dst, ea, we, wh, rec_g, tab, Ag, Bg, whT,
                                                      N, E, G, CH, NPB, nGemm);
    k_main<<<NB, KB_THREADS, sizeof(KbSmem), stream>>>(h, pos, we, wx, rec_g, tab, Ag, Bg, whT,
                                                      out_h, out_pos, N, G, CH, NPB);
}
```

```cpp
#include <hip/hip_runtime.h>
#include <hip/hip_bf16.h>
#include <stdint.h>


typedef unsigned short u16;
typedef unsigned int u32;
typedef __attribute__((ext_vector_type(8))) short bf16x8;
typedef __attribute__((ext_vector_type(4))) float f32x4;

#define D 128
#define NB 768
#define NPBMAX 32
#define KA_THREADS 512
#define KA_ITERS 8
#define KB_THREADS 256
#define KB_WAVES (KB_THREADS / 64)
#define CAP 768
#define RPT (CAP / KB_THREADS)
#define AST (D + 4)

__device__ __forceinline__ float bf2f(u16 v) { return __uint_as_float(((u32)v) << 16); }
__device__ __forceinline__ u16 f2bf(float f) {
    __hip_bfloat16 b = __float2bfloat16(f);
    return *reinterpret_cast<u16*>(&b);
}
__device__ __forceinline__ float silu_f(float x) {
    float e = __builtin_amdgcn_exp2f(x * -1.4426950408889634f);
    return x * __builtin_amdgcn_rcpf(1.0f + e);
}
__device__ __forceinline__ u32 pack2(float a, float b) {
    return (u32)f2bf(a) | ((u32)f2bf(b) << 16);
}
__device__ __forceinline__ bf16x8 as_bf16x8(uint4 v) { return *reinterpret_cast<bf16x8*>(&v); }

__global__ void __launch_bounds__(KA_THREADS)
k_part(const u16* __restrict__ h, const int* __restrict__ src, const int* __restrict__ dst,
       const u16* __restrict__ ea, const u16* __restrict__ we, const u16* __restrict__ wh,
       uint4* __restrict__ rec_g, u32* __restrict__ tab, float* __restrict__ Ag, u16* __restrict__ Bg,
       u16* __restrict__ whT,
       int N, int E, int G, int CH, int NPB, int nGemm) {
    __shared__ __align__(16) u16 smem[2 * D * (D + 8)];
    __shared__ int wsum[KA_THREADS / 64];
    int t = threadIdx.x;
    int bid = blockIdx.x;
    if (bid < G) {
        int* hist = (int*)smem;
        hist[t] = 0; hist[t + KA_THREADS] = 0;
        __syncthreads();
        int e0 = bid * CH;
        int e1 = min(E, e0 + CH);
        int sv[KA_ITERS], bv[KA_ITERS], rk[KA_ITERS], lo[KA_ITERS];
        uint2 eav[KA_ITERS];
#pragma unroll
        for (int i = 0; i < KA_ITERS; ++i) {
            int e = e0 + t + i * KA_THREADS;
            bv[i] = -1;
            if (e < e1) {
                int d = dst[e];
                sv[i] = src[e];
                eav[i] = *reinterpret_cast<const uint2*>(ea + (size_t)e * 4);
                int b = d / NPB;
                bv[i] = b;
                lo[i] = d - b * NPB;
            }
        }
#pragma unroll
        for (int i = 0; i < KA_ITERS; ++i)
            if (bv[i] >= 0) rk[i] = atomicAdd(&hist[bv[i]], 1);
        __syncthreads();
        int cnt0 = hist[2 * t], cnt1 = hist[2 * t + 1];
        int cnt = cnt0 + cnt1;
        int lane = t & 63, wv = t >> 6;
        int x = cnt;
#pragma unroll
        for (int o = 1; o < 64; o <<= 1) {
            int y = __shfl_up(x, o);
            if (lane >= o) x += y;
        }
        if (lane == 63) wsum[wv] = x;
        __syncthreads();
        int base = 0;
        for (int w = 0; w < wv; ++w) base += wsum[w];
        int off = base + x - cnt;
        hist[2 * t] = off; hist[2 * t + 1] = off + cnt0;
        if (2 * t < NB) tab[(size_t)(2 * t) * G + bid] = ((u32)off << 16) | (u32)cnt0;
        if (2 * t + 1 < NB) tab[(size_t)(2 * t + 1) * G + bid] = ((u32)(off + cnt0) << 16) | (u32)cnt1;
        __syncthreads();
#pragma unroll
        for (int i = 0; i < KA_ITERS; ++i) {
            if (bv[i] >= 0) {
                int e = e0 + t + i * KA_THREADS;
                int p = hist[bv[i]] + rk[i];
                uint4 r;
                r.x = (u32)sv[i]; r.y = eav[i].x; r.z = eav[i].y;
                r.w = ((u32)e << 8) | (u32)lo[i];
                rec_g[(size_t)bid * CH + p] = r;
            }
        }
        return;
    }
    bid -= G;
    if (bid < nGemm) {
        {
            uint4 wv8[8];
#pragma unroll
            for (int i = 0; i < 8; ++i) {
                int v = t + i * KA_THREADS;
                wv8[i] = *reinterpret_cast<const uint4*>(we + (size_t)v * 8);
            }
#pragma unroll
            for (int i = 0; i < 8; ++i) {
                int v = t + i * KA_THREADS;
                int kk = v >> 4, c0 = (v & 15) * 8;
                int half = kk >> 7, k = kk & 127;
                u16* dstp = &smem[(half * D + c0) * (D + 8) + k];
                dstp[0 * (D + 8)] = (u16)(wv8[i].x & 0xffffu); dstp[1 * (D + 8)] = (u16)(wv8[i].x >> 16);
                dstp[2 * (D + 8)] = (u16)(wv8[i].y & 0xffffu); dstp[3 * (D + 8)] = (u16)(wv8[i].y >> 16);
                dstp[4 * (D + 8)] = (u16)(wv8[i].z & 0xffffu); dstp[5 * (D + 8)] = (u16)(wv8[i].z >> 16);
                dstp[6 * (D + 8)] = (u16)(wv8[i].w & 0xffffu); dstp[7 * (D + 8)] = (u16)(wv8[i].w >> 16);
            }
        }
        __syncthreads();
        int lane = t & 63, wv = t >> 6;
        int e16 = lane & 15, g = lane >> 4;
        int rt = wv & 3;
        int half = wv >> 2;
        int n = bid * 64 + rt * 16 + e16;
        int nn = n < N ? n : N - 1;
        bf16x8 af[4];
#pragma unroll
        for (int ks = 0; ks < 4; ++ks)
            af[ks] = *reinterpret_cast<const bf16x8*>(h + (size_t)nn * D + ks * 32 + g * 8);
#pragma unroll
        for (int ct = 0; ct < 8; ++ct) {
            int c0 = ct * 16;
            f32x4 acc = {0.f, 0.f, 0.f, 0.f};
#pragma unroll
            for (int ks = 0; ks < 4; ++ks) {
                bf16x8 bfr = *reinterpret_cast<const bf16x8*>(&smem[(half * D + c0 + e16) * (D + 8) + ks * 32 + g * 8]);
                acc = __builtin_amdgcn_mfma_f32_16x16x32_bf16(af[ks], bfr, acc, 0, 0, 0);
            }
#pragma unroll
            for (int r = 0; r < 4; ++r) {
                int row = bid * 64 + rt * 16 + g * 4 + r;
                if (row < N) {
                    if (half == 0) Ag[(size_t)row * D + c0 + e16] = acc[r];
                    else Bg[(size_t)row * D + c0 + e16] = f2bf(acc[r]);
                }
            }
        }
        return;
    }
    bid -= nGemm;
    {
        int i = bid * KA_THREADS + t;
        if (i < 2 * D * D) {
            int k = i >> 7, c = i & 127;
            whT[(size_t)c * 2 * D + k] = wh[i];
        }
    }
}

struct KbSmem {
    uint4 bufA[CAP];
    uint4 bufB[CAP];
    float Al[NPBMAX][AST];
    float macc[NPBMAX][AST];
    float head[KB_WAVES][AST];
    float pacc[NPBMAX][4];
    float posl[NPBMAX][4];
    uint4 w5s[8][16];
    float wxs[D];
    uint2 wdd[KB_WAVES][16];
    int segoff[KB_THREADS + 1];
    int ncnt[NPBMAX + 1];
    int nstart[NPBMAX + 1];
    int headnode[KB_WAVES];
    int wsum[KB_WAVES];
};

__global__ void __launch_bounds__(KB_THREADS, 3)
k_main(const u16* __restrict__ h, const u16* __restrict__ pos,
       const u16* __restrict__ we, const u16* __restrict__ wx,
       const uint4* __restrict__ rec_g, const u32* __restrict__ tab,
       const float* __restrict__ Ag, const u16* __restrict__ Bg,
       const u16* __restrict__ whT,
       u16* __restrict__ out_h, u16* __restrict__ out_pos,
       int N, int G, int CH, int NPB) {
    extern __shared__ __align__(16) char smem_raw[];
    KbSmem& S = *reinterpret_cast<KbSmem*>(smem_raw);
    const int t = threadIdx.x, lane = t & 63, wv = t >> 6;
    const int e16 = lane & 15, g = lane >> 4;
    const int b = blockIdx.x;
    const int nb0 = b * NPB;
    const int nnodes = min(NPB, N - nb0);
    if (nnodes <= 0) return;

    u32 tv = (t < G) ? tab[(size_t)b * G + t] : 0u;
    for (int i = t; i < NPBMAX * D; i += KB_THREADS) {
        int n = i >> 7, c = i & 127;
        S.Al[n][c] = (n < nnodes) ? Ag[(size_t)(nb0 + n) * D + c] : 0.f;
    }
    if (t < 8 * 16) {
        int tt = t >> 4, c16 = t & 15, c = tt * 16 + c16;
        u32 v[6];
#pragma unroll
        for (int k = 0; k < 6; ++k) {
            int kr = (k == 5) ? 0 : k;
            v[k] = (u32)we[(size_t)(256 + kr) * D + c];
        }
        S.w5s[tt][c16] = make_uint4(v[0] | (v[1] << 16), v[2] | (v[3] << 16), v[4] | (v[5] << 16), 0u);
    }
    if (t < D) S.wxs[t] = bf2f(wx[t]);
    if (t < NPBMAX * 4) {
        int n = t >> 2, cpt = t & 3;
        S.posl[n][cpt] = (n < nnodes && cpt < 3) ? bf2f(pos[(nb0 + n) * 3 + cpt]) : 0.f;
        S.pacc[n][cpt] = 0.f;
    }
    for (int i = t; i < NPBMAX * AST; i += KB_THREADS) (&S.macc[0][0])[i] = 0.f;
    {
        int scnt = (int)(tv & 0xffffu);
        int x = scnt;
#pragma unroll
        for (int o = 1; o < 64; o <<= 1) {
            int y = __shfl_up(x, o);
            if (lane >= o) x += y;
        }
        if (lane == 63) S.wsum[wv] = x;
        __syncthreads();
        int base = 0;
        for (int w = 0; w < wv; ++w) base += S.wsum[w];
        S.segoff[t] = base + x - scnt;
        if (t == KB_THREADS - 1) S.segoff[KB_THREADS] = base + x;
    }
    __syncthreads();
    const int T = S.segoff[KB_THREADS];

    for (int base = 0; base < T; base += CAP) {
        const int Tr = min(CAP, T - base);
        {
            int c = t >> 1, sub = t & 1;
            if (c < G) {
                int so = S.segoff[c], sc = S.segoff[c + 1] - so;
                u32 tvc = tab[(size_t)b * G + c];
                int st = (int)(tvc >> 16);
                for (int i = sub; i < sc; i += 2) {
                    int fp = so + i - base;
                    if (fp >= 0 && fp < CAP) S.bufA[fp] = rec_g[(size_t)c * CH + st + i];
                }
            }
        }
        if (t <= NPBMAX) S.ncnt[t] = 0;
        __syncthreads();
        int myrank[RPT], mynode[RPT];
#pragma unroll
        for (int i = 0; i < RPT; ++i) {
            int p = t + i * KB_THREADS;
            mynode[i] = -1;
            if (p < Tr) {
                mynode[i] = (int)(S.bufA[p].w & 0xffu);
                myrank[i] = atomicAdd(&S.ncnt[mynode[i]], 1);
            }
        }
        __syncthreads();
        if (wv == 0) {
            int c = (lane < NPBMAX) ? S.ncnt[lane] : 0;
            int x = c;
#pragma unroll
            for (int o = 1; o < 64; o <<= 1) {
                int y = __shfl_up(x, o);
                if (lane >= o) x += y;
            }
            if (lane < NPBMAX) S.nstart[lane] = x - c;
            if (lane == NPBMAX - 1) S.nstart[NPBMAX] = x;
        }
        __syncthreads();
#pragma unroll
        for (int i = 0; i < RPT; ++i) {
            int p = t + i * KB_THREADS;
            if (mynode[i] >= 0) S.bufB[S.nstart[mynode[i]] + myrank[i]] = S.bufA[p];
        }
        __syncthreads();
#pragma unroll
        for (int i = 0; i < RPT; ++i) {
            int p = t + i * KB_THREADS;
            if (p < Tr) {
                uint4 r = S.bufB[p];
                int node = (int)(r.w & 0xffu);
                u32 key = r.w >> 8;
                int s0 = S.nstart[node], s1 = S.nstart[node + 1];
                int rank = 0;
                for (int q = s0; q < s1; ++q) rank += ((S.bufB[q].w >> 8) < key) ? 1 : 0;
                S.bufA[s0 + rank] = r;
            }
        }
        __syncthreads();

        int lq = lane;
        asm volatile("" : "+v"(lq));
        bf16x8 selLo, selHi;
#pragma unroll
        for (int j = 0; j < 8; ++j) {
            int k = 8 * (lq >> 4) + j;
            selLo[j] = (k == (lq & 15)) ? (short)0x3F80 : (short)0;
            selHi[j] = (k == (lq & 15) + 16) ? (short)0x3F80 : (short)0;
        }
        const int nst = (Tr + 15) >> 4;
        const int st0 = (wv * nst) / KB_WAVES;
        const int st1 = ((wv + 1) * nst) / KB_WAVES;
        int headnode = -1;
        if (st0 < st1) headnode = (int)(S.bufA[st0 * 16].w & 0xffu);
        if (lane == 0) S.headnode[wv] = headnode;
        for (int i = lane; i < D; i += 64) S.head[wv][i] = 0.f;
        float phead0 = 0.f, phead1 = 0.f, phead2 = 0.f;

        bf16x8 gcur[4], gnxt[4];
        u32 pcur0, pcur1, pnxt0 = 0, pnxt1 = 0;
        auto issue = [&](int st, bf16x8 (&gat)[4], u32& q0, u32& q1) {
            int p = st * 16 + e16;
            int sidx = (p < Tr) ? (int)S.bufA[p].x : 0;
            const u16* brow = Bg + (size_t)sidx * D + g * 8;
#pragma unroll
            for (int tp = 0; tp < 4; ++tp)
                gat[tp] = *reinterpret_cast<const bf16x8*>(brow + tp * 32);
            const u16* pp = pos + (size_t)sidx * 3;
            q0 = (u32)pp[0] | ((u32)pp[1] << 16);
            q1 = (u32)pp[2];
        };
        if (st0 < st1) issue(st0, gcur, pcur0, pcur1);
        for (int st = st0; st < st1; ++st) {
            if (st + 1 < st1) issue(st + 1, gnxt, pnxt0, pnxt1);
            const int p0 = st * 16;
            const int pme = p0 + e16;
            const bool vme = pme < Tr;
            const uint4 rme = S.bufA[vme ? pme : 0];
            const int nme = (int)(rme.w & 0xffu);
            float dx = S.posl[nme][0] - bf2f((u16)(pcur0 & 0xffffu));
            float dy = S.posl[nme][1] - bf2f((u16)(pcur0 >> 16));
            float dz = S.posl[nme][2] - bf2f((u16)pcur1);
            float d2v = dx * dx + dy * dy + dz * dz;
            u16 d2h = f2bf(d2v);
            u16 d2l = f2bf(d2v - bf2f(d2h));
            if (g == 0)
                S.wdd[wv][e16] = make_uint2((u32)f2bf(dx) | ((u32)f2bf(dy) << 16),
                                            (u32)f2bf(dz) | ((u32)nme << 16));
            uint4 xu = make_uint4(0u, 0u, 0u, 0u);
            if (vme && g == 0) {
                xu.x = (u32)d2h | (rme.y << 16);
                xu.y = (rme.y >> 16) | (rme.z << 16);
                xu.z = (rme.z >> 16) | ((u32)d2l << 16);
            }
            const bf16x8 x5 = as_bf16x8(xu);
            asm volatile("s_waitcnt lgkmcnt(0)" ::: "memory");
            int arow[4];
#pragma unroll
            for (int r4 = 0; r4 < 4; ++r4)
                arow[r4] = (int)(S.wdd[wv][g * 4 + r4].y >> 16) * AST + e16;
            const int node0 = (int)(S.wdd[wv][0].y >> 16);
            const int nodeL = (int)(S.wdd[wv][min(15, Tr - 1 - p0)].y >> 16);
            const int slot = e16 >> 2, comp = e16 & 3;
            for (int nb = node0; nb <= nodeL; nb += 4) {
                u32 sw[4];
#pragma unroll
                for (int j = 0; j < 4; ++j) {
                    int ql = 4 * g + j;
                    u32 w = 0u;
                    if (p0 + ql < Tr) {
                        uint2 dq = S.wdd[wv][ql];
                        if ((int)(dq.y >> 16) == nb + slot)
                            w = comp == 0 ? 0x3F80u : comp == 1 ? (dq.x & 0xffffu)
                              : comp == 2 ? (dq.x >> 16) : (dq.y & 0xffffu);
                    }
                    sw[j] = w;
                }
                const bf16x8 sop = as_bf16x8(make_uint4(sw[0] | (sw[1] << 16), sw[2] | (sw[3] << 16), 0u, 0u));
                const int mynd = nb + g;
                const bool ndok = mynd <= nodeL;
                float* mbase = (mynd == headnode) ? &S.head[wv][e16] : &S.macc[ndok ? mynd : 0][e16];
                float pv0 = 0.f, pv1 = 0.f, pv2 = 0.f;
#pragma unroll
                for (int tp = 0; tp < 4; ++tp) {
#pragma unroll
                    for (int u = 0; u < 2; ++u) {
                        const int tt = tp * 2 + u;
                        bf16x8 w5 = as_bf16x8(g == 0 ? S.w5s[tt][e16] : make_uint4(0u, 0u, 0u, 0u));
                        f32x4 acc;
#pragma unroll
                        for (int r4 = 0; r4 < 4; ++r4) acc[r4] = (&S.Al[0][0])[arow[r4] + tt * 16];
                        acc = __builtin_amdgcn_mfma_f32_16x16x32_bf16(gcur[tp], u ? selHi : selLo, acc, 0, 0, 0);
                        acc = __builtin_amdgcn_mfma_f32_16x16x32_bf16(x5, w5, acc, 0, 0, 0);
                        float m0 = silu_f(acc[0]), m1 = silu_f(acc[1]), m2 = silu_f(acc[2]), m3 = silu_f(acc[3]);
                        bf16x8 mb = as_bf16x8(make_uint4(pack2(m0, m1), pack2(m2, m3), 0u, 0u));
                        f32x4 z = {0.f, 0.f, 0.f, 0.f};
                        f32x4 d2 = __builtin_amdgcn_mfma_f32_16x16x32_bf16(sop, mb, z, 0, 0, 0);
                        float wxc = S.wxs[tt * 16 + e16];
                        if (ndok) mbase[tt * 16] += d2[0];
                        pv0 += d2[1] * wxc;
                        pv1 += d2[2] * wxc;
                        pv2 += d2[3] * wxc;
                    }
                }
#pragma unroll
                for (int o = 8; o > 0; o >>= 1) {
                    pv0 += __shfl_xor(pv0, o);
                    pv1 += __shfl_xor(pv1, o);
                    pv2 += __shfl_xor(pv2, o);
                }
                if (ndok && e16 == 0) {
                    if (mynd == headnode) { phead0 += pv0; phead1 += pv1; phead2 += pv2; }
                    else { S.pacc[mynd][0] += pv0; S.pacc[mynd][1] += pv1; S.pacc[mynd][2] += pv2; }
                }
            }
#pragma unroll
            for (int tp = 0; tp < 4; ++tp) gcur[tp] = gnxt[tp];
            pcur0 = pnxt0; pcur1 = pnxt1;
        }
        if (lane == 0 && headnode >= 0) {
            S.head[wv][D + 0] = phead0; S.head[wv][D + 1] = phead1; S.head[wv][D + 2] = phead2;
        }
        __syncthreads();
        if (t < D + 3) {
            for (int w = 0; w < KB_WAVES; ++w) {
                int hn = S.headnode[w];
                if (hn >= 0) {
                    if (t < D) S.macc[hn][t] += S.head[w][t];
                    else S.pacc[hn][t - D] += S.head[w][t];
                }
            }
        }
        __syncthreads();
    }

    if (t < nnodes * 3) {
        int n = t / 3, cpt = t - n * 3;
        out_pos[(size_t)(nb0 + n) * 3 + cpt] = f2bf(S.posl[n][cpt] + S.pacc[n][cpt]);
    }
    {
        int rt = wv & 1;
        if (rt * 16 < nnodes) {
            int n = nb0 + rt * 16 + e16;
            int nn = n < N ? n : N - 1;
            bf16x8 af[8];
#pragma unroll
            for (int ks = 0; ks < 4; ++ks)
                af[ks] = *reinterpret_cast<const bf16x8*>(h + (size_t)nn * D + ks * 32 + g * 8);
#pragma unroll
            for (int ks = 0; ks < 4; ++ks) {
                const float* mp = &S.macc[rt * 16 + e16][ks * 32 + g * 8];
                af[4 + ks] = as_bf16x8(make_uint4(pack2(mp[0], mp[1]), pack2(mp[2], mp[3]),
                                                  pack2(mp[4], mp[5]), pack2(mp[6], mp[7])));
            }
#pragma unroll
            for (int q = 0; q < 4; ++q) {
                int ct = (wv >> 1) * 4 + q;
                f32x4 acc = {0.f, 0.f, 0.f, 0.f};
#pragma unroll
                for (int ks = 0; ks < 8; ++ks) {
                    bf16x8 bfr = *reinterpret_cast<const bf16x8*>(whT + (size_t)(ct * 16 + e16) * 2 * D + ks * 32 + g * 8);
                    acc = __builtin_amdgcn_mfma_f32_16x16x32_bf16(af[ks], bfr, acc, 0, 0, 0);
                }
#pragma unroll
                for (int r = 0; r < 4; ++r) {
                    int ln = rt * 16 + g * 4 + r;
                    int nd = nb0 + ln;
                    if (ln < nnodes) {
                        int c = ct * 16 + e16;
                        float hv = bf2f(h[(size_t)nd * D + c]);
                        out_h[(size_t)nd * D + c] = f2bf(hv + silu_f(acc[r]));
                    }
                }
            }
        }
    }
}

extern "C" void kernel_launch(void* const* d_in, const int* in_sizes, int n_in,
                              void* d_out, int out_size, void* d_ws, size_t ws_size,
                              hipStream_t stream) {
    const u16* h   = (const u16*)d_in[0];
    const u16* pos = (const u16*)d_in[1];
    const u16* ea  = (const u16*)d_in[2];
    const int* src = (const int*)d_in[3];
    const int* dst = (const int*)d_in[4];
    const u16* we  = (const u16*)d_in[5];
    const u16* wx  = (const u16*)d_in[6];
    const u16* wh  = (const u16*)d_in[7];
    int N = in_sizes[0] / D;
    int E = in_sizes[3];
    u16* out_h = (u16*)d_out;
    u16* out_pos = out_h + (size_t)N * D;

    int G = 128;
    int CH = (E + G - 1) / G;
    int NPB = (N + NB - 1) / NB;
    int nGemm = (N + 63) / 64;
    int nT = (2 * D * D + KA_THREADS - 1) / KA_THREADS;

    char* ws = (char*)d_ws;
    size_t off = 0;
    auto take = [&](size_t bytes) { char* p = ws + off; off += (bytes + 255) & ~(size_t)255; return p; };
    uint4* rec_g = (uint4*)take((size_t)G * CH * 16);
    u32* tab = (u32*)take((size_t)NB * G * 4);
    float* Ag = (float*)take((size_t)N * D * 4);
    u16* Bg = (u16*)take((size_t)N * D * 2);
    u16* whT = (u16*)take((size_t)2 * D * D * 2);

    k_part<<<G + nGemm + nT, KA_THREADS, 0, stream>>>(h, src, dst, ea, we, wh, rec_g, tab, Ag, Bg, whT,
                                                      N, E, G, CH, NPB, nGemm);
    k_main<<<NB, KB_THREADS, sizeof(KbSmem), stream>>>(h, pos, we, wx, rec_g, tab, Ag, Bg, whT,
                                                      out_h, out_pos, N, G, CH, NPB);
}
```

```cpp
#include <hip/hip_runtime.h>
#include <hip/hip_bf16.h>
#include <stdint.h>
#include <type_traits>


typedef unsigned short u16;
typedef unsigned int u32;
typedef __attribute__((ext_vector_type(8))) short bf16x8;
typedef __attribute__((ext_vector_type(4))) float f32x4;
typedef __attribute__((ext_vector_type(4))) short bf16x4;
typedef __attribute__((ext_vector_type(2))) float f32x2;

#define D 128
#define KA_THREADS 512
#define KA_ITERS 8
#define NBW_MAX 8192
#define KB_THREADS 256
#define KB_WAVES 4
#define NPW 5
#define RPL 3
#define CAP (RPL * 64)
#define CAPS 272
#define CAPR (CAP + 32)
#define GI_MAX 4
#define AST (D + 4)
#define CBAR() asm volatile("" ::: "memory")
#define NLOG2E (-1.4426950408889634f)
#define NLN2 (-0.6931471805599453f)

__device__ __forceinline__ float bf2f(u16 v) { return __uint_as_float(((u32)v) << 16); }
__device__ __forceinline__ u16 f2bf(float f) {
    __hip_bfloat16 b = __float2bfloat16(f);
    return *reinterpret_cast<u16*>(&b);
}
__device__ __forceinline__ float silu_f(float x) {
    float e = __builtin_amdgcn_exp2f(x * NLOG2E);
    return x * __builtin_amdgcn_rcpf(1.0f + e);
}
__device__ __forceinline__ u32 pack2(float a, float b) {
    return (u32)f2bf(a) | ((u32)f2bf(b) << 16);
}
typedef __attribute__((ext_vector_type(2))) __bf16 bf16x2;
__device__ __forceinline__ u32 cvt_pk(f32x2 v) {
    bf16x2 p = __builtin_convertvector(v, bf16x2);
    return *reinterpret_cast<u32*>(&p);
}
__device__ __forceinline__ bf16x8 as_bf16x8(uint4 v) { return *reinterpret_cast<bf16x8*>(&v); }
__device__ __forceinline__ bf16x4 as_bf16x4(uint2 v) { return *reinterpret_cast<bf16x4*>(&v); }
__device__ __forceinline__ u32 mdiv(u32 x, unsigned long long m) {
    return (u32)(((unsigned long long)x * m) >> 40);
}
__device__ __forceinline__ int node_start(int b, int N, int NBW, unsigned long long mNBW) {
    return (int)mdiv((u32)b * (u32)N + (u32)NBW - 1u, mNBW);
}

__global__ void __launch_bounds__(KA_THREADS)
k_part(const u16* __restrict__ h, const int* __restrict__ src, const int* __restrict__ dst,
       const u16* __restrict__ ea, const u16* __restrict__ we, const u16* __restrict__ wh,
       uint4* __restrict__ rec_g, u16* __restrict__ tab, float* __restrict__ Ag, u16* __restrict__ Bg,
       u16* __restrict__ whT, uint4* __restrict__ w5g,
       unsigned long long mN, unsigned long long mNBW,
       int N, int E, int G, int CH, int NBW, int nGemm) {
    __shared__ __align__(16) u16 smem[2 * D * (D + 8)];
    __shared__ int wsum[KA_THREADS / 64];
    int t = threadIdx.x;
    int bid = blockIdx.x;
    if (bid < G) {
        int* hist = (int*)smem;
        for (int i = t; i < NBW; i += KA_THREADS) hist[i] = 0;
        int e0 = bid * CH;
        int e1 = min(E, e0 + CH);
        int sv[KA_ITERS], bv[KA_ITERS], rk[KA_ITERS], lo[KA_ITERS];
        uint2 eav[KA_ITERS];
#pragma unroll
        for (int i = 0; i < KA_ITERS; ++i) {
            int e = e0 + t + i * KA_THREADS;
            bv[i] = -1;
            if (e < e1) {
                int d = dst[e];
                sv[i] = src[e];
                eav[i] = *reinterpret_cast<const uint2*>(ea + (size_t)e * 4);
                int b = (int)mdiv((u32)d * (u32)NBW, mN);
                bv[i] = b;
                lo[i] = d - node_start(b, N, NBW, mNBW);
            }
        }
        __syncthreads();
#pragma unroll
        for (int i = 0; i < KA_ITERS; ++i)
            if (bv[i] >= 0) rk[i] = atomicAdd(&hist[bv[i]], 1);
        __syncthreads();
        const int per = (NBW + KA_THREADS - 1) / KA_THREADS;
        int b0 = t * per, b1 = min(NBW, b0 + per);
        int cnt = 0;
        for (int b = b0; b < b1; ++b) cnt += hist[b];
        int lane = t & 63, wv = t >> 6;
        int x = cnt;
#pragma unroll
        for (int o = 1; o < 64; o <<= 1) {
            int y = __shfl_up(x, o);
            if (lane >= o) x += y;
        }
        if (lane == 63) wsum[wv] = x;
        __syncthreads();
        int base = 0;
        for (int w = 0; w < wv; ++w) base += wsum[w];
        int off = base + x - cnt;
        u16* trow = tab + (size_t)bid * (NBW + 1);
        for (int b = b0; b < b1; ++b) {
            int c = hist[b];
            hist[b] = off;
            trow[b] = (u16)off;
            off += c;
        }
        if (t == KA_THREADS - 1) trow[NBW] = (u16)(base + x);
        __syncthreads();
#pragma unroll
        for (int i = 0; i < KA_ITERS; ++i) {
            if (bv[i] >= 0) {
                int e = e0 + t + i * KA_THREADS;
                int p = hist[bv[i]] + rk[i];
                uint4 r;
                r.x = (u32)sv[i]; r.y = eav[i].x; r.z = eav[i].y;
                r.w = ((u32)e << 3) | (u32)lo[i];
                rec_g[(size_t)bid * CH + p] = r;
            }
        }
        return;
    }
    bid -= G;
    if (bid < nGemm) {
        {
            uint4 wv8[8];
#pragma unroll
            for (int i = 0; i < 8; ++i) {
                int v = t + i * KA_THREADS;
                wv8[i] = *reinterpret_cast<const uint4*>(we + (size_t)v * 8);
            }
#pragma unroll
            for (int i = 0; i < 8; ++i) {
                int v = t + i * KA_THREADS;
                int kk = v >> 4, c0 = (v & 15) * 8;
                int half = kk >> 7, k = kk & 127;
                u16* dstp = &smem[(half * D + c0) * (D + 8) + k];
                dstp[0 * (D + 8)] = (u16)(wv8[i].x & 0xffffu); dstp[1 * (D + 8)] = (u16)(wv8[i].x >> 16);
                dstp[2 * (D + 8)] = (u16)(wv8[i].y & 0xffffu); dstp[3 * (D + 8)] = (u16)(wv8[i].y >> 16);
                dstp[4 * (D + 8)] = (u16)(wv8[i].z & 0xffffu); dstp[5 * (D + 8)] = (u16)(wv8[i].z >> 16);
                dstp[6 * (D + 8)] = (u16)(wv8[i].w & 0xffffu); dstp[7 * (D + 8)] = (u16)(wv8[i].w >> 16);
            }
        }
        __syncthreads();
        int lane = t & 63, wv = t >> 6;
        int e16 = lane & 15, g = lane >> 4;
        int rt = wv & 3;
        int half = wv >> 2;
        int n = bid * 64 + rt * 16 + e16;
        int nn = n < N ? n : N - 1;
        bf16x8 af[4];
#pragma unroll
        for (int ks = 0; ks < 4; ++ks)
            af[ks] = *reinterpret_cast<const bf16x8*>(h + (size_t)nn * D + ks * 32 + g * 8);
#pragma unroll
        for (int ct = 0; ct < 8; ++ct) {
            int c0 = ct * 16;
            f32x4 acc = {0.f, 0.f, 0.f, 0.f};
#pragma unroll
            for (int ks = 0; ks < 4; ++ks) {
                bf16x8 bfr = *reinterpret_cast<const bf16x8*>(&smem[(half * D + c0 + e16) * (D + 8) + ks * 32 + g * 8]);
                acc = __builtin_amdgcn_mfma_f32_16x16x32_bf16(af[ks], bfr, acc, 0, 0, 0);
            }
#pragma unroll
            for (int r = 0; r < 4; ++r) {
                int row = bid * 64 + rt * 16 + g * 4 + r;
                if (row < N) {
                    float v = acc[r] * NLOG2E;
                    if (half == 0) Ag[(size_t)row * D + c0 + e16] = v;
                    else Bg[(size_t)row * D + c0 + e16] = f2bf(v);
                }
            }
        }
        return;
    }
    bid -= nGemm;
    {
        int i = bid * KA_THREADS + t;
        if (i < 2 * D * D) {
            int k = i >> 7, c = i & 127;
            whT[(size_t)c * 2 * D + k] = wh[i];
        }
        if (bid == 0 && t < 2 * D) {
            int gq = t >> 7, c = t & 127;
            u32 v[6];
#pragma unroll
            for (int j = 0; j < 6; ++j) {
                int kr = (j == 5) ? 0 : j;
                float w = bf2f(we[(size_t)(256 + kr) * D + c]) * NLOG2E;
                u16 hi = f2bf(w);
                u16 lo = f2bf(w - bf2f(hi));
                v[j] = (u32)(gq ? lo : hi);
            }
            w5g[t] = make_uint4(v[0] | (v[1] << 16), v[2] | (v[3] << 16), v[4] | (v[5] << 16), 0u);
        }
    }
}

struct KbSmem {
    uint4 rec[KB_WAVES][CAPR];
    u32 key[KB_WAVES][CAPS];
    uint4 At[KB_WAVES][2][D];
    uint4 w5s[2][D];
    float macc[KB_WAVES * NPW][AST];
    float pacc[KB_WAVES][NPW][4];
    float posl[KB_WAVES][NPW + 3][4];
    float wxs[D];
    uint2 M[KB_WAVES][16][4];
};

__global__ void __launch_bounds__(KB_THREADS, 3)
k_main(const u16* __restrict__ h, const u16* __restrict__ pos,
       const u16* __restrict__ wx,
       const uint4* __restrict__ rec_g, const u16* __restrict__ tab,
       const float* __restrict__ Ag, const u16* __restrict__ Bg,
       const u16* __restrict__ whT, const uint4* __restrict__ w5g,
       u16* __restrict__ out_h, u16* __restrict__ out_pos,
       unsigned long long mNBW, int N, int G, int CH, int NBW) {
    __shared__ KbSmem S;
    const int t = threadIdx.x, lane = t & 63;
    const int wv = __builtin_amdgcn_readfirstlane(t >> 6);
    const int e16 = lane & 15, g = lane >> 4;
    const int wb = blockIdx.x * KB_WAVES + wv;
    const int N0 = node_start(blockIdx.x * KB_WAVES, N, NBW, mNBW);
    const int N1 = node_start(blockIdx.x * KB_WAVES + KB_WAVES, N, NBW, mNBW);
    const int n0 = node_start(wb, N, NBW, mNBW);
    const int nn = node_start(wb + 1, N, NBW, mNBW) - n0;
    const int rowb = n0 - N0;

    int so[GI_MAX], sc[GI_MAX];
    auto load_tab = [&]() {
        int lo_ = lane;
        asm volatile("" : "+v"(lo_));
#pragma unroll
        for (int ci = 0; ci < GI_MAX; ++ci) {
            int c = lo_ + 64 * ci;
            so[ci] = 0; sc[ci] = 0;
            if (c < G) {
                const u16* tp = tab + (size_t)c * (NBW + 1) + wb;
                so[ci] = tp[0];
                sc[ci] = tp[1];
            }
        }
    };
    load_tab();
    float a0[NPW], a1[NPW];
#pragma unroll
    for (int j = 0; j < NPW; ++j) {
        a0[j] = 0.f; a1[j] = 0.f;
        if (j < nn) {
            a0[j] = Ag[(size_t)(n0 + j) * D + lane];
            a1[j] = Ag[(size_t)(n0 + j) * D + 64 + lane];
        }
    }
    const uint4 w40 = w5g[lane], w41 = w5g[lane + 64], w42 = w5g[lane + 128], w43 = w5g[lane + 192];
    u16 wx0 = wx[lane], wx1 = wx[lane + 64];
    u16 pz16 = 0;
    {
        int n = lane >> 2, cpt = lane & 3;
        if (n < nn && cpt < 3) pz16 = pos[(n0 + n) * 3 + cpt];
    }
    auto stage = [&]() {
#pragma unroll
        for (int hf = 0; hf < 2; ++hf) {
            u32 hi[NPW], mi[NPW], lw[NPW];
#pragma unroll
            for (int j = 0; j < NPW; ++j) {
                float a = hf ? a1[j] : a0[j];
                u16 x = f2bf(a);
                float r1 = a - bf2f(x);
                u16 y = f2bf(r1);
                u16 z = f2bf(r1 - bf2f(y));
                hi[j] = x; mi[j] = y; lw[j] = z;
            }
            int c = hf * 64 + lane;
            S.At[wv][0][c] = make_uint4(hi[0] | (hi[1] << 16), hi[2] | (hi[3] << 16), mi[0] | (mi[1] << 16), mi[2] | (mi[3] << 16));
            S.At[wv][1][c] = make_uint4(lw[0] | (lw[1] << 16), lw[2] | (lw[3] << 16), hi[4] | (mi[4] << 16), lw[4]);
#pragma unroll
            for (int j = 0; j < NPW; ++j)
                if (j < nn) S.macc[rowb + j][c] = 0.f;
        }
        uint4* wq = &S.w5s[0][0] + lane;
        wq[0] = w40; wq[64] = w41; wq[128] = w42; wq[192] = w43;
        S.wxs[lane] = bf2f(wx0);
        S.wxs[lane + 64] = bf2f(wx1);
        if (lane < (NPW + 3) * 4) S.posl[wv][lane >> 2][lane & 3] = bf2f(pz16);
        if (lane < NPW * 4) S.pacc[wv][lane >> 2][lane & 3] = 0.f;
    };
    const unsigned long long ltmask = (1ull << lane) - 1ull;
    uint4* rec = S.rec[wv];
    u32* key = S.key[wv];

    CBAR();
    auto round = [&](const int base, auto firstc) __attribute__((always_inline)) -> bool {
        constexpr bool FIRST = decltype(firstc)::value;
        int T;
        int fo[GI_MAX];
        {
            int tot = 0;
#pragma unroll
            for (int ci = 0; ci < GI_MAX; ++ci) { sc[ci] -= so[ci]; tot += sc[ci]; }
            int x = tot;
            int ls = lane;
            asm volatile("" : "+v"(ls));
#pragma unroll
            for (int o = 1; o < 64; o <<= 1) {
                int y = __builtin_amdgcn_ds_bpermute((ls - o) << 2, x);
                if (ls >= o) x += y;
            }
            T = __builtin_amdgcn_readlane(x, 63);
            int off = x - tot;
#pragma unroll
            for (int ci = 0; ci < GI_MAX; ++ci) { fo[ci] = off; off += sc[ci]; }
        }
        const int Tr = min(CAP, T - base);
        int lo2 = lane;
        asm volatile("" : "+v"(lo2));
#pragma unroll
        for (int ci = 0; ci < GI_MAX; ++ci) {
            int gs = (lo2 + 64 * ci) * CH + so[ci];
            for (int q = 0; q < sc[ci]; ++q) {
                int fp = fo[ci] + q - base;
                if (fp >= 0 && fp < CAP) key[fp] = (u32)(gs + q);
            }
        }
        CBAR();
        uint4 r[RPL];
#pragma unroll
        for (int i = 0; i < RPL; ++i) {
            int p = lane + 64 * i;
            r[i] = make_uint4(0u, 0u, 0u, 0xffffffffu);
            if (p < Tr) r[i] = rec_g[key[p]];
        }
        if (FIRST) stage();
        int posn[RPL];
        int cntj[NPW], stj[NPW + 1];
        stj[0] = 0;
#pragma unroll
        for (int i = 0; i < RPL; ++i) posn[i] = 0;
#pragma unroll
        for (int j = 0; j < NPW; ++j) {
            int c = 0;
#pragma unroll
            for (int i = 0; i < RPL; ++i) {
                bool mt = (r[i].w != 0xffffffffu) && ((int)(r[i].w & 7u) == j);
                unsigned long long m = __ballot(mt);
                if (mt) posn[i] = stj[j] + c + __popcll(m & ltmask);
                c += __popcll(m);
            }
            cntj[j] = c;
            stj[j + 1] = stj[j] + ((c + 15) & ~15);
        }
        CBAR();
#pragma unroll
        for (int i = 0; i < (CAPS + 63) / 64; ++i) {
            int p = lane + 64 * i;
            if (p < CAPS) key[p] = 0xffffffffu;
        }
#pragma unroll
        for (int i = 0; i < RPL; ++i)
            if (r[i].w != 0xffffffffu) key[posn[i]] = r[i].w >> 3;
        CBAR();
        const int c0 = cntj[0] + cntj[1] + cntj[2] + cntj[3];
        const int g1s = (c0 + 15) & ~15;
        const int g1e = (g1s + cntj[4] + 15) & ~15;
        {
            int s0[RPL], c4[RPL], cs[RPL], rank[RPL];
            u32 k0[RPL];
            int mx = 0;
#pragma unroll
            for (int j = 0; j < NPW; ++j) mx = max(mx, cntj[j]);
#pragma unroll
            for (int i = 0; i < RPL; ++i) {
                int nd = (int)(r[i].w & 7u);
                s0[i] = 0; c4[i] = 0; cs[i] = 0; rank[i] = 0;
                int cc = 0;
#pragma unroll
                for (int j = 0; j < NPW; ++j) {
                    if (nd == j) { s0[i] = stj[j]; c4[i] = cntj[j]; cs[i] = (j == 4) ? g1s : cc; }
                    cc += cntj[j];
                }
                k0[i] = r[i].w >> 3;
            }
#pragma clang loop unroll_count(2) vectorize(disable) interleave(disable)
            for (int q = 0; q < mx; q += 4) {
#pragma unroll
                for (int i = 0; i < RPL; ++i) {
                    uint4 kk = *reinterpret_cast<const uint4*>(&key[s0[i] + (q < c4[i] ? q : 0)]);
                    int a = (kk.x < k0[i] ? 1 : 0) + (kk.y < k0[i] ? 1 : 0) + (kk.z < k0[i] ? 1 : 0) + (kk.w < k0[i] ? 1 : 0);
                    rank[i] += (q < c4[i]) ? a : 0;
                }
            }
#pragma unroll
            for (int i = 0; i < RPL; ++i)
                if (r[i].w != 0xffffffffu) rec[cs[i] + rank[i]] = r[i];
        }
        if (lane < 16) {
            if (c0 + lane < g1s) rec[c0 + lane] = make_uint4(0u, 0u, 0u, 0xffffffffu);
            if (g1s + cntj[4] + lane < g1e) rec[g1s + cntj[4] + lane] = make_uint4(0u, 0u, 0u, 0xffffffffu);
        }
        CBAR();

        int lq = lane;
        asm volatile("" : "+v"(lq));
        bf16x8 selLo, selHi;
        {
            int dl = (lq & 15) - 8 * (lq >> 4);
            unsigned long long one = 0x3F80ull;
            u32 l0 = 0, l1 = 0, l2 = 0, l3 = 0, h0 = 0, h1 = 0, h2 = 0, h3 = 0;
            if (dl >= 0 && dl < 4) { unsigned long long v = one << (16 * dl); l0 = (u32)v; l1 = (u32)(v >> 32); }
            if (dl >= 4 && dl < 8) { unsigned long long v = one << (16 * (dl - 4)); l2 = (u32)v; l3 = (u32)(v >> 32); }
            int dh = dl + 16;
            if (dh >= 0 && dh < 4) { unsigned long long v = one << (16 * dh); h0 = (u32)v; h1 = (u32)(v >> 32); }
            if (dh >= 4 && dh < 8) { unsigned long long v = one << (16 * (dh - 4)); h2 = (u32)v; h3 = (u32)(v >> 32); }
            selLo = as_bf16x8(make_uint4(l0, l1, l2, l3));
            selHi = as_bf16x8(make_uint4(h0, h1, h2, h3));
        }
        const uint4* w5p = (g < 2) ? &S.w5s[g][e16] : &S.At[wv][g - 2][e16];
        bf16x8 gA[4], gB[4];
        u32 pA0 = 0, pA1 = 0, pB0 = 0, pB1 = 0;
        auto issue = [&](int tl, bf16x8 (&gat)[4], u32& q0, u32& q1) {
            int sidx = (int)rec[tl * 16 + e16].x;
            const u16* brow = Bg + (size_t)sidx * D + g * 8;
#pragma unroll
            for (int tp = 0; tp < 4; ++tp)
                gat[tp] = *reinterpret_cast<const bf16x8*>(brow + tp * 32);
            const u16* pp = pos + (size_t)sidx * 3;
            q0 = (u32)pp[0] | ((u32)pp[1] << 16);
            q1 = (u32)pp[2];
        };
        f32x4 acc2[8];
        auto compute = [&](int tl, bf16x8 (&gat)[4], u32 q0, u32 q1) {
            const uint4 rme = rec[tl * 16 + e16];
            const bool vme = rme.w != 0xffffffffu;
            const int nd = (int)(rme.w & 7u);
            const float4 pn = *reinterpret_cast<const float4*>(&S.posl[wv][nd][0]);
            float dx = pn.x - bf2f((u16)(q0 & 0xffffu));
            float dy = pn.y - bf2f((u16)(q0 >> 16));
            float dz = pn.z - bf2f((u16)q1);
            float d2v = dx * dx + dy * dy + dz * dz;
            u16 d2h = f2bf(d2v);
            u16 d2l = f2bf(d2v - bf2f(d2h));
            {
                bool own = vme && ((nd & 3) == g);
                S.M[wv][e16][g] = own ? make_uint2(0x3F80u | ((u32)f2bf(dx) << 16), (u32)f2bf(dy) | ((u32)f2bf(dz) << 16))
                                      : make_uint2(0u, 0u);
            }
            uint4 xu;
            {
                unsigned long long oh = (nd < 4) ? (0x3F80ull << (16 * nd)) : 0ull;
                u32 o01 = (u32)oh, o23 = (u32)(oh >> 32);
                u32 f4 = (nd == 4) ? 0x3F80u : 0u;
                if (g < 2) xu = make_uint4((u32)d2h | (rme.y << 16), (rme.y >> 16) | (rme.z << 16), (rme.z >> 16) | ((u32)d2l << 16), 0u);
                else if (g == 2) xu = make_uint4(o01, o23, o01, o23);
                else xu = make_uint4(o01, o23, f4 | (f4 << 16), f4);
                if (!vme) xu = make_uint4(0u, 0u, 0u, 0u);
            }
            const bf16x8 x5 = as_bf16x8(xu);
            asm volatile("s_waitcnt lgkmcnt(0)" ::: "memory");
            u32 sw01, sw23;
            {
                const u16* wp = reinterpret_cast<const u16*>(&S.M[wv][g * 4][0]) + e16;
                sw01 = (u32)wp[0] | ((u32)wp[16] << 16);
                sw23 = (u32)wp[32] | ((u32)wp[48] << 16);
            }
            const bf16x4 sop = as_bf16x4(make_uint2(sw01, sw23));
#pragma unroll
            for (int tp = 0; tp < 4; ++tp) {
#pragma unroll
                for (int u = 0; u < 2; ++u) {
                    const int tt = tp * 2 + u;
                    bf16x8 w5 = as_bf16x8(w5p[tt * 16]);
                    f32x4 acc = __builtin_amdgcn_mfma_f32_16x16x32_bf16(x5, w5, (f32x4){0.f, 0.f, 0.f, 0.f}, 0, 0, 0);
                    acc = __builtin_amdgcn_mfma_f32_16x16x32_bf16(gat[tp], u ? selHi : selLo, acc, 0, 0, 0);
                    f32x2 t01 = {acc[0], acc[1]}, t23 = {acc[2], acc[3]};
                    f32x2 e01 = {__builtin_amdgcn_exp2f(acc[0]), __builtin_amdgcn_exp2f(acc[1])};
                    f32x2 e23 = {__builtin_amdgcn_exp2f(acc[2]), __builtin_amdgcn_exp2f(acc[3])};
                    e01 = e01 + (f32x2){1.f, 1.f};
                    e23 = e23 + (f32x2){1.f, 1.f};
                    f32x2 r01 = {__builtin_amdgcn_rcpf(e01[0]), __builtin_amdgcn_rcpf(e01[1])};
                    f32x2 r23 = {__builtin_amdgcn_rcpf(e23[0]), __builtin_amdgcn_rcpf(e23[1])};
                    f32x2 m01 = t01 * r01, m23 = t23 * r23;
                    u32 k01 = cvt_pk(m01), k23 = cvt_pk(m23);
                    bf16x4 mb = as_bf16x4(make_uint2(k01, k23));
                    acc2[tt] = __builtin_amdgcn_mfma_f32_16x16x16bf16_1k(sop, mb, acc2[tt], 0, 0, 0);
                }
            }
        };
        for (int grp = 0; grp < 2; ++grp) {
            const int t0 = grp ? (g1s >> 4) : 0;
            const int t1 = grp ? (g1e >> 4) : (g1s >> 4);
            if (t1 <= t0) continue;
#pragma unroll
            for (int tt = 0; tt < 8; ++tt) acc2[tt] = (f32x4){0.f, 0.f, 0.f, 0.f};
            const int lastt = t1 - 1;
            issue(t0, gA, pA0, pA1);
            int tl = t0;
            for (; tl + 1 < t1; tl += 2) {
                issue(tl + 1, gB, pB0, pB1);
                compute(tl, gA, pA0, pA1);
                issue(min(tl + 2, lastt), gA, pA0, pA1);
                compute(tl + 1, gB, pB0, pB1);
            }
            if (tl < t1) compute(tl, gA, pA0, pA1);
            const int nl = grp * 4 + g;
            float pv0 = 0.f, pv1 = 0.f, pv2 = 0.f;
#pragma unroll
            for (int tt = 0; tt < 8; ++tt) {
                float wxc = S.wxs[tt * 16 + e16];
                if (nl < nn) S.macc[rowb + nl][tt * 16 + e16] += acc2[tt][0] * NLN2;
                pv0 += acc2[tt][1] * wxc;
                pv1 += acc2[tt][2] * wxc;
                pv2 += acc2[tt][3] * wxc;
            }
#pragma unroll
            for (int o = 8; o > 0; o >>= 1) {
                pv0 += __shfl_xor(pv0, o);
                pv1 += __shfl_xor(pv1, o);
                pv2 += __shfl_xor(pv2, o);
            }
            if (e16 == 0 && nl < nn) {
                S.pacc[wv][nl][0] += pv0 * NLN2;
                S.pacc[wv][nl][1] += pv1 * NLN2;
                S.pacc[wv][nl][2] += pv2 * NLN2;
            }
        }
        return base + CAP < T;
    };
    if (round(0, std::integral_constant<bool, true>())) {
        for (int base = CAP;; base += CAP) {
            load_tab();
            if (!round(base, std::integral_constant<bool, false>())) break;
        }
    }

    int le = lane;
    asm volatile("" : "+v"(le));
    const int f16 = le & 15, gg = le >> 4;
    if (le < nn * 3) {
        int n = le / 3, cpt = le - n * 3;
        out_pos[(size_t)(n0 + n) * 3 + cpt] = f2bf(S.posl[wv][n][cpt] + S.pacc[wv][n][cpt]);
    }
    bf16x8 bw[2][8];
#pragma unroll
    for (int q = 0; q < 2; ++q)
#pragma unroll
        for (int ks = 0; ks < 8; ++ks)
            bw[q][ks] = *reinterpret_cast<const bf16x8*>(whT + (size_t)((wv * 2 + q) * 16 + f16) * 2 * D + ks * 32 + gg * 8);
    const int NR = N1 - N0;
    bf16x8 hf[2][4];
#pragma unroll
    for (int rt = 0; rt < 2; ++rt) {
        int lr = rt * 16 + f16;
        int n = N0 + (lr < NR ? lr : 0);
#pragma unroll
        for (int ks = 0; ks < 4; ++ks)
            hf[rt][ks] = *reinterpret_cast<const bf16x8*>(h + (size_t)n * D + ks * 32 + gg * 8);
    }
    u16 hvv[2][2][4];
#pragma unroll
    for (int rt = 0; rt < 2; ++rt)
#pragma unroll
        for (int q = 0; q < 2; ++q)
#pragma unroll
            for (int r = 0; r < 4; ++r) {
                int ln = rt * 16 + gg * 4 + r;
                hvv[rt][q][r] = h[(size_t)(N0 + (ln < NR ? ln : 0)) * D + (wv * 2 + q) * 16 + f16];
            }
    __syncthreads();
#pragma unroll
    for (int rt = 0; rt < 2; ++rt) {
        if (rt * 16 < NR) {
            bf16x8 af[4];
            int lr = rt * 16 + f16;
#pragma unroll
            for (int ks = 0; ks < 4; ++ks) {
                const float* mp = &S.macc[lr < NR ? lr : 0][ks * 32 + gg * 8];
                af[ks] = as_bf16x8(make_uint4(pack2(mp[0], mp[1]), pack2(mp[2], mp[3]),
                                              pack2(mp[4], mp[5]), pack2(mp[6], mp[7])));
            }
#pragma unroll
            for (int q = 0; q < 2; ++q) {
                int ct = wv * 2 + q;
                f32x4 acc = {0.f, 0.f, 0.f, 0.f};
#pragma unroll
                for (int ks = 0; ks < 4; ++ks)
                    acc = __builtin_amdgcn_mfma_f32_16x16x32_bf16(hf[rt][ks], bw[q][ks], acc, 0, 0, 0);
#pragma unroll
                for (int ks = 0; ks < 4; ++ks)
                    acc = __builtin_amdgcn_mfma_f32_16x16x32_bf16(af[ks], bw[q][4 + ks], acc, 0, 0, 0);
#pragma unroll
                for (int r = 0; r < 4; ++r) {
                    int ln = rt * 16 + gg * 4 + r;
                    if (ln < NR) {
                        int nd = N0 + ln;
                        int c = ct * 16 + f16;
                        float hv = bf2f(hvv[rt][q][r]);
                        out_h[(size_t)nd * D + c] = f2bf(hv + silu_f(acc[r]));
                    }
                }
            }
        }
    }
}

extern "C" void kernel_launch(void* const* d_in, const int* in_sizes, int n_in,
                              void* d_out, int out_size, void* d_ws, size_t ws_size,
                              hipStream_t stream) {
    const u16* h   = (const u16*)d_in[0];
    const u16* pos = (const u16*)d_in[1];
    const u16* ea  = (const u16*)d_in[2];
    const int* src = (const int*)d_in[3];
    const int* dst = (const int*)d_in[4];
    const u16* we  = (const u16*)d_in[5];
    const u16* wx  = (const u16*)d_in[6];
    const u16* wh  = (const u16*)d_in[7];
    int N = in_sizes[0] / D;
    int E = in_sizes[3];
    u16* out_h = (u16*)d_out;
    u16* out_pos = out_h + (size_t)N * D;

    int G = 128;
    while ((E + G - 1) / G > KA_THREADS * KA_ITERS && G < 64 * GI_MAX) G += 64;
    int CH = (E + G - 1) / G;
    int NBW = 3072;
    while (NBW * NPW < N && NBW < NBW_MAX) NBW += 4;
    int nGemm = (N + 63) / 64;
    int nT = (2 * D * D + KA_THREADS - 1) / KA_THREADS;
    unsigned long long mN = (1ull << 40) / (unsigned long long)N + 1ull;
    unsigned long long mNBW = (1ull << 40) / (unsigned long long)NBW + 1ull;

    char* ws = (char*)d_ws;
    size_t off = 0;
    auto take = [&](size_t bytes) { char* p = ws + off; off += (bytes + 255) & ~(size_t)255; return p; };
    uint4* rec_g = (uint4*)take((size_t)G * CH * 16);
    u16* tab = (u16*)take((size_t)G * (NBW + 1) * 2);
    float* Ag = (float*)take((size_t)N * D * 4);
    u16* Bg = (u16*)take((size_t)N * D * 2);
    u16* whT = (u16*)take((size_t)2 * D * D * 2);
    uint4* w5g = (uint4*)take((size_t)256 * 16);

    k_part<<<G + nGemm + nT, KA_THREADS, 0, stream>>>(h, src, dst, ea, we, wh, rec_g, tab, Ag, Bg, whT, w5g,
                                                      mN, mNBW, N, E, G, CH, NBW, nGemm);
    k_main<<<NBW / KB_WAVES, KB_THREADS, 0, stream>>>(h, pos, wx, rec_g, tab, Ag, Bg, whT, w5g,
                                                     out_h, out_pos, mNBW, N, G, CH, NBW);
}
```

```cpp
#include <hip/hip_runtime.h>
#include <hip/hip_bf16.h>
#include <stdint.h>
#include <type_traits>


typedef unsigned short u16;
typedef unsigned int u32;
typedef __attribute__((ext_vector_type(8))) short bf16x8;
typedef __attribute__((ext_vector_type(4))) float f32x4;
typedef __attribute__((ext_vector_type(4))) short bf16x4;
typedef __attribute__((ext_vector_type(2))) float f32x2;

#define D 128
#define KA_THREADS 1024
#define KA_ITERS 4
#define NBW_MAX 8192
#define KB_THREADS 256
#define KB_WAVES 4
#define NPW 5
#define RPL 4
#define CAP (RPL * 64)
#define CAPS 272
#define CAPR (CAP + 32)
#define GI_MAX 4
#define AST (D + 4)
#define CBAR() asm volatile("" ::: "memory")
#define NLOG2E (-1.4426950408889634f)
#define NLN2 (-0.6931471805599453f)

__device__ __forceinline__ float bf2f(u16 v) { return __uint_as_float(((u32)v) << 16); }
__device__ __forceinline__ u16 f2bf(float f) {
    __hip_bfloat16 b = __float2bfloat16(f);
    return *reinterpret_cast<u16*>(&b);
}
__device__ __forceinline__ float silu_f(float x) {
    float e = __builtin_amdgcn_exp2f(x * NLOG2E);
    return x * __builtin_amdgcn_rcpf(1.0f + e);
}
__device__ __forceinline__ u32 pack2(float a, float b) {
    return (u32)f2bf(a) | ((u32)f2bf(b) << 16);
}
typedef __attribute__((ext_vector_type(2))) __bf16 bf16x2;
__device__ __forceinline__ u32 cvt_pk(f32x2 v) {
    bf16x2 p = __builtin_convertvector(v, bf16x2);
    return *reinterpret_cast<u32*>(&p);
}
__device__ __forceinline__ bf16x8 as_bf16x8(uint4 v) { return *reinterpret_cast<bf16x8*>(&v); }
__device__ __forceinline__ bf16x4 as_bf16x4(uint2 v) { return *reinterpret_cast<bf16x4*>(&v); }
__device__ __forceinline__ u32 mdiv(u32 x, unsigned long long m) {
    return (u32)(((unsigned long long)x * m) >> 40);
}
__device__ __forceinline__ int node_start(int b, int N, int NBW, unsigned long long mNBW) {
    return (int)mdiv((u32)b * (u32)N + (u32)NBW - 1u, mNBW);
}

__global__ void __launch_bounds__(KA_THREADS)
k_part(const u16* __restrict__ h, const int* __restrict__ src, const int* __restrict__ dst,
       const u16* __restrict__ ea, const u16* __restrict__ we, const u16* __restrict__ wh,
       uint4* __restrict__ rec_g, u16* __restrict__ tab, float* __restrict__ Ag, u16* __restrict__ Bg,
       u16* __restrict__ whT, uint4* __restrict__ w5g,
       unsigned long long mN, unsigned long long mNBW,
       int N, int E, int G, int CH, int NBW, int nGemm) {
    __shared__ __align__(16) u16 smem[2 * D * (D + 8)];
    __shared__ int wsum[KA_THREADS / 64];
    int t = threadIdx.x;
    int bid = blockIdx.x;
    if (bid < G) {
        int* hist = (int*)smem;
        for (int i = t; i < NBW; i += KA_THREADS) hist[i] = 0;
        int e0 = bid * CH;
        int e1 = min(E, e0 + CH);
        int sv[KA_ITERS], bv[KA_ITERS], rk[KA_ITERS], lo[KA_ITERS];
        uint2 eav[KA_ITERS];
#pragma unroll
        for (int i = 0; i < KA_ITERS; ++i) {
            int e = e0 + t + i * KA_THREADS;
            bv[i] = -1;
            if (e < e1) {
                int d = dst[e];
                sv[i] = src[e];
                eav[i] = *reinterpret_cast<const uint2*>(ea + (size_t)e * 4);
                int b = (int)mdiv((u32)d * (u32)NBW, mN);
                bv[i] = b;
                lo[i] = d - node_start(b, N, NBW, mNBW);
            }
        }
        __syncthreads();
#pragma unroll
        for (int i = 0; i < KA_ITERS; ++i)
            if (bv[i] >= 0) rk[i] = atomicAdd(&hist[bv[i]], 1);
        __syncthreads();
        const int per = (NBW + KA_THREADS - 1) / KA_THREADS;
        int b0 = t * per, b1 = min(NBW, b0 + per);
        int cnt = 0;
        for (int b = b0; b < b1; ++b) cnt += hist[b];
        int lane = t & 63, wv = t >> 6;
        int x = cnt;
#pragma unroll
        for (int o = 1; o < 64; o <<= 1) {
            int y = __shfl_up(x, o);
            if (lane >= o) x += y;
        }
        if (lane == 63) wsum[wv] = x;
        __syncthreads();
        int base = 0;
#pragma unroll
        for (int w = 0; w < KA_THREADS / 64; ++w) base += (w < wv) ? wsum[w] : 0;
        int off = base + x - cnt;
        u16* trow = tab + (size_t)bid * (NBW + 1);
        for (int b = b0; b < b1; ++b) {
            int c = hist[b];
            hist[b] = off;
            trow[b] = (u16)off;
            off += c;
        }
        if (t == KA_THREADS - 1) { trow[NBW] = (u16)(base + x); hist[NBW] = base + x; }
        __syncthreads();
        u16* ids = reinterpret_cast<u16*>(hist + NBW_MAX + 8);
#pragma unroll
        for (int i = 0; i < KA_ITERS; ++i)
            if (bv[i] >= 0) ids[hist[bv[i]] + rk[i]] = (u16)(t + i * KA_THREADS);
        __syncthreads();
#pragma unroll
        for (int i = 0; i < KA_ITERS; ++i) {
            if (bv[i] >= 0) {
                int e = e0 + t + i * KA_THREADS;
                int s0 = hist[bv[i]], s1 = hist[bv[i] + 1];
                int me = t + i * KA_THREADS, rnk = 0;
                for (int q = s0; q < s1; ++q) rnk += ((int)ids[q] < me) ? 1 : 0;
                int p = s0 + rnk;
                uint4 r;
                r.x = (u32)sv[i]; r.y = eav[i].x; r.z = eav[i].y;
                r.w = ((u32)e << 3) | (u32)lo[i];
                rec_g[(size_t)bid * CH + p] = r;
            }
        }
        return;
    }
    bid -= G;
    if (bid < nGemm) {
        {
            const int c2 = t & 63, kb = t >> 6;
            u32 wr[16];
#pragma unroll
            for (int j = 0; j < 16; ++j)
                wr[j] = *reinterpret_cast<const u32*>(we + (size_t)(kb * 16 + j) * D + c2 * 2);
#pragma unroll
            for (int sb = 0; sb < 2; ++sb) {
                int kk = kb * 16 + sb * 8;
                int half = kk >> 7, k = kk & 127;
                const u32* w = &wr[sb * 8];
                uint4 lo4 = make_uint4((w[0] & 0xffffu) | (w[1] << 16), (w[2] & 0xffffu) | (w[3] << 16),
                                       (w[4] & 0xffffu) | (w[5] << 16), (w[6] & 0xffffu) | (w[7] << 16));
                uint4 hi4 = make_uint4((w[0] >> 16) | (w[1] & 0xffff0000u), (w[2] >> 16) | (w[3] & 0xffff0000u),
                                       (w[4] >> 16) | (w[5] & 0xffff0000u), (w[6] >> 16) | (w[7] & 0xffff0000u));
                *reinterpret_cast<uint4*>(&smem[(half * D + c2 * 2) * (D + 8) + k]) = lo4;
                *reinterpret_cast<uint4*>(&smem[(half * D + c2 * 2 + 1) * (D + 8) + k]) = hi4;
            }
        }
        __syncthreads();
        int lane = t & 63, wv = t >> 6;
        int e16 = lane & 15, g = lane >> 4;
        int rt = wv & 7;
        int half = wv >> 3;
        int n = bid * 128 + rt * 16 + e16;
        int nn = n < N ? n : N - 1;
        bf16x8 af[4];
#pragma unroll
        for (int ks = 0; ks < 4; ++ks)
            af[ks] = *reinterpret_cast<const bf16x8*>(h + (size_t)nn * D + ks * 32 + g * 8);
#pragma unroll
        for (int ct = 0; ct < 8; ++ct) {
            int c0 = ct * 16;
            f32x4 acc = {0.f, 0.f, 0.f, 0.f};
#pragma unroll
            for (int ks = 0; ks < 4; ++ks) {
                bf16x8 bfr = *reinterpret_cast<const bf16x8*>(&smem[(half * D + c0 + e16) * (D + 8) + ks * 32 + g * 8]);
                acc = __builtin_amdgcn_mfma_f32_16x16x32_bf16(af[ks], bfr, acc, 0, 0, 0);
            }
#pragma unroll
            for (int r = 0; r < 4; ++r) {
                int row = bid * 128 + rt * 16 + g * 4 + r;
                if (row < N) {
                    float v = acc[r] * NLOG2E;
                    if (half == 0) Ag[(size_t)row * D + c0 + e16] = v;
                    else Bg[(size_t)row * D + c0 + e16] = f2bf(v);
                }
            }
        }
        return;
    }
    bid -= nGemm;
    {
        int i = bid * KA_THREADS + t;
        if (i < 2 * D * D) {
            int k = i >> 7, c = i & 127;
            whT[(size_t)c * 2 * D + k] = wh[i];
        }
        if (bid == 0 && t < 2 * D) {
            int gq = t >> 7, c = t & 127;
            u32 v[6];
#pragma unroll
            for (int j = 0; j < 6; ++j) {
                int kr = (j == 5) ? 0 : j;
                float w = bf2f(we[(size_t)(256 + kr) * D + c]) * NLOG2E;
                u16 hi = f2bf(w);
                u16 lo = f2bf(w - bf2f(hi));
                v[j] = (u32)(gq ? lo : hi);
            }
            w5g[t] = make_uint4(v[0] | (v[1] << 16), v[2] | (v[3] << 16), v[4] | (v[5] << 16), 0u);
        }
    }
}

struct KbSmem {
    uint4 rec[KB_WAVES][CAPR];
    uint4 At[KB_WAVES][2][D];
    uint4 w5s[2][D];
    float macc[KB_WAVES * NPW][AST];
    float pacc[KB_WAVES][NPW][4];
    float posl[KB_WAVES][NPW + 3][4];
    float wxs[D];
    uint2 M[KB_WAVES][16][4];
};

__global__ void __launch_bounds__(KB_THREADS, 3)
k_main(const u16* __restrict__ h, const u16* __restrict__ pos,
       const u16* __restrict__ wx,
       const uint4* __restrict__ rec_g, const u16* __restrict__ tab,
       const float* __restrict__ Ag, const u16* __restrict__ Bg,
       const u16* __restrict__ whT, const uint4* __restrict__ w5g,
       u16* __restrict__ out_h, u16* __restrict__ out_pos,
       unsigned long long mNBW, int N, int G, int CH, int NBW) {
    __shared__ KbSmem S;
    const int t = threadIdx.x, lane = t & 63;
    const int wv = __builtin_amdgcn_readfirstlane(t >> 6);
    const int e16 = lane & 15, g = lane >> 4;
    const int wb = blockIdx.x * KB_WAVES + wv;
    const int N0 = node_start(blockIdx.x * KB_WAVES, N, NBW, mNBW);
    const int N1 = node_start(blockIdx.x * KB_WAVES + KB_WAVES, N, NBW, mNBW);
    const int n0 = node_start(wb, N, NBW, mNBW);
    const int nn = node_start(wb + 1, N, NBW, mNBW) - n0;
    const int rowb = n0 - N0;

    int so[GI_MAX], sc[GI_MAX];
    auto load_tab = [&]() {
        int lo_ = lane;
        asm volatile("" : "+v"(lo_));
#pragma unroll
        for (int ci = 0; ci < GI_MAX; ++ci) {
            int c = lo_ + 64 * ci;
            so[ci] = 0; sc[ci] = 0;
            if (c < G) {
                const u16* tp = tab + (size_t)c * (NBW + 1) + wb;
                so[ci] = tp[0];
                sc[ci] = tp[1];
            }
        }
    };
    load_tab();
    float a0[NPW], a1[NPW];
#pragma unroll
    for (int j = 0; j < NPW; ++j) {
        a0[j] = 0.f; a1[j] = 0.f;
        if (j < nn) {
            a0[j] = Ag[(size_t)(n0 + j) * D + lane];
            a1[j] = Ag[(size_t)(n0 + j) * D + 64 + lane];
        }
    }
    const uint4 w40 = w5g[lane], w41 = w5g[lane + 64], w42 = w5g[lane + 128], w43 = w5g[lane + 192];
    u16 wx0 = wx[lane], wx1 = wx[lane + 64];
    u16 pz16 = 0;
    {
        int n = lane >> 2, cpt = lane & 3;
        if (n < nn && cpt < 3) pz16 = pos[(n0 + n) * 3 + cpt];
    }
    auto stage = [&]() {
#pragma unroll
        for (int hf = 0; hf < 2; ++hf) {
            u32 hi[NPW], mi[NPW], lw[NPW];
#pragma unroll
            for (int j = 0; j < NPW; ++j) {
                float a = hf ? a1[j] : a0[j];
                u16 x = f2bf(a);
                float r1 = a - bf2f(x);
                u16 y = f2bf(r1);
                u16 z = f2bf(r1 - bf2f(y));
                hi[j] = x; mi[j] = y; lw[j] = z;
            }
            int c = hf * 64 + lane;
            S.At[wv][0][c] = make_uint4(hi[0] | (hi[1] << 16), hi[2] | (hi[3] << 16), mi[0] | (mi[1] << 16), mi[2] | (mi[3] << 16));
            S.At[wv][1][c] = make_uint4(lw[0] | (lw[1] << 16), lw[2] | (lw[3] << 16), hi[4] | (mi[4] << 16), lw[4]);
#pragma unroll
            for (int j = 0; j < NPW; ++j)
                if (j < nn) S.macc[rowb + j][c] = 0.f;
        }
        uint4* wq = &S.w5s[0][0] + lane;
        wq[0] = w40; wq[64] = w41; wq[128] = w42; wq[192] = w43;
        S.wxs[lane] = bf2f(wx0);
        S.wxs[lane + 64] = bf2f(wx1);
        if (lane < (NPW + 3) * 4) S.posl[wv][lane >> 2][lane & 3] = bf2f(pz16);
        if (lane < NPW * 4) S.pacc[wv][lane >> 2][lane & 3] = 0.f;
    };
    const unsigned long long ltmask = (1ull << lane) - 1ull;
    uint4* rec = S.rec[wv];
    u32* key = reinterpret_cast<u32*>(S.rec[wv]);

    CBAR();
    auto round = [&](const int base, auto firstc) __attribute__((always_inline)) -> bool {
        constexpr bool FIRST = decltype(firstc)::value;
        int T;
        int fo[GI_MAX];
        {
            int tot = 0;
#pragma unroll
            for (int ci = 0; ci < GI_MAX; ++ci) { sc[ci] -= so[ci]; tot += sc[ci]; }
            int x = tot;
            int ls = lane;
            asm volatile("" : "+v"(ls));
#pragma unroll
            for (int o = 1; o < 64; o <<= 1) {
                int y = __builtin_amdgcn_ds_bpermute((ls - o) << 2, x);
                if (ls >= o) x += y;
            }
            T = __builtin_amdgcn_readlane(x, 63);
            int off = x - tot;
#pragma unroll
            for (int ci = 0; ci < GI_MAX; ++ci) { fo[ci] = off; off += sc[ci]; }
        }
        const int Tr = min(CAP, T - base);
        int lo2 = lane;
        asm volatile("" : "+v"(lo2));
#pragma unroll
        for (int ci = 0; ci < GI_MAX; ++ci) {
            int gs = (lo2 + 64 * ci) * CH + so[ci];
            for (int q = 0; q < sc[ci]; ++q) {
                int fp = fo[ci] + q - base;
                if (fp >= 0 && fp < CAP) key[fp] = (u32)(gs + q);
            }
        }
        CBAR();
        uint4 r[RPL];
#pragma unroll
        for (int i = 0; i < RPL; ++i) {
            int p = lane + 64 * i;
            r[i] = make_uint4(0u, 0u, 0u, 0xffffffffu);
            if (p < Tr) r[i] = rec_g[key[p]];
        }
        if (FIRST) stage();
        int posn[RPL];
        int cst = 0, cnt4 = 0, c0 = 0, g1s = 0;
#pragma unroll
        for (int i = 0; i < RPL; ++i) posn[i] = 0;
#pragma unroll
        for (int j = 0; j < NPW; ++j) {
            if (j == 4) { c0 = cst; g1s = (cst + 15) & ~15; cst = g1s; }
            int c = 0;
#pragma unroll
            for (int i = 0; i < RPL; ++i) {
                bool mt = (r[i].w != 0xffffffffu) && ((int)(r[i].w & 7u) == j);
                unsigned long long m = __ballot(mt);
                if (mt) posn[i] = cst + c + __popcll(m & ltmask);
                c += __popcll(m);
            }
            if (j == 4) cnt4 = c;
            cst += c;
        }
        const int g1e = (g1s + cnt4 + 15) & ~15;
        CBAR();
#pragma unroll
        for (int i = 0; i < RPL; ++i)
            if (r[i].w != 0xffffffffu) rec[posn[i]] = r[i];
        if (lane < 16) {
            if (c0 + lane < g1s) rec[c0 + lane] = make_uint4(0u, 0u, 0u, 0xffffffffu);
            if (g1s + cnt4 + lane < g1e) rec[g1s + cnt4 + lane] = make_uint4(0u, 0u, 0u, 0xffffffffu);
        }
        CBAR();

        int lq = lane;
        asm volatile("" : "+v"(lq));
        bf16x8 selLo, selHi;
        {
            int dl = (lq & 15) - 8 * (lq >> 4);
            unsigned long long one = 0x3F80ull;
            u32 l0 = 0, l1 = 0, l2 = 0, l3 = 0, h0 = 0, h1 = 0, h2 = 0, h3 = 0;
            if (dl >= 0 && dl < 4) { unsigned long long v = one << (16 * dl); l0 = (u32)v; l1 = (u32)(v >> 32); }
            if (dl >= 4 && dl < 8) { unsigned long long v = one << (16 * (dl - 4)); l2 = (u32)v; l3 = (u32)(v >> 32); }
            int dh = dl + 16;
            if (dh >= 0 && dh < 4) { unsigned long long v = one << (16 * dh); h0 = (u32)v; h1 = (u32)(v >> 32); }
            if (dh >= 4 && dh < 8) { unsigned long long v = one << (16 * (dh - 4)); h2 = (u32)v; h3 = (u32)(v >> 32); }
            selLo = as_bf16x8(make_uint4(l0, l1, l2, l3));
            selHi = as_bf16x8(make_uint4(h0, h1, h2, h3));
        }
        const uint4* w5p = (g < 2) ? &S.w5s[g][e16] : &S.At[wv][g - 2][e16];
        bf16x8 gA[4], gB[4];
        u32 pA0 = 0, pA1 = 0, pB0 = 0, pB1 = 0;
        auto issue = [&](int tl, bf16x8 (&gat)[4], u32& q0, u32& q1) {
            int sidx = (int)rec[tl * 16 + e16].x;
            const u16* brow = Bg + (size_t)sidx * D + g * 8;
#pragma unroll
            for (int tp = 0; tp < 4; ++tp)
                gat[tp] = *reinterpret_cast<const bf16x8*>(brow + tp * 32);
            const u16* pp = pos + (size_t)sidx * 3;
            q0 = (u32)pp[0] | ((u32)pp[1] << 16);
            q1 = (u32)pp[2];
        };
        f32x4 acc2[8];
        auto compute = [&](int tl, bf16x8 (&gat)[4], u32 q0, u32 q1) {
            const uint4 rme = rec[tl * 16 + e16];
            const bool vme = rme.w != 0xffffffffu;
            const int nd = (int)(rme.w & 7u);
            const float4 pn = *reinterpret_cast<const float4*>(&S.posl[wv][nd][0]);
            float dx = pn.x - bf2f((u16)(q0 & 0xffffu));
            float dy = pn.y - bf2f((u16)(q0 >> 16));
            float dz = pn.z - bf2f((u16)q1);
            float d2v = dx * dx + dy * dy + dz * dz;
            u16 d2h = f2bf(d2v);
            u16 d2l = f2bf(d2v - bf2f(d2h));
            {
                bool own = vme && ((nd & 3) == g);
                S.M[wv][e16][g] = own ? make_uint2(0x3F80u | ((u32)f2bf(dx) << 16), (u32)f2bf(dy) | ((u32)f2bf(dz) << 16))
                                      : make_uint2(0u, 0u);
            }
            uint4 xu;
            {
                unsigned long long oh = (nd < 4) ? (0x3F80ull << (16 * nd)) : 0ull;
                u32 o01 = (u32)oh, o23 = (u32)(oh >> 32);
                u32 f4 = (nd == 4) ? 0x3F80u : 0u;
                if (g < 2) xu = make_uint4((u32)d2h | (rme.y << 16), (rme.y >> 16) | (rme.z << 16), (rme.z >> 16) | ((u32)d2l << 16), 0u);
                else if (g == 2) xu = make_uint4(o01, o23, o01, o23);
                else xu = make_uint4(o01, o23, f4 | (f4 << 16), f4);
                if (!vme) xu = make_uint4(0u, 0u, 0u, 0u);
            }
            const bf16x8 x5 = as_bf16x8(xu);
            asm volatile("s_waitcnt lgkmcnt(0)" ::: "memory");
            u32 sw01, sw23;
            {
                const u16* wp = reinterpret_cast<const u16*>(&S.M[wv][g * 4][0]) + e16;
                sw01 = (u32)wp[0] | ((u32)wp[16] << 16);
                sw23 = (u32)wp[32] | ((u32)wp[48] << 16);
            }
            const bf16x4 sop = as_bf16x4(make_uint2(sw01, sw23));
#pragma unroll
            for (int tp = 0; tp < 4; ++tp) {
#pragma unroll
                for (int u = 0; u < 2; ++u) {
                    const int tt = tp * 2 + u;
                    bf16x8 w5 = as_bf16x8(w5p[tt * 16]);
                    f32x4 acc = __builtin_amdgcn_mfma_f32_16x16x32_bf16(x5, w5, (f32x4){0.f, 0.f, 0.f, 0.f}, 0, 0, 0);
                    acc = __builtin_amdgcn_mfma_f32_16x16x32_bf16(gat[tp], u ? selHi : selLo, acc, 0, 0, 0);
                    f32x2 t01 = {acc[0], acc[1]}, t23 = {acc[2], acc[3]};
                    f32x2 e01 = {__builtin_amdgcn_exp2f(acc[0]), __builtin_amdgcn_exp2f(acc[1])};
                    f32x2 e23 = {__builtin_amdgcn_exp2f(acc[2]), __builtin_amdgcn_exp2f(acc[3])};
                    e01 = e01 + (f32x2){1.f, 1.f};
                    e23 = e23 + (f32x2){1.f, 1.f};
                    f32x2 r01 = {__builtin_amdgcn_rcpf(e01[0]), __builtin_amdgcn_rcpf(e01[1])};
                    f32x2 r23 = {__builtin_amdgcn_rcpf(e23[0]), __builtin_amdgcn_rcpf(e23[1])};
                    f32x2 m01 = t01 * r01, m23 = t23 * r23;
                    u32 k01 = cvt_pk(m01), k23 = cvt_pk(m23);
                    bf16x4 mb = as_bf16x4(make_uint2(k01, k23));
                    acc2[tt] = __builtin_amdgcn_mfma_f32_16x16x16bf16_1k(sop, mb, acc2[tt], 0, 0, 0);
                }
            }
        };
        for (int grp = 0; grp < 2; ++grp) {
            const int t0 = grp ? (g1s >> 4) : 0;
            const int t1 = grp ? (g1e >> 4) : (g1s >> 4);
            if (t1 <= t0) continue;
#pragma unroll
            for (int tt = 0; tt < 8; ++tt) acc2[tt] = (f32x4){0.f, 0.f, 0.f, 0.f};
            const int lastt = t1 - 1;
            issue(t0, gA, pA0, pA1);
            int tl = t0;
            for (; tl + 1 < t1; tl += 2) {
                {
                    const int rem = (g1e >> 4) - tl;
                    if (rem >= 9) __builtin_amdgcn_s_setprio(3);
                    else if (rem >= 6) __builtin_amdgcn_s_setprio(2);
                    else if (rem >= 3) __builtin_amdgcn_s_setprio(1);
                    else __builtin_amdgcn_s_setprio(0);
                }
                issue(tl + 1, gB, pB0, pB1);
                compute(tl, gA, pA0, pA1);
                issue(min(tl + 2, lastt), gA, pA0, pA1);
                compute(tl + 1, gB, pB0, pB1);
            }
            if (tl < t1) compute(tl, gA, pA0, pA1);
            const int nl = grp * 4 + g;
            float pv0 = 0.f, pv1 = 0.f, pv2 = 0.f;
#pragma unroll
            for (int tt = 0; tt < 8; ++tt) {
                float wxc = S.wxs[tt * 16 + e16];
                if (nl < nn) S.macc[rowb + nl][tt * 16 + e16] += acc2[tt][0] * NLN2;
                pv0 += acc2[tt][1] * wxc;
                pv1 += acc2[tt][2] * wxc;
                pv2 += acc2[tt][3] * wxc;
            }
#pragma unroll
            for (int o = 8; o > 0; o >>= 1) {
                pv0 += __shfl_xor(pv0, o);
                pv1 += __shfl_xor(pv1, o);
                pv2 += __shfl_xor(pv2, o);
            }
            if (e16 == 0 && nl < nn) {
                S.pacc[wv][nl][0] += pv0 * NLN2;
                S.pacc[wv][nl][1] += pv1 * NLN2;
                S.pacc[wv][nl][2] += pv2 * NLN2;
            }
        }
        return base + CAP < T;
    };
    if (round(0, std::integral_constant<bool, true>())) {
        for (int base = CAP;; base += CAP) {
            load_tab();
            if (!round(base, std::integral_constant<bool, false>())) break;
        }
    }

    int le = lane;
    asm volatile("" : "+v"(le));
    const int f16 = le & 15, gg = le >> 4;
    if (le < nn * 3) {
        int n = le / 3, cpt = le - n * 3;
        out_pos[(size_t)(n0 + n) * 3 + cpt] = f2bf(S.posl[wv][n][cpt] + S.pacc[wv][n][cpt]);
    }
    bf16x8 bw[2][8];
#pragma unroll
    for (int q = 0; q < 2; ++q)
#pragma unroll
        for (int ks = 0; ks < 8; ++ks)
            bw[q][ks] = *reinterpret_cast<const bf16x8*>(whT + (size_t)((wv * 2 + q) * 16 + f16) * 2 * D + ks * 32 + gg * 8);
    const int NR = N1 - N0;
    bf16x8 hf[2][4];
#pragma unroll
    for (int rt = 0; rt < 2; ++rt) {
        int lr = rt * 16 + f16;
        int n = N0 + (lr < NR ? lr : 0);
#pragma unroll
        for (int ks = 0; ks < 4; ++ks)
            hf[rt][ks] = *reinterpret_cast<const bf16x8*>(h + (size_t)n * D + ks * 32 + gg * 8);
    }
    u16 hvv[2][2][4];
#pragma unroll
    for (int rt = 0; rt < 2; ++rt)
#pragma unroll
        for (int q = 0; q < 2; ++q)
#pragma unroll
            for (int r = 0; r < 4; ++r) {
                int ln = rt * 16 + gg * 4 + r;
                hvv[rt][q][r] = h[(size_t)(N0 + (ln < NR ? ln : 0)) * D + (wv * 2 + q) * 16 + f16];
            }
    __syncthreads();
#pragma unroll
    for (int rt = 0; rt < 2; ++rt) {
        if (rt * 16 < NR) {
            bf16x8 af[4];
            int lr = rt * 16 + f16;
#pragma unroll
            for (int ks = 0; ks < 4; ++ks) {
                const float* mp = &S.macc[lr < NR ? lr : 0][ks * 32 + gg * 8];
                af[ks] = as_bf16x8(make_uint4(pack2(mp[0], mp[1]), pack2(mp[2], mp[3]),
                                              pack2(mp[4], mp[5]), pack2(mp[6], mp[7])));
            }
#pragma unroll
            for (int q = 0; q < 2; ++q) {
                int ct = wv * 2 + q;
                f32x4 acc = {0.f, 0.f, 0.f, 0.f};
#pragma unroll
                for (int ks = 0; ks < 4; ++ks)
                    acc = __builtin_amdgcn_mfma_f32_16x16x32_bf16(hf[rt][ks], bw[q][ks], acc, 0, 0, 0);
#pragma unroll
                for (int ks = 0; ks < 4; ++ks)
                    acc = __builtin_amdgcn_mfma_f32_16x16x32_bf16(af[ks], bw[q][4 + ks], acc, 0, 0, 0);
#pragma unroll
                for (int r = 0; r < 4; ++r) {
                    int ln = rt * 16 + gg * 4 + r;
                    if (ln < NR) {
                        int nd = N0 + ln;
                        int c = ct * 16 + f16;
                        float hv = bf2f(hvv[rt][q][r]);
                        out_h[(size_t)nd * D + c] = f2bf(hv + silu_f(acc[r]));
                    }
                }
            }
        }
    }
}

extern "C" void kernel_launch(void* const* d_in, const int* in_sizes, int n_in,
                              void* d_out, int out_size, void* d_ws, size_t ws_size,
                              hipStream_t stream) {
    const u16* h   = (const u16*)d_in[0];
    const u16* pos = (const u16*)d_in[1];
    const u16* ea  = (const u16*)d_in[2];
    const int* src = (const int*)d_in[3];
    const int* dst = (const int*)d_in[4];
    const u16* we  = (const u16*)d_in[5];
    const u16* wx  = (const u16*)d_in[6];
    const u16* wh  = (const u16*)d_in[7];
    int N = in_sizes[0] / D;
    int E = in_sizes[3];
    u16* out_h = (u16*)d_out;
    u16* out_pos = out_h + (size_t)N * D;

    int G = 128;
    while ((E + G - 1) / G > KA_THREADS * KA_ITERS && G < 64 * GI_MAX) G += 64;
    int CH = (E + G - 1) / G;
    int NBW = 3072;
    while (NBW * NPW < N && NBW < NBW_MAX) NBW += 4;
    int nGemm = (N + 127) / 128;
    int nT = (2 * D * D + KA_THREADS - 1) / KA_THREADS;
    unsigned long long mN = (1ull << 40) / (unsigned long long)N + 1ull;
    unsigned long long mNBW = (1ull << 40) / (unsigned long long)NBW + 1ull;

    char* ws = (char*)d_ws;
    size_t off = 0;
    auto take = [&](size_t bytes) { char* p = ws + off; off += (bytes + 255) & ~(size_t)255; return p; };
    uint4* rec_g = (uint4*)take((size_t)G * CH * 16);
    u16* tab = (u16*)take((size_t)G * (NBW + 1) * 2);
    float* Ag = (float*)take((size_t)N * D * 4);
    u16* Bg = (u16*)take((size_t)N * D * 2);
    u16* whT = (u16*)take((size_t)2 * D * D * 2);
    uint4* w5g = (uint4*)take((size_t)256 * 16);

    k_part<<<G + nGemm + nT, KA_THREADS, 0, stream>>>(h, src, dst, ea, we, wh, rec_g, tab, Ag, Bg, whT, w5g,
                                                      mN, mNBW, N, E, G, CH, NBW, nGemm);
    k_main<<<NBW / KB_WAVES, KB_THREADS, 0, stream>>>(h, pos, wx, rec_g, tab, Ag, Bg, whT, w5g,
                                                     out_h, out_pos, mNBW, N, G, CH, NBW);
}
```

```cpp
#include <hip/hip_runtime.h>
#include <hip/hip_bf16.h>
#include <stdint.h>
#include <type_traits>


typedef unsigned short u16;
typedef unsigned int u32;
typedef __attribute__((ext_vector_type(8))) short bf16x8;
typedef __attribute__((ext_vector_type(4))) float f32x4;
typedef __attribute__((ext_vector_type(4))) short bf16x4;
typedef __attribute__((ext_vector_type(2))) float f32x2;

#define D 128
#define KA_THREADS 1024
#define KA_ITERS 4
#define NBW_MAX 8192
#define KB_THREADS 256
#define KB_WAVES 4
#define NPW 5
#define RPL 4
#define CAP (RPL * 64)
#define CAPS 272
#define CAPR (CAP + 32)
#define GI_MAX 2
#define AST (D + 4)
#define CBAR() asm volatile("" ::: "memory")
#define NLOG2E (-1.4426950408889634f)
#define NLN2 (-0.6931471805599453f)

__device__ __forceinline__ float bf2f(u16 v) { return __uint_as_float(((u32)v) << 16); }
__device__ __forceinline__ u16 f2bf(float f) {
    __hip_bfloat16 b = __float2bfloat16(f);
    return *reinterpret_cast<u16*>(&b);
}
__device__ __forceinline__ float silu_f(float x) {
    float e = __builtin_amdgcn_exp2f(x * NLOG2E);
    return x * __builtin_amdgcn_rcpf(1.0f + e);
}
__device__ __forceinline__ u32 pack2(float a, float b) {
    return (u32)f2bf(a) | ((u32)f2bf(b) << 16);
}
typedef __attribute__((ext_vector_type(2))) __bf16 bf16x2;
__device__ __forceinline__ u32 cvt_pk(f32x2 v) {
    bf16x2 p = __builtin_convertvector(v, bf16x2);
    return *reinterpret_cast<u32*>(&p);
}
__device__ __forceinline__ bf16x8 as_bf16x8(uint4 v) { return *reinterpret_cast<bf16x8*>(&v); }
__device__ __forceinline__ bf16x4 as_bf16x4(uint2 v) { return *reinterpret_cast<bf16x4*>(&v); }
__device__ __forceinline__ u32 mdiv(u32 x, unsigned long long m) {
    return (u32)(((unsigned long long)x * m) >> 40);
}
__device__ __forceinline__ int node_start(int b, int N, int NBW, unsigned long long mNBW) {
    return (int)mdiv((u32)b * (u32)N + (u32)NBW - 1u, mNBW);
}

__global__ void __launch_bounds__(KA_THREADS)
k_part(const u16* __restrict__ h, const int* __restrict__ src, const int* __restrict__ dst,
       const u16* __restrict__ ea, const u16* __restrict__ we, const u16* __restrict__ wh,
       uint4* __restrict__ rec_g, u16* __restrict__ tab, float* __restrict__ Ag, u16* __restrict__ Bg,
       u16* __restrict__ whT, uint4* __restrict__ w5g,
       unsigned long long mN, unsigned long long mNBW,
       int N, int E, int G, int CH, int NBW, int nGemm) {
    __shared__ __align__(16) u16 smem[(NBW_MAX + 8) * 2 + KA_THREADS * KA_ITERS + KA_THREADS * KA_ITERS * 8];
    __shared__ int wsum[KA_THREADS / 64];
    int t = threadIdx.x;
    int bid = blockIdx.x;
    if (bid < G) {
        int* hist = (int*)smem;
        for (int i = t; i < NBW; i += KA_THREADS) hist[i] = 0;
        int e0 = bid * CH;
        int e1 = min(E, e0 + CH);
        int sv[KA_ITERS], bv[KA_ITERS], rk[KA_ITERS], lo[KA_ITERS];
        uint2 eav[KA_ITERS];
#pragma unroll
        for (int i = 0; i < KA_ITERS; ++i) {
            int e = e0 + t + i * KA_THREADS;
            bv[i] = -1;
            if (e < e1) {
                int d = dst[e];
                sv[i] = src[e];
                eav[i] = *reinterpret_cast<const uint2*>(ea + (size_t)e * 4);
                int b = (int)mdiv((u32)d * (u32)NBW, mN);
                bv[i] = b;
                lo[i] = d - node_start(b, N, NBW, mNBW);
            }
        }
        __syncthreads();
#pragma unroll
        for (int i = 0; i < KA_ITERS; ++i)
            if (bv[i] >= 0) rk[i] = atomicAdd(&hist[bv[i]], 1);
        __syncthreads();
        const int per = (NBW + KA_THREADS - 1) / KA_THREADS;
        int b0 = t * per, b1 = min(NBW, b0 + per);
        int cnt = 0;
        for (int b = b0; b < b1; ++b) cnt += hist[b];
        int lane = t & 63, wv = t >> 6;
        int x = cnt;
#pragma unroll
        for (int o = 1; o < 64; o <<= 1) {
            int y = __shfl_up(x, o);
            if (lane >= o) x += y;
        }
        if (lane == 63) wsum[wv] = x;
        __syncthreads();
        int base = 0;
#pragma unroll
        for (int w = 0; w < KA_THREADS / 64; ++w) base += (w < wv) ? wsum[w] : 0;
        int off = base + x - cnt;
        u16* trow = tab + (size_t)bid * (NBW + 4);
        for (int b = b0; b < b1; ++b) {
            int c = hist[b];
            hist[b] = off;
            trow[b] = (u16)off;
            off += c;
        }
        if (t == KA_THREADS - 1) { trow[NBW] = (u16)(base + x); hist[NBW] = base + x; }
        __syncthreads();
        u16* ids = reinterpret_cast<u16*>(hist + NBW_MAX + 8);
        uint4* lrec = reinterpret_cast<uint4*>(ids + KA_THREADS * KA_ITERS);
#pragma unroll
        for (int i = 0; i < KA_ITERS; ++i)
            if (bv[i] >= 0) ids[hist[bv[i]] + rk[i]] = (u16)(t + i * KA_THREADS);
        __syncthreads();
        int cs0[KA_ITERS], cn[KA_ITERS], rnk[KA_ITERS];
        int mxn = 0;
#pragma unroll
        for (int i = 0; i < KA_ITERS; ++i) {
            cs0[i] = 0; cn[i] = 0; rnk[i] = 0;
            if (bv[i] >= 0) { cs0[i] = hist[bv[i]]; cn[i] = hist[bv[i] + 1] - cs0[i]; }
            mxn = max(mxn, cn[i]);
        }
#pragma clang loop unroll(disable) vectorize(disable) interleave(disable)
        for (int q = 0; q < mxn; ++q) {
#pragma unroll
            for (int i = 0; i < KA_ITERS; ++i) {
                int v = (int)ids[cs0[i] + (q < cn[i] ? q : 0)];
                rnk[i] += (q < cn[i] && v < t + i * KA_THREADS) ? 1 : 0;
            }
        }
#pragma unroll
        for (int i = 0; i < KA_ITERS; ++i) {
            if (bv[i] >= 0) {
                int e = e0 + t + i * KA_THREADS;
                int p = cs0[i] + rnk[i];
                uint4 r;
                r.x = (u32)sv[i]; r.y = eav[i].x; r.z = eav[i].y;
                r.w = ((u32)e << 3) | (u32)lo[i];
                lrec[p] = r;
            }
        }
        __syncthreads();
#pragma unroll
        for (int i = 0; i < KA_ITERS; ++i) {
            int p = t + i * KA_THREADS;
            if (p < e1 - e0) rec_g[(size_t)bid * CH + p] = lrec[p];
        }
        return;
    }
    bid -= G;
    if (bid < nGemm) {
        {
            int i = bid * KA_THREADS + t;
            if (i < 2 * D * D) {
                int k = i >> 7, c = i & 127;
                whT[(size_t)c * 2 * D + k] = wh[i];
            }
            if (bid == nGemm - 1 && t < 2 * D) {
                int gq = t >> 7, c = t & 127;
                u32 v[6];
#pragma unroll
                for (int j = 0; j < 6; ++j) {
                    int kr = (j == 5) ? 0 : j;
                    float w = bf2f(we[(size_t)(256 + kr) * D + c]) * NLOG2E;
                    u16 hi = f2bf(w);
                    u16 lo = f2bf(w - bf2f(hi));
                    v[j] = (u32)(gq ? lo : hi);
                }
                w5g[t] = make_uint4(v[0] | (v[1] << 16), v[2] | (v[3] << 16), v[4] | (v[5] << 16), 0u);
            }
        }
        {
            const int c2 = t & 63, kb = t >> 6;
            u32 wr[16];
#pragma unroll
            for (int j = 0; j < 16; ++j)
                wr[j] = *reinterpret_cast<const u32*>(we + (size_t)(kb * 16 + j) * D + c2 * 2);
#pragma unroll
            for (int sb = 0; sb < 2; ++sb) {
                int kk = kb * 16 + sb * 8;
                int half = kk >> 7, k = kk & 127;
                const u32* w = &wr[sb * 8];
                uint4 lo4 = make_uint4((w[0] & 0xffffu) | (w[1] << 16), (w[2] & 0xffffu) | (w[3] << 16),
                                       (w[4] & 0xffffu) | (w[5] << 16), (w[6] & 0xffffu) | (w[7] << 16));
                uint4 hi4 = make_uint4((w[0] >> 16) | (w[1] & 0xffff0000u), (w[2] >> 16) | (w[3] & 0xffff0000u),
                                       (w[4] >> 16) | (w[5] & 0xffff0000u), (w[6] >> 16) | (w[7] & 0xffff0000u));
                *reinterpret_cast<uint4*>(&smem[(half * D + c2 * 2) * (D + 8) + k]) = lo4;
                *reinterpret_cast<uint4*>(&smem[(half * D + c2 * 2 + 1) * (D + 8) + k]) = hi4;
            }
        }
        __syncthreads();
        int lane = t & 63, wv = t >> 6;
        int e16 = lane & 15, g = lane >> 4;
        int rt = wv & 7;
        int half = wv >> 3;
        int n = bid * 128 + rt * 16 + e16;
        int nn = n < N ? n : N - 1;
        bf16x8 af[4];
#pragma unroll
        for (int ks = 0; ks < 4; ++ks)
            af[ks] = *reinterpret_cast<const bf16x8*>(h + (size_t)nn * D + ks * 32 + g * 8);
#pragma unroll
        for (int ct = 0; ct < 8; ++ct) {
            int c0 = ct * 16;
            f32x4 acc = {0.f, 0.f, 0.f, 0.f};
#pragma unroll
            for (int ks = 0; ks < 4; ++ks) {
                bf16x8 bfr = *reinterpret_cast<const bf16x8*>(&smem[(half * D + c0 + e16) * (D + 8) + ks * 32 + g * 8]);
                acc = __builtin_amdgcn_mfma_f32_16x16x32_bf16(af[ks], bfr, acc, 0, 0, 0);
            }
#pragma unroll
            for (int r = 0; r < 4; ++r) {
                int row = bid * 128 + rt * 16 + g * 4 + r;
                if (row < N) {
                    float v = acc[r] * NLOG2E;
                    if (half == 0) Ag[(size_t)row * D + c0 + e16] = v;
                    else Bg[(size_t)row * D + c0 + e16] = f2bf(v);
                }
            }
        }
        return;
    }
}

struct KbSmem {
    uint4 rec[KB_WAVES][CAPR];
    uint4 At[KB_WAVES][2][D];
    uint4 w5s[2][D];
    float macc[KB_WAVES * NPW][AST];
    float pacc[KB_WAVES][NPW][4];
    float posl[KB_WAVES][NPW + 3][4];
    float wxs[D];
    uint2 M[KB_WAVES][16][4];
};

__global__ void __launch_bounds__(KB_THREADS, 3)
k_main(const u16* __restrict__ h, const u16* __restrict__ pos,
       const u16* __restrict__ wx,
       const uint4* __restrict__ rec_g, const u16* __restrict__ tab,
       const float* __restrict__ Ag, const u16* __restrict__ Bg,
       const u16* __restrict__ whT, const uint4* __restrict__ w5g,
       u16* __restrict__ out_h, u16* __restrict__ out_pos,
       unsigned long long mNBW, int N, int G, int CH, int NBW) {
    __shared__ KbSmem S;
    const int t = threadIdx.x, lane = t & 63;
    const int wv = __builtin_amdgcn_readfirstlane(t >> 6);
    const int e16 = lane & 15, g = lane >> 4;
    const int wb = blockIdx.x * KB_WAVES + wv;
    const int N0 = node_start(blockIdx.x * KB_WAVES, N, NBW, mNBW);
    const int N1 = node_start(blockIdx.x * KB_WAVES + KB_WAVES, N, NBW, mNBW);
    const int n0 = node_start(wb, N, NBW, mNBW);
    const int nn = node_start(wb + 1, N, NBW, mNBW) - n0;
    const int rowb = n0 - N0;

    int so[GI_MAX], sc[GI_MAX];
    u16 (*tabs)[5] = reinterpret_cast<u16 (*)[5]>(&S.M[0][0][0]);
    auto fetch_tab = [&]() {
        if (t < G) {
            const u16* tp = tab + (size_t)t * (NBW + 4) + blockIdx.x * KB_WAVES;
            uint2 v = *reinterpret_cast<const uint2*>(tp);
            u32 v4 = tp[4];
            tabs[t][0] = (u16)v.x; tabs[t][1] = (u16)(v.x >> 16);
            tabs[t][2] = (u16)v.y; tabs[t][3] = (u16)(v.y >> 16);
            tabs[t][4] = (u16)v4;
        }
    };
    auto load_tab = [&]() {
        int lo_ = lane;
        asm volatile("" : "+v"(lo_));
#pragma unroll
        for (int ci = 0; ci < GI_MAX; ++ci) {
            int c = lo_ + 64 * ci;
            so[ci] = 0; sc[ci] = 0;
            if (c < G) {
                so[ci] = tabs[c][wv];
                sc[ci] = tabs[c][wv + 1];
            }
        }
    };
    auto load_tab_g = [&]() {
        int lo_ = lane;
        asm volatile("" : "+v"(lo_));
#pragma unroll
        for (int ci = 0; ci < GI_MAX; ++ci) {
            int c = lo_ + 64 * ci;
            so[ci] = 0; sc[ci] = 0;
            if (c < G) {
                const u16* tp = tab + (size_t)c * (NBW + 4) + wb;
                so[ci] = tp[0];
                sc[ci] = tp[1];
            }
        }
    };
    fetch_tab();
    float a0[NPW], a1[NPW];
#pragma unroll
    for (int j = 0; j < NPW; ++j) {
        a0[j] = 0.f; a1[j] = 0.f;
        if (j < nn) {
            a0[j] = Ag[(size_t)(n0 + j) * D + lane];
            a1[j] = Ag[(size_t)(n0 + j) * D + 64 + lane];
        }
    }
    const uint4 w40 = w5g[lane], w41 = w5g[lane + 64], w42 = w5g[lane + 128], w43 = w5g[lane + 192];
    u16 wx0 = wx[lane], wx1 = wx[lane + 64];
    u16 pz16 = 0;
    {
        int n = lane >> 2, cpt = lane & 3;
        if (n < nn && cpt < 3) pz16 = pos[(n0 + n) * 3 + cpt];
    }
    auto stage = [&]() {
#pragma unroll
        for (int hf = 0; hf < 2; ++hf) {
            u32 hi[NPW], mi[NPW], lw[NPW];
#pragma unroll
            for (int j = 0; j < NPW; ++j) {
                float a = hf ? a1[j] : a0[j];
                u16 x = f2bf(a);
                float r1 = a - bf2f(x);
                u16 y = f2bf(r1);
                u16 z = f2bf(r1 - bf2f(y));
                hi[j] = x; mi[j] = y; lw[j] = z;
            }
            int c = hf * 64 + lane;
            S.At[wv][0][c] = make_uint4(hi[0] | (hi[1] << 16), hi[2] | (hi[3] << 16), mi[0] | (mi[1] << 16), mi[2] | (mi[3] << 16));
            S.At[wv][1][c] = make_uint4(lw[0] | (lw[1] << 16), lw[2] | (lw[3] << 16), hi[4] | (mi[4] << 16), lw[4]);
#pragma unroll
            for (int j = 0; j < NPW; ++j)
                if (j < nn) S.macc[rowb + j][c] = 0.f;
        }
        uint4* wq = &S.w5s[0][0] + lane;
        wq[0] = w40; wq[64] = w41; wq[128] = w42; wq[192] = w43;
        S.wxs[lane] = bf2f(wx0);
        S.wxs[lane + 64] = bf2f(wx1);
        if (lane < (NPW + 3) * 4) S.posl[wv][lane >> 2][lane & 3] = bf2f(pz16);
        if (lane < NPW * 4) S.pacc[wv][lane >> 2][lane & 3] = 0.f;
    };
    const unsigned long long ltmask = (1ull << lane) - 1ull;
    uint4* rec = S.rec[wv];
    u32* key = reinterpret_cast<u32*>(S.rec[wv]);

    __syncthreads();
    load_tab();
    __syncthreads();
    auto round = [&](const int base, auto firstc) __attribute__((always_inline)) -> bool {
        constexpr bool FIRST = decltype(firstc)::value;
        int T;
        int fo[GI_MAX];
        {
            int tot = 0;
#pragma unroll
            for (int ci = 0; ci < GI_MAX; ++ci) { sc[ci] -= so[ci]; tot += sc[ci]; }
            int x = tot;
            int ls = lane;
            asm volatile("" : "+v"(ls));
#pragma unroll
            for (int o = 1; o < 64; o <<= 1) {
                int y = __builtin_amdgcn_ds_bpermute((ls - o) << 2, x);
                if (ls >= o) x += y;
            }
            T = __builtin_amdgcn_readlane(x, 63);
            int off = x - tot;
#pragma unroll
            for (int ci = 0; ci < GI_MAX; ++ci) { fo[ci] = off; off += sc[ci]; }
        }
        const int Tr = min(CAP, T - base);
        int lo2 = lane;
        asm volatile("" : "+v"(lo2));
#pragma unroll
        for (int ci = 0; ci < GI_MAX; ++ci) {
            int gs = (lo2 + 64 * ci) * CH + so[ci];
            for (int q = 0; q < sc[ci]; ++q) {
                int fp = fo[ci] + q - base;
                if (fp >= 0 && fp < CAP) key[fp] = (u32)(gs + q);
            }
        }
        CBAR();
        uint4 r[RPL];
#pragma unroll
        for (int i = 0; i < RPL; ++i) {
            int p = lane + 64 * i;
            r[i] = make_uint4(0u, 0u, 0u, 0xffffffffu);
            if (p < Tr) r[i] = rec_g[key[p]];
        }
        if (FIRST) stage();
        int posn[RPL];
        int cst = 0, cnt4 = 0, c0 = 0, g1s = 0;
#pragma unroll
        for (int i = 0; i < RPL; ++i) posn[i] = 0;
#pragma unroll
        for (int j = 0; j < NPW; ++j) {
            if (j == 4) { c0 = cst; g1s = (cst + 15) & ~15; cst = g1s; }
            int c = 0;
#pragma unroll
            for (int i = 0; i < RPL; ++i) {
                bool mt = (r[i].w != 0xffffffffu) && ((int)(r[i].w & 7u) == j);
                unsigned long long m = __ballot(mt);
                if (mt) posn[i] = cst + c + __popcll(m & ltmask);
                c += __popcll(m);
            }
            if (j == 4) cnt4 = c;
            cst += c;
        }
        const int g1e = (g1s + cnt4 + 15) & ~15;
        CBAR();
#pragma unroll
        for (int i = 0; i < RPL; ++i)
            if (r[i].w != 0xffffffffu) rec[posn[i]] = r[i];
        if (lane < 16) {
            if (c0 + lane < g1s) rec[c0 + lane] = make_uint4(0u, 0u, 0u, 0xffffffffu);
            if (g1s + cnt4 + lane < g1e) rec[g1s + cnt4 + lane] = make_uint4(0u, 0u, 0u, 0xffffffffu);
        }
        CBAR();

        int lq = lane;
        asm volatile("" : "+v"(lq));
        bf16x8 selLo, selHi;
        {
            int dl = (lq & 15) - 8 * (lq >> 4);
            unsigned long long one = 0x3F80ull;
            u32 l0 = 0, l1 = 0, l2 = 0, l3 = 0, h0 = 0, h1 = 0, h2 = 0, h3 = 0;
            if (dl >= 0 && dl < 4) { unsigned long long v = one << (16 * dl); l0 = (u32)v; l1 = (u32)(v >> 32); }
            if (dl >= 4 && dl < 8) { unsigned long long v = one << (16 * (dl - 4)); l2 = (u32)v; l3 = (u32)(v >> 32); }
            int dh = dl + 16;
            if (dh >= 0 && dh < 4) { unsigned long long v = one << (16 * dh); h0 = (u32)v; h1 = (u32)(v >> 32); }
            if (dh >= 4 && dh < 8) { unsigned long long v = one << (16 * (dh - 4)); h2 = (u32)v; h3 = (u32)(v >> 32); }
            selLo = as_bf16x8(make_uint4(l0, l1, l2, l3));
            selHi = as_bf16x8(make_uint4(h0, h1, h2, h3));
        }
        const uint4* w5p = (g < 2) ? &S.w5s[g][e16] : &S.At[wv][g - 2][e16];
        bf16x8 gA[4], gB[4];
        u32 pA0 = 0, pA1 = 0, pB0 = 0, pB1 = 0;
        auto issue = [&](int tl, bf16x8 (&gat)[4], u32& q0, u32& q1) {
            int sidx = (int)rec[tl * 16 + e16].x;
            const u16* brow = Bg + (size_t)sidx * D + g * 8;
#pragma unroll
            for (int tp = 0; tp < 4; ++tp)
                gat[tp] = *reinterpret_cast<const bf16x8*>(brow + tp * 32);
            const u16* pp = pos + (size_t)sidx * 3;
            q0 = (u32)pp[0] | ((u32)pp[1] << 16);
            q1 = (u32)pp[2];
        };
        f32x4 acc2[8];
        auto compute = [&](int tl, bf16x8 (&gat)[4], u32 q0, u32 q1) {
            const uint4 rme = rec[tl * 16 + e16];
            const bool vme = rme.w != 0xffffffffu;
            const int nd = (int)(rme.w & 7u);
            const float4 pn = *reinterpret_cast<const float4*>(&S.posl[wv][nd][0]);
            float dx = pn.x - bf2f((u16)(q0 & 0xffffu));
            float dy = pn.y - bf2f((u16)(q0 >> 16));
            float dz = pn.z - bf2f((u16)q1);
            float d2v = dx * dx + dy * dy + dz * dz;
            u16 d2h = f2bf(d2v);
            u16 d2l = f2bf(d2v - bf2f(d2h));
            {
                bool own = vme && ((nd & 3) == g);
                S.M[wv][e16][g] = own ? make_uint2(0x3F80u | ((u32)f2bf(dx) << 16), (u32)f2bf(dy) | ((u32)f2bf(dz) << 16))
                                      : make_uint2(0u, 0u);
            }
            uint4 xu;
            {
                unsigned long long oh = (nd < 4) ? (0x3F80ull << (16 * nd)) : 0ull;
                u32 o01 = (u32)oh, o23 = (u32)(oh >> 32);
                u32 f4 = (nd == 4) ? 0x3F80u : 0u;
                if (g < 2) xu = make_uint4((u32)d2h | (rme.y << 16), (rme.y >> 16) | (rme.z << 16), (rme.z >> 16) | ((u32)d2l << 16), 0u);
                else if (g == 2) xu = make_uint4(o01, o23, o01, o23);
                else xu = make_uint4(o01, o23, f4 | (f4 << 16), f4);
                if (!vme) xu = make_uint4(0u, 0u, 0u, 0u);
            }
            const bf16x8 x5 = as_bf16x8(xu);
            asm volatile("s_waitcnt lgkmcnt(0)" ::: "memory");
            u32 sw01, sw23;
            {
                const u16* wp = reinterpret_cast<const u16*>(&S.M[wv][g * 4][0]) + e16;
                sw01 = (u32)wp[0] | ((u32)wp[16] << 16);
                sw23 = (u32)wp[32] | ((u32)wp[48] << 16);
            }
            const bf16x4 sop = as_bf16x4(make_uint2(sw01, sw23));
#pragma unroll
            for (int tp = 0; tp < 4; ++tp) {
#pragma unroll
                for (int u = 0; u < 2; ++u) {
                    const int tt = tp * 2 + u;
                    bf16x8 w5 = as_bf16x8(w5p[tt * 16]);
                    f32x4 acc = __builtin_amdgcn_mfma_f32_16x16x32_bf16(x5, w5, (f32x4){0.f, 0.f, 0.f, 0.f}, 0, 0, 0);
                    acc = __builtin_amdgcn_mfma_f32_16x16x32_bf16(gat[tp], u ? selHi : selLo, acc, 0, 0, 0);
                    f32x2 t01 = {acc[0], acc[1]}, t23 = {acc[2], acc[3]};
                    f32x2 e01 = {__builtin_amdgcn_exp2f(acc[0]), __builtin_amdgcn_exp2f(acc[1])};
                    f32x2 e23 = {__builtin_amdgcn_exp2f(acc[2]), __builtin_amdgcn_exp2f(acc[3])};
                    e01 = e01 + (f32x2){1.f, 1.f};
                    e23 = e23 + (f32x2){1.f, 1.f};
                    f32x2 r01 = {__builtin_amdgcn_rcpf(e01[0]), __builtin_amdgcn_rcpf(e01[1])};
                    f32x2 r23 = {__builtin_amdgcn_rcpf(e23[0]), __builtin_amdgcn_rcpf(e23[1])};
                    f32x2 m01 = t01 * r01, m23 = t23 * r23;
                    u32 k01 = cvt_pk(m01), k23 = cvt_pk(m23);
                    bf16x4 mb = as_bf16x4(make_uint2(k01, k23));
                    acc2[tt] = __builtin_amdgcn_mfma_f32_16x16x16bf16_1k(sop, mb, acc2[tt], 0, 0, 0);
                }
            }
        };
        for (int grp = 0; grp < 2; ++grp) {
            const int t0 = grp ? (g1s >> 4) : 0;
            const int t1 = grp ? (g1e >> 4) : (g1s >> 4);
            if (t1 <= t0) continue;
#pragma unroll
            for (int tt = 0; tt < 8; ++tt) acc2[tt] = (f32x4){0.f, 0.f, 0.f, 0.f};
            const int lastt = t1 - 1;
            issue(t0, gA, pA0, pA1);
            int tl = t0;
            for (; tl + 1 < t1; tl += 2) {
                {
                    const int rem = (g1e >> 4) - tl;
                    if (rem >= 9) __builtin_amdgcn_s_setprio(3);
                    else if (rem >= 6) __builtin_amdgcn_s_setprio(2);
                    else if (rem >= 3) __builtin_amdgcn_s_setprio(1);
                    else __builtin_amdgcn_s_setprio(0);
                }
                issue(tl + 1, gB, pB0, pB1);
                compute(tl, gA, pA0, pA1);
                issue(min(tl + 2, lastt), gA, pA0, pA1);
                compute(tl + 1, gB, pB0, pB1);
            }
            if (tl < t1) compute(tl, gA, pA0, pA1);
            const int nl = grp * 4 + g;
            float pv0 = 0.f, pv1 = 0.f, pv2 = 0.f;
#pragma unroll
            for (int tt = 0; tt < 8; ++tt) {
                float wxc = S.wxs[tt * 16 + e16];
                if (nl < nn) S.macc[rowb + nl][tt * 16 + e16] += acc2[tt][0] * NLN2;
                pv0 += acc2[tt][1] * wxc;
                pv1 += acc2[tt][2] * wxc;
                pv2 += acc2[tt][3] * wxc;
            }
#pragma unroll
            for (int o = 8; o > 0; o >>= 1) {
                pv0 += __shfl_xor(pv0, o);
                pv1 += __shfl_xor(pv1, o);
                pv2 += __shfl_xor(pv2, o);
            }
            if (e16 == 0 && nl < nn) {
                S.pacc[wv][nl][0] += pv0 * NLN2;
                S.pacc[wv][nl][1] += pv1 * NLN2;
                S.pacc[wv][nl][2] += pv2 * NLN2;
            }
        }
        return base + CAP < T;
    };
    if (round(0, std::integral_constant<bool, true>())) {
        for (int base = CAP;; base += CAP) {
            load_tab_g();
            if (!round(base, std::integral_constant<bool, false>())) break;
        }
    }

    int le = lane;
    asm volatile("" : "+v"(le));
    const int f16 = le & 15, gg = le >> 4;
    if (le < nn * 3) {
        int n = le / 3, cpt = le - n * 3;
        out_pos[(size_t)(n0 + n) * 3 + cpt] = f2bf(S.posl[wv][n][cpt] + S.pacc[wv][n][cpt]);
    }
    bf16x8 bw[2][8];
#pragma unroll
    for (int q = 0; q < 2; ++q)
#pragma unroll
        for (int ks = 0; ks < 8; ++ks)
            bw[q][ks] = *reinterpret_cast<const bf16x8*>(whT + (size_t)((wv * 2 + q) * 16 + f16) * 2 * D + ks * 32 + gg * 8);
    const int NR = N1 - N0;
    bf16x8 hf[2][4];
#pragma unroll
    for (int rt = 0; rt < 2; ++rt) {
        int lr = rt * 16 + f16;
        int n = N0 + (lr < NR ? lr : 0);
#pragma unroll
        for (int ks = 0; ks < 4; ++ks)
            hf[rt][ks] = *reinterpret_cast<const bf16x8*>(h + (size_t)n * D + ks * 32 + gg * 8);
    }
    u16 hvv[2][2][4];
#pragma unroll
    for (int rt = 0; rt < 2; ++rt)
#pragma unroll
        for (int q = 0; q < 2; ++q)
#pragma unroll
            for (int r = 0; r < 4; ++r) {
                int ln = rt * 16 + gg * 4 + r;
                hvv[rt][q][r] = h[(size_t)(N0 + (ln < NR ? ln : 0)) * D + (wv * 2 + q) * 16 + f16];
            }
    __syncthreads();
#pragma unroll
    for (int rt = 0; rt < 2; ++rt) {
        if (rt * 16 < NR) {
            bf16x8 af[4];
            int lr = rt * 16 + f16;
#pragma unroll
            for (int ks = 0; ks < 4; ++ks) {
                const float* mp = &S.macc[lr < NR ? lr : 0][ks * 32 + gg * 8];
                af[ks] = as_bf16x8(make_uint4(pack2(mp[0], mp[1]), pack2(mp[2], mp[3]),
                                              pack2(mp[4], mp[5]), pack2(mp[6], mp[7])));
            }
#pragma unroll
            for (int q = 0; q < 2; ++q) {
                int ct = wv * 2 + q;
                f32x4 acc = {0.f, 0.f, 0.f, 0.f};
#pragma unroll
                for (int ks = 0; ks < 4; ++ks)
                    acc = __builtin_amdgcn_mfma_f32_16x16x32_bf16(hf[rt][ks], bw[q][ks], acc, 0, 0, 0);
#pragma unroll
                for (int ks = 0; ks < 4; ++ks)
                    acc = __builtin_amdgcn_mfma_f32_16x16x32_bf16(af[ks], bw[q][4 + ks], acc, 0, 0, 0);
#pragma unroll
                for (int r = 0; r < 4; ++r) {
                    int ln = rt * 16 + gg * 4 + r;
                    if (ln < NR) {
                        int nd = N0 + ln;
                        int c = ct * 16 + f16;
                        float hv = bf2f(hvv[rt][q][r]);
                        out_h[(size_t)nd * D + c] = f2bf(hv + silu_f(acc[r]));
                    }
                }
            }
        }
    }
}

extern "C" void kernel_launch(void* const* d_in, const int* in_sizes, int n_in,
                              void* d_out, int out_size, void* d_ws, size_t ws_size,
                              hipStream_t stream) {
    const u16* h   = (const u16*)d_in[0];
    const u16* pos = (const u16*)d_in[1];
    const u16* ea  = (const u16*)d_in[2];
    const int* src = (const int*)d_in[3];
    const int* dst = (const int*)d_in[4];
    const u16* we  = (const u16*)d_in[5];
    const u16* wx  = (const u16*)d_in[6];
    const u16* wh  = (const u16*)d_in[7];
    int N = in_sizes[0] / D;
    int E = in_sizes[3];
    u16* out_h = (u16*)d_out;
    u16* out_pos = out_h + (size_t)N * D;

    int G = 128;
    while ((E + G - 1) / G > KA_THREADS * KA_ITERS && G < 64 * GI_MAX) G += 64;
    int CH = (E + G - 1) / G;
    int NBW = 3072;
    while (NBW * NPW < N && NBW < NBW_MAX) NBW += 4;
    int nGemm = (N + 127) / 128;
    if (nGemm < 2 * D * D / KA_THREADS) nGemm = 2 * D * D / KA_THREADS;
    unsigned long long mN = (1ull << 40) / (unsigned long long)N + 1ull;
    unsigned long long mNBW = (1ull << 40) / (unsigned long long)NBW + 1ull;

    char* ws = (char*)d_ws;
    size_t off = 0;
    auto take = [&](size_t bytes) { char* p = ws + off; off += (bytes + 255) & ~(size_t)255; return p; };
    uint4* rec_g = (uint4*)take((size_t)G * CH * 16);
    u16* tab = (u16*)take((size_t)G * (NBW + 4) * 2);
    float* Ag = (float*)take((size_t)N * D * 4);
    u16* Bg = (u16*)take((size_t)N * D * 2);
    u16* whT = (u16*)take((size_t)2 * D * D * 2);
    uint4* w5g = (uint4*)take((size_t)256 * 16);

    k_part<<<G + nGemm, KA_THREADS, 0, stream>>>(h, src, dst, ea, we, wh, rec_g, tab, Ag, Bg, whT, w5g,
                                                      mN, mNBW, N, E, G, CH, NBW, nGemm);
    k_main<<<NBW / KB_WAVES, KB_THREADS, 0, stream>>>(h, pos, wx, rec_g, tab, Ag, Bg, whT, w5g,
                                                     out_h, out_pos, mNBW, N, G, CH, NBW);
}
```

```cpp
#include <hip/hip_runtime.h>
#include <hip/hip_bf16.h>
#include <stdint.h>
#include <type_traits>


typedef unsigned short u16;
typedef unsigned int u32;
typedef __attribute__((ext_vector_type(8))) short bf16x8;
typedef __attribute__((ext_vector_type(4))) float f32x4;
typedef __attribute__((ext_vector_type(4))) short bf16x4;
typedef __attribute__((ext_vector_type(2))) float f32x2;

#define D 128
#define KA_THREADS 1024
#define KA_ITERS 4
#define NBW_MAX 8192
#define KB_THREADS 256
#define KB_WAVES 4
#define NPW 5
#define RPL 4
#define CAP (RPL * 64)
#define CAPS 272
#define CAPR (CAP + 32)
#define GI_MAX 2
#define AST (D + 4)
#define CBAR() asm volatile("" ::: "memory")
#define NLOG2E (-1.4426950408889634f)
#define NLN2 (-0.6931471805599453f)

__device__ __forceinline__ float bf2f(u16 v) { return __uint_as_float(((u32)v) << 16); }
__device__ __forceinline__ u16 f2bf(float f) {
    __hip_bfloat16 b = __float2bfloat16(f);
    return *reinterpret_cast<u16*>(&b);
}
__device__ __forceinline__ float silu_f(float x) {
    float e = __builtin_amdgcn_exp2f(x * NLOG2E);
    return x * __builtin_amdgcn_rcpf(1.0f + e);
}
__device__ __forceinline__ u32 pack2(float a, float b) {
    return (u32)f2bf(a) | ((u32)f2bf(b) << 16);
}
typedef __attribute__((ext_vector_type(2))) __bf16 bf16x2;
__device__ __forceinline__ u32 cvt_pk(f32x2 v) {
    bf16x2 p = __builtin_convertvector(v, bf16x2);
    return *reinterpret_cast<u32*>(&p);
}
__device__ __forceinline__ bf16x8 as_bf16x8(uint4 v) { return *reinterpret_cast<bf16x8*>(&v); }
__device__ __forceinline__ bf16x4 as_bf16x4(uint2 v) { return *reinterpret_cast<bf16x4*>(&v); }
template <int CTRL>
__device__ __forceinline__ float dpp_f(float x) {
    return __int_as_float(__builtin_amdgcn_update_dpp(0, __float_as_int(x), CTRL, 0xf, 0xf, true));
}
__device__ __forceinline__ float rsum16(float x) {
    x += dpp_f<0x128>(x);
    x += dpp_f<0x124>(x);
    x += dpp_f<0x122>(x);
    x += dpp_f<0x121>(x);
    return x;
}
__device__ __forceinline__ u32 mdiv(u32 x, unsigned long long m) {
    return (u32)(((unsigned long long)x * m) >> 40);
}
__device__ __forceinline__ int node_start(int b, int N, int NBW, unsigned long long mNBW) {
    return (int)mdiv((u32)b * (u32)N + (u32)NBW - 1u, mNBW);
}

__global__ void __launch_bounds__(KA_THREADS)
k_part(const u16* __restrict__ h, const int* __restrict__ src, const int* __restrict__ dst,
       const u16* __restrict__ ea, const u16* __restrict__ we, const u16* __restrict__ wh,
       uint4* __restrict__ rec_g, u16* __restrict__ tab, float* __restrict__ Ag, u16* __restrict__ Bg,
       u16* __restrict__ whT, uint4* __restrict__ w5g, u32* __restrict__ UT,
       unsigned long long mN, unsigned long long mNBW,
       int N, int E, int G, int CH, int NBW, int nGemm) {
    __shared__ __align__(16) u16 smem[(NBW_MAX + 8) * 2 + KA_THREADS * KA_ITERS + KA_THREADS * KA_ITERS * 8];
    __shared__ int wsum[KA_THREADS / 64];
    int t = threadIdx.x;
    int bid = blockIdx.x;
    if (bid < G) {
        int* hist = (int*)smem;
        for (int i = t; i < NBW; i += KA_THREADS) hist[i] = 0;
        int e0 = bid * CH;
        int e1 = min(E, e0 + CH);
        int sv[KA_ITERS], bv[KA_ITERS], rk[KA_ITERS], lo[KA_ITERS];
        uint2 eav[KA_ITERS];
#pragma unroll
        for (int i = 0; i < KA_ITERS; ++i) {
            int e = e0 + t + i * KA_THREADS;
            bv[i] = -1;
            if (e < e1) {
                int d = dst[e];
                sv[i] = src[e];
                eav[i] = *reinterpret_cast<const uint2*>(ea + (size_t)e * 4);
                int b = (int)mdiv((u32)d * (u32)NBW, mN);
                bv[i] = b;
                lo[i] = d - node_start(b, N, NBW, mNBW);
            }
        }
        __syncthreads();
#pragma unroll
        for (int i = 0; i < KA_ITERS; ++i)
            if (bv[i] >= 0) rk[i] = atomicAdd(&hist[bv[i]], 1);
        __syncthreads();
        const int per = (NBW + KA_THREADS - 1) / KA_THREADS;
        int b0 = t * per, b1 = min(NBW, b0 + per);
        int cnt = 0;
        for (int b = b0; b < b1; ++b) cnt += hist[b];
        int lane = t & 63, wv = t >> 6;
        int x = cnt;
#pragma unroll
        for (int o = 1; o < 64; o <<= 1) {
            int y = __shfl_up(x, o);
            if (lane >= o) x += y;
        }
        if (lane == 63) wsum[wv] = x;
        __syncthreads();
        int base = 0;
#pragma unroll
        for (int w = 0; w < KA_THREADS / 64; ++w) base += (w < wv) ? wsum[w] : 0;
        int off = base + x - cnt;
        u16* trow = tab + (size_t)bid * (NBW + 4);
        for (int b = b0; b < b1; ++b) {
            int c = hist[b];
            hist[b] = off;
            trow[b] = (u16)off;
            off += c;
        }
        if (t == KA_THREADS - 1) { trow[NBW] = (u16)(base + x); hist[NBW] = base + x; }
        __syncthreads();
        u16* ids = reinterpret_cast<u16*>(hist + NBW_MAX + 8);
        uint4* lrec = reinterpret_cast<uint4*>(ids + KA_THREADS * KA_ITERS);
#pragma unroll
        for (int i = 0; i < KA_ITERS; ++i)
            if (bv[i] >= 0) ids[hist[bv[i]] + rk[i]] = (u16)(t + i * KA_THREADS);
        __syncthreads();
        int cs0[KA_ITERS], cn[KA_ITERS], rnk[KA_ITERS];
        int mxn = 0;
#pragma unroll
        for (int i = 0; i < KA_ITERS; ++i) {
            cs0[i] = 0; cn[i] = 0; rnk[i] = 0;
            if (bv[i] >= 0) { cs0[i] = hist[bv[i]]; cn[i] = hist[bv[i] + 1] - cs0[i]; }
            mxn = max(mxn, cn[i]);
        }
#pragma clang loop unroll(disable) vectorize(disable) interleave(disable)
        for (int q = 0; q < mxn; ++q) {
#pragma unroll
            for (int i = 0; i < KA_ITERS; ++i) {
                int v = (int)ids[cs0[i] + (q < cn[i] ? q : 0)];
                rnk[i] += (q < cn[i] && v < t + i * KA_THREADS) ? 1 : 0;
            }
        }
#pragma unroll
        for (int i = 0; i < KA_ITERS; ++i) {
            if (bv[i] >= 0) {
                int e = e0 + t + i * KA_THREADS;
                int p = cs0[i] + rnk[i];
                uint4 r;
                r.x = (u32)sv[i]; r.y = eav[i].x; r.z = eav[i].y;
                r.w = ((u32)e << 3) | (u32)lo[i];
                lrec[p] = r;
            }
        }
        __syncthreads();
#pragma unroll
        for (int i = 0; i < KA_ITERS; ++i) {
            int p = t + i * KA_THREADS;
            if (p < e1 - e0) rec_g[(size_t)bid * CH + p] = lrec[p];
        }
        return;
    }
    bid -= G;
    if (bid < nGemm) {
        {
            int i = bid * KA_THREADS + t;
            if (i < 2 * D * D) {
                int k = i >> 7, c = i & 127;
                whT[(size_t)c * 2 * D + k] = wh[i];
            }
            if (bid == nGemm - 1 && t < 2 * D) {
                int gq = t >> 7, c = t & 127;
                u32 v[6];
#pragma unroll
                for (int j = 0; j < 6; ++j) {
                    int kr = (j == 5) ? 0 : j;
                    float w = bf2f(we[(size_t)(256 + kr) * D + c]) * NLOG2E;
                    u16 hi = f2bf(w);
                    u16 lo = f2bf(w - bf2f(hi));
                    v[j] = (u32)(gq ? lo : hi);
                }
                w5g[t] = make_uint4(v[0] | (v[1] << 16), v[2] | (v[3] << 16), v[4] | (v[5] << 16), 0u);
            }
        }
        {
            const int c2 = t & 63, kb = t >> 6;
            u32 wr[16];
#pragma unroll
            for (int j = 0; j < 16; ++j)
                wr[j] = *reinterpret_cast<const u32*>(we + (size_t)(kb * 16 + j) * D + c2 * 2);
#pragma unroll
            for (int sb = 0; sb < 2; ++sb) {
                int kk = kb * 16 + sb * 8;
                int half = kk >> 7, k = kk & 127;
                const u32* w = &wr[sb * 8];
                uint4 lo4 = make_uint4((w[0] & 0xffffu) | (w[1] << 16), (w[2] & 0xffffu) | (w[3] << 16),
                                       (w[4] & 0xffffu) | (w[5] << 16), (w[6] & 0xffffu) | (w[7] << 16));
                uint4 hi4 = make_uint4((w[0] >> 16) | (w[1] & 0xffff0000u), (w[2] >> 16) | (w[3] & 0xffff0000u),
                                       (w[4] >> 16) | (w[5] & 0xffff0000u), (w[6] >> 16) | (w[7] & 0xffff0000u));
                *reinterpret_cast<uint4*>(&smem[(half * D + c2 * 2) * (D + 8) + k]) = lo4;
                *reinterpret_cast<uint4*>(&smem[(half * D + c2 * 2 + 1) * (D + 8) + k]) = hi4;
            }
        }
        {
            const int c2 = t & 63, kb = t >> 6;
            u32 w[8];
#pragma unroll
            for (int j = 0; j < 8; ++j)
                w[j] = *reinterpret_cast<const u32*>(wh + (size_t)(kb * 8 + j) * D + c2 * 2);
            int k = kb * 8;
            uint4 lo4 = make_uint4((w[0] & 0xffffu) | (w[1] << 16), (w[2] & 0xffffu) | (w[3] << 16),
                                   (w[4] & 0xffffu) | (w[5] << 16), (w[6] & 0xffffu) | (w[7] << 16));
            uint4 hi4 = make_uint4((w[0] >> 16) | (w[1] & 0xffff0000u), (w[2] >> 16) | (w[3] & 0xffff0000u),
                                   (w[4] >> 16) | (w[5] & 0xffff0000u), (w[6] >> 16) | (w[7] & 0xffff0000u));
            *reinterpret_cast<uint4*>(&smem[(2 * D + c2 * 2) * (D + 8) + k]) = lo4;
            *reinterpret_cast<uint4*>(&smem[(2 * D + c2 * 2 + 1) * (D + 8) + k]) = hi4;
        }
        __syncthreads();
        int lane = t & 63, wv = t >> 6;
        int e16 = lane & 15, g = lane >> 4;
        int rt = wv & 7;
        int half = wv >> 3;
        int n = bid * 128 + rt * 16 + e16;
        int nn = n < N ? n : N - 1;
        bf16x8 af[4];
#pragma unroll
        for (int ks = 0; ks < 4; ++ks)
            af[ks] = *reinterpret_cast<const bf16x8*>(h + (size_t)nn * D + ks * 32 + g * 8);
#pragma unroll
        for (int ct = 0; ct < 8; ++ct) {
            int c0 = ct * 16;
            f32x4 acc = {0.f, 0.f, 0.f, 0.f};
#pragma unroll
            for (int ks = 0; ks < 4; ++ks) {
                bf16x8 bfr = *reinterpret_cast<const bf16x8*>(&smem[(half * D + c0 + e16) * (D + 8) + ks * 32 + g * 8]);
                acc = __builtin_amdgcn_mfma_f32_16x16x32_bf16(af[ks], bfr, acc, 0, 0, 0);
            }
#pragma unroll
            for (int r = 0; r < 4; ++r) {
                int row = bid * 128 + rt * 16 + g * 4 + r;
                if (row < N) {
                    float v = acc[r] * NLOG2E;
                    if (half == 0) Ag[(size_t)row * D + c0 + e16] = v;
                    else Bg[(size_t)row * D + c0 + e16] = f2bf(v);
                }
            }
        }
        const int NP = (N + 35) & ~3;
#pragma unroll
        for (int cq = 0; cq < 4; ++cq) {
            int c0 = (half * 4 + cq) * 16;
            f32x4 acc = {0.f, 0.f, 0.f, 0.f};
#pragma unroll
            for (int ks = 0; ks < 4; ++ks) {
                bf16x8 bfr = *reinterpret_cast<const bf16x8*>(&smem[(2 * D + c0 + e16) * (D + 8) + ks * 32 + g * 8]);
                acc = __builtin_amdgcn_mfma_f32_16x16x32_bf16(af[ks], bfr, acc, 0, 0, 0);
            }
            int row = bid * 128 + rt * 16 + g * 4;
            u32 o[4];
#pragma unroll
            for (int r = 0; r < 4; ++r) {
                int rr = row + r < N ? row + r : N - 1;
                o[r] = (u32)f2bf(acc[r]) | ((u32)h[(size_t)rr * D + c0 + e16] << 16);
            }
            if (row < N) *reinterpret_cast<uint4*>(UT + (size_t)(c0 + e16) * NP + row) = make_uint4(o[0], o[1], o[2], o[3]);
        }
        return;
    }
}

struct KbSmem {
    uint4 rec[KB_WAVES][CAPR];
    uint4 At[KB_WAVES][2][D];
    uint4 w5s[2][D];
    float macc[KB_WAVES * NPW][AST];
    float pacc[KB_WAVES][NPW][4];
    float posl[KB_WAVES][NPW + 3][4];
    float wxs[D];
    uint2 M[KB_WAVES][16][4];
    int nt[KB_WAVES][4];
};

__global__ void __launch_bounds__(KB_THREADS, 3)
k_main(const u16* __restrict__ h, const u16* __restrict__ pos,
       const u16* __restrict__ wx,
       const uint4* __restrict__ rec_g, const u16* __restrict__ tab,
       const float* __restrict__ Ag, const u16* __restrict__ Bg,
       const u16* __restrict__ whT, const uint4* __restrict__ w5g, const u32* __restrict__ UT,
       u16* __restrict__ out_h, u16* __restrict__ out_pos,
       unsigned long long mNBW, int N, int G, int CH, int NBW) {
    __shared__ KbSmem S;
    const int t = threadIdx.x, lane = t & 63;
    const int wv = __builtin_amdgcn_readfirstlane(t >> 6);
    const int e16 = lane & 15, g = lane >> 4;
    const int wb = blockIdx.x * KB_WAVES + wv;
    const int N0 = node_start(blockIdx.x * KB_WAVES, N, NBW, mNBW);
    const int N1 = node_start(blockIdx.x * KB_WAVES + KB_WAVES, N, NBW, mNBW);
    const int n0 = node_start(wb, N, NBW, mNBW);
    const int nn = node_start(wb + 1, N, NBW, mNBW) - n0;
    const int rowb = n0 - N0;

    int so[GI_MAX], sc[GI_MAX];
    u16 (*tabs)[5] = reinterpret_cast<u16 (*)[5]>(&S.M[0][0][0]);
    auto fetch_tab = [&]() {
        if (t < G) {
            const u16* tp = tab + (size_t)t * (NBW + 4) + blockIdx.x * KB_WAVES;
            uint2 v = *reinterpret_cast<const uint2*>(tp);
            u32 v4 = tp[4];
            tabs[t][0] = (u16)v.x; tabs[t][1] = (u16)(v.x >> 16);
            tabs[t][2] = (u16)v.y; tabs[t][3] = (u16)(v.y >> 16);
            tabs[t][4] = (u16)v4;
        }
    };
    auto load_tab = [&]() {
        int lo_ = lane;
        asm volatile("" : "+v"(lo_));
#pragma unroll
        for (int ci = 0; ci < GI_MAX; ++ci) {
            int c = lo_ + 64 * ci;
            so[ci] = 0; sc[ci] = 0;
            if (c < G) {
                so[ci] = tabs[c][wv];
                sc[ci] = tabs[c][wv + 1];
            }
        }
    };
    auto load_tab_g = [&]() {
        int lo_ = lane;
        asm volatile("" : "+v"(lo_));
#pragma unroll
        for (int ci = 0; ci < GI_MAX; ++ci) {
            int c = lo_ + 64 * ci;
            so[ci] = 0; sc[ci] = 0;
            if (c < G) {
                const u16* tp = tab + (size_t)c * (NBW + 4) + wb;
                so[ci] = tp[0];
                sc[ci] = tp[1];
            }
        }
    };
    fetch_tab();
    float a0[NPW], a1[NPW];
#pragma unroll
    for (int j = 0; j < NPW; ++j) {
        a0[j] = 0.f; a1[j] = 0.f;
        if (j < nn) {
            a0[j] = Ag[(size_t)(n0 + j) * D + lane];
            a1[j] = Ag[(size_t)(n0 + j) * D + 64 + lane];
        }
    }
    const uint4 w40 = w5g[lane], w41 = w5g[lane + 64], w42 = w5g[lane + 128], w43 = w5g[lane + 192];
    u16 wx0 = wx[lane], wx1 = wx[lane + 64];
    u16 pz16 = 0;
    {
        int n = lane >> 2, cpt = lane & 3;
        if (n < nn && cpt < 3) pz16 = pos[(n0 + n) * 3 + cpt];
    }
    auto stage = [&]() {
#pragma unroll
        for (int hf = 0; hf < 2; ++hf) {
            u32 hi[NPW], mi[NPW], lw[NPW];
#pragma unroll
            for (int j = 0; j < NPW; ++j) {
                float a = hf ? a1[j] : a0[j];
                u16 x = f2bf(a);
                float r1 = a - bf2f(x);
                u16 y = f2bf(r1);
                u16 z = f2bf(r1 - bf2f(y));
                hi[j] = x; mi[j] = y; lw[j] = z;
            }
            int c = hf * 64 + lane;
            S.At[wv][0][c] = make_uint4(hi[0] | (hi[1] << 16), hi[2] | (hi[3] << 16), mi[0] | (mi[1] << 16), mi[2] | (mi[3] << 16));
            S.At[wv][1][c] = make_uint4(lw[0] | (lw[1] << 16), lw[2] | (lw[3] << 16), hi[4] | (mi[4] << 16), lw[4]);
#pragma unroll
            for (int j = 0; j < NPW; ++j)
                if (j < nn) S.macc[rowb + j][c] = 0.f;
        }
        uint4* wq = &S.w5s[0][0] + lane;
        wq[0] = w40; wq[64] = w41; wq[128] = w42; wq[192] = w43;
        S.wxs[lane] = bf2f(wx0);
        S.wxs[lane + 64] = bf2f(wx1);
        if (lane < (NPW + 3) * 4) S.posl[wv][lane >> 2][lane & 3] = bf2f(pz16);
        if (lane < NPW * 4) S.pacc[wv][lane >> 2][lane & 3] = 0.f;
    };
    const unsigned long long ltmask = (1ull << lane) - 1ull;
    uint4* rec = S.rec[wv];
    u32* key = reinterpret_cast<u32*>(S.rec[wv]);

    __syncthreads();
    load_tab();
    __syncthreads();
    f32x4 acc2[8];
    int dHead = 0, hflags = 0;
    float hm[8], hp0, hp1, hp2;
    auto reduce = [&]() __attribute__((always_inline)) {
        float wxc[8];
#pragma unroll
        for (int tt = 0; tt < 8; ++tt) wxc[tt] = S.wxs[tt * 16 + e16];
        float pv0 = 0.f, pv1 = 0.f, pv2 = 0.f;
#pragma unroll
        for (int tt = 0; tt < 8; ++tt) {
            hm[tt] = acc2[tt][0] * NLN2;
            pv0 += acc2[tt][1] * wxc[tt];
            pv1 += acc2[tt][2] * wxc[tt];
            pv2 += acc2[tt][3] * wxc[tt];
            acc2[tt] = (f32x4){0.f, 0.f, 0.f, 0.f};
        }
        hp0 = rsum16(pv0) * NLN2; hp1 = rsum16(pv1) * NLN2; hp2 = rsum16(pv2) * NLN2;
    };
    auto commit = [&](int d, auto accc) __attribute__((always_inline)) {
        constexpr bool ACC = decltype(accc)::value;
        const int bk = (d >> 16) & 3;
        const int nl = ((d >> 18) & 1) * 4 + g;
        const int rb = (d >> 20) & 31, nb = (d >> 25) & 7;
        const bool on = nl < nb;
        float* mrow = &S.macc[rb + (on ? nl : 0)][e16];
        float* prow = &S.pacc[bk][on ? nl : 0][0];
        float old[8], po[3];
        if (ACC) {
#pragma unroll
            for (int tt = 0; tt < 8; ++tt) old[tt] = mrow[tt * 16];
            po[0] = prow[0]; po[1] = prow[1]; po[2] = prow[2];
            CBAR();
        }
        if (on) {
#pragma unroll
            for (int tt = 0; tt < 8; ++tt) mrow[tt * 16] = ACC ? old[tt] + hm[tt] : hm[tt];
            if (e16 == 0) {
                prow[0] = (ACC ? po[0] : 0.f) + hp0;
                prow[1] = (ACC ? po[1] : 0.f) + hp1;
                prow[2] = (ACC ? po[2] : 0.f) + hp2;
            }
        }
    };
    auto flush = [&](int d, auto accc) __attribute__((always_inline)) { reduce(); commit(d, accc); };
    auto finish = [&]() __attribute__((always_inline)) {
        const std::integral_constant<bool, true> accc;
        __syncthreads();
        for (int w = 1; w < KB_WAVES; ++w) {
            if ((hflags & 1) && (wv == w || !(hflags & 2))) commit(dHead, accc);
            __syncthreads();
            if (!(hflags & 2)) break;
        }
    };
    auto round = [&](const int base, auto firstc) __attribute__((always_inline)) -> bool {
        constexpr bool FIRST = decltype(firstc)::value;
        int T;
        int fo[GI_MAX];
        {
            int tot = 0;
#pragma unroll
            for (int ci = 0; ci < GI_MAX; ++ci) { sc[ci] -= so[ci]; tot += sc[ci]; }
            int x = tot;
            int ls = lane;
            asm volatile("" : "+v"(ls));
#pragma unroll
            for (int o = 1; o < 64; o <<= 1) {
                int y = __builtin_amdgcn_ds_bpermute((ls - o) << 2, x);
                if (ls >= o) x += y;
            }
            T = __builtin_amdgcn_readlane(x, 63);
            int off = x - tot;
#pragma unroll
            for (int ci = 0; ci < GI_MAX; ++ci) { fo[ci] = off; off += sc[ci]; }
        }
        const int Tr = max(0, min(CAP, T - base));
        int lo2 = lane;
        asm volatile("" : "+v"(lo2));
#pragma unroll
        for (int ci = 0; ci < GI_MAX; ++ci) {
            int gs = (lo2 + 64 * ci) * CH + so[ci];
            for (int q = 0; q < sc[ci]; ++q) {
                int fp = fo[ci] + q - base;
                if (fp >= 0 && fp < CAP) key[fp] = (u32)(gs + q);
            }
        }
        CBAR();
        uint4 r[RPL];
#pragma unroll
        for (int i = 0; i < RPL; ++i) {
            int p = lane + 64 * i;
            r[i] = make_uint4(0u, 0u, 0u, 0xffffffffu);
            if (p < Tr) r[i] = rec_g[key[p]];
        }
        if (FIRST) stage();
        int posn[RPL];
        int cst = 0, cnt4 = 0, c0 = 0, g1s = 0;
#pragma unroll
        for (int i = 0; i < RPL; ++i) posn[i] = 0;
#pragma unroll
        for (int j = 0; j < NPW; ++j) {
            if (j == 4) { c0 = cst; g1s = (cst + 15) & ~15; cst = g1s; }
            int c = 0;
#pragma unroll
            for (int i = 0; i < RPL; ++i) {
                bool mt = (r[i].w != 0xffffffffu) && ((int)(r[i].w & 7u) == j);
                unsigned long long m = __ballot(mt);
                if (mt) posn[i] = cst + c + __popcll(m & ltmask);
                c += __popcll(m);
            }
            if (j == 4) cnt4 = c;
            cst += c;
        }
        const int g1e = (g1s + cnt4 + 15) & ~15;
        CBAR();
#pragma unroll
        for (int i = 0; i < RPL; ++i)
            if (r[i].w != 0xffffffffu) rec[posn[i]] = r[i];
        if (lane < 16) {
            if (c0 + lane < g1s) rec[c0 + lane] = make_uint4(0u, 0u, 0u, 0xffffffffu);
            if (g1s + cnt4 + lane < g1e) rec[g1s + cnt4 + lane] = make_uint4(0u, 0u, 0u, 0xffffffffu);
        }
        if (lane == 0) {
            S.nt[wv][0] = g1s >> 4;
            S.nt[wv][1] = g1e >> 4;
            S.nt[wv][2] = (base + CAP < T) ? 1 : 0;
        }
        __syncthreads();

        int sb1, sb2, sb3, sb4, sb5, sb6, sb7, TT, anymore;
        {
            const int4 q0 = *reinterpret_cast<const int4*>(&S.nt[0][0]);
            const int4 q1 = *reinterpret_cast<const int4*>(&S.nt[1][0]);
            const int4 q2 = *reinterpret_cast<const int4*>(&S.nt[2][0]);
            const int4 q3 = *reinterpret_cast<const int4*>(&S.nt[3][0]);
            sb1 = __builtin_amdgcn_readfirstlane(q0.x);
            sb2 = __builtin_amdgcn_readfirstlane(q0.y);
            sb3 = sb2 + __builtin_amdgcn_readfirstlane(q1.x);
            sb4 = sb2 + __builtin_amdgcn_readfirstlane(q1.y);
            sb5 = sb4 + __builtin_amdgcn_readfirstlane(q2.x);
            sb6 = sb4 + __builtin_amdgcn_readfirstlane(q2.y);
            sb7 = sb6 + __builtin_amdgcn_readfirstlane(q3.x);
            TT = sb6 + __builtin_amdgcn_readfirstlane(q3.y);
            anymore = __builtin_amdgcn_readfirstlane(q0.z | q1.z | q2.z | q3.z);
        }
        auto seg_of = [&](int x) __attribute__((always_inline)) {
            return (x >= sb1) + (x >= sb2) + (x >= sb3) + (x >= sb4) + (x >= sb5) + (x >= sb6) + (x >= sb7);
        };
        auto is_start = [&](int x) __attribute__((always_inline)) {
            return (x == 0) | (x == sb1) | (x == sb2) | (x == sb3) | (x == sb4) | (x == sb5) | (x == sb6) | (x == sb7);
        };
        auto seg_end = [&](int x) __attribute__((always_inline)) {
            int r = TT;
            r = (x < sb7) ? +sb7 : +r; r = (x < sb6) ? +sb6 : +r; r = (x < sb5) ? +sb5 : +r; r = (x < sb4) ? +sb4 : +r;
            r = (x < sb3) ? +sb3 : +r; r = (x < sb2) ? +sb2 : +r; r = (x < sb1) ? +sb1 : +r;
            return r;
        };
        const int ts = (TT * wv) >> 2, te = (TT * (wv + 1)) >> 2;
        const int nst = te - ts;
        const bool has_head = (nst > 0) && !is_start(ts);
        const int th = has_head ? min(te, seg_end(ts)) : +ts;
        bool serial = false;
        {
            const int s1 = TT >> 2, s2 = (TT * 2) >> 2, s3 = (TT * 3) >> 2;
            serial = (!is_start(s1) && seg_end(s1) > s2) || (!is_start(s2) && seg_end(s2) > s3);
        }
        int lq = lane;
        asm volatile("" : "+v"(lq));
        int descv;
        {
            const int ns1 = node_start(blockIdx.x * KB_WAVES + 1, N, NBW, mNBW) - N0;
            const int ns2 = node_start(blockIdx.x * KB_WAVES + 2, N, NBW, mNBW) - N0;
            const int ns3 = node_start(blockIdx.x * KB_WAVES + 3, N, NBW, mNBW) - N0;
            const int ns4 = N1 - N0;
            const int nh = te - th;
            const int tl = (lq < nh) ? (th + lq) : (ts + lq - nh);
            const int k = seg_of(tl);
            const int b = k >> 1;
            const int cb = (b == 0) ? 0 : (b == 1) ? +sb2 : (b == 2) ? +sb4 : +sb6;
            const int r0 = (b == 0) ? 0 : (b == 1) ? +ns1 : (b == 2) ? +ns2 : +ns3;
            const int r1 = (b == 0) ? +ns1 : (b == 1) ? +ns2 : (b == 2) ? +ns3 : +ns4;
            const int fl = (lq < nh) && ((tl + 1 == te) || is_start(tl + 1));
            descv = ((b * CAPR + (tl - cb) * 16) * 16) | (b << 16) | ((k & 1) << 18) | (fl << 19) | (r0 << 20) | ((r1 - r0) << 25);
        }
        bf16x8 selLo, selHi;
        {
            int dl = (lq & 15) - 8 * (lq >> 4);
            unsigned long long one = 0x3F80ull;
            u32 l0 = 0, l1 = 0, l2 = 0, l3 = 0, h0 = 0, h1 = 0, h2 = 0, h3 = 0;
            if (dl >= 0 && dl < 4) { unsigned long long v = one << (16 * dl); l0 = (u32)v; l1 = (u32)(v >> 32); }
            if (dl >= 4 && dl < 8) { unsigned long long v = one << (16 * (dl - 4)); l2 = (u32)v; l3 = (u32)(v >> 32); }
            int dh = dl + 16;
            if (dh >= 0 && dh < 4) { unsigned long long v = one << (16 * dh); h0 = (u32)v; h1 = (u32)(v >> 32); }
            if (dh >= 4 && dh < 8) { unsigned long long v = one << (16 * (dh - 4)); h2 = (u32)v; h3 = (u32)(v >> 32); }
            selLo = as_bf16x8(make_uint4(l0, l1, l2, l3));
            selHi = as_bf16x8(make_uint4(h0, h1, h2, h3));
        }
        const char* w5b = (g < 2) ? reinterpret_cast<const char*>(&S.w5s[g][e16]) : reinterpret_cast<const char*>(&S.At[0][g - 2][e16]);
        const int w5m = (g < 2) ? 0 : (int)sizeof(S.At[0]);
        const char* recb = reinterpret_cast<const char*>(&S.rec[0][e16]);
        bf16x8 gA[4], gB[4];
        u32 pA0 = 0, pA1 = 0, pB0 = 0, pB1 = 0;
        auto issue = [&](int d, bf16x8 (&gat)[4], u32& q0, u32& q1) __attribute__((always_inline)) {
            int sidx = (int)reinterpret_cast<const uint4*>(recb + (d & 0xffff))->x;
            const u16* brow = Bg + (size_t)sidx * D + g * 8;
#pragma unroll
            for (int tp = 0; tp < 4; ++tp)
                gat[tp] = *reinterpret_cast<const bf16x8*>(brow + tp * 32);
            const u16* pp = pos + (size_t)sidx * 3;
            q0 = (u32)pp[0] | ((u32)pp[1] << 16);
            q1 = (u32)pp[2];
        };
#pragma unroll
        for (int tt = 0; tt < 8; ++tt) acc2[tt] = (f32x4){0.f, 0.f, 0.f, 0.f};
        auto compute = [&](int d, bf16x8 (&gat)[4], u32 q0, u32 q1) __attribute__((always_inline)) {
            const int bk = (d >> 16) & 3;
            const uint4 rme = *reinterpret_cast<const uint4*>(recb + (d & 0xffff));
            const bool vme = rme.w != 0xffffffffu;
            const int nd = (int)(rme.w & 7u);
            const float4 pn = *reinterpret_cast<const float4*>(&S.posl[bk][nd][0]);
            const uint4* w5p = reinterpret_cast<const uint4*>(w5b + w5m * bk);
            float dx = pn.x - bf2f((u16)(q0 & 0xffffu));
            float dy = pn.y - bf2f((u16)(q0 >> 16));
            float dz = pn.z - bf2f((u16)q1);
            float d2v = dx * dx + dy * dy + dz * dz;
            u16 d2h = f2bf(d2v);
            u16 d2l = f2bf(d2v - bf2f(d2h));
            {
                bool own = vme && ((nd & 3) == g);
                S.M[wv][e16][g] = own ? make_uint2(0x3F80u | ((u32)f2bf(dx) << 16), (u32)f2bf(dy) | ((u32)f2bf(dz) << 16))
                                      : make_uint2(0u, 0u);
            }
            uint4 xu;
            {
                unsigned long long oh = (nd < 4) ? (0x3F80ull << (16 * nd)) : 0ull;
                u32 o01 = (u32)oh, o23 = (u32)(oh >> 32);
                u32 f4 = (nd == 4) ? 0x3F80u : 0u;
                if (g < 2) xu = make_uint4((u32)d2h | (rme.y << 16), (rme.y >> 16) | (rme.z << 16), (rme.z >> 16) | ((u32)d2l << 16), 0u);
                else if (g == 2) xu = make_uint4(o01, o23, o01, o23);
                else xu = make_uint4(o01, o23, f4 | (f4 << 16), f4);
                if (!vme) xu = make_uint4(0u, 0u, 0u, 0u);
            }
            const bf16x8 x5 = as_bf16x8(xu);
            asm volatile("s_waitcnt lgkmcnt(0)" ::: "memory");
            u32 sw01, sw23;
            {
                const u16* wp = reinterpret_cast<const u16*>(&S.M[wv][g * 4][0]) + e16;
                sw01 = (u32)wp[0] | ((u32)wp[16] << 16);
                sw23 = (u32)wp[32] | ((u32)wp[48] << 16);
            }
            const bf16x4 sop = as_bf16x4(make_uint2(sw01, sw23));
#pragma unroll
            for (int tp = 0; tp < 4; ++tp) {
#pragma unroll
                for (int u = 0; u < 2; ++u) {
                    const int tt = tp * 2 + u;
                    bf16x8 w5 = as_bf16x8(w5p[tt * 16]);
                    f32x4 acc = __builtin_amdgcn_mfma_f32_16x16x32_bf16(x5, w5, (f32x4){0.f, 0.f, 0.f, 0.f}, 0, 0, 0);
                    acc = __builtin_amdgcn_mfma_f32_16x16x32_bf16(gat[tp], u ? selHi : selLo, acc, 0, 0, 0);
                    f32x2 t01 = {acc[0], acc[1]}, t23 = {acc[2], acc[3]};
                    f32x2 e01 = {__builtin_amdgcn_exp2f(acc[0]), __builtin_amdgcn_exp2f(acc[1])};
                    f32x2 e23 = {__builtin_amdgcn_exp2f(acc[2]), __builtin_amdgcn_exp2f(acc[3])};
                    e01 = e01 + (f32x2){1.f, 1.f};
                    e23 = e23 + (f32x2){1.f, 1.f};
                    f32x2 r01 = {__builtin_amdgcn_rcpf(e01[0]), __builtin_amdgcn_rcpf(e01[1])};
                    f32x2 r23 = {__builtin_amdgcn_rcpf(e23[0]), __builtin_amdgcn_rcpf(e23[1])};
                    f32x2 m01 = t01 * r01, m23 = t23 * r23;
                    u32 k01 = cvt_pk(m01), k23 = cvt_pk(m23);
                    bf16x4 mb = as_bf16x4(make_uint2(k01, k23));
                    acc2[tt] = __builtin_amdgcn_mfma_f32_16x16x16bf16_1k(sop, mb, acc2[tt], 0, 0, 0);
                }
            }
        };
        const std::integral_constant<bool, !FIRST> dirc;
        if (nst > 0) {
            issue(__builtin_amdgcn_readlane(descv, 0), gA, pA0, pA1);
            int i = 0;
            for (; i + 1 < nst; i += 2) {
                {
                    const int rem = nst - i;
                    if (rem >= 9) __builtin_amdgcn_s_setprio(3);
                    else if (rem >= 6) __builtin_amdgcn_s_setprio(2);
                    else if (rem >= 3) __builtin_amdgcn_s_setprio(1);
                    else __builtin_amdgcn_s_setprio(0);
                }
                const int dA = __builtin_amdgcn_readlane(descv, i);
                const int dB = __builtin_amdgcn_readlane(descv, i + 1);
                const int dC = __builtin_amdgcn_readlane(descv, min(i + 2, nst - 1));
                issue(dB, gB, pB0, pB1);
                compute(dA, gA, pA0, pA1);
                if (dA & (1 << 19)) flush(dA, dirc);
                issue(dC, gA, pA0, pA1);
                compute(dB, gB, pB0, pB1);
                if (dB & (1 << 19)) flush(dB, dirc);
            }
            if (i < nst) {
                const int dA = __builtin_amdgcn_readlane(descv, i);
                compute(dA, gA, pA0, pA1);
                if (dA & (1 << 19)) flush(dA, dirc);
            }
        }
        dHead = __builtin_amdgcn_readlane(descv, max(nst - 1, 0));
        hflags = (has_head ? 1 : 0) | (serial ? 2 : 0);
        reduce();
        return anymore != 0;
    };
    if (round(0, std::integral_constant<bool, true>())) {
        for (int base = CAP;; base += CAP) {
            finish();
            load_tab_g();
            if (!round(base, std::integral_constant<bool, false>())) break;
        }
    }

    int le = lane;
    asm volatile("" : "+v"(le));
    const int f16 = le & 15, gg = le >> 4;
    bf16x8 bw[2][4];
#pragma unroll
    for (int q = 0; q < 2; ++q)
#pragma unroll
        for (int ks = 0; ks < 4; ++ks)
            bw[q][ks] = *reinterpret_cast<const bf16x8*>(whT + (size_t)((wv * 2 + q) * 16 + f16) * 2 * D + D + ks * 32 + gg * 8);
    const int NR = N1 - N0;
    const int NP = (N + 35) & ~3;
    u32 uh[2][2][4];
#pragma unroll
    for (int rt = 0; rt < 2; ++rt)
#pragma unroll
        for (int q = 0; q < 2; ++q) {
            const u32* p = UT + (size_t)((wv * 2 + q) * 16 + f16) * NP + N0 + rt * 16 + gg * 4;
#pragma unroll
            for (int r = 0; r < 4; ++r) uh[rt][q][r] = p[r];
        }
    finish();
    if (le < nn * 3) {
        int n = le / 3, cpt = le - n * 3;
        out_pos[(size_t)(n0 + n) * 3 + cpt] = f2bf(S.posl[wv][n][cpt] + S.pacc[wv][n][cpt]);
    }
#pragma unroll
    for (int rt = 0; rt < 2; ++rt) {
        if (rt * 16 < NR) {
            bf16x8 af[4];
            int lr = rt * 16 + f16;
#pragma unroll
            for (int ks = 0; ks < 4; ++ks) {
                const float* mp = &S.macc[lr < NR ? lr : 0][ks * 32 + gg * 8];
                af[ks] = as_bf16x8(make_uint4(pack2(mp[0], mp[1]), pack2(mp[2], mp[3]),
                                              pack2(mp[4], mp[5]), pack2(mp[6], mp[7])));
            }
#pragma unroll
            for (int q = 0; q < 2; ++q) {
                int ct = wv * 2 + q;
                f32x4 acc = {0.f, 0.f, 0.f, 0.f};
#pragma unroll
                for (int ks = 0; ks < 4; ++ks)
                    acc = __builtin_amdgcn_mfma_f32_16x16x32_bf16(af[ks], bw[q][ks], acc, 0, 0, 0);
#pragma unroll
                for (int r = 0; r < 4; ++r) {
                    int ln = rt * 16 + gg * 4 + r;
                    if (ln < NR) {
                        int nd = N0 + ln;
                        int c = ct * 16 + f16;
                        float hv = __uint_as_float(uh[rt][q][r] & 0xffff0000u);
                        float uv = __uint_as_float(uh[rt][q][r] << 16);
                        out_h[(size_t)nd * D + c] = f2bf(hv + silu_f(uv + acc[r]));
                    }
                }
            }
        }
    }
}

extern "C" void kernel_launch(void* const* d_in, const int* in_sizes, int n_in,
                              void* d_out, int out_size, void* d_ws, size_t ws_size,
                              hipStream_t stream) {
    const u16* h   = (const u16*)d_in[0];
    const u16* pos = (const u16*)d_in[1];
    const u16* ea  = (const u16*)d_in[2];
    const int* src = (const int*)d_in[3];
    const int* dst = (const int*)d_in[4];
    const u16* we  = (const u16*)d_in[5];
    const u16* wx  = (const u16*)d_in[6];
    const u16* wh  = (const u16*)d_in[7];
    int N = in_sizes[0] / D;
    int E = in_sizes[3];
    u16* out_h = (u16*)d_out;
    u16* out_pos = out_h + (size_t)N * D;

    int G = 128;
    while ((E + G - 1) / G > KA_THREADS * KA_ITERS && G < 64 * GI_MAX) G += 64;
    int CH = (E + G - 1) / G;
    int NBW = 3072;
    while (NBW * NPW < N && NBW < NBW_MAX) NBW += 4;
    int nGemm = (N + 127) / 128;
    if (nGemm < 2 * D * D / KA_THREADS) nGemm = 2 * D * D / KA_THREADS;
    unsigned long long mN = (1ull << 40) / (unsigned long long)N + 1ull;
    unsigned long long mNBW = (1ull << 40) / (unsigned long long)NBW + 1ull;

    char* ws = (char*)d_ws;
    size_t off = 0;
    auto take = [&](size_t bytes) { char* p = ws + off; off += (bytes + 255) & ~(size_t)255; return p; };
    uint4* rec_g = (uint4*)take((size_t)G * CH * 16);
    u16* tab = (u16*)take((size_t)G * (NBW + 4) * 2);
    float* Ag = (float*)take((size_t)N * D * 4);
    u16* Bg = (u16*)take((size_t)N * D * 2);
    u16* whT = (u16*)take((size_t)2 * D * D * 2);
    uint4* w5g = (uint4*)take((size_t)256 * 16);
    u32* UT = (u32*)take((size_t)D * ((N + 35) & ~3) * 4);

    k_part<<<G + nGemm, KA_THREADS, 0, stream>>>(h, src, dst, ea, we, wh, rec_g, tab, Ag, Bg, whT, w5g, UT,
                                                      mN, mNBW, N, E, G, CH, NBW, nGemm);
    k_main<<<NBW / KB_WAVES, KB_THREADS, 0, stream>>>(h, pos, wx, rec_g, tab, Ag, Bg, whT, w5g, UT,
                                                     out_h, out_pos, mNBW, N, G, CH, NBW);
}
```

```cpp
#include <hip/hip_runtime.h>
#include <hip/hip_bf16.h>
#include <stdint.h>
#include <type_traits>


typedef unsigned short u16;
typedef unsigned int u32;
typedef __attribute__((ext_vector_type(8))) short bf16x8;
typedef __attribute__((ext_vector_type(4))) float f32x4;
typedef __attribute__((ext_vector_type(4))) short bf16x4;
typedef __attribute__((ext_vector_type(2))) float f32x2;

#define D 128
#define KA_THREADS 1024
#define KA_ITERS 4
#define NBW_MAX 8192
#define KB_THREADS 256
#define KB_WAVES 4
#define NPW 5
#define RPL 4
#define CAP (RPL * 64)
#define CAPS 272
#define CAPR (CAP + 32)
#define GI_MAX 2
#define AST (D + 4)
#define CBAR() asm volatile("" ::: "memory")
#define NLOG2E (-1.4426950408889634f)
#define NLN2 (-0.6931471805599453f)

__device__ __forceinline__ float bf2f(u16 v) { return __uint_as_float(((u32)v) << 16); }
__device__ __forceinline__ u16 f2bf(float f) {
    __hip_bfloat16 b = __float2bfloat16(f);
    return *reinterpret_cast<u16*>(&b);
}
__device__ __forceinline__ float silu_f(float x) {
    float e = __builtin_amdgcn_exp2f(x * NLOG2E);
    return x * __builtin_amdgcn_rcpf(1.0f + e);
}
__device__ __forceinline__ u32 pack2(float a, float b) {
    return (u32)f2bf(a) | ((u32)f2bf(b) << 16);
}
typedef __attribute__((ext_vector_type(2))) __bf16 bf16x2;
__device__ __forceinline__ u32 cvt_pk(f32x2 v) {
    bf16x2 p = __builtin_convertvector(v, bf16x2);
    return *reinterpret_cast<u32*>(&p);
}
__device__ __forceinline__ bf16x8 as_bf16x8(uint4 v) { return *reinterpret_cast<bf16x8*>(&v); }
__device__ __forceinline__ bf16x4 as_bf16x4(uint2 v) { return *reinterpret_cast<bf16x4*>(&v); }
template <int CTRL>
__device__ __forceinline__ float dpp_f(float x) {
    return __int_as_float(__builtin_amdgcn_update_dpp(0, __float_as_int(x), CTRL, 0xf, 0xf, true));
}
__device__ __forceinline__ float rsum16(float x) {
    x += dpp_f<0x128>(x);
    x += dpp_f<0x124>(x);
    x += dpp_f<0x122>(x);
    x += dpp_f<0x121>(x);
    return x;
}
__device__ __forceinline__ u32 mdiv(u32 x, unsigned long long m) {
    return (u32)(((unsigned long long)x * m) >> 40);
}
__device__ __forceinline__ int node_start(int b, int N, int NBW, unsigned long long mNBW) {
    return (int)mdiv((u32)b * (u32)N + (u32)NBW - 1u, mNBW);
}

__global__ void __launch_bounds__(KA_THREADS)
k_part(const u16* __restrict__ h, const int* __restrict__ src, const int* __restrict__ dst,
       const u16* __restrict__ ea, const u16* __restrict__ we, const u16* __restrict__ wh,
       uint4* __restrict__ rec_g, u16* __restrict__ tab, u16* __restrict__ ABU,
       u16* __restrict__ whT, uint4* __restrict__ w5g,
       unsigned long long mN, unsigned long long mNBW,
       int N, int E, int G, int CH, int NBW, int nGemm) {
    __shared__ __align__(16) u16 smem[(NBW_MAX + 8) * 2 + KA_THREADS * KA_ITERS + KA_THREADS * KA_ITERS * 8];
    __shared__ int wsum[KA_THREADS / 64];
    int t = threadIdx.x;
    int bid = blockIdx.x;
    if (bid < G) {
        int* hist = (int*)smem;
        for (int i = t; i < NBW; i += KA_THREADS) hist[i] = 0;
        int e0 = bid * CH;
        int e1 = min(E, e0 + CH);
        int sv[KA_ITERS], bv[KA_ITERS], rk[KA_ITERS], lo[KA_ITERS];
        uint2 eav[KA_ITERS];
#pragma unroll
        for (int i = 0; i < KA_ITERS; ++i) {
            int e = e0 + t + i * KA_THREADS;
            bv[i] = -1;
            if (e < e1) {
                int d = dst[e];
                sv[i] = src[e];
                eav[i] = *reinterpret_cast<const uint2*>(ea + (size_t)e * 4);
                int b = (int)mdiv((u32)d * (u32)NBW, mN);
                bv[i] = b;
                lo[i] = d - node_start(b, N, NBW, mNBW);
            }
        }
        __syncthreads();
#pragma unroll
        for (int i = 0; i < KA_ITERS; ++i)
            if (bv[i] >= 0) rk[i] = atomicAdd(&hist[bv[i]], 1);
        __syncthreads();
        const int per = (NBW + KA_THREADS - 1) / KA_THREADS;
        int b0 = t * per, b1 = min(NBW, b0 + per);
        int cnt = 0;
        for (int b = b0; b < b1; ++b) cnt += hist[b];
        int lane = t & 63, wv = t >> 6;
        int x = cnt;
#pragma unroll
        for (int o = 1; o < 64; o <<= 1) {
            int y = __shfl_up(x, o);
            if (lane >= o) x += y;
        }
        if (lane == 63) wsum[wv] = x;
        __syncthreads();
        int base = 0;
#pragma unroll
        for (int w = 0; w < KA_THREADS / 64; ++w) base += (w < wv) ? wsum[w] : 0;
        int off = base + x - cnt;
        u16* trow = tab + (size_t)bid * (NBW + 4);
        for (int b = b0; b < b1; ++b) {
            int c = hist[b];
            hist[b] = off;
            trow[b] = (u16)off;
            off += c;
        }
        if (t == KA_THREADS - 1) { trow[NBW] = (u16)(base + x); hist[NBW] = base + x; }
        __syncthreads();
        u16* ids = reinterpret_cast<u16*>(hist + NBW_MAX + 8);
        uint4* lrec = reinterpret_cast<uint4*>(ids + KA_THREADS * KA_ITERS);
#pragma unroll
        for (int i = 0; i < KA_ITERS; ++i)
            if (bv[i] >= 0) ids[hist[bv[i]] + rk[i]] = (u16)(t + i * KA_THREADS);
        __syncthreads();
        int cs0[KA_ITERS], cn[KA_ITERS], rnk[KA_ITERS];
        int mxn = 0;
#pragma unroll
        for (int i = 0; i < KA_ITERS; ++i) {
            cs0[i] = 0; cn[i] = 0; rnk[i] = 0;
            if (bv[i] >= 0) { cs0[i] = hist[bv[i]]; cn[i] = hist[bv[i] + 1] - cs0[i]; }
            mxn = max(mxn, cn[i]);
        }
#pragma clang loop unroll(disable) vectorize(disable) interleave(disable)
        for (int q = 0; q < mxn; ++q) {
#pragma unroll
            for (int i = 0; i < KA_ITERS; ++i) {
                int v = (int)ids[cs0[i] + (q < cn[i] ? q : 0)];
                rnk[i] += (q < cn[i] && v < t + i * KA_THREADS) ? 1 : 0;
            }
        }
#pragma unroll
        for (int i = 0; i < KA_ITERS; ++i) {
            if (bv[i] >= 0) {
                int e = e0 + t + i * KA_THREADS;
                int p = cs0[i] + rnk[i];
                uint4 r;
                r.x = (u32)sv[i]; r.y = eav[i].x; r.z = eav[i].y;
                r.w = ((u32)e << 3) | (u32)lo[i];
                lrec[p] = r;
            }
        }
        __syncthreads();
#pragma unroll
        for (int i = 0; i < KA_ITERS; ++i) {
            int p = t + i * KA_THREADS;
            if (p < e1 - e0) rec_g[(size_t)bid * CH + p] = lrec[p];
        }
        return;
    }
    bid -= G;
    if (bid < nGemm) {
        {
            int i = bid * KA_THREADS + t;
            if (i < D * D) {
                int k = i >> 7, c = i & 127;
                whT[(size_t)c * D + k] = wh[D * D + i];
            }
            if (bid == nGemm - 1 && t < 2 * D) {
                int gq = t >> 7, c = t & 127;
                u32 v[6];
#pragma unroll
                for (int j = 0; j < 6; ++j) {
                    int kr = (j == 5) ? 0 : j;
                    float w = bf2f(we[(size_t)(256 + kr) * D + c]) * NLOG2E;
                    u16 hi = f2bf(w);
                    u16 lo = f2bf(w - bf2f(hi));
                    v[j] = (u32)(gq ? lo : hi);
                }
                w5g[t] = make_uint4(v[0] | (v[1] << 16), v[2] | (v[3] << 16), v[4] | (v[5] << 16), 0u);
            }
        }
        {
            const int c2 = t & 63, kb = t >> 6;
            u32 wr[16];
#pragma unroll
            for (int j = 0; j < 16; ++j)
                wr[j] = *reinterpret_cast<const u32*>(we + (size_t)(kb * 16 + j) * D + c2 * 2);
#pragma unroll
            for (int sb = 0; sb < 2; ++sb) {
                int kk = kb * 16 + sb * 8;
                int half = kk >> 7, k = kk & 127;
                const u32* w = &wr[sb * 8];
                uint4 lo4 = make_uint4((w[0] & 0xffffu) | (w[1] << 16), (w[2] & 0xffffu) | (w[3] << 16),
                                       (w[4] & 0xffffu) | (w[5] << 16), (w[6] & 0xffffu) | (w[7] << 16));
                uint4 hi4 = make_uint4((w[0] >> 16) | (w[1] & 0xffff0000u), (w[2] >> 16) | (w[3] & 0xffff0000u),
                                       (w[4] >> 16) | (w[5] & 0xffff0000u), (w[6] >> 16) | (w[7] & 0xffff0000u));
                *reinterpret_cast<uint4*>(&smem[(half * D + c2 * 2) * (D + 8) + k]) = lo4;
                *reinterpret_cast<uint4*>(&smem[(half * D + c2 * 2 + 1) * (D + 8) + k]) = hi4;
            }
        }
        {
            const int c2 = t & 63, kb = t >> 6;
            u32 w[8];
#pragma unroll
            for (int j = 0; j < 8; ++j)
                w[j] = *reinterpret_cast<const u32*>(wh + (size_t)(kb * 8 + j) * D + c2 * 2);
            int k = kb * 8;
            uint4 lo4 = make_uint4((w[0] & 0xffffu) | (w[1] << 16), (w[2] & 0xffffu) | (w[3] << 16),
                                   (w[4] & 0xffffu) | (w[5] << 16), (w[6] & 0xffffu) | (w[7] << 16));
            uint4 hi4 = make_uint4((w[0] >> 16) | (w[1] & 0xffff0000u), (w[2] >> 16) | (w[3] & 0xffff0000u),
                                   (w[4] >> 16) | (w[5] & 0xffff0000u), (w[6] >> 16) | (w[7] & 0xffff0000u));
            *reinterpret_cast<uint4*>(&smem[(2 * D + c2 * 2) * (D + 8) + k]) = lo4;
            *reinterpret_cast<uint4*>(&smem[(2 * D + c2 * 2 + 1) * (D + 8) + k]) = hi4;
        }
        __syncthreads();
        int lane = t & 63, wv = t >> 6;
        int e16 = lane & 15, g = lane >> 4;
        int rt = wv & 7;
        int half = wv >> 3;
        int n = bid * 128 + rt * 16 + e16;
        int nn = n < N ? n : N - 1;
        bf16x8 af[4];
#pragma unroll
        for (int ks = 0; ks < 4; ++ks)
            af[ks] = *reinterpret_cast<const bf16x8*>(h + (size_t)nn * D + ks * 32 + g * 8);
        const int wr0 = (e16 >> 2) * 8 + (e16 & 3);
#pragma unroll
        for (int jj = 0; jj < 6; ++jj) {
            const int jb = half * 6 + jj;
            f32x4 acc0 = {0.f, 0.f, 0.f, 0.f}, acc1 = {0.f, 0.f, 0.f, 0.f};
#pragma unroll
            for (int ks = 0; ks < 4; ++ks) {
                const u16* wp = &smem[(jb * 32 + wr0) * (D + 8) + ks * 32 + g * 8];
                bf16x8 w0 = *reinterpret_cast<const bf16x8*>(wp);
                bf16x8 w1 = *reinterpret_cast<const bf16x8*>(wp + 4 * (D + 8));
                acc0 = __builtin_amdgcn_mfma_f32_16x16x32_bf16(w0, af[ks], acc0, 0, 0, 0);
                acc1 = __builtin_amdgcn_mfma_f32_16x16x32_bf16(w1, af[ks], acc1, 0, 0, 0);
            }
            const float sc = (jb < 8) ? NLOG2E : 1.0f;
            uint4 o = make_uint4(cvt_pk((f32x2){acc0[0] * sc, acc0[1] * sc}), cvt_pk((f32x2){acc0[2] * sc, acc0[3] * sc}),
                                 cvt_pk((f32x2){acc1[0] * sc, acc1[1] * sc}), cvt_pk((f32x2){acc1[2] * sc, acc1[3] * sc}));
            if (n < N)
                *reinterpret_cast<uint4*>(ABU + ((size_t)(jb >> 2) * N + n) * D + (jb & 3) * 32 + g * 8) = o;
        }
        return;
    }
}

struct KbSmem {
    uint4 rec[KB_WAVES][CAPR];
    uint4 At[KB_WAVES][D];
    uint4 w5s[2][D];
    float macc[KB_WAVES * NPW][AST];
    float pacc[KB_WAVES][NPW][4];
    float posl[KB_WAVES][NPW + 3][4];
    float wxs[D];
    uint2 M[KB_WAVES][16][4];
    int nt[KB_WAVES][4];
};

__global__ void __launch_bounds__(KB_THREADS, 3)
k_main(const u16* __restrict__ h, const u16* __restrict__ pos,
       const u16* __restrict__ wx,
       const uint4* __restrict__ rec_g, const u16* __restrict__ tab,
       const u16* __restrict__ ABU,
       const u16* __restrict__ whT, const uint4* __restrict__ w5g,
       u16* __restrict__ out_h, u16* __restrict__ out_pos,
       unsigned long long mNBW, int N, int G, int CH, int NBW) {
    __shared__ KbSmem S;
    const u16* __restrict__ Bg = ABU + (size_t)N * D;
    const u16* __restrict__ Ug = ABU + (size_t)2 * N * D;
    const int t = threadIdx.x, lane = t & 63;
    const int wv = __builtin_amdgcn_readfirstlane(t >> 6);
    const int e16 = lane & 15, g = lane >> 4;
    const int wb = blockIdx.x * KB_WAVES + wv;
    const int N0 = node_start(blockIdx.x * KB_WAVES, N, NBW, mNBW);
    const int N1 = node_start(blockIdx.x * KB_WAVES + KB_WAVES, N, NBW, mNBW);
    const int n0 = node_start(wb, N, NBW, mNBW);
    const int nn = node_start(wb + 1, N, NBW, mNBW) - n0;
    const int rowb = n0 - N0;

    int so[GI_MAX], sc[GI_MAX];
    u16 (*tabs)[5] = reinterpret_cast<u16 (*)[5]>(&S.M[0][0][0]);
    auto fetch_tab = [&]() {
        if (t < G) {
            const u16* tp = tab + (size_t)t * (NBW + 4) + blockIdx.x * KB_WAVES;
            uint2 v = *reinterpret_cast<const uint2*>(tp);
            u32 v4 = tp[4];
            tabs[t][0] = (u16)v.x; tabs[t][1] = (u16)(v.x >> 16);
            tabs[t][2] = (u16)v.y; tabs[t][3] = (u16)(v.y >> 16);
            tabs[t][4] = (u16)v4;
        }
    };
    auto load_tab = [&]() {
        int lo_ = lane;
        asm volatile("" : "+v"(lo_));
#pragma unroll
        for (int ci = 0; ci < GI_MAX; ++ci) {
            int c = lo_ + 64 * ci;
            so[ci] = 0; sc[ci] = 0;
            if (c < G) {
                so[ci] = tabs[c][wv];
                sc[ci] = tabs[c][wv + 1];
            }
        }
    };
    auto load_tab_g = [&]() {
        int lo_ = lane;
        asm volatile("" : "+v"(lo_));
#pragma unroll
        for (int ci = 0; ci < GI_MAX; ++ci) {
            int c = lo_ + 64 * ci;
            so[ci] = 0; sc[ci] = 0;
            if (c < G) {
                const u16* tp = tab + (size_t)c * (NBW + 4) + wb;
                so[ci] = tp[0];
                sc[ci] = tp[1];
            }
        }
    };
    fetch_tab();
    u32 a0[NPW];
#pragma unroll
    for (int j = 0; j < NPW; ++j) {
        a0[j] = 0u;
        if (j < nn) a0[j] = *reinterpret_cast<const u32*>(ABU + (size_t)(n0 + j) * D + lane * 2);
    }
    const uint4 w40 = w5g[lane], w41 = w5g[lane + 64], w42 = w5g[lane + 128], w43 = w5g[lane + 192];
    u16 wx0 = wx[lane], wx1 = wx[lane + 64];
    u16 pz16 = 0;
    {
        int n = lane >> 2, cpt = lane & 3;
        if (n < nn && cpt < 3) pz16 = pos[(n0 + n) * 3 + cpt];
    }
    auto stage = [&]() {
        S.At[wv][lane * 2] = make_uint4((a0[0] & 0xffffu) | (a0[1] << 16), (a0[2] & 0xffffu) | (a0[3] << 16), a0[4] & 0xffffu, 0u);
        S.At[wv][lane * 2 + 1] = make_uint4((a0[0] >> 16) | (a0[1] & 0xffff0000u), (a0[2] >> 16) | (a0[3] & 0xffff0000u), a0[4] >> 16, 0u);
#pragma unroll
        for (int j = 0; j < NPW; ++j)
            if (j < nn) { S.macc[rowb + j][lane] = 0.f; S.macc[rowb + j][64 + lane] = 0.f; }
        uint4* wq = &S.w5s[0][0] + lane;
        wq[0] = w40; wq[64] = w41; wq[128] = w42; wq[192] = w43;
        S.wxs[lane] = bf2f(wx0);
        S.wxs[lane + 64] = bf2f(wx1);
        if (lane < (NPW + 3) * 4) S.posl[wv][lane >> 2][lane & 3] = bf2f(pz16);
        if (lane < NPW * 4) S.pacc[wv][lane >> 2][lane & 3] = 0.f;
    };
    const unsigned long long ltmask = (1ull << lane) - 1ull;
    uint4* rec = S.rec[wv];
    u32* key = reinterpret_cast<u32*>(S.rec[wv]);

    __syncthreads();
    load_tab();
    __syncthreads();
    f32x4 acc2[8];
    int dHead = 0, hflags = 0;
    float hm[8], hp0, hp1, hp2;
    auto reduce = [&]() __attribute__((always_inline)) {
        float wxc[8];
#pragma unroll
        for (int tt = 0; tt < 8; ++tt) wxc[tt] = S.wxs[tt * 16 + e16];
        float pv0 = 0.f, pv1 = 0.f, pv2 = 0.f;
#pragma unroll
        for (int tt = 0; tt < 8; ++tt) {
            hm[tt] = acc2[tt][0] * NLN2;
            pv0 += acc2[tt][1] * wxc[tt];
            pv1 += acc2[tt][2] * wxc[tt];
            pv2 += acc2[tt][3] * wxc[tt];
            acc2[tt] = (f32x4){0.f, 0.f, 0.f, 0.f};
        }
        hp0 = rsum16(pv0) * NLN2; hp1 = rsum16(pv1) * NLN2; hp2 = rsum16(pv2) * NLN2;
    };
    auto commit = [&](int d, auto accc) __attribute__((always_inline)) {
        constexpr bool ACC = decltype(accc)::value;
        const int bk = (d >> 16) & 3;
        const int nl = ((d >> 18) & 1) * 4 + g;
        const int rb = (d >> 20) & 31, nb = (d >> 25) & 7;
        const bool on = nl < nb;
        float* mrow = &S.macc[rb + (on ? nl : 0)][e16];
        float* prow = &S.pacc[bk][on ? nl : 0][0];
        float old[8], po[3];
        if (ACC) {
#pragma unroll
            for (int tt = 0; tt < 8; ++tt) old[tt] = mrow[tt * 16];
            po[0] = prow[0]; po[1] = prow[1]; po[2] = prow[2];
            CBAR();
        }
        if (on) {
#pragma unroll
            for (int tt = 0; tt < 8; ++tt) mrow[tt * 16] = ACC ? old[tt] + hm[tt] : hm[tt];
            if (e16 == 0) {
                prow[0] = (ACC ? po[0] : 0.f) + hp0;
                prow[1] = (ACC ? po[1] : 0.f) + hp1;
                prow[2] = (ACC ? po[2] : 0.f) + hp2;
            }
        }
    };
    auto flush = [&](int d, auto accc) __attribute__((always_inline)) { reduce(); commit(d, accc); };
    auto finish = [&]() __attribute__((always_inline)) {
        const std::integral_constant<bool, true> accc;
        __syncthreads();
        for (int w = 1; w < KB_WAVES; ++w) {
            if ((hflags & 1) && (wv == w || !(hflags & 2))) commit(dHead, accc);
            __syncthreads();
            if (!(hflags & 2)) break;
        }
    };
    auto round = [&](const int base, auto firstc) __attribute__((always_inline)) -> bool {
        constexpr bool FIRST = decltype(firstc)::value;
        int T;
        int fo[GI_MAX];
        {
            int tot = 0;
#pragma unroll
            for (int ci = 0; ci < GI_MAX; ++ci) { sc[ci] -= so[ci]; tot += sc[ci]; }
            int x = tot;
            int ls = lane;
            asm volatile("" : "+v"(ls));
#pragma unroll
            for (int o = 1; o < 64; o <<= 1) {
                int y = __builtin_amdgcn_ds_bpermute((ls - o) << 2, x);
                if (ls >= o) x += y;
            }
            T = __builtin_amdgcn_readlane(x, 63);
            int off = x - tot;
#pragma unroll
            for (int ci = 0; ci < GI_MAX; ++ci) { fo[ci] = off; off += sc[ci]; }
        }
        const int Tr = max(0, min(CAP, T - base));
        int lo2 = lane;
        asm volatile("" : "+v"(lo2));
#pragma unroll
        for (int ci = 0; ci < GI_MAX; ++ci) {
            int gs = (lo2 + 64 * ci) * CH + so[ci];
            for (int q = 0; q < sc[ci]; ++q) {
                int fp = fo[ci] + q - base;
                if (fp >= 0 && fp < CAP) key[fp] = (u32)(gs + q);
            }
        }
        CBAR();
        uint4 r[RPL];
#pragma unroll
        for (int i = 0; i < RPL; ++i) {
            int p = lane + 64 * i;
            r[i] = make_uint4(0u, 0u, 0u, 0xffffffffu);
            if (p < Tr) r[i] = rec_g[key[p]];
        }
        if (FIRST) stage();
        int posn[RPL];
        int cst = 0, cnt4 = 0, c0 = 0, g1s = 0;
#pragma unroll
        for (int i = 0; i < RPL; ++i) posn[i] = 0;
#pragma unroll
        for (int j = 0; j < NPW; ++j) {
            if (j == 4) { c0 = cst; g1s = (cst + 15) & ~15; cst = g1s; }
            int c = 0;
#pragma unroll
            for (int i = 0; i < RPL; ++i) {
                bool mt = (r[i].w != 0xffffffffu) && ((int)(r[i].w & 7u) == j);
                unsigned long long m = __ballot(mt);
                if (mt) posn[i] = cst + c + __popcll(m & ltmask);
                c += __popcll(m);
            }
            if (j == 4) cnt4 = c;
            cst += c;
        }
        const int g1e = (g1s + cnt4 + 15) & ~15;
        CBAR();
#pragma unroll
        for (int i = 0; i < RPL; ++i)
            if (r[i].w != 0xffffffffu) rec[posn[i]] = r[i];
        if (lane < 16) {
            if (c0 + lane < g1s) rec[c0 + lane] = make_uint4(0u, 0u, 0u, 0xffffffffu);
            if (g1s + cnt4 + lane < g1e) rec[g1s + cnt4 + lane] = make_uint4(0u, 0u, 0u, 0xffffffffu);
        }
        if (lane == 0) {
            S.nt[wv][0] = g1s >> 4;
            S.nt[wv][1] = g1e >> 4;
            S.nt[wv][2] = (base + CAP < T) ? 1 : 0;
        }
        __syncthreads();

        int sb1, sb2, sb3, sb4, sb5, sb6, sb7, TT, anymore;
        {
            const int4 q0 = *reinterpret_cast<const int4*>(&S.nt[0][0]);
            const int4 q1 = *reinterpret_cast<const int4*>(&S.nt[1][0]);
            const int4 q2 = *reinterpret_cast<const int4*>(&S.nt[2][0]);
            const int4 q3 = *reinterpret_cast<const int4*>(&S.nt[3][0]);
            sb1 = __builtin_amdgcn_readfirstlane(q0.x);
            sb2 = __builtin_amdgcn_readfirstlane(q0.y);
            sb3 = sb2 + __builtin_amdgcn_readfirstlane(q1.x);
            sb4 = sb2 + __builtin_amdgcn_readfirstlane(q1.y);
            sb5 = sb4 + __builtin_amdgcn_readfirstlane(q2.x);
            sb6 = sb4 + __builtin_amdgcn_readfirstlane(q2.y);
            sb7 = sb6 + __builtin_amdgcn_readfirstlane(q3.x);
            TT = sb6 + __builtin_amdgcn_readfirstlane(q3.y);
            anymore = __builtin_amdgcn_readfirstlane(q0.z | q1.z | q2.z | q3.z);
        }
        auto seg_of = [&](int x) __attribute__((always_inline)) {
            return (x >= sb1) + (x >= sb2) + (x >= sb3) + (x >= sb4) + (x >= sb5) + (x >= sb6) + (x >= sb7);
        };
        auto is_start = [&](int x) __attribute__((always_inline)) {
            return (x == 0) | (x == sb1) | (x == sb2) | (x == sb3) | (x == sb4) | (x == sb5) | (x == sb6) | (x == sb7);
        };
        auto seg_end = [&](int x) __attribute__((always_inline)) {
            int r = TT;
            r = (x < sb7) ? +sb7 : +r; r = (x < sb6) ? +sb6 : +r; r = (x < sb5) ? +sb5 : +r; r = (x < sb4) ? +sb4 : +r;
            r = (x < sb3) ? +sb3 : +r; r = (x < sb2) ? +sb2 : +r; r = (x < sb1) ? +sb1 : +r;
            return r;
        };
        const int ts = (TT * wv) >> 2, te = (TT * (wv + 1)) >> 2;
        const int nst = te - ts;
        const bool has_head = (nst > 0) && !is_start(ts);
        const int th = has_head ? min(te, seg_end(ts)) : +ts;
        bool serial = false;
        {
            const int s1 = TT >> 2, s2 = (TT * 2) >> 2, s3 = (TT * 3) >> 2;
            serial = (!is_start(s1) && seg_end(s1) > s2) || (!is_start(s2) && seg_end(s2) > s3);
        }
        int lq = lane;
        asm volatile("" : "+v"(lq));
        int descv;
        {
            const int ns1 = node_start(blockIdx.x * KB_WAVES + 1, N, NBW, mNBW) - N0;
            const int ns2 = node_start(blockIdx.x * KB_WAVES + 2, N, NBW, mNBW) - N0;
            const int ns3 = node_start(blockIdx.x * KB_WAVES + 3, N, NBW, mNBW) - N0;
            const int ns4 = N1 - N0;
            const int nh = te - th;
            const int tl = (lq < nh) ? (th + lq) : (ts + lq - nh);
            const int k = seg_of(tl);
            const int b = k >> 1;
            const int cb = (b == 0) ? 0 : (b == 1) ? +sb2 : (b == 2) ? +sb4 : +sb6;
            const int r0 = (b == 0) ? 0 : (b == 1) ? +ns1 : (b == 2) ? +ns2 : +ns3;
            const int r1 = (b == 0) ? +ns1 : (b == 1) ? +ns2 : (b == 2) ? +ns3 : +ns4;
            const int fl = (lq < nh) && ((tl + 1 == te) || is_start(tl + 1));
            descv = ((b * CAPR + (tl - cb) * 16) * 16) | (b << 16) | ((k & 1) << 18) | (fl << 19) | (r0 << 20) | ((r1 - r0) << 25);
        }
        bf16x8 selLo, selHi;
        {
            int dl = (lq & 15) - 8 * (lq >> 4);
            unsigned long long one = 0x3F80ull;
            u32 l0 = 0, l1 = 0, l2 = 0, l3 = 0, h0 = 0, h1 = 0, h2 = 0, h3 = 0;
            if (dl >= 0 && dl < 4) { unsigned long long v = one << (16 * dl); l0 = (u32)v; l1 = (u32)(v >> 32); }
            if (dl >= 4 && dl < 8) { unsigned long long v = one << (16 * (dl - 4)); l2 = (u32)v; l3 = (u32)(v >> 32); }
            int dh = dl + 16;
            if (dh >= 0 && dh < 4) { unsigned long long v = one << (16 * dh); h0 = (u32)v; h1 = (u32)(v >> 32); }
            if (dh >= 4 && dh < 8) { unsigned long long v = one << (16 * (dh - 4)); h2 = (u32)v; h3 = (u32)(v >> 32); }
            selLo = as_bf16x8(make_uint4(l0, l1, l2, l3));
            selHi = as_bf16x8(make_uint4(h0, h1, h2, h3));
        }
        const char* w5b = (g < 2) ? reinterpret_cast<const char*>(&S.w5s[g][e16]) : reinterpret_cast<const char*>(&S.At[0][e16]);
        const int w5m = (g < 2) ? 0 : (int)sizeof(S.At[0]);
        const char* recb = reinterpret_cast<const char*>(&S.rec[0][e16]);
        bf16x8 gA[4], gB[4];
        u32 pA0 = 0, pA1 = 0, pB0 = 0, pB1 = 0;
        auto issue = [&](int d, bf16x8 (&gat)[4], u32& q0, u32& q1) __attribute__((always_inline)) {
            int sidx = (int)reinterpret_cast<const uint4*>(recb + (d & 0xffff))->x;
            const u16* brow = Bg + (size_t)sidx * D + g * 8;
#pragma unroll
            for (int tp = 0; tp < 4; ++tp)
                gat[tp] = *reinterpret_cast<const bf16x8*>(brow + tp * 32);
            const u16* pp = pos + (size_t)sidx * 3;
            q0 = (u32)pp[0] | ((u32)pp[1] << 16);
            q1 = (u32)pp[2];
        };
#pragma unroll
        for (int tt = 0; tt < 8; ++tt) acc2[tt] = (f32x4){0.f, 0.f, 0.f, 0.f};
        auto compute = [&](int d, bf16x8 (&gat)[4], u32 q0, u32 q1) __attribute__((always_inline)) {
            const int bk = (d >> 16) & 3;
            const uint4 rme = *reinterpret_cast<const uint4*>(recb + (d & 0xffff));
            const bool vme = rme.w != 0xffffffffu;
            const int nd = (int)(rme.w & 7u);
            const float4 pn = *reinterpret_cast<const float4*>(&S.posl[bk][nd][0]);
            const uint4* w5p = reinterpret_cast<const uint4*>(w5b + w5m * bk);
            float dx = pn.x - bf2f((u16)(q0 & 0xffffu));
            float dy = pn.y - bf2f((u16)(q0 >> 16));
            float dz = pn.z - bf2f((u16)q1);
            float d2v = dx * dx + dy * dy + dz * dz;
            u16 d2h = f2bf(d2v);
            u16 d2l = f2bf(d2v - bf2f(d2h));
            {
                bool own = vme && ((nd & 3) == g);
                S.M[wv][e16][g] = own ? make_uint2(0x3F80u | ((u32)f2bf(dx) << 16), (u32)f2bf(dy) | ((u32)f2bf(dz) << 16))
                                      : make_uint2(0u, 0u);
            }
            uint4 xu;
            {
                unsigned long long oh = (nd < 4) ? (0x3F80ull << (16 * nd)) : 0ull;
                u32 o01 = (u32)oh, o23 = (u32)(oh >> 32);
                u32 f4 = (nd == 4) ? 0x3F80u : 0u;
                if (g < 2) xu = make_uint4((u32)d2h | (rme.y << 16), (rme.y >> 16) | (rme.z << 16), (rme.z >> 16) | ((u32)d2l << 16), 0u);
                else xu = make_uint4(o01, o23, f4, 0u);
                if (!vme || g == 3) xu = make_uint4(0u, 0u, 0u, 0u);
            }
            const bf16x8 x5 = as_bf16x8(xu);
            asm volatile("s_waitcnt lgkmcnt(0)" ::: "memory");
            u32 sw01, sw23;
            {
                const u16* wp = reinterpret_cast<const u16*>(&S.M[wv][g * 4][0]) + e16;
                sw01 = (u32)wp[0] | ((u32)wp[16] << 16);
                sw23 = (u32)wp[32] | ((u32)wp[48] << 16);
            }
            const bf16x4 sop = as_bf16x4(make_uint2(sw01, sw23));
#pragma unroll
            for (int tp = 0; tp < 4; ++tp) {
#pragma unroll
                for (int u = 0; u < 2; ++u) {
                    const int tt = tp * 2 + u;
                    bf16x8 w5 = as_bf16x8(w5p[tt * 16]);
                    f32x4 acc = __builtin_amdgcn_mfma_f32_16x16x32_bf16(x5, w5, (f32x4){0.f, 0.f, 0.f, 0.f}, 0, 0, 0);
                    acc = __builtin_amdgcn_mfma_f32_16x16x32_bf16(gat[tp], u ? selHi : selLo, acc, 0, 0, 0);
                    f32x2 t01 = {acc[0], acc[1]}, t23 = {acc[2], acc[3]};
                    f32x2 e01 = {__builtin_amdgcn_exp2f(acc[0]), __builtin_amdgcn_exp2f(acc[1])};
                    f32x2 e23 = {__builtin_amdgcn_exp2f(acc[2]), __builtin_amdgcn_exp2f(acc[3])};
                    e01 = e01 + (f32x2){1.f, 1.f};
                    e23 = e23 + (f32x2){1.f, 1.f};
                    f32x2 r01 = {__builtin_amdgcn_rcpf(e01[0]), __builtin_amdgcn_rcpf(e01[1])};
                    f32x2 r23 = {__builtin_amdgcn_rcpf(e23[0]), __builtin_amdgcn_rcpf(e23[1])};
                    f32x2 m01 = t01 * r01, m23 = t23 * r23;
                    u32 k01 = cvt_pk(m01), k23 = cvt_pk(m23);
                    bf16x4 mb = as_bf16x4(make_uint2(k01, k23));
                    acc2[tt] = __builtin_amdgcn_mfma_f32_16x16x16bf16_1k(sop, mb, acc2[tt], 0, 0, 0);
                }
            }
        };
        const std::integral_constant<bool, !FIRST> dirc;
        if (nst > 0) {
            issue(__builtin_amdgcn_readlane(descv, 0), gA, pA0, pA1);
            int i = 0;
            for (; i + 1 < nst; i += 2) {
                {
                    const int rem = nst - i;
                    if (rem >= 9) __builtin_amdgcn_s_setprio(3);
                    else if (rem >= 6) __builtin_amdgcn_s_setprio(2);
                    else if (rem >= 3) __builtin_amdgcn_s_setprio(1);
                    else __builtin_amdgcn_s_setprio(0);
                }
                const int dA = __builtin_amdgcn_readlane(descv, i);
                const int dB = __builtin_amdgcn_readlane(descv, i + 1);
                const int dC = __builtin_amdgcn_readlane(descv, min(i + 2, nst - 1));
                issue(dB, gB, pB0, pB1);
                compute(dA, gA, pA0, pA1);
                if (dA & (1 << 19)) flush(dA, dirc);
                issue(dC, gA, pA0, pA1);
                compute(dB, gB, pB0, pB1);
                if (dB & (1 << 19)) flush(dB, dirc);
            }
            if (i < nst) {
                const int dA = __builtin_amdgcn_readlane(descv, i);
                compute(dA, gA, pA0, pA1);
                if (dA & (1 << 19)) flush(dA, dirc);
            }
        }
        dHead = __builtin_amdgcn_readlane(descv, max(nst - 1, 0));
        hflags = (has_head ? 1 : 0) | (serial ? 2 : 0);
        reduce();
        return anymore != 0;
    };
    if (round(0, std::integral_constant<bool, true>())) {
        for (int base = CAP;; base += CAP) {
            finish();
            load_tab_g();
            if (!round(base, std::integral_constant<bool, false>())) break;
        }
    }

    int le = lane;
    asm volatile("" : "+v"(le));
    const int f16 = le & 15, gg = le >> 4;
    bf16x8 bw[2][4];
#pragma unroll
    for (int q = 0; q < 2; ++q)
#pragma unroll
        for (int ks = 0; ks < 4; ++ks)
            bw[q][ks] = *reinterpret_cast<const bf16x8*>(whT + (size_t)(wv * 32 + (f16 >> 2) * 8 + q * 4 + (f16 & 3)) * D + ks * 32 + gg * 8);
    const int NR = N1 - N0;
    uint4 hv4[2], uv4[2];
#pragma unroll
    for (int rt = 0; rt < 2; ++rt) {
        int lr = rt * 16 + f16;
        size_t o = (size_t)(N0 + (lr < NR ? lr : 0)) * D + wv * 32 + gg * 8;
        hv4[rt] = *reinterpret_cast<const uint4*>(h + o);
        uv4[rt] = *reinterpret_cast<const uint4*>(Ug + o);
    }
    finish();
    if (le < nn * 3) {
        int n = le / 3, cpt = le - n * 3;
        out_pos[(size_t)(n0 + n) * 3 + cpt] = f2bf(S.posl[wv][n][cpt] + S.pacc[wv][n][cpt]);
    }
#pragma unroll
    for (int rt = 0; rt < 2; ++rt) {
        if (rt * 16 < NR) {
            bf16x8 af[4];
            int lr = rt * 16 + f16;
#pragma unroll
            for (int ks = 0; ks < 4; ++ks) {
                const float* mp = &S.macc[lr < NR ? lr : 0][ks * 32 + gg * 8];
                af[ks] = as_bf16x8(make_uint4(pack2(mp[0], mp[1]), pack2(mp[2], mp[3]),
                                              pack2(mp[4], mp[5]), pack2(mp[6], mp[7])));
            }
            u32 ov[4];
#pragma unroll
            for (int q = 0; q < 2; ++q) {
                const u32 u0 = q ? uv4[rt].z : uv4[rt].x, u1 = q ? uv4[rt].w : uv4[rt].y;
                const u32 h0 = q ? hv4[rt].z : hv4[rt].x, h1 = q ? hv4[rt].w : hv4[rt].y;
                f32x4 acc = {__uint_as_float(u0 << 16), __uint_as_float(u0 & 0xffff0000u),
                             __uint_as_float(u1 << 16), __uint_as_float(u1 & 0xffff0000u)};
#pragma unroll
                for (int ks = 0; ks < 4; ++ks)
                    acc = __builtin_amdgcn_mfma_f32_16x16x32_bf16(bw[q][ks], af[ks], acc, 0, 0, 0);
                ov[q * 2] = pack2(__uint_as_float(h0 << 16) + silu_f(acc[0]), __uint_as_float(h0 & 0xffff0000u) + silu_f(acc[1]));
                ov[q * 2 + 1] = pack2(__uint_as_float(h1 << 16) + silu_f(acc[2]), __uint_as_float(h1 & 0xffff0000u) + silu_f(acc[3]));
            }
            if (lr < NR)
                *reinterpret_cast<uint4*>(out_h + (size_t)(N0 + lr) * D + wv * 32 + gg * 8) = make_uint4(ov[0], ov[1], ov[2], ov[3]);
        }
    }
}

extern "C" void kernel_launch(void* const* d_in, const int* in_sizes, int n_in,
                              void* d_out, int out_size, void* d_ws, size_t ws_size,
                              hipStream_t stream) {
    const u16* h   = (const u16*)d_in[0];
    const u16* pos = (const u16*)d_in[1];
    const u16* ea  = (const u16*)d_in[2];
    const int* src = (const int*)d_in[3];
    const int* dst = (const int*)d_in[4];
    const u16* we  = (const u16*)d_in[5];
    const u16* wx  = (const u16*)d_in[6];
    const u16* wh  = (const u16*)d_in[7];
    int N = in_sizes[0] / D;
    int E = in_sizes[3];
    u16* out_h = (u16*)d_out;
    u16* out_pos = out_h + (size_t)N * D;

    int G = 128;
    while ((E + G - 1) / G > KA_THREADS * KA_ITERS && G < 64 * GI_MAX) G += 64;
    int CH = (E + G - 1) / G;
    int NBW = 3072;
    while (NBW * NPW < N && NBW < NBW_MAX) NBW += 4;
    int nGemm = (N + 127) / 128;
    if (nGemm < D * D / KA_THREADS) nGemm = D * D / KA_THREADS;
    unsigned long long mN = (1ull << 40) / (unsigned long long)N + 1ull;
    unsigned long long mNBW = (1ull << 40) / (unsigned long long)NBW + 1ull;

    char* ws = (char*)d_ws;
    size_t off = 0;
    auto take = [&](size_t bytes) { char* p = ws + off; off += (bytes + 255) & ~(size_t)255; return p; };
    uint4* rec_g = (uint4*)take((size_t)G * CH * 16);
    u16* tab = (u16*)take((size_t)G * (NBW + 4) * 2);
    u16* ABU = (u16*)take((size_t)3 * N * D * 2);
    u16* whT = (u16*)take((size_t)D * D * 2);
    uint4* w5g = (uint4*)take((size_t)256 * 16);

    k_part<<<G + nGemm, KA_THREADS, 0, stream>>>(h, src, dst, ea, we, wh, rec_g, tab, ABU, whT, w5g,
                                                      mN, mNBW, N, E, G, CH, NBW, nGemm);
    k_main<<<NBW / KB_WAVES, KB_THREADS, 0, stream>>>(h, pos, wx, rec_g, tab, ABU, whT, w5g,
                                                     out_h, out_pos, mNBW, N, G, CH, NBW);
}
```

```cpp
#include <hip/hip_runtime.h>
#include <hip/hip_bf16.h>
#include <stdint.h>
#include <type_traits>


typedef unsigned short u16;
typedef unsigned int u32;
typedef __attribute__((ext_vector_type(8))) short bf16x8;
typedef __attribute__((ext_vector_type(4))) float f32x4;
typedef __attribute__((ext_vector_type(4))) short bf16x4;
typedef __attribute__((ext_vector_type(2))) float f32x2;
typedef __attribute__((ext_vector_type(4))) unsigned int u32x4;
__device__ __forceinline__ void st_nt(void* p, uint4 v) { u32x4 x = {v.x, v.y, v.z, v.w}; __builtin_nontemporal_store(x, reinterpret_cast<u32x4*>(p)); }

#define D 128
#define KA_THREADS 1024
#define KA_ITERS 4
#define NBW_MAX 8192
#define KB_THREADS 256
#define KB_WAVES 4
#define NPW 5
#define RPL 4
#define CAP (RPL * 64)
#define CAPS 272
#define CAPR (CAP + 32)
#define GI_MAX 2
#define AST (D + 4)
#define CBAR() asm volatile("" ::: "memory")
#define NLOG2E (-1.4426950408889634f)
#define NLN2 (-0.6931471805599453f)

__device__ __forceinline__ float bf2f(u16 v) { return __uint_as_float(((u32)v) << 16); }
__device__ __forceinline__ u16 f2bf(float f) {
    __hip_bfloat16 b = __float2bfloat16(f);
    return *reinterpret_cast<u16*>(&b);
}
__device__ __forceinline__ float silu_f(float x) {
    float e = __builtin_amdgcn_exp2f(x * NLOG2E);
    return x * __builtin_amdgcn_rcpf(1.0f + e);
}
__device__ __forceinline__ u32 pack2(float a, float b) {
    return (u32)f2bf(a) | ((u32)f2bf(b) << 16);
}
typedef __attribute__((ext_vector_type(2))) __bf16 bf16x2;
__device__ __forceinline__ u32 cvt_pk(f32x2 v) {
    bf16x2 p = __builtin_convertvector(v, bf16x2);
    return *reinterpret_cast<u32*>(&p);
}
__device__ __forceinline__ bf16x8 as_bf16x8(uint4 v) { return *reinterpret_cast<bf16x8*>(&v); }
__device__ __forceinline__ bf16x4 as_bf16x4(uint2 v) { return *reinterpret_cast<bf16x4*>(&v); }
template <int CTRL>
__device__ __forceinline__ float dpp_f(float x) {
    return __int_as_float(__builtin_amdgcn_update_dpp(0, __float_as_int(x), CTRL, 0xf, 0xf, true));
}
__device__ __forceinline__ float rsum16(float x) {
    x += dpp_f<0x128>(x);
    x += dpp_f<0x124>(x);
    x += dpp_f<0x122>(x);
    x += dpp_f<0x121>(x);
    return x;
}
__device__ __forceinline__ u32 mdiv(u32 x, unsigned long long m) {
    return (u32)(((unsigned long long)x * m) >> 40);
}
__device__ __forceinline__ int node_start(int b, int N, int NBW, unsigned long long mNBW) {
    return (int)mdiv((u32)b * (u32)N + (u32)NBW - 1u, mNBW);
}

__global__ void __launch_bounds__(KA_THREADS)
k_part(const u16* __restrict__ h, const int* __restrict__ src, const int* __restrict__ dst,
       const u16* __restrict__ ea, const u16* __restrict__ we, const u16* __restrict__ wh,
       uint4* __restrict__ rec_g, u16* __restrict__ tab, u16* __restrict__ ABU,
       u16* __restrict__ whT, uint4* __restrict__ w5g,
       unsigned long long mN, unsigned long long mNBW,
       int N, int E, int G, int CH, int NBW, int nGemm) {
    __shared__ __align__(16) u16 smem[(NBW_MAX + 8) * 2 + KA_THREADS * KA_ITERS + KA_THREADS * KA_ITERS * 8];
    __shared__ int wsum[KA_THREADS / 64];
    int t = threadIdx.x;
    int bid = blockIdx.x;
    if (bid < G) {
        int* hist = (int*)smem;
        for (int i = t; i < NBW; i += KA_THREADS) hist[i] = 0;
        int e0 = bid * CH;
        int e1 = min(E, e0 + CH);
        int sv[KA_ITERS], bv[KA_ITERS], rk[KA_ITERS], lo[KA_ITERS];
        uint2 eav[KA_ITERS];
#pragma unroll
        for (int i = 0; i < KA_ITERS; ++i) {
            int e = e0 + t + i * KA_THREADS;
            bv[i] = -1;
            if (e < e1) {
                int d = dst[e];
                sv[i] = src[e];
                eav[i] = *reinterpret_cast<const uint2*>(ea + (size_t)e * 4);
                int b = (int)mdiv((u32)d * (u32)NBW, mN);
                bv[i] = b;
                lo[i] = d - node_start(b, N, NBW, mNBW);
            }
        }
        __syncthreads();
#pragma unroll
        for (int i = 0; i < KA_ITERS; ++i)
            if (bv[i] >= 0) rk[i] = atomicAdd(&hist[bv[i]], 1);
        __syncthreads();
        const int per = (NBW + KA_THREADS - 1) / KA_THREADS;
        int b0 = t * per, b1 = min(NBW, b0 + per);
        int cnt = 0;
        for (int b = b0; b < b1; ++b) cnt += hist[b];
        int lane = t & 63, wv = t >> 6;
        int x = cnt;
#pragma unroll
        for (int o = 1; o < 64; o <<= 1) {
            int y = __shfl_up(x, o);
            if (lane >= o) x += y;
        }
        if (lane == 63) wsum[wv] = x;
        __syncthreads();
        int base = 0;
#pragma unroll
        for (int w = 0; w < KA_THREADS / 64; ++w) base += (w < wv) ? wsum[w] : 0;
        int off = base + x - cnt;
        u16* trow = tab + (size_t)bid * (NBW + 4);
        for (int b = b0; b < b1; ++b) {
            int c = hist[b];
            hist[b] = off;
            trow[b] = (u16)off;
            off += c;
        }
        if (t == KA_THREADS - 1) { trow[NBW] = (u16)(base + x); hist[NBW] = base + x; }
        __syncthreads();
        u16* ids = reinterpret_cast<u16*>(hist + NBW_MAX + 8);
        uint4* lrec = reinterpret_cast<uint4*>(ids + KA_THREADS * KA_ITERS);
#pragma unroll
        for (int i = 0; i < KA_ITERS; ++i)
            if (bv[i] >= 0) ids[hist[bv[i]] + rk[i]] = (u16)(t + i * KA_THREADS);
        __syncthreads();
        int cs0[KA_ITERS], cn[KA_ITERS], rnk[KA_ITERS];
        int mxn = 0;
#pragma unroll
        for (int i = 0; i < KA_ITERS; ++i) {
            cs0[i] = 0; cn[i] = 0; rnk[i] = 0;
            if (bv[i] >= 0) { cs0[i] = hist[bv[i]]; cn[i] = hist[bv[i] + 1] - cs0[i]; }
            mxn = max(mxn, cn[i]);
        }
#pragma clang loop unroll(disable) vectorize(disable) interleave(disable)
        for (int q = 0; q < mxn; ++q) {
#pragma unroll
            for (int i = 0; i < KA_ITERS; ++i) {
                int v = (int)ids[cs0[i] + (q < cn[i] ? q : 0)];
                rnk[i] += (q < cn[i] && v < t + i * KA_THREADS) ? 1 : 0;
            }
        }
#pragma unroll
        for (int i = 0; i < KA_ITERS; ++i) {
            if (bv[i] >= 0) {
                int e = e0 + t + i * KA_THREADS;
                int p = cs0[i] + rnk[i];
                uint4 r;
                r.x = (u32)sv[i]; r.y = eav[i].x; r.z = eav[i].y;
                r.w = ((u32)e << 3) | (u32)lo[i];
                lrec[p] = r;
            }
        }
        __syncthreads();
#pragma unroll
        for (int i = 0; i < KA_ITERS; ++i) {
            int p = t + i * KA_THREADS;
            if (p < e1 - e0) rec_g[(size_t)bid * CH + p] = lrec[p];
        }
        return;
    }
    bid -= G;
    if (bid < nGemm) {
        {
            int i = bid * KA_THREADS + t;
            if (i < D * D) {
                int k = i >> 7, c = i & 127;
                whT[(size_t)c * D + k] = wh[D * D + i];
            }
            if (bid == nGemm - 1 && t < 2 * D) {
                int gq = t >> 7, c = t & 127;
                u32 v[6];
#pragma unroll
                for (int j = 0; j < 6; ++j) {
                    int kr = (j == 5) ? 0 : j;
                    float w = bf2f(we[(size_t)(256 + kr) * D + c]) * NLOG2E;
                    u16 hi = f2bf(w);
                    u16 lo = f2bf(w - bf2f(hi));
                    v[j] = (u32)(gq ? lo : hi);
                }
                w5g[t] = make_uint4(v[0] | (v[1] << 16), v[2] | (v[3] << 16), v[4] | (v[5] << 16), 0u);
            }
        }
        {
            const int c2 = t & 63, kb = t >> 6;
            u32 wr[16];
#pragma unroll
            for (int j = 0; j < 16; ++j)
                wr[j] = *reinterpret_cast<const u32*>(we + (size_t)(kb * 16 + j) * D + c2 * 2);
#pragma unroll
            for (int sb = 0; sb < 2; ++sb) {
                int kk = kb * 16 + sb * 8;
                int half = kk >> 7, k = kk & 127;
                const u32* w = &wr[sb * 8];
                uint4 lo4 = make_uint4((w[0] & 0xffffu) | (w[1] << 16), (w[2] & 0xffffu) | (w[3] << 16),
                                       (w[4] & 0xffffu) | (w[5] << 16), (w[6] & 0xffffu) | (w[7] << 16));
                uint4 hi4 = make_uint4((w[0] >> 16) | (w[1] & 0xffff0000u), (w[2] >> 16) | (w[3] & 0xffff0000u),
                                       (w[4] >> 16) | (w[5] & 0xffff0000u), (w[6] >> 16) | (w[7] & 0xffff0000u));
                *reinterpret_cast<uint4*>(&smem[(half * D + c2 * 2) * (D + 8) + k]) = lo4;
                *reinterpret_cast<uint4*>(&smem[(half * D + c2 * 2 + 1) * (D + 8) + k]) = hi4;
            }
        }
        {
            const int c2 = t & 63, kb = t >> 6;
            u32 w[8];
#pragma unroll
            for (int j = 0; j < 8; ++j)
                w[j] = *reinterpret_cast<const u32*>(wh + (size_t)(kb * 8 + j) * D + c2 * 2);
            int k = kb * 8;
            uint4 lo4 = make_uint4((w[0] & 0xffffu) | (w[1] << 16), (w[2] & 0xffffu) | (w[3] << 16),
                                   (w[4] & 0xffffu) | (w[5] << 16), (w[6] & 0xffffu) | (w[7] << 16));
            uint4 hi4 = make_uint4((w[0] >> 16) | (w[1] & 0xffff0000u), (w[2] >> 16) | (w[3] & 0xffff0000u),
                                   (w[4] >> 16) | (w[5] & 0xffff0000u), (w[6] >> 16) | (w[7] & 0xffff0000u));
            *reinterpret_cast<uint4*>(&smem[(2 * D + c2 * 2) * (D + 8) + k]) = lo4;
            *reinterpret_cast<uint4*>(&smem[(2 * D + c2 * 2 + 1) * (D + 8) + k]) = hi4;
        }
        __syncthreads();
        int lane = t & 63, wv = t >> 6;
        int e16 = lane & 15, g = lane >> 4;
        int rt = wv & 7;
        int half = wv >> 3;
        int n = bid * 128 + rt * 16 + e16;
        int nn = n < N ? n : N - 1;
        bf16x8 af[4];
#pragma unroll
        for (int ks = 0; ks < 4; ++ks)
            af[ks] = *reinterpret_cast<const bf16x8*>(h + (size_t)nn * D + ks * 32 + g * 8);
        const int wr0 = (e16 >> 2) * 8 + (e16 & 3);
#pragma unroll
        for (int jj = 0; jj < 6; ++jj) {
            const int jb = half * 6 + jj;
            f32x4 acc0 = {0.f, 0.f, 0.f, 0.f}, acc1 = {0.f, 0.f, 0.f, 0.f};
#pragma unroll
            for (int ks = 0; ks < 4; ++ks) {
                const u16* wp = &smem[(jb * 32 + wr0) * (D + 8) + ks * 32 + g * 8];
                bf16x8 w0 = *reinterpret_cast<const bf16x8*>(wp);
                bf16x8 w1 = *reinterpret_cast<const bf16x8*>(wp + 4 * (D + 8));
                acc0 = __builtin_amdgcn_mfma_f32_16x16x32_bf16(w0, af[ks], acc0, 0, 0, 0);
                acc1 = __builtin_amdgcn_mfma_f32_16x16x32_bf16(w1, af[ks], acc1, 0, 0, 0);
            }
            const float sc = (jb < 8) ? NLOG2E : 1.0f;
            uint4 o = make_uint4(cvt_pk((f32x2){acc0[0] * sc, acc0[1] * sc}), cvt_pk((f32x2){acc0[2] * sc, acc0[3] * sc}),
                                 cvt_pk((f32x2){acc1[0] * sc, acc1[1] * sc}), cvt_pk((f32x2){acc1[2] * sc, acc1[3] * sc}));
            if (n < N) {
                u16* op = ABU + ((size_t)(jb >> 2) * N + n) * D + (jb & 3) * 32 + g * 8;
                if ((jb >> 2) == 1) *reinterpret_cast<uint4*>(op) = o;
                else st_nt(op, o);
            }
        }
        return;
    }
}

struct KbSmem {
    uint4 rec[KB_WAVES][CAPR];
    uint4 At[KB_WAVES][D];
    uint4 w5s[2][D];
    float macc[KB_WAVES * NPW][AST];
    float pacc[KB_WAVES][NPW][4];
    float posl[KB_WAVES][NPW + 3][4];
    float wxs[D];
    uint2 M[KB_WAVES][16][4];
    int nt[KB_WAVES][4];
};

__global__ void __launch_bounds__(KB_THREADS, 3)
k_main(const u16* __restrict__ h, const u16* __restrict__ pos,
       const u16* __restrict__ wx,
       const uint4* __restrict__ rec_g, const u16* __restrict__ tab,
       const u16* __restrict__ ABU,
       const u16* __restrict__ whT, const uint4* __restrict__ w5g,
       u16* __restrict__ out_h, u16* __restrict__ out_pos,
       unsigned long long mNBW, int N, int G, int CH, int NBW) {
    __shared__ KbSmem S;
    const u16* __restrict__ Bg = ABU + (size_t)N * D;
    const u16* __restrict__ Ug = ABU + (size_t)2 * N * D;
    const int t = threadIdx.x, lane = t & 63;
    const int wv = __builtin_amdgcn_readfirstlane(t >> 6);
    const int e16 = lane & 15, g = lane >> 4;
    const int nwg = NBW / KB_WAVES, per8 = nwg >> 3;
    const int bx = ((int)blockIdx.x < per8 * 8) ? ((int)(blockIdx.x & 7) * per8 + (int)(blockIdx.x >> 3)) : (int)blockIdx.x;
    const int wb = bx * KB_WAVES + wv;
    const int N0 = node_start(bx * KB_WAVES, N, NBW, mNBW);
    const int N1 = node_start(bx * KB_WAVES + KB_WAVES, N, NBW, mNBW);
    const int n0 = node_start(wb, N, NBW, mNBW);
    const int nn = node_start(wb + 1, N, NBW, mNBW) - n0;
    const int rowb = n0 - N0;

    int so[GI_MAX], sc[GI_MAX];
    u16 (*tabs)[5] = reinterpret_cast<u16 (*)[5]>(&S.M[0][0][0]);
    auto fetch_tab = [&]() {
        if (t < G) {
            const u16* tp = tab + (size_t)t * (NBW + 4) + bx * KB_WAVES;
            uint2 v = *reinterpret_cast<const uint2*>(tp);
            u32 v4 = tp[4];
            tabs[t][0] = (u16)v.x; tabs[t][1] = (u16)(v.x >> 16);
            tabs[t][2] = (u16)v.y; tabs[t][3] = (u16)(v.y >> 16);
            tabs[t][4] = (u16)v4;
        }
    };
    auto load_tab = [&]() {
        int lo_ = lane;
        asm volatile("" : "+v"(lo_));
#pragma unroll
        for (int ci = 0; ci < GI_MAX; ++ci) {
            int c = lo_ + 64 * ci;
            so[ci] = 0; sc[ci] = 0;
            if (c < G) {
                so[ci] = tabs[c][wv];
                sc[ci] = tabs[c][wv + 1];
            }
        }
    };
    auto load_tab_g = [&]() {
        int lo_ = lane;
        asm volatile("" : "+v"(lo_));
#pragma unroll
        for (int ci = 0; ci < GI_MAX; ++ci) {
            int c = lo_ + 64 * ci;
            so[ci] = 0; sc[ci] = 0;
            if (c < G) {
                const u16* tp = tab + (size_t)c * (NBW + 4) + wb;
                so[ci] = tp[0];
                sc[ci] = tp[1];
            }
        }
    };
    fetch_tab();
    u32 a0[NPW];
#pragma unroll
    for (int j = 0; j < NPW; ++j) {
        a0[j] = 0u;
        if (j < nn) a0[j] = *reinterpret_cast<const u32*>(ABU + (size_t)(n0 + j) * D + lane * 2);
    }
    const uint4 w40 = w5g[lane], w41 = w5g[lane + 64], w42 = w5g[lane + 128], w43 = w5g[lane + 192];
    u16 wx0 = wx[lane], wx1 = wx[lane + 64];
    u16 pz16 = 0;
    {
        int n = lane >> 2, cpt = lane & 3;
        if (n < nn && cpt < 3) pz16 = pos[(n0 + n) * 3 + cpt];
    }
    auto stage = [&]() {
        S.At[wv][lane * 2] = make_uint4((a0[0] & 0xffffu) | (a0[1] << 16), (a0[2] & 0xffffu) | (a0[3] << 16), a0[4] & 0xffffu, 0u);
        S.At[wv][lane * 2 + 1] = make_uint4((a0[0] >> 16) | (a0[1] & 0xffff0000u), (a0[2] >> 16) | (a0[3] & 0xffff0000u), a0[4] >> 16, 0u);
#pragma unroll
        for (int j = 0; j < NPW; ++j)
            if (j < nn) { S.macc[rowb + j][lane] = 0.f; S.macc[rowb + j][64 + lane] = 0.f; }
        uint4* wq = &S.w5s[0][0] + lane;
        wq[0] = w40; wq[64] = w41; wq[128] = w42; wq[192] = w43;
        S.wxs[lane] = bf2f(wx0);
        S.wxs[lane + 64] = bf2f(wx1);
        if (lane < (NPW + 3) * 4) S.posl[wv][lane >> 2][lane & 3] = bf2f(pz16);
        if (lane < NPW * 4) S.pacc[wv][lane >> 2][lane & 3] = 0.f;
    };
    const unsigned long long ltmask = (1ull << lane) - 1ull;
    uint4* rec = S.rec[wv];
    u32* key = reinterpret_cast<u32*>(S.rec[wv]);

    __syncthreads();
    load_tab();
    __syncthreads();
    f32x4 acc2[8];
    int dHead = 0, hflags = 0;
    float hm[8], hp0, hp1, hp2;
    auto reduce = [&]() __attribute__((always_inline)) {
        float wxc[8];
#pragma unroll
        for (int tt = 0; tt < 8; ++tt) wxc[tt] = S.wxs[tt * 16 + e16];
        float pv0 = 0.f, pv1 = 0.f, pv2 = 0.f;
#pragma unroll
        for (int tt = 0; tt < 8; ++tt) {
            hm[tt] = acc2[tt][0] * NLN2;
            pv0 += acc2[tt][1] * wxc[tt];
            pv1 += acc2[tt][2] * wxc[tt];
            pv2 += acc2[tt][3] * wxc[tt];
            acc2[tt] = (f32x4){0.f, 0.f, 0.f, 0.f};
        }
        hp0 = rsum16(pv0) * NLN2; hp1 = rsum16(pv1) * NLN2; hp2 = rsum16(pv2) * NLN2;
    };
    auto commit = [&](int d, auto accc) __attribute__((always_inline)) {
        constexpr bool ACC = decltype(accc)::value;
        const int bk = (d >> 16) & 3;
        const int nl = ((d >> 18) & 1) * 4 + g;
        const int rb = (d >> 20) & 31, nb = (d >> 25) & 7;
        const bool on = nl < nb;
        float* mrow = &S.macc[rb + (on ? nl : 0)][e16];
        float* prow = &S.pacc[bk][on ? nl : 0][0];
        float old[8], po[3];
        if (ACC) {
#pragma unroll
            for (int tt = 0; tt < 8; ++tt) old[tt] = mrow[tt * 16];
            po[0] = prow[0]; po[1] = prow[1]; po[2] = prow[2];
            CBAR();
        }
        if (on) {
#pragma unroll
            for (int tt = 0; tt < 8; ++tt) mrow[tt * 16] = ACC ? old[tt] + hm[tt] : hm[tt];
            if (e16 == 0) {
                prow[0] = (ACC ? po[0] : 0.f) + hp0;
                prow[1] = (ACC ? po[1] : 0.f) + hp1;
                prow[2] = (ACC ? po[2] : 0.f) + hp2;
            }
        }
    };
    auto flush = [&](int d, auto accc) __attribute__((always_inline)) { reduce(); commit(d, accc); };
    auto finish = [&]() __attribute__((always_inline)) {
        const std::integral_constant<bool, true> accc;
        __syncthreads();
        for (int w = 1; w < KB_WAVES; ++w) {
            if ((hflags & 1) && (wv == w || !(hflags & 2))) commit(dHead, accc);
            __syncthreads();
            if (!(hflags & 2)) break;
        }
    };
    auto round = [&](const int base, auto firstc) __attribute__((always_inline)) -> bool {
        constexpr bool FIRST = decltype(firstc)::value;
        int T;
        int fo[GI_MAX];
        {
            int tot = 0;
#pragma unroll
            for (int ci = 0; ci < GI_MAX; ++ci) { sc[ci] -= so[ci]; tot += sc[ci]; }
            int x = tot;
            int ls = lane;
            asm volatile("" : "+v"(ls));
#pragma unroll
            for (int o = 1; o < 64; o <<= 1) {
                int y = __builtin_amdgcn_ds_bpermute((ls - o) << 2, x);
                if (ls >= o) x += y;
            }
            T = __builtin_amdgcn_readlane(x, 63);
            int off = x - tot;
#pragma unroll
            for (int ci = 0; ci < GI_MAX; ++ci) { fo[ci] = off; off += sc[ci]; }
        }
        const int Tr = max(0, min(CAP, T - base));
        int lo2 = lane;
        asm volatile("" : "+v"(lo2));
#pragma unroll
        for (int ci = 0; ci < GI_MAX; ++ci) {
            int gs = (lo2 + 64 * ci) * CH + so[ci];
            for (int q = 0; q < sc[ci]; ++q) {
                int fp = fo[ci] + q - base;
                if (fp >= 0 && fp < CAP) key[fp] = (u32)(gs + q);
            }
        }
        CBAR();
        uint4 r[RPL];
#pragma unroll
        for (int i = 0; i < RPL; ++i) {
            int p = lane + 64 * i;
            r[i] = make_uint4(0u, 0u, 0u, 0xffffffffu);
            if (p < Tr) r[i] = rec_g[key[p]];
        }
        if (FIRST) stage();
        int posn[RPL];
        int cst = 0, cnt4 = 0, c0 = 0, g1s = 0;
#pragma unroll
        for (int i = 0; i < RPL; ++i) posn[i] = 0;
#pragma unroll
        for (int j = 0; j < NPW; ++j) {
            if (j == 4) { c0 = cst; g1s = (cst + 15) & ~15; cst = g1s; }
            int c = 0;
#pragma unroll
            for (int i = 0; i < RPL; ++i) {
                bool mt = (r[i].w != 0xffffffffu) && ((int)(r[i].w & 7u) == j);
                unsigned long long m = __ballot(mt);
                if (mt) posn[i] = cst + c + __popcll(m & ltmask);
                c += __popcll(m);
            }
            if (j == 4) cnt4 = c;
            cst += c;
        }
        const int g1e = (g1s + cnt4 + 15) & ~15;
        CBAR();
#pragma unroll
        for (int i = 0; i < RPL; ++i)
            if (r[i].w != 0xffffffffu) rec[posn[i]] = r[i];
        if (lane < 16) {
            if (c0 + lane < g1s) rec[c0 + lane] = make_uint4(0u, 0u, 0u, 0xffffffffu);
            if (g1s + cnt4 + lane < g1e) rec[g1s + cnt4 + lane] = make_uint4(0u, 0u, 0u, 0xffffffffu);
        }
        if (lane == 0) {
            S.nt[wv][0] = g1s >> 4;
            S.nt[wv][1] = g1e >> 4;
            S.nt[wv][2] = (base + CAP < T) ? 1 : 0;
        }
        __syncthreads();

        int sb1, sb2, sb3, sb4, sb5, sb6, sb7, TT, anymore;
        {
            const int4 q0 = *reinterpret_cast<const int4*>(&S.nt[0][0]);
            const int4 q1 = *reinterpret_cast<const int4*>(&S.nt[1][0]);
            const int4 q2 = *reinterpret_cast<const int4*>(&S.nt[2][0]);
            const int4 q3 = *reinterpret_cast<const int4*>(&S.nt[3][0]);
            sb1 = __builtin_amdgcn_readfirstlane(q0.x);
            sb2 = __builtin_amdgcn_readfirstlane(q0.y);
            sb3 = sb2 + __builtin_amdgcn_readfirstlane(q1.x);
            sb4 = sb2 + __builtin_amdgcn_readfirstlane(q1.y);
            sb5 = sb4 + __builtin_amdgcn_readfirstlane(q2.x);
            sb6 = sb4 + __builtin_amdgcn_readfirstlane(q2.y);
            sb7 = sb6 + __builtin_amdgcn_readfirstlane(q3.x);
            TT = sb6 + __builtin_amdgcn_readfirstlane(q3.y);
            anymore = __builtin_amdgcn_readfirstlane(q0.z | q1.z | q2.z | q3.z);
        }
        auto seg_of = [&](int x) __attribute__((always_inline)) {
            return (x >= sb1) + (x >= sb2) + (x >= sb3) + (x >= sb4) + (x >= sb5) + (x >= sb6) + (x >= sb7);
        };
        auto is_start = [&](int x) __attribute__((always_inline)) {
            return (x == 0) | (x == sb1) | (x == sb2) | (x == sb3) | (x == sb4) | (x == sb5) | (x == sb6) | (x == sb7);
        };
        auto seg_end = [&](int x) __attribute__((always_inline)) {
            int r = TT;
            r = (x < sb7) ? +sb7 : +r; r = (x < sb6) ? +sb6 : +r; r = (x < sb5) ? +sb5 : +r; r = (x < sb4) ? +sb4 : +r;
            r = (x < sb3) ? +sb3 : +r; r = (x < sb2) ? +sb2 : +r; r = (x < sb1) ? +sb1 : +r;
            return r;
        };
        const int ts = (TT * wv) >> 2, te = (TT * (wv + 1)) >> 2;
        const int nst = te - ts;
        const bool has_head = (nst > 0) && !is_start(ts);
        const int th = has_head ? min(te, seg_end(ts)) : +ts;
        bool serial = false;
        {
            const int s1 = TT >> 2, s2 = (TT * 2) >> 2, s3 = (TT * 3) >> 2;
            serial = (!is_start(s1) && seg_end(s1) > s2) || (!is_start(s2) && seg_end(s2) > s3);
        }
        int lq = lane;
        asm volatile("" : "+v"(lq));
        int descv;
        {
            const int ns1 = node_start(bx * KB_WAVES + 1, N, NBW, mNBW) - N0;
            const int ns2 = node_start(bx * KB_WAVES + 2, N, NBW, mNBW) - N0;
            const int ns3 = node_start(bx * KB_WAVES + 3, N, NBW, mNBW) - N0;
            const int ns4 = N1 - N0;
            const int nh = te - th;
            const int tl = (lq < nh) ? (th + lq) : (ts + lq - nh);
            const int k = seg_of(tl);
            const int b = k >> 1;
            const int cb = (b == 0) ? 0 : (b == 1) ? +sb2 : (b == 2) ? +sb4 : +sb6;
            const int r0 = (b == 0) ? 0 : (b == 1) ? +ns1 : (b == 2) ? +ns2 : +ns3;
            const int r1 = (b == 0) ? +ns1 : (b == 1) ? +ns2 : (b == 2) ? +ns3 : +ns4;
            const int fl = (lq < nh) && ((tl + 1 == te) || is_start(tl + 1));
            descv = ((b * CAPR + (tl - cb) * 16) * 16) | (b << 16) | ((k & 1) << 18) | (fl << 19) | (r0 << 20) | ((r1 - r0) << 25);
        }
        bf16x8 selLo, selHi;
        {
            int dl = (lq & 15) - 8 * (lq >> 4);
            unsigned long long one = 0x3F80ull;
            u32 l0 = 0, l1 = 0, l2 = 0, l3 = 0, h0 = 0, h1 = 0, h2 = 0, h3 = 0;
            if (dl >= 0 && dl < 4) { unsigned long long v = one << (16 * dl); l0 = (u32)v; l1 = (u32)(v >> 32); }
            if (dl >= 4 && dl < 8) { unsigned long long v = one << (16 * (dl - 4)); l2 = (u32)v; l3 = (u32)(v >> 32); }
            int dh = dl + 16;
            if (dh >= 0 && dh < 4) { unsigned long long v = one << (16 * dh); h0 = (u32)v; h1 = (u32)(v >> 32); }
            if (dh >= 4 && dh < 8) { unsigned long long v = one << (16 * (dh - 4)); h2 = (u32)v; h3 = (u32)(v >> 32); }
            selLo = as_bf16x8(make_uint4(l0, l1, l2, l3));
            selHi = as_bf16x8(make_uint4(h0, h1, h2, h3));
        }
        const char* w5b = (g < 2) ? reinterpret_cast<const char*>(&S.w5s[g][e16]) : reinterpret_cast<const char*>(&S.At[0][e16]);
        const int w5m = (g < 2) ? 0 : (int)sizeof(S.At[0]);
        const char* recb = reinterpret_cast<const char*>(&S.rec[0][e16]);
        bf16x8 gA[4], gB[4];
        u32 pA0 = 0, pA1 = 0, pB0 = 0, pB1 = 0;
        auto issue = [&](int d, bf16x8 (&gat)[4], u32& q0, u32& q1) __attribute__((always_inline)) {
            int sidx = (int)reinterpret_cast<const uint4*>(recb + (d & 0xffff))->x;
            const u16* brow = Bg + (size_t)sidx * D + g * 8;
#pragma unroll
            for (int tp = 0; tp < 4; ++tp)
                gat[tp] = *reinterpret_cast<const bf16x8*>(brow + tp * 32);
            const u16* pp = pos + (size_t)sidx * 3;
            q0 = (u32)pp[0] | ((u32)pp[1] << 16);
            q1 = (u32)pp[2];
        };
#pragma unroll
        for (int tt = 0; tt < 8; ++tt) acc2[tt] = (f32x4){0.f, 0.f, 0.f, 0.f};
        auto compute = [&](int d, bf16x8 (&gat)[4], u32 q0, u32 q1) __attribute__((always_inline)) {
            const int bk = (d >> 16) & 3;
            const uint4 rme = *reinterpret_cast<const uint4*>(recb + (d & 0xffff));
            const bool vme = rme.w != 0xffffffffu;
            const int nd = (int)(rme.w & 7u);
            const float4 pn = *reinterpret_cast<const float4*>(&S.posl[bk][nd][0]);
            const uint4* w5p = reinterpret_cast<const uint4*>(w5b + w5m * bk);
            float dx = pn.x - bf2f((u16)(q0 & 0xffffu));
            float dy = pn.y - bf2f((u16)(q0 >> 16));
            float dz = pn.z - bf2f((u16)q1);
            float d2v = dx * dx + dy * dy + dz * dz;
            u16 d2h = f2bf(d2v);
            u16 d2l = f2bf(d2v - bf2f(d2h));
            {
                bool own = vme && ((nd & 3) == g);
                S.M[wv][e16][g] = own ? make_uint2(0x3F80u | ((u32)f2bf(dx) << 16), (u32)f2bf(dy) | ((u32)f2bf(dz) << 16))
                                      : make_uint2(0u, 0u);
            }
            uint4 xu;
            {
                unsigned long long oh = (nd < 4) ? (0x3F80ull << (16 * nd)) : 0ull;
                u32 o01 = (u32)oh, o23 = (u32)(oh >> 32);
                u32 f4 = (nd == 4) ? 0x3F80u : 0u;
                if (g < 2) xu = make_uint4((u32)d2h | (rme.y << 16), (rme.y >> 16) | (rme.z << 16), (rme.z >> 16) | ((u32)d2l << 16), 0u);
                else xu = make_uint4(o01, o23, f4, 0u);
                if (!vme || g == 3) xu = make_uint4(0u, 0u, 0u, 0u);
            }
            const bf16x8 x5 = as_bf16x8(xu);
            asm volatile("s_waitcnt lgkmcnt(0)" ::: "memory");
            u32 sw01, sw23;
            {
                const u16* wp = reinterpret_cast<const u16*>(&S.M[wv][g * 4][0]) + e16;
                sw01 = (u32)wp[0] | ((u32)wp[16] << 16);
                sw23 = (u32)wp[32] | ((u32)wp[48] << 16);
            }
            const bf16x4 sop = as_bf16x4(make_uint2(sw01, sw23));
#pragma unroll
            for (int tp = 0; tp < 4; ++tp) {
#pragma unroll
                for (int u = 0; u < 2; ++u) {
                    const int tt = tp * 2 + u;
                    bf16x8 w5 = as_bf16x8(w5p[tt * 16]);
                    f32x4 acc = __builtin_amdgcn_mfma_f32_16x16x32_bf16(x5, w5, (f32x4){0.f, 0.f, 0.f, 0.f}, 0, 0, 0);
                    acc = __builtin_amdgcn_mfma_f32_16x16x32_bf16(gat[tp], u ? selHi : selLo, acc, 0, 0, 0);
                    f32x2 t01 = {acc[0], acc[1]}, t23 = {acc[2], acc[3]};
                    f32x2 e01 = {__builtin_amdgcn_exp2f(acc[0]), __builtin_amdgcn_exp2f(acc[1])};
                    f32x2 e23 = {__builtin_amdgcn_exp2f(acc[2]), __builtin_amdgcn_exp2f(acc[3])};
                    e01 = e01 + (f32x2){1.f, 1.f};
                    e23 = e23 + (f32x2){1.f, 1.f};
                    f32x2 r01 = {__builtin_amdgcn_rcpf(e01[0]), __builtin_amdgcn_rcpf(e01[1])};
                    f32x2 r23 = {__builtin_amdgcn_rcpf(e23[0]), __builtin_amdgcn_rcpf(e23[1])};
                    f32x2 m01 = t01 * r01, m23 = t23 * r23;
                    u32 k01 = cvt_pk(m01), k23 = cvt_pk(m23);
                    bf16x4 mb = as_bf16x4(make_uint2(k01, k23));
                    acc2[tt] = __builtin_amdgcn_mfma_f32_16x16x16bf16_1k(sop, mb, acc2[tt], 0, 0, 0);
                }
            }
        };
        const std::integral_constant<bool, !FIRST> dirc;
        if (nst > 0) {
            issue(__builtin_amdgcn_readlane(descv, 0), gA, pA0, pA1);
            int i = 0;
            for (; i + 1 < nst; i += 2) {
                {
                    const int rem = nst - i;
                    if (rem >= 9) __builtin_amdgcn_s_setprio(3);
                    else if (rem >= 6) __builtin_amdgcn_s_setprio(2);
                    else if (rem >= 3) __builtin_amdgcn_s_setprio(1);
                    else __builtin_amdgcn_s_setprio(0);
                }
                const int dA = __builtin_amdgcn_readlane(descv, i);
                const int dB = __builtin_amdgcn_readlane(descv, i + 1);
                const int dC = __builtin_amdgcn_readlane(descv, min(i + 2, nst - 1));
                issue(dB, gB, pB0, pB1);
                compute(dA, gA, pA0, pA1);
                if (dA & (1 << 19)) flush(dA, dirc);
                issue(dC, gA, pA0, pA1);
                compute(dB, gB, pB0, pB1);
                if (dB & (1 << 19)) flush(dB, dirc);
            }
            if (i < nst) {
                const int dA = __builtin_amdgcn_readlane(descv, i);
                compute(dA, gA, pA0, pA1);
                if (dA & (1 << 19)) flush(dA, dirc);
            }
        }
        dHead = __builtin_amdgcn_readlane(descv, max(nst - 1, 0));
        hflags = (has_head ? 1 : 0) | (serial ? 2 : 0);
        reduce();
        return anymore != 0;
    };
    if (round(0, std::integral_constant<bool, true>())) {
        for (int base = CAP;; base += CAP) {
            finish();
            load_tab_g();
            if (!round(base, std::integral_constant<bool, false>())) break;
        }
    }

    int le = lane;
    asm volatile("" : "+v"(le));
    const int f16 = le & 15, gg = le >> 4;
    bf16x8 bw[2][4];
#pragma unroll
    for (int q = 0; q < 2; ++q)
#pragma unroll
        for (int ks = 0; ks < 4; ++ks)
            bw[q][ks] = *reinterpret_cast<const bf16x8*>(whT + (size_t)(wv * 32 + (f16 >> 2) * 8 + q * 4 + (f16 & 3)) * D + ks * 32 + gg * 8);
    const int NR = N1 - N0;
    uint4 hv4[2], uv4[2];
#pragma unroll
    for (int rt = 0; rt < 2; ++rt) {
        int lr = rt * 16 + f16;
        size_t o = (size_t)(N0 + (lr < NR ? lr : 0)) * D + wv * 32 + gg * 8;
        hv4[rt] = *reinterpret_cast<const uint4*>(h + o);
        uv4[rt] = *reinterpret_cast<const uint4*>(Ug + o);
    }
    finish();
    if (le < nn * 3) {
        int n = le / 3, cpt = le - n * 3;
        out_pos[(size_t)(n0 + n) * 3 + cpt] = f2bf(S.posl[wv][n][cpt] + S.pacc[wv][n][cpt]);
    }
#pragma unroll
    for (int rt = 0; rt < 2; ++rt) {
        if (rt * 16 < NR) {
            bf16x8 af[4];
            int lr = rt * 16 + f16;
#pragma unroll
            for (int ks = 0; ks < 4; ++ks) {
                const float* mp = &S.macc[lr < NR ? lr : 0][ks * 32 + gg * 8];
                af[ks] = as_bf16x8(make_uint4(pack2(mp[0], mp[1]), pack2(mp[2], mp[3]),
                                              pack2(mp[4], mp[5]), pack2(mp[6], mp[7])));
            }
            u32 ov[4];
#pragma unroll
            for (int q = 0; q < 2; ++q) {
                const u32 u0 = q ? uv4[rt].z : uv4[rt].x, u1 = q ? uv4[rt].w : uv4[rt].y;
                const u32 h0 = q ? hv4[rt].z : hv4[rt].x, h1 = q ? hv4[rt].w : hv4[rt].y;
                f32x4 acc = {__uint_as_float(u0 << 16), __uint_as_float(u0 & 0xffff0000u),
                             __uint_as_float(u1 << 16), __uint_as_float(u1 & 0xffff0000u)};
#pragma unroll
                for (int ks = 0; ks < 4; ++ks)
                    acc = __builtin_amdgcn_mfma_f32_16x16x32_bf16(bw[q][ks], af[ks], acc, 0, 0, 0);
                ov[q * 2] = pack2(__uint_as_float(h0 << 16) + silu_f(acc[0]), __uint_as_float(h0 & 0xffff0000u) + silu_f(acc[1]));
                ov[q * 2 + 1] = pack2(__uint_as_float(h1 << 16) + silu_f(acc[2]), __uint_as_float(h1 & 0xffff0000u) + silu_f(acc[3]));
            }
            if (lr < NR)
                *reinterpret_cast<uint4*>(out_h + (size_t)(N0 + lr) * D + wv * 32 + gg * 8) = make_uint4(ov[0], ov[1], ov[2], ov[3]);
        }
    }
}

extern "C" void kernel_launch(void* const* d_in, const int* in_sizes, int n_in,
                              void* d_out, int out_size, void* d_ws, size_t ws_size,
                              hipStream_t stream) {
    const u16* h   = (const u16*)d_in[0];
    const u16* pos = (const u16*)d_in[1];
    const u16* ea  = (const u16*)d_in[2];
    const int* src = (const int*)d_in[3];
    const int* dst = (const int*)d_in[4];
    const u16* we  = (const u16*)d_in[5];
    const u16* wx  = (const u16*)d_in[6];
    const u16* wh  = (const u16*)d_in[7];
    int N = in_sizes[0] / D;
    int E = in_sizes[3];
    u16* out_h = (u16*)d_out;
    u16* out_pos = out_h + (size_t)N * D;

    int G = 128;
    while ((E + G - 1) / G > KA_THREADS * KA_ITERS && G < 64 * GI_MAX) G += 64;
    int CH = (E + G - 1) / G;
    int NBW = 3072;
    while (NBW * NPW < N && NBW < NBW_MAX) NBW += 4;
    int nGemm = (N + 127) / 128;
    if (nGemm < D * D / KA_THREADS) nGemm = D * D / KA_THREADS;
    unsigned long long mN = (1ull << 40) / (unsigned long long)N + 1ull;
    unsigned long long mNBW = (1ull << 40) / (unsigned long long)NBW + 1ull;

    char* ws = (char*)d_ws;
    size_t off = 0;
    auto take = [&](size_t bytes) { char* p = ws + off; off += (bytes + 255) & ~(size_t)255; return p; };
    uint4* rec_g = (uint4*)take((size_t)G * CH * 16);
    u16* tab = (u16*)take((size_t)G * (NBW + 4) * 2);
    u16* ABU = (u16*)take((size_t)3 * N * D * 2);
    u16* whT = (u16*)take((size_t)D * D * 2);
    uint4* w5g = (uint4*)take((size_t)256 * 16);

    k_part<<<G + nGemm, KA_THREADS, 0, stream>>>(h, src, dst, ea, we, wh, rec_g, tab, ABU, whT, w5g,
                                                      mN, mNBW, N, E, G, CH, NBW, nGemm);
    k_main<<<NBW / KB_WAVES, KB_THREADS, 0, stream>>>(h, pos, wx, rec_g, tab, ABU, whT, w5g,
                                                     out_h, out_pos, mNBW, N, G, CH, NBW);
}
```

```cpp
#include <hip/hip_runtime.h>
#include <hip/hip_bf16.h>
#include <stdint.h>
#include <type_traits>


typedef unsigned short u16;
typedef unsigned int u32;
typedef __attribute__((ext_vector_type(8))) short bf16x8;
typedef __attribute__((ext_vector_type(4))) float f32x4;
typedef __attribute__((ext_vector_type(4))) short bf16x4;
typedef __attribute__((ext_vector_type(2))) float f32x2;
typedef __attribute__((ext_vector_type(4))) unsigned int u32x4;
__device__ __forceinline__ void st_nt(void* p, uint4 v) { u32x4 x = {v.x, v.y, v.z, v.w}; __builtin_nontemporal_store(x, reinterpret_cast<u32x4*>(p)); }

#define D 128
#define KA_THREADS 1024
#define KA_ITERS 4
#define NBW_MAX 8192
#define KB_THREADS 256
#define KB_WAVES 4
#define NPW 5
#define RPL 4
#define CAP 240
#define CAPS 272
#define CAPR (CAP + 32)
#define RW 3
#define GI_MAX 2
#define AST (D + 4)
#define CBAR() asm volatile("" ::: "memory")
#define NLOG2E (-1.4426950408889634f)
#define NLN2 (-0.6931471805599453f)

__device__ __forceinline__ float bf2f(u16 v) { return __uint_as_float(((u32)v) << 16); }
__device__ __forceinline__ u16 f2bf(float f) {
    __hip_bfloat16 b = __float2bfloat16(f);
    return *reinterpret_cast<u16*>(&b);
}
__device__ __forceinline__ float silu_f(float x) {
    float e = __builtin_amdgcn_exp2f(x * NLOG2E);
    return x * __builtin_amdgcn_rcpf(1.0f + e);
}
__device__ __forceinline__ u32 pack2(float a, float b) {
    return (u32)f2bf(a) | ((u32)f2bf(b) << 16);
}
typedef __attribute__((ext_vector_type(2))) __bf16 bf16x2;
__device__ __forceinline__ u32 cvt_pk(f32x2 v) {
    bf16x2 p = __builtin_convertvector(v, bf16x2);
    return *reinterpret_cast<u32*>(&p);
}
__device__ __forceinline__ bf16x8 as_bf16x8(uint4 v) { return *reinterpret_cast<bf16x8*>(&v); }
__device__ __forceinline__ bf16x4 as_bf16x4(uint2 v) { return *reinterpret_cast<bf16x4*>(&v); }
template <int CTRL>
__device__ __forceinline__ float dpp_f(float x) {
    return __int_as_float(__builtin_amdgcn_update_dpp(0, __float_as_int(x), CTRL, 0xf, 0xf, true));
}
__device__ __forceinline__ float rsum16(float x) {
    x += dpp_f<0x128>(x);
    x += dpp_f<0x124>(x);
    x += dpp_f<0x122>(x);
    x += dpp_f<0x121>(x);
    return x;
}
__device__ __forceinline__ u32 mdiv(u32 x, unsigned long long m) {
    return (u32)(((unsigned long long)x * m) >> 40);
}
__device__ __forceinline__ int node_start(int b, int N, int NBW, unsigned long long mNBW) {
    return (int)mdiv((u32)b * (u32)N + (u32)NBW - 1u, mNBW);
}

__global__ void __launch_bounds__(KA_THREADS)
k_part(const u16* __restrict__ h, const u16* __restrict__ pos, const int* __restrict__ src, const int* __restrict__ dst,
       const u16* __restrict__ ea, const u16* __restrict__ we, const u16* __restrict__ wh,
       u32* __restrict__ rec_g, u16* __restrict__ tab, u16* __restrict__ ABU,
       u16* __restrict__ whT, uint4* __restrict__ w5g, uint2* __restrict__ pos8,
       unsigned long long mN, unsigned long long mNBW,
       int N, int E, int G, int CH, int NBW, int nGemm) {
    __shared__ __align__(16) u16 smem[(NBW_MAX + 8) * 2 + KA_THREADS * KA_ITERS + KA_THREADS * KA_ITERS * 8];
    __shared__ int wsum[KA_THREADS / 64];
    int t = threadIdx.x;
    int bid = blockIdx.x;
    if (bid < G) {
        int* hist = (int*)smem;
        for (int i = t; i < NBW; i += KA_THREADS) hist[i] = 0;
        int e0 = bid * CH;
        int e1 = min(E, e0 + CH);
        int sv[KA_ITERS], bv[KA_ITERS], rk[KA_ITERS], lo[KA_ITERS];
        uint2 eav[KA_ITERS];
#pragma unroll
        for (int i = 0; i < KA_ITERS; ++i) {
            int e = e0 + t + i * KA_THREADS;
            bv[i] = -1;
            if (e < e1) {
                int d = dst[e];
                sv[i] = src[e];
                eav[i] = *reinterpret_cast<const uint2*>(ea + (size_t)e * 4);
                int b = (int)mdiv((u32)d * (u32)NBW, mN);
                bv[i] = b;
                lo[i] = d - node_start(b, N, NBW, mNBW);
            }
        }
        __syncthreads();
#pragma unroll
        for (int i = 0; i < KA_ITERS; ++i)
            if (bv[i] >= 0) rk[i] = atomicAdd(&hist[bv[i]], 1);
        __syncthreads();
        const int per = (NBW + KA_THREADS - 1) / KA_THREADS;
        int b0 = t * per, b1 = min(NBW, b0 + per);
        int cnt = 0;
        for (int b = b0; b < b1; ++b) cnt += hist[b];
        int lane = t & 63, wv = t >> 6;
        int x = cnt;
#pragma unroll
        for (int o = 1; o < 64; o <<= 1) {
            int y = __shfl_up(x, o);
            if (lane >= o) x += y;
        }
        if (lane == 63) wsum[wv] = x;
        __syncthreads();
        int base = 0;
#pragma unroll
        for (int w = 0; w < KA_THREADS / 64; ++w) base += (w < wv) ? wsum[w] : 0;
        int off = base + x - cnt;
        u16* trow = tab + (size_t)bid * (NBW + 4);
        for (int b = b0; b < b1; ++b) {
            int c = hist[b];
            hist[b] = off;
            trow[b] = (u16)off;
            off += c;
        }
        if (t == KA_THREADS - 1) { trow[NBW] = (u16)(base + x); hist[NBW] = base + x; }
        __syncthreads();
        u16* ids = reinterpret_cast<u16*>(hist + NBW_MAX + 8);
        u32* lrec = reinterpret_cast<u32*>(ids + KA_THREADS * KA_ITERS);
#pragma unroll
        for (int i = 0; i < KA_ITERS; ++i)
            if (bv[i] >= 0) ids[hist[bv[i]] + rk[i]] = (u16)(t + i * KA_THREADS);
        __syncthreads();
        int cs0[KA_ITERS], cn[KA_ITERS], rnk[KA_ITERS];
        int mxn = 0;
#pragma unroll
        for (int i = 0; i < KA_ITERS; ++i) {
            cs0[i] = 0; cn[i] = 0; rnk[i] = 0;
            if (bv[i] >= 0) { cs0[i] = hist[bv[i]]; cn[i] = hist[bv[i] + 1] - cs0[i]; }
            mxn = max(mxn, cn[i]);
        }
#pragma clang loop unroll(disable) vectorize(disable) interleave(disable)
        for (int q = 0; q < mxn; ++q) {
#pragma unroll
            for (int i = 0; i < KA_ITERS; ++i) {
                int v = (int)ids[cs0[i] + (q < cn[i] ? q : 0)];
                rnk[i] += (q < cn[i] && v < t + i * KA_THREADS) ? 1 : 0;
            }
        }
#pragma unroll
        for (int i = 0; i < KA_ITERS; ++i) {
            if (bv[i] >= 0) {
                u32* q = lrec + (cs0[i] + rnk[i]) * RW;
                q[0] = ((u32)sv[i] << 3) | (u32)lo[i];
                q[1] = eav[i].x; q[2] = eav[i].y;
            }
        }
        __syncthreads();
        {
            const int CHB = (CH * RW + 3) >> 2;
            const int nq = ((e1 - e0) * RW + 3) >> 2;
            uint4* og = reinterpret_cast<uint4*>(rec_g) + (size_t)bid * CHB;
            const uint4* lq4 = reinterpret_cast<const uint4*>(lrec);
#pragma unroll
            for (int i = 0; i < KA_ITERS * RW / 4; ++i) {
                int p = t + i * KA_THREADS;
                if (p < nq) og[p] = lq4[p];
            }
        }
        return;
    }
    bid -= G;
    if (bid < nGemm) {
        {
            int i = bid * KA_THREADS + t;
            if (i < D * D) {
                int k = i >> 7, c = i & 127;
                whT[(size_t)c * D + k] = wh[D * D + i];
            }
            if (t < 128 && bid * 128 + t < N) {
                const u16* pp = pos + (size_t)(bid * 128 + t) * 3;
                pos8[bid * 128 + t] = make_uint2((u32)pp[0] | ((u32)pp[1] << 16), (u32)pp[2]);
            }
            if (bid == nGemm - 1 && t < 2 * D) {
                int gq = t >> 7, c = t & 127;
                u32 v[6];
#pragma unroll
                for (int j = 0; j < 6; ++j) {
                    int kr = (j == 5) ? 0 : j;
                    float w = bf2f(we[(size_t)(256 + kr) * D + c]) * NLOG2E;
                    u16 hi = f2bf(w);
                    u16 lo = f2bf(w - bf2f(hi));
                    v[j] = (u32)(gq ? lo : hi);
                }
                w5g[t] = make_uint4(v[0] | (v[1] << 16), v[2] | (v[3] << 16), v[4] | (v[5] << 16), 0u);
            }
        }
        {
            const int c2 = t & 63, kb = t >> 6;
            u32 wr[16];
#pragma unroll
            for (int j = 0; j < 16; ++j)
                wr[j] = *reinterpret_cast<const u32*>(we + (size_t)(kb * 16 + j) * D + c2 * 2);
#pragma unroll
            for (int sb = 0; sb < 2; ++sb) {
                int kk = kb * 16 + sb * 8;
                int half = kk >> 7, k = kk & 127;
                const u32* w = &wr[sb * 8];
                uint4 lo4 = make_uint4((w[0] & 0xffffu) | (w[1] << 16), (w[2] & 0xffffu) | (w[3] << 16),
                                       (w[4] & 0xffffu) | (w[5] << 16), (w[6] & 0xffffu) | (w[7] << 16));
                uint4 hi4 = make_uint4((w[0] >> 16) | (w[1] & 0xffff0000u), (w[2] >> 16) | (w[3] & 0xffff0000u),
                                       (w[4] >> 16) | (w[5] & 0xffff0000u), (w[6] >> 16) | (w[7] & 0xffff0000u));
                *reinterpret_cast<uint4*>(&smem[(half * D + c2 * 2) * (D + 8) + k]) = lo4;
                *reinterpret_cast<uint4*>(&smem[(half * D + c2 * 2 + 1) * (D + 8) + k]) = hi4;
            }
        }
        {
            const int c2 = t & 63, kb = t >> 6;
            u32 w[8];
#pragma unroll
            for (int j = 0; j < 8; ++j)
                w[j] = *reinterpret_cast<const u32*>(wh + (size_t)(kb * 8 + j) * D + c2 * 2);
            int k = kb * 8;
            uint4 lo4 = make_uint4((w[0] & 0xffffu) | (w[1] << 16), (w[2] & 0xffffu) | (w[3] << 16),
                                   (w[4] & 0xffffu) | (w[5] << 16), (w[6] & 0xffffu) | (w[7] << 16));
            uint4 hi4 = make_uint4((w[0] >> 16) | (w[1] & 0xffff0000u), (w[2] >> 16) | (w[3] & 0xffff0000u),
                                   (w[4] >> 16) | (w[5] & 0xffff0000u), (w[6] >> 16) | (w[7] & 0xffff0000u));
            *reinterpret_cast<uint4*>(&smem[(2 * D + c2 * 2) * (D + 8) + k]) = lo4;
            *reinterpret_cast<uint4*>(&smem[(2 * D + c2 * 2 + 1) * (D + 8) + k]) = hi4;
        }
        __syncthreads();
        int lane = t & 63, wv = t >> 6;
        int e16 = lane & 15, g = lane >> 4;
        int rt = wv & 7;
        int half = wv >> 3;
        int n = bid * 128 + rt * 16 + e16;
        int nn = n < N ? n : N - 1;
        bf16x8 af[4];
#pragma unroll
        for (int ks = 0; ks < 4; ++ks)
            af[ks] = *reinterpret_cast<const bf16x8*>(h + (size_t)nn * D + ks * 32 + g * 8);
        const int wr0 = (e16 >> 2) * 8 + (e16 & 3);
#pragma unroll
        for (int jj = 0; jj < 6; ++jj) {
            const int jb = half * 6 + jj;
            f32x4 acc0 = {0.f, 0.f, 0.f, 0.f}, acc1 = {0.f, 0.f, 0.f, 0.f};
#pragma unroll
            for (int ks = 0; ks < 4; ++ks) {
                const u16* wp = &smem[(jb * 32 + wr0) * (D + 8) + ks * 32 + g * 8];
                bf16x8 w0 = *reinterpret_cast<const bf16x8*>(wp);
                bf16x8 w1 = *reinterpret_cast<const bf16x8*>(wp + 4 * (D + 8));
                acc0 = __builtin_amdgcn_mfma_f32_16x16x32_bf16(w0, af[ks], acc0, 0, 0, 0);
                acc1 = __builtin_amdgcn_mfma_f32_16x16x32_bf16(w1, af[ks], acc1, 0, 0, 0);
            }
            const float sc = (jb < 8) ? NLOG2E : 1.0f;
            uint4 o = make_uint4(cvt_pk((f32x2){acc0[0] * sc, acc0[1] * sc}), cvt_pk((f32x2){acc0[2] * sc, acc0[3] * sc}),
                                 cvt_pk((f32x2){acc1[0] * sc, acc1[1] * sc}), cvt_pk((f32x2){acc1[2] * sc, acc1[3] * sc}));
            if (n < N) {
                u16* op = ABU + ((size_t)(jb >> 2) * N + n) * D + (jb & 3) * 32 + g * 8;
                if ((jb >> 2) == 1) *reinterpret_cast<uint4*>(op) = o;
                else st_nt(op, o);
            }
        }
        return;
    }
}

struct KbSmem {
    uint4 rec[KB_WAVES][CAPR];
    uint2 geo[KB_WAVES][CAPR];
    uint4 lut[8];
    uint4 At[KB_WAVES][D];
    uint4 w5s[2][D];
    float macc[KB_WAVES * NPW][AST];
    float pacc[KB_WAVES][NPW][4];
    float posl[KB_WAVES][NPW + 3][4];
    float wxs[D];
    uint2 M[KB_WAVES][16][4];
    int nt[KB_WAVES][4];
};

__global__ void __launch_bounds__(KB_THREADS, 3)
k_main(const u16* __restrict__ h, const u16* __restrict__ pos,
       const u16* __restrict__ wx,
       const u32* __restrict__ rec_g, const u16* __restrict__ tab,
       const u16* __restrict__ ABU,
       const u16* __restrict__ whT, const uint4* __restrict__ w5g, const uint2* __restrict__ pos8,
       u16* __restrict__ out_h, u16* __restrict__ out_pos,
       unsigned long long mNBW, int N, int G, int CH, int NBW) {
    __shared__ KbSmem S;
    const u16* __restrict__ Bg = ABU + (size_t)N * D;
    const u16* __restrict__ Ug = ABU + (size_t)2 * N * D;
    const int t = threadIdx.x, lane = t & 63;
    const int wv = __builtin_amdgcn_readfirstlane(t >> 6);
    const int e16 = lane & 15, g = lane >> 4;
    const int nwg = NBW / KB_WAVES, per8 = nwg >> 3;
    const int bx = ((int)blockIdx.x < per8 * 8) ? ((int)(blockIdx.x & 7) * per8 + (int)(blockIdx.x >> 3)) : (int)blockIdx.x;
    const int wb = bx * KB_WAVES + wv;
    const int N0 = node_start(bx * KB_WAVES, N, NBW, mNBW);
    const int N1 = node_start(bx * KB_WAVES + KB_WAVES, N, NBW, mNBW);
    const int n0 = node_start(wb, N, NBW, mNBW);
    const int nn = node_start(wb + 1, N, NBW, mNBW) - n0;
    const int rowb = n0 - N0;

    int so[GI_MAX], sc[GI_MAX];
    u16 (*tabs)[5] = reinterpret_cast<u16 (*)[5]>(&S.M[0][0][0]);
    auto fetch_tab = [&]() {
        if (t < G) {
            const u16* tp = tab + (size_t)t * (NBW + 4) + bx * KB_WAVES;
            uint2 v = *reinterpret_cast<const uint2*>(tp);
            u32 v4 = tp[4];
            tabs[t][0] = (u16)v.x; tabs[t][1] = (u16)(v.x >> 16);
            tabs[t][2] = (u16)v.y; tabs[t][3] = (u16)(v.y >> 16);
            tabs[t][4] = (u16)v4;
        }
    };
    auto load_tab = [&]() {
        int lo_ = lane;
        asm volatile("" : "+v"(lo_));
#pragma unroll
        for (int ci = 0; ci < GI_MAX; ++ci) {
            int c = lo_ + 64 * ci;
            so[ci] = 0; sc[ci] = 0;
            if (c < G) {
                so[ci] = tabs[c][wv];
                sc[ci] = tabs[c][wv + 1];
            }
        }
    };
    auto load_tab_g = [&]() {
        int lo_ = lane;
        asm volatile("" : "+v"(lo_));
#pragma unroll
        for (int ci = 0; ci < GI_MAX; ++ci) {
            int c = lo_ + 64 * ci;
            so[ci] = 0; sc[ci] = 0;
            if (c < G) {
                const u16* tp = tab + (size_t)c * (NBW + 4) + wb;
                so[ci] = tp[0];
                sc[ci] = tp[1];
            }
        }
    };
    fetch_tab();
    u32 a0[NPW];
#pragma unroll
    for (int j = 0; j < NPW; ++j) {
        a0[j] = 0u;
        if (j < nn) a0[j] = *reinterpret_cast<const u32*>(ABU + (size_t)(n0 + j) * D + lane * 2);
    }
    const uint4 w40 = w5g[lane], w41 = w5g[lane + 64], w42 = w5g[lane + 128], w43 = w5g[lane + 192];
    u16 wx0 = wx[lane], wx1 = wx[lane + 64];
    u16 pz16 = 0;
    {
        int n = lane >> 2, cpt = lane & 3;
        if (n < nn && cpt < 3) pz16 = pos[(n0 + n) * 3 + cpt];
    }
    auto stage = [&]() {
        S.At[wv][lane * 2] = make_uint4((a0[0] & 0xffffu) | (a0[1] << 16), (a0[2] & 0xffffu) | (a0[3] << 16), a0[4] & 0xffffu, 0u);
        S.At[wv][lane * 2 + 1] = make_uint4((a0[0] >> 16) | (a0[1] & 0xffff0000u), (a0[2] >> 16) | (a0[3] & 0xffff0000u), a0[4] >> 16, 0u);
#pragma unroll
        for (int j = 0; j < NPW; ++j)
            if (j < nn) { S.macc[rowb + j][lane] = 0.f; S.macc[rowb + j][64 + lane] = 0.f; }
        uint4* wq = &S.w5s[0][0] + lane;
        wq[0] = w40; wq[64] = w41; wq[128] = w42; wq[192] = w43;
        S.wxs[lane] = bf2f(wx0);
        S.wxs[lane + 64] = bf2f(wx1);
        if (lane < (NPW + 3) * 4) S.posl[wv][lane >> 2][lane & 3] = bf2f(pz16);
        if (lane < NPW * 4) S.pacc[wv][lane >> 2][lane & 3] = 0.f;
        if (lane < 32) {
            const int n = lane >> 2, w = lane & 3;
            reinterpret_cast<u32*>(S.lut)[lane] = (n < 5 && (n >> 1) == w) ? (0x3F80u << (16 * (n & 1))) : 0u;
        }
    };
    const unsigned long long ltmask = (1ull << lane) - 1ull;
    const int CHB4 = ((CH * RW + 3) >> 2) << 2;
    uint4* rec = S.rec[wv];
    u32* key = reinterpret_cast<u32*>(S.rec[wv]);

    __syncthreads();
    load_tab();
    __syncthreads();
    f32x4 acc2[8];
    int dHead = 0, hflags = 0;
    float hm[8], hp0, hp1, hp2;
    auto reduce = [&]() __attribute__((always_inline)) {
        float wxc[8];
#pragma unroll
        for (int tt = 0; tt < 8; ++tt) wxc[tt] = S.wxs[tt * 16 + e16];
        float pv0 = 0.f, pv1 = 0.f, pv2 = 0.f;
#pragma unroll
        for (int tt = 0; tt < 8; ++tt) {
            hm[tt] = acc2[tt][0] * NLN2;
            pv0 += acc2[tt][1] * wxc[tt];
            pv1 += acc2[tt][2] * wxc[tt];
            pv2 += acc2[tt][3] * wxc[tt];
            acc2[tt] = (f32x4){0.f, 0.f, 0.f, 0.f};
        }
        hp0 = rsum16(pv0) * NLN2; hp1 = rsum16(pv1) * NLN2; hp2 = rsum16(pv2) * NLN2;
    };
    auto commit = [&](int d, auto accc) __attribute__((always_inline)) {
        constexpr bool ACC = decltype(accc)::value;
        const int bk = (d >> 16) & 3;
        const int nl = ((d >> 18) & 1) * 4 + g;
        const int rb = (d >> 20) & 31, nb = (d >> 25) & 7;
        const bool on = nl < nb;
        float* mrow = &S.macc[rb + (on ? nl : 0)][e16];
        float* prow = &S.pacc[bk][on ? nl : 0][0];
        float old[8], po[3];
        if (ACC) {
#pragma unroll
            for (int tt = 0; tt < 8; ++tt) old[tt] = mrow[tt * 16];
            po[0] = prow[0]; po[1] = prow[1]; po[2] = prow[2];
            CBAR();
        }
        if (on) {
#pragma unroll
            for (int tt = 0; tt < 8; ++tt) mrow[tt * 16] = ACC ? old[tt] + hm[tt] : hm[tt];
            if (e16 == 0) {
                prow[0] = (ACC ? po[0] : 0.f) + hp0;
                prow[1] = (ACC ? po[1] : 0.f) + hp1;
                prow[2] = (ACC ? po[2] : 0.f) + hp2;
            }
        }
    };
    auto flush = [&](int d, auto accc) __attribute__((always_inline)) { reduce(); commit(d, accc); };
    auto finish = [&]() __attribute__((always_inline)) {
        const std::integral_constant<bool, true> accc;
        __syncthreads();
        for (int w = 1; w < KB_WAVES; ++w) {
            if ((hflags & 1) && (wv == w || !(hflags & 2))) commit(dHead, accc);
            __syncthreads();
            if (!(hflags & 2)) break;
        }
    };
    auto round = [&](const int base, auto firstc) __attribute__((always_inline)) -> bool {
        constexpr bool FIRST = decltype(firstc)::value;
        int T;
        int fo[GI_MAX];
        {
            int tot = 0;
#pragma unroll
            for (int ci = 0; ci < GI_MAX; ++ci) { sc[ci] -= so[ci]; tot += sc[ci]; }
            int x = tot;
            int ls = lane;
            asm volatile("" : "+v"(ls));
#pragma unroll
            for (int o = 1; o < 64; o <<= 1) {
                int y = __builtin_amdgcn_ds_bpermute((ls - o) << 2, x);
                if (ls >= o) x += y;
            }
            T = __builtin_amdgcn_readlane(x, 63);
            int off = x - tot;
#pragma unroll
            for (int ci = 0; ci < GI_MAX; ++ci) { fo[ci] = off; off += sc[ci]; }
        }
        const int Tr = max(0, min(CAP, T - base));
        int lo2 = lane;
        asm volatile("" : "+v"(lo2));
#pragma unroll
        for (int ci = 0; ci < GI_MAX; ++ci) {
            int gs = (lo2 + 64 * ci) * CHB4 + so[ci] * RW;
            for (int q = 0; q < sc[ci]; ++q) {
                int fp = fo[ci] + q - base;
                if (fp >= 0 && fp < CAP) key[fp] = (u32)(gs + q * RW);
            }
        }
        CBAR();
        uint4 r[RPL];
        uint2 ps[RPL];
#pragma unroll
        for (int i = 0; i < RPL; ++i) {
            int p = lane + 64 * i;
            r[i] = make_uint4(0xffffffffu, 0u, 0u, 0u);
            if (p < Tr) {
                const u32* rp = rec_g + key[p];
                r[i] = make_uint4(rp[0], rp[1], rp[2], 0u);
            }
        }
        if (FIRST) stage();
#pragma unroll
        for (int i = 0; i < RPL; ++i) {
            ps[i] = make_uint2(0u, 0u);
            if (r[i].x != 0xffffffffu) ps[i] = pos8[r[i].x >> 3];
        }
        int posn[RPL];
        int cst = 0, cnt4 = 0, c0 = 0, g1s = 0;
#pragma unroll
        for (int i = 0; i < RPL; ++i) posn[i] = 0;
#pragma unroll
        for (int j = 0; j < NPW; ++j) {
            if (j == 4) { c0 = cst; g1s = (cst + 15) & ~15; cst = g1s; }
            int c = 0;
#pragma unroll
            for (int i = 0; i < RPL; ++i) {
                bool mt = (r[i].x != 0xffffffffu) && ((int)(r[i].x & 7u) == j);
                unsigned long long m = __ballot(mt);
                if (mt) posn[i] = cst + c + __popcll(m & ltmask);
                c += __popcll(m);
            }
            if (j == 4) cnt4 = c;
            cst += c;
        }
        const int g1e = (g1s + cnt4 + 15) & ~15;
        CBAR();
#pragma unroll
        for (int i = 0; i < RPL; ++i)
            if (r[i].x != 0xffffffffu) {
                const float4 pn = *reinterpret_cast<const float4*>(&S.posl[wv][r[i].x & 7u][0]);
                float dx = pn.x - __uint_as_float(ps[i].x << 16), dy = pn.y - __uint_as_float(ps[i].x & 0xffff0000u);
                float dz = pn.z - __uint_as_float(ps[i].y << 16);
                float d2v = dx * dx + dy * dy + dz * dz;
                u16 d2h = f2bf(d2v);
                u16 d2l = f2bf(d2v - bf2f(d2h));
                rec[posn[i]] = make_uint4((u32)d2h | (r[i].y << 16), (r[i].y >> 16) | (r[i].z << 16), (r[i].z >> 16) | ((u32)d2l << 16), r[i].x);
                S.geo[wv][posn[i]] = make_uint2(0x3F80u | ((u32)f2bf(dx) << 16), pack2(dy, dz));
            }
        if (lane < 16) {
            if (c0 + lane < g1s) { rec[c0 + lane] = make_uint4(0u, 0u, 0u, 7u); S.geo[wv][c0 + lane] = make_uint2(0u, 0u); }
            if (g1s + cnt4 + lane < g1e) { rec[g1s + cnt4 + lane] = make_uint4(0u, 0u, 0u, 7u); S.geo[wv][g1s + cnt4 + lane] = make_uint2(0u, 0u); }
        }
        if (lane == 0) {
            S.nt[wv][0] = g1s >> 4;
            S.nt[wv][1] = g1e >> 4;
            S.nt[wv][2] = (base + CAP < T) ? 1 : 0;
        }
        __syncthreads();

        int sb1, sb2, sb3, sb4, sb5, sb6, sb7, TT, anymore;
        {
            const int4 q0 = *reinterpret_cast<const int4*>(&S.nt[0][0]);
            const int4 q1 = *reinterpret_cast<const int4*>(&S.nt[1][0]);
            const int4 q2 = *reinterpret_cast<const int4*>(&S.nt[2][0]);
            const int4 q3 = *reinterpret_cast<const int4*>(&S.nt[3][0]);
            sb1 = __builtin_amdgcn_readfirstlane(q0.x);
            sb2 = __builtin_amdgcn_readfirstlane(q0.y);
            sb3 = sb2 + __builtin_amdgcn_readfirstlane(q1.x);
            sb4 = sb2 + __builtin_amdgcn_readfirstlane(q1.y);
            sb5 = sb4 + __builtin_amdgcn_readfirstlane(q2.x);
            sb6 = sb4 + __builtin_amdgcn_readfirstlane(q2.y);
            sb7 = sb6 + __builtin_amdgcn_readfirstlane(q3.x);
            TT = sb6 + __builtin_amdgcn_readfirstlane(q3.y);
            anymore = __builtin_amdgcn_readfirstlane(q0.z | q1.z | q2.z | q3.z);
        }
        auto seg_of = [&](int x) __attribute__((always_inline)) {
            return (x >= sb1) + (x >= sb2) + (x >= sb3) + (x >= sb4) + (x >= sb5) + (x >= sb6) + (x >= sb7);
        };
        auto is_start = [&](int x) __attribute__((always_inline)) {
            return (x == 0) | (x == sb1) | (x == sb2) | (x == sb3) | (x == sb4) | (x == sb5) | (x == sb6) | (x == sb7);
        };
        auto seg_end = [&](int x) __attribute__((always_inline)) {
            int r = TT;
            r = (x < sb7) ? +sb7 : +r; r = (x < sb6) ? +sb6 : +r; r = (x < sb5) ? +sb5 : +r; r = (x < sb4) ? +sb4 : +r;
            r = (x < sb3) ? +sb3 : +r; r = (x < sb2) ? +sb2 : +r; r = (x < sb1) ? +sb1 : +r;
            return r;
        };
        const int ts = (TT * wv) >> 2, te = (TT * (wv + 1)) >> 2;
        const int nst = te - ts;
        const bool has_head = (nst > 0) && !is_start(ts);
        const int th = has_head ? min(te, seg_end(ts)) : +ts;
        bool serial = false;
        {
            const int s1 = TT >> 2, s2 = (TT * 2) >> 2, s3 = (TT * 3) >> 2;
            serial = (!is_start(s1) && seg_end(s1) > s2) || (!is_start(s2) && seg_end(s2) > s3);
        }
        int lq = lane;
        asm volatile("" : "+v"(lq));
        int descv;
        {
            const int ns1 = node_start(bx * KB_WAVES + 1, N, NBW, mNBW) - N0;
            const int ns2 = node_start(bx * KB_WAVES + 2, N, NBW, mNBW) - N0;
            const int ns3 = node_start(bx * KB_WAVES + 3, N, NBW, mNBW) - N0;
            const int ns4 = N1 - N0;
            const int nh = te - th;
            const int tl = (lq < nh) ? (th + lq) : (ts + lq - nh);
            const int k = seg_of(tl);
            const int b = k >> 1;
            const int cb = (b == 0) ? 0 : (b == 1) ? +sb2 : (b == 2) ? +sb4 : +sb6;
            const int r0 = (b == 0) ? 0 : (b == 1) ? +ns1 : (b == 2) ? +ns2 : +ns3;
            const int r1 = (b == 0) ? +ns1 : (b == 1) ? +ns2 : (b == 2) ? +ns3 : +ns4;
            const int fl = (lq < nh) && ((tl + 1 == te) || is_start(tl + 1));
            descv = ((b * CAPR + (tl - cb) * 16) * 16) | (b << 16) | ((k & 1) << 18) | (fl << 19) | (r0 << 20) | ((r1 - r0) << 25);
        }
        bf16x8 selLo, selHi;
        {
            int dl = (lq & 15) - 8 * (lq >> 4);
            unsigned long long one = 0x3F80ull;
            u32 l0 = 0, l1 = 0, l2 = 0, l3 = 0, h0 = 0, h1 = 0, h2 = 0, h3 = 0;
            if (dl >= 0 && dl < 4) { unsigned long long v = one << (16 * dl); l0 = (u32)v; l1 = (u32)(v >> 32); }
            if (dl >= 4 && dl < 8) { unsigned long long v = one << (16 * (dl - 4)); l2 = (u32)v; l3 = (u32)(v >> 32); }
            int dh = dl + 16;
            if (dh >= 0 && dh < 4) { unsigned long long v = one << (16 * dh); h0 = (u32)v; h1 = (u32)(v >> 32); }
            if (dh >= 4 && dh < 8) { unsigned long long v = one << (16 * (dh - 4)); h2 = (u32)v; h3 = (u32)(v >> 32); }
            selLo = as_bf16x8(make_uint4(l0, l1, l2, l3));
            selHi = as_bf16x8(make_uint4(h0, h1, h2, h3));
        }
        const char* w5b = (g < 2) ? reinterpret_cast<const char*>(&S.w5s[g][e16]) : reinterpret_cast<const char*>(&S.At[0][e16]);
        const int w5m = (g < 2) ? 0 : (int)sizeof(S.At[0]);
        const char* recb = reinterpret_cast<const char*>(&S.rec[0][e16]);
        const char* geob = reinterpret_cast<const char*>(&S.geo[0][e16]);
        const char* Bb = reinterpret_cast<const char*>(Bg);
        const u32 g16 = (u32)g * 16u;
        const u32 ndor = (g == 3) ? 7u : 0u;
        const u32 wmask = (g < 2) ? 0u : 0xffffffffu;
        bf16x8 gA[4], gB[4];
        u32 pA0 = 0, pB0 = 0;
        auto issue = [&](int d, bf16x8 (&gat)[4], u32& q0) __attribute__((always_inline)) {
            q0 = reinterpret_cast<const uint4*>(recb + (d & 0xffff))->w;
            const u32 off = ((q0 >> 3) << 8) + g16;
#pragma unroll
            for (int tp = 0; tp < 4; ++tp)
                gat[tp] = *reinterpret_cast<const bf16x8*>(Bb + off + tp * 64);
        };
#pragma unroll
        for (int tt = 0; tt < 8; ++tt) acc2[tt] = (f32x4){0.f, 0.f, 0.f, 0.f};
        auto compute = [&](int d, bf16x8 (&gat)[4], u32 q0) __attribute__((always_inline)) {
            const int bk = (d >> 16) & 3;
            const u32 nd = q0 & 7u;
            const char* xp = (g < 2) ? (recb + (d & 0xffff)) : (reinterpret_cast<const char*>(S.lut) + ((nd | ndor) << 4));
            uint4 xu = *reinterpret_cast<const uint4*>(xp);
            const uint2 gq = *reinterpret_cast<const uint2*>(geob + ((d & 0xffff) >> 1));
            const uint4* w5p = reinterpret_cast<const uint4*>(w5b + w5m * bk);
            S.M[wv][e16][g] = ((nd & 3u) == (u32)g) ? gq : make_uint2(0u, 0u);
            xu.w &= wmask;
            const bf16x8 x5 = as_bf16x8(xu);
            asm volatile("s_waitcnt lgkmcnt(0)" ::: "memory");
            u32 sw01, sw23;
            {
                const u16* wp = reinterpret_cast<const u16*>(&S.M[wv][g * 4][0]) + e16;
                sw01 = (u32)wp[0] | ((u32)wp[16] << 16);
                sw23 = (u32)wp[32] | ((u32)wp[48] << 16);
            }
            const bf16x4 sop = as_bf16x4(make_uint2(sw01, sw23));
#pragma unroll
            for (int tp = 0; tp < 4; ++tp) {
#pragma unroll
                for (int u = 0; u < 2; ++u) {
                    const int tt = tp * 2 + u;
                    bf16x8 w5 = as_bf16x8(w5p[tt * 16]);
                    f32x4 acc = __builtin_amdgcn_mfma_f32_16x16x32_bf16(x5, w5, (f32x4){0.f, 0.f, 0.f, 0.f}, 0, 0, 0);
                    acc = __builtin_amdgcn_mfma_f32_16x16x32_bf16(gat[tp], u ? selHi : selLo, acc, 0, 0, 0);
                    f32x2 t01 = {acc[0], acc[1]}, t23 = {acc[2], acc[3]};
                    f32x2 e01 = {__builtin_amdgcn_exp2f(acc[0]), __builtin_amdgcn_exp2f(acc[1])};
                    f32x2 e23 = {__builtin_amdgcn_exp2f(acc[2]), __builtin_amdgcn_exp2f(acc[3])};
                    e01 = e01 + (f32x2){1.f, 1.f};
                    e23 = e23 + (f32x2){1.f, 1.f};
                    f32x2 r01 = {__builtin_amdgcn_rcpf(e01[0]), __builtin_amdgcn_rcpf(e01[1])};
                    f32x2 r23 = {__builtin_amdgcn_rcpf(e23[0]), __builtin_amdgcn_rcpf(e23[1])};
                    f32x2 m01 = t01 * r01, m23 = t23 * r23;
                    u32 k01 = cvt_pk(m01), k23 = cvt_pk(m23);
                    bf16x4 mb = as_bf16x4(make_uint2(k01, k23));
                    acc2[tt] = __builtin_amdgcn_mfma_f32_16x16x16bf16_1k(sop, mb, acc2[tt], 0, 0, 0);
                }
            }
        };
        const std::integral_constant<bool, !FIRST> dirc;
        if (nst > 0) {
            issue(__builtin_amdgcn_readlane(descv, 0), gA, pA0);
            int i = 0;
            for (; i + 1 < nst; i += 2) {
                {
                    const int rem = nst - i;
                    if (rem >= 9) __builtin_amdgcn_s_setprio(3);
                    else if (rem >= 6) __builtin_amdgcn_s_setprio(2);
                    else if (rem >= 3) __builtin_amdgcn_s_setprio(1);
                    else __builtin_amdgcn_s_setprio(0);
                }
                const int dA = __builtin_amdgcn_readlane(descv, i);
                const int dB = __builtin_amdgcn_readlane(descv, i + 1);
                const int dC = __builtin_amdgcn_readlane(descv, min(i + 2, nst - 1));
                issue(dB, gB, pB0);
                compute(dA, gA, pA0);
                if (dA & (1 << 19)) flush(dA, dirc);
                issue(dC, gA, pA0);
                compute(dB, gB, pB0);
                if (dB & (1 << 19)) flush(dB, dirc);
            }
            if (i < nst) {
                const int dA = __builtin_amdgcn_readlane(descv, i);
                compute(dA, gA, pA0);
                if (dA & (1 << 19)) flush(dA, dirc);
            }
        }
        dHead = __builtin_amdgcn_readlane(descv, max(nst - 1, 0));
        hflags = (has_head ? 1 : 0) | (serial ? 2 : 0);
        reduce();
        return anymore != 0;
    };
    if (round(0, std::integral_constant<bool, true>())) {
        for (int base = CAP;; base += CAP) {
            finish();
            load_tab_g();
            if (!round(base, std::integral_constant<bool, false>())) break;
        }
    }

    int le = lane;
    asm volatile("" : "+v"(le));
    const int f16 = le & 15, gg = le >> 4;
    bf16x8 bw[2][4];
#pragma unroll
    for (int q = 0; q < 2; ++q)
#pragma unroll
        for (int ks = 0; ks < 4; ++ks)
            bw[q][ks] = *reinterpret_cast<const bf16x8*>(whT + (size_t)(wv * 32 + (f16 >> 2) * 8 + q * 4 + (f16 & 3)) * D + ks * 32 + gg * 8);
    const int NR = N1 - N0;
    uint4 hv4[2], uv4[2];
#pragma unroll
    for (int rt = 0; rt < 2; ++rt) {
        int lr = rt * 16 + f16;
        size_t o = (size_t)(N0 + (lr < NR ? lr : 0)) * D + wv * 32 + gg * 8;
        hv4[rt] = *reinterpret_cast<const uint4*>(h + o);
        uv4[rt] = *reinterpret_cast<const uint4*>(Ug + o);
    }
    finish();
    if (le < nn * 3) {
        int n = le / 3, cpt = le - n * 3;
        out_pos[(size_t)(n0 + n) * 3 + cpt] = f2bf(S.posl[wv][n][cpt] + S.pacc[wv][n][cpt]);
    }
#pragma unroll
    for (int rt = 0; rt < 2; ++rt) {
        if (rt * 16 < NR) {
            bf16x8 af[4];
            int lr = rt * 16 + f16;
#pragma unroll
            for (int ks = 0; ks < 4; ++ks) {
                const float* mp = &S.macc[lr < NR ? lr : 0][ks * 32 + gg * 8];
                af[ks] = as_bf16x8(make_uint4(pack2(mp[0], mp[1]), pack2(mp[2], mp[3]),
                                              pack2(mp[4], mp[5]), pack2(mp[6], mp[7])));
            }
            u32 ov[4];
#pragma unroll
            for (int q = 0; q < 2; ++q) {
                const u32 u0 = q ? uv4[rt].z : uv4[rt].x, u1 = q ? uv4[rt].w : uv4[rt].y;
                const u32 h0 = q ? hv4[rt].z : hv4[rt].x, h1 = q ? hv4[rt].w : hv4[rt].y;
                f32x4 acc = {__uint_as_float(u0 << 16), __uint_as_float(u0 & 0xffff0000u),
                             __uint_as_float(u1 << 16), __uint_as_float(u1 & 0xffff0000u)};
#pragma unroll
                for (int ks = 0; ks < 4; ++ks)
                    acc = __builtin_amdgcn_mfma_f32_16x16x32_bf16(bw[q][ks], af[ks], acc, 0, 0, 0);
                ov[q * 2] = pack2(__uint_as_float(h0 << 16) + silu_f(acc[0]), __uint_as_float(h0 & 0xffff0000u) + silu_f(acc[1]));
                ov[q * 2 + 1] = pack2(__uint_as_float(h1 << 16) + silu_f(acc[2]), __uint_as_float(h1 & 0xffff0000u) + silu_f(acc[3]));
            }
            if (lr < NR)
                *reinterpret_cast<uint4*>(out_h + (size_t)(N0 + lr) * D + wv * 32 + gg * 8) = make_uint4(ov[0], ov[1], ov[2], ov[3]);
        }
    }
}

extern "C" void kernel_launch(void* const* d_in, const int* in_sizes, int n_in,
                              void* d_out, int out_size, void* d_ws, size_t ws_size,
                              hipStream_t stream) {
    const u16* h   = (const u16*)d_in[0];
    const u16* pos = (const u16*)d_in[1];
    const u16* ea  = (const u16*)d_in[2];
    const int* src = (const int*)d_in[3];
    const int* dst = (const int*)d_in[4];
    const u16* we  = (const u16*)d_in[5];
    const u16* wx  = (const u16*)d_in[6];
    const u16* wh  = (const u16*)d_in[7];
    int N = in_sizes[0] / D;
    int E = in_sizes[3];
    u16* out_h = (u16*)d_out;
    u16* out_pos = out_h + (size_t)N * D;

    int G = 128;
    while ((E + G - 1) / G > KA_THREADS * KA_ITERS && G < 64 * GI_MAX) G += 64;
    int CH = (E + G - 1) / G;
    int NBW = 3072;
    while (NBW * NPW < N && NBW < NBW_MAX) NBW += 4;
    int nGemm = (N + 127) / 128;
    if (nGemm < D * D / KA_THREADS) nGemm = D * D / KA_THREADS;
    unsigned long long mN = (1ull << 40) / (unsigned long long)N + 1ull;
    unsigned long long mNBW = (1ull << 40) / (unsigned long long)NBW + 1ull;

    char* ws = (char*)d_ws;
    size_t off = 0;
    auto take = [&](size_t bytes) { char* p = ws + off; off += (bytes + 255) & ~(size_t)255; return p; };
    u32* rec_g = (u32*)take((size_t)G * ((CH * RW + 3) >> 2) * 16 + 64);
    u16* tab = (u16*)take((size_t)G * (NBW + 4) * 2);
    u16* ABU = (u16*)take((size_t)3 * N * D * 2);
    u16* whT = (u16*)take((size_t)D * D * 2);
    uint4* w5g = (uint4*)take((size_t)256 * 16);
    uint2* pos8 = (uint2*)take((size_t)N * 8);

    k_part<<<G + nGemm, KA_THREADS, 0, stream>>>(h, pos, src, dst, ea, we, wh, rec_g, tab, ABU, whT, w5g, pos8,
                                                      mN, mNBW, N, E, G, CH, NBW, nGemm);
    k_main<<<NBW / KB_WAVES, KB_THREADS, 0, stream>>>(h, pos, wx, rec_g, tab, ABU, whT, w5g, pos8,
                                                     out_h, out_pos, mNBW, N, G, CH, NBW);
}
```

```cpp
#include <hip/hip_runtime.h>
#include <hip/hip_bf16.h>
#include <stdint.h>
#include <type_traits>


typedef unsigned short u16;
typedef unsigned int u32;
typedef __attribute__((ext_vector_type(8))) short bf16x8;
typedef __attribute__((ext_vector_type(4))) float f32x4;
typedef __attribute__((ext_vector_type(4))) short bf16x4;
typedef __attribute__((ext_vector_type(2))) float f32x2;
typedef __attribute__((ext_vector_type(4))) unsigned int u32x4;
__device__ __forceinline__ void st_nt(void* p, uint4 v) { u32x4 x = {v.x, v.y, v.z, v.w}; __builtin_nontemporal_store(x, reinterpret_cast<u32x4*>(p)); }

#define D 128
#define KA_THREADS 1024
#define KA_ITERS 4
#define NBW_MAX 8192
#define KB_THREADS 256
#define KB_WAVES 4
#define NPW 5
#define RPL 4
#define CAP 240
#define CAPS 272
#define CAPR (CAP + 32)
#define RW 3
#define GI_MAX 2
#define AST (D + 4)
#define CBAR() asm volatile("" ::: "memory")
#define NLOG2E (-1.4426950408889634f)
#define NLN2 (-0.6931471805599453f)

__device__ __forceinline__ float bf2f(u16 v) { return __uint_as_float(((u32)v) << 16); }
__device__ __forceinline__ u16 f2bf(float f) {
    __hip_bfloat16 b = __float2bfloat16(f);
    return *reinterpret_cast<u16*>(&b);
}
__device__ __forceinline__ float silu_f(float x) {
    float e = __builtin_amdgcn_exp2f(x * NLOG2E);
    return x * __builtin_amdgcn_rcpf(1.0f + e);
}
__device__ __forceinline__ u32 pack2(float a, float b) {
    return (u32)f2bf(a) | ((u32)f2bf(b) << 16);
}
typedef __attribute__((ext_vector_type(2))) __bf16 bf16x2;
__device__ __forceinline__ u32 cvt_pk(f32x2 v) {
    bf16x2 p = __builtin_convertvector(v, bf16x2);
    return *reinterpret_cast<u32*>(&p);
}
__device__ __forceinline__ bf16x8 as_bf16x8(uint4 v) { return *reinterpret_cast<bf16x8*>(&v); }
__device__ __forceinline__ bf16x4 as_bf16x4(uint2 v) { return *reinterpret_cast<bf16x4*>(&v); }
template <int CTRL>
__device__ __forceinline__ float dpp_f(float x) {
    return __int_as_float(__builtin_amdgcn_update_dpp(0, __float_as_int(x), CTRL, 0xf, 0xf, true));
}
__device__ __forceinline__ float rsum16(float x) {
    x += dpp_f<0x128>(x);
    x += dpp_f<0x124>(x);
    x += dpp_f<0x122>(x);
    x += dpp_f<0x121>(x);
    return x;
}
__device__ __forceinline__ u32 mdiv(u32 x, unsigned long long m) {
    return (u32)(((unsigned long long)x * m) >> 40);
}
__device__ __forceinline__ int node_start(int b, int N, int NBW, unsigned long long mNBW) {
    return (int)mdiv((u32)b * (u32)N + (u32)NBW - 1u, mNBW);
}

__global__ void __launch_bounds__(KA_THREADS)
k_part(const u16* __restrict__ h, const u16* __restrict__ pos, const int* __restrict__ src, const int* __restrict__ dst,
       const u16* __restrict__ ea, const u16* __restrict__ we, const u16* __restrict__ wh,
       u32* __restrict__ rec_g, u16* __restrict__ tab, u16* __restrict__ ABU,
       u16* __restrict__ whT, uint4* __restrict__ w5g, uint2* __restrict__ pos8,
       unsigned long long mN, unsigned long long mNBW,
       int N, int E, int G, int CH, int NBW, int nGemm) {
    __shared__ __align__(16) u16 smem[(NBW_MAX + 8) * 2 + KA_THREADS * KA_ITERS + KA_THREADS * KA_ITERS * 8];
    __shared__ int wsum[KA_THREADS / 64];
    int t = threadIdx.x;
    int bid = blockIdx.x;
    if (bid < G) {
        int* hist = (int*)smem;
        for (int i = t; i < NBW; i += KA_THREADS) hist[i] = 0;
        int e0 = bid * CH;
        int e1 = min(E, e0 + CH);
        int sv[KA_ITERS], bv[KA_ITERS], rk[KA_ITERS], lo[KA_ITERS];
        uint2 eav[KA_ITERS];
#pragma unroll
        for (int i = 0; i < KA_ITERS; ++i) {
            int e = e0 + t + i * KA_THREADS;
            bv[i] = -1;
            if (e < e1) {
                int d = dst[e];
                sv[i] = src[e];
                eav[i] = *reinterpret_cast<const uint2*>(ea + (size_t)e * 4);
                int b = (int)mdiv((u32)d * (u32)NBW, mN);
                bv[i] = b;
                lo[i] = d - node_start(b, N, NBW, mNBW);
            }
        }
        __syncthreads();
#pragma unroll
        for (int i = 0; i < KA_ITERS; ++i)
            if (bv[i] >= 0) rk[i] = atomicAdd(&hist[bv[i]], 1);
        __syncthreads();
        const int per = (NBW + KA_THREADS - 1) / KA_THREADS;
        int b0 = t * per, b1 = min(NBW, b0 + per);
        int cnt = 0;
        for (int b = b0; b < b1; ++b) cnt += hist[b];
        int lane = t & 63, wv = t >> 6;
        int x = cnt;
#pragma unroll
        for (int o = 1; o < 64; o <<= 1) {
            int y = __shfl_up(x, o);
            if (lane >= o) x += y;
        }
        if (lane == 63) wsum[wv] = x;
        __syncthreads();
        int base = 0;
#pragma unroll
        for (int w = 0; w < KA_THREADS / 64; ++w) base += (w < wv) ? wsum[w] : 0;
        int off = base + x - cnt;
        u16* trow = tab + (size_t)bid * (NBW + 4);
        for (int b = b0; b < b1; ++b) {
            int c = hist[b];
            hist[b] = off;
            trow[b] = (u16)off;
            off += c;
        }
        if (t == KA_THREADS - 1) { trow[NBW] = (u16)(base + x); hist[NBW] = base + x; }
        __syncthreads();
        u16* ids = reinterpret_cast<u16*>(hist + NBW_MAX + 8);
        u32* lrec = reinterpret_cast<u32*>(ids + KA_THREADS * KA_ITERS);
#pragma unroll
        for (int i = 0; i < KA_ITERS; ++i)
            if (bv[i] >= 0) ids[hist[bv[i]] + rk[i]] = (u16)(t + i * KA_THREADS);
        __syncthreads();
        int cs0[KA_ITERS], cn[KA_ITERS], rnk[KA_ITERS];
        int mxn = 0;
#pragma unroll
        for (int i = 0; i < KA_ITERS; ++i) {
            cs0[i] = 0; cn[i] = 0; rnk[i] = 0;
            if (bv[i] >= 0) { cs0[i] = hist[bv[i]]; cn[i] = hist[bv[i] + 1] - cs0[i]; }
            mxn = max(mxn, cn[i]);
        }
#pragma clang loop unroll(disable) vectorize(disable) interleave(disable)
        for (int q = 0; q < mxn; ++q) {
#pragma unroll
            for (int i = 0; i < KA_ITERS; ++i) {
                int v = (int)ids[cs0[i] + (q < cn[i] ? q : 0)];
                rnk[i] += (q < cn[i] && v < t + i * KA_THREADS) ? 1 : 0;
            }
        }
#pragma unroll
        for (int i = 0; i < KA_ITERS; ++i) {
            if (bv[i] >= 0) {
                u32* q = lrec + (cs0[i] + rnk[i]) * RW;
                q[0] = ((u32)sv[i] << 3) | (u32)lo[i];
                q[1] = eav[i].x; q[2] = eav[i].y;
            }
        }
        __syncthreads();
        {
            const int CHB = (CH * RW + 3) >> 2;
            const int nq = ((e1 - e0) * RW + 3) >> 2;
            uint4* og = reinterpret_cast<uint4*>(rec_g) + (size_t)bid * CHB;
            const uint4* lq4 = reinterpret_cast<const uint4*>(lrec);
#pragma unroll
            for (int i = 0; i < KA_ITERS * RW / 4; ++i) {
                int p = t + i * KA_THREADS;
                if (p < nq) og[p] = lq4[p];
            }
        }
        return;
    }
    bid -= G;
    if (bid < nGemm) {
        {
            int i = bid * KA_THREADS + t;
            if (i < D * D) {
                int k = i >> 7, c = i & 127;
                whT[(size_t)c * D + k] = wh[D * D + i];
            }
            if (t < 128 && bid * 128 + t < N) {
                const u16* pp = pos + (size_t)(bid * 128 + t) * 3;
                pos8[bid * 128 + t] = make_uint2((u32)pp[0] | ((u32)pp[1] << 16), (u32)pp[2]);
            }
            if (bid == nGemm - 1 && t < 2 * D) {
                int gq = t >> 7, c = t & 127;
                u32 v[6];
#pragma unroll
                for (int j = 0; j < 6; ++j) {
                    int kr = (j == 5) ? 0 : j;
                    float w = bf2f(we[(size_t)(256 + kr) * D + c]) * NLOG2E;
                    u16 hi = f2bf(w);
                    u16 lo = f2bf(w - bf2f(hi));
                    v[j] = (u32)(gq ? lo : hi);
                }
                w5g[t] = make_uint4(v[0] | (v[1] << 16), v[2] | (v[3] << 16), v[4] | (v[5] << 16), 0u);
            }
        }
        {
            const int c2 = t & 63, kb = t >> 6;
            u32 wr[16];
#pragma unroll
            for (int j = 0; j < 16; ++j)
                wr[j] = *reinterpret_cast<const u32*>(we + (size_t)(kb * 16 + j) * D + c2 * 2);
#pragma unroll
            for (int sb = 0; sb < 2; ++sb) {
                int kk = kb * 16 + sb * 8;
                int half = kk >> 7, k = kk & 127;
                const u32* w = &wr[sb * 8];
                uint4 lo4 = make_uint4((w[0] & 0xffffu) | (w[1] << 16), (w[2] & 0xffffu) | (w[3] << 16),
                                       (w[4] & 0xffffu) | (w[5] << 16), (w[6] & 0xffffu) | (w[7] << 16));
                uint4 hi4 = make_uint4((w[0] >> 16) | (w[1] & 0xffff0000u), (w[2] >> 16) | (w[3] & 0xffff0000u),
                                       (w[4] >> 16) | (w[5] & 0xffff0000u), (w[6] >> 16) | (w[7] & 0xffff0000u));
                *reinterpret_cast<uint4*>(&smem[(half * D + c2 * 2) * (D + 8) + k]) = lo4;
                *reinterpret_cast<uint4*>(&smem[(half * D + c2 * 2 + 1) * (D + 8) + k]) = hi4;
            }
        }
        {
            const int c2 = t & 63, kb = t >> 6;
            u32 w[8];
#pragma unroll
            for (int j = 0; j < 8; ++j)
                w[j] = *reinterpret_cast<const u32*>(wh + (size_t)(kb * 8 + j) * D + c2 * 2);
            int k = kb * 8;
            uint4 lo4 = make_uint4((w[0] & 0xffffu) | (w[1] << 16), (w[2] & 0xffffu) | (w[3] << 16),
                                   (w[4] & 0xffffu) | (w[5] << 16), (w[6] & 0xffffu) | (w[7] << 16));
            uint4 hi4 = make_uint4((w[0] >> 16) | (w[1] & 0xffff0000u), (w[2] >> 16) | (w[3] & 0xffff0000u),
                                   (w[4] >> 16) | (w[5] & 0xffff0000u), (w[6] >> 16) | (w[7] & 0xffff0000u));
            *reinterpret_cast<uint4*>(&smem[(2 * D + c2 * 2) * (D + 8) + k]) = lo4;
            *reinterpret_cast<uint4*>(&smem[(2 * D + c2 * 2 + 1) * (D + 8) + k]) = hi4;
        }
        __syncthreads();
        int lane = t & 63, wv = t >> 6;
        int e16 = lane & 15, g = lane >> 4;
        int rt = wv & 7;
        int half = wv >> 3;
        int n = bid * 128 + rt * 16 + e16;
        int nn = n < N ? n : N - 1;
        bf16x8 af[4];
#pragma unroll
        for (int ks = 0; ks < 4; ++ks)
            af[ks] = *reinterpret_cast<const bf16x8*>(h + (size_t)nn * D + ks * 32 + g * 8);
        const int wr0 = (e16 >> 2) * 8 + (e16 & 3);
#pragma unroll
        for (int jj = 0; jj < 6; ++jj) {
            const int jb = half * 6 + jj;
            f32x4 acc0 = {0.f, 0.f, 0.f, 0.f}, acc1 = {0.f, 0.f, 0.f, 0.f};
#pragma unroll
            for (int ks = 0; ks < 4; ++ks) {
                const u16* wp = &smem[(jb * 32 + wr0) * (D + 8) + ks * 32 + g * 8];
                bf16x8 w0 = *reinterpret_cast<const bf16x8*>(wp);
                bf16x8 w1 = *reinterpret_cast<const bf16x8*>(wp + 4 * (D + 8));
                acc0 = __builtin_amdgcn_mfma_f32_16x16x32_bf16(w0, af[ks], acc0, 0, 0, 0);
                acc1 = __builtin_amdgcn_mfma_f32_16x16x32_bf16(w1, af[ks], acc1, 0, 0, 0);
            }
            const float sc = (jb < 8) ? NLOG2E : 1.0f;
            uint4 o = make_uint4(cvt_pk((f32x2){acc0[0] * sc, acc0[1] * sc}), cvt_pk((f32x2){acc0[2] * sc, acc0[3] * sc}),
                                 cvt_pk((f32x2){acc1[0] * sc, acc1[1] * sc}), cvt_pk((f32x2){acc1[2] * sc, acc1[3] * sc}));
            if (n < N) {
                u16* op = ABU + ((size_t)(jb >> 2) * N + n) * D + (jb & 3) * 32 + g * 8;
                if ((jb >> 2) == 1) *reinterpret_cast<uint4*>(op) = o;
                else st_nt(op, o);
            }
        }
        return;
    }
}

struct KbSmem {
    uint4 rec[KB_WAVES][CAPR];
    uint2 geo[KB_WAVES][CAPR];
    uint4 lut[8];
    uint4 At[KB_WAVES][D];
    uint4 w5s[2][D];
    float macc[KB_WAVES * NPW][AST];
    float pacc[KB_WAVES][NPW][4];
    float posl[KB_WAVES][NPW + 3][4];
    float wxs[D];
    uint2 M[KB_WAVES][16][4];
    int nt[KB_WAVES][4];
};

__global__ void __launch_bounds__(KB_THREADS, 3)
k_main(const u16* __restrict__ h, const u16* __restrict__ pos,
       const u16* __restrict__ wx,
       const u32* __restrict__ rec_g, const u16* __restrict__ tab,
       const u16* __restrict__ ABU,
       const u16* __restrict__ whT, const uint4* __restrict__ w5g, const uint2* __restrict__ pos8,
       u16* __restrict__ out_h, u16* __restrict__ out_pos,
       unsigned long long mNBW, int N, int G, int CH, int NBW) {
    __shared__ KbSmem S;
    const u16* __restrict__ Bg = ABU + (size_t)N * D;
    const u16* __restrict__ Ug = ABU + (size_t)2 * N * D;
    const int t = threadIdx.x, lane = t & 63;
    const int wv = __builtin_amdgcn_readfirstlane(t >> 6);
    const int e16 = lane & 15, g = lane >> 4;
    const int nwg = NBW / KB_WAVES, per8 = nwg >> 3;
    const int bx = ((int)blockIdx.x < per8 * 8) ? ((int)(blockIdx.x & 7) * per8 + (int)(blockIdx.x >> 3)) : (int)blockIdx.x;
    const int wb = bx * KB_WAVES + wv;
    const int N0 = node_start(bx * KB_WAVES, N, NBW, mNBW);
    const int N1 = node_start(bx * KB_WAVES + KB_WAVES, N, NBW, mNBW);
    const int n0 = node_start(wb, N, NBW, mNBW);
    const int nn = node_start(wb + 1, N, NBW, mNBW) - n0;
    const int rowb = n0 - N0;

    int so[GI_MAX], sc[GI_MAX];
    u16 (*tabs)[5] = reinterpret_cast<u16 (*)[5]>(&S.M[0][0][0]);
    auto fetch_tab = [&]() {
        if (t < G) {
            const u16* tp = tab + (size_t)t * (NBW + 4) + bx * KB_WAVES;
            uint2 v = *reinterpret_cast<const uint2*>(tp);
            u32 v4 = tp[4];
            tabs[t][0] = (u16)v.x; tabs[t][1] = (u16)(v.x >> 16);
            tabs[t][2] = (u16)v.y; tabs[t][3] = (u16)(v.y >> 16);
            tabs[t][4] = (u16)v4;
        }
    };
    auto load_tab = [&]() {
        int lo_ = lane;
        asm volatile("" : "+v"(lo_));
#pragma unroll
        for (int ci = 0; ci < GI_MAX; ++ci) {
            int c = lo_ + 64 * ci;
            so[ci] = 0; sc[ci] = 0;
            if (c < G) {
                so[ci] = tabs[c][wv];
                sc[ci] = tabs[c][wv + 1];
            }
        }
    };
    auto load_tab_g = [&]() {
        int lo_ = lane;
        asm volatile("" : "+v"(lo_));
#pragma unroll
        for (int ci = 0; ci < GI_MAX; ++ci) {
            int c = lo_ + 64 * ci;
            so[ci] = 0; sc[ci] = 0;
            if (c < G) {
                const u16* tp = tab + (size_t)c * (NBW + 4) + wb;
                so[ci] = tp[0];
                sc[ci] = tp[1];
            }
        }
    };
    fetch_tab();
    u32 a0[NPW];
#pragma unroll
    for (int j = 0; j < NPW; ++j) {
        a0[j] = 0u;
        if (j < nn) a0[j] = *reinterpret_cast<const u32*>(ABU + (size_t)(n0 + j) * D + lane * 2);
    }
    const uint4 w40 = w5g[lane], w41 = w5g[lane + 64], w42 = w5g[lane + 128], w43 = w5g[lane + 192];
    u16 wx0 = wx[lane], wx1 = wx[lane + 64];
    u16 pz16 = 0;
    {
        int n = lane >> 2, cpt = lane & 3;
        if (n < nn && cpt < 3) pz16 = pos[(n0 + n) * 3 + cpt];
    }
    const int NR = N1 - N0;
    uint4 hv4[2], uv4[2];
#pragma unroll
    for (int rt = 0; rt < 2; ++rt) {
        int lr = rt * 16 + e16;
        size_t o = (size_t)(N0 + (lr < NR ? lr : 0)) * D + wv * 32 + g * 8;
        hv4[rt] = *reinterpret_cast<const uint4*>(h + o);
        uv4[rt] = *reinterpret_cast<const uint4*>(Ug + o);
    }
    auto stage = [&]() {
        S.At[wv][lane * 2] = make_uint4((a0[0] & 0xffffu) | (a0[1] << 16), (a0[2] & 0xffffu) | (a0[3] << 16), a0[4] & 0xffffu, 0u);
        S.At[wv][lane * 2 + 1] = make_uint4((a0[0] >> 16) | (a0[1] & 0xffff0000u), (a0[2] >> 16) | (a0[3] & 0xffff0000u), a0[4] >> 16, 0u);
#pragma unroll
        for (int j = 0; j < NPW; ++j)
            if (j < nn) { S.macc[rowb + j][lane] = 0.f; S.macc[rowb + j][64 + lane] = 0.f; }
        uint4* wq = &S.w5s[0][0] + lane;
        wq[0] = w40; wq[64] = w41; wq[128] = w42; wq[192] = w43;
        S.wxs[lane] = bf2f(wx0);
        S.wxs[lane + 64] = bf2f(wx1);
        if (lane < (NPW + 3) * 4) S.posl[wv][lane >> 2][lane & 3] = bf2f(pz16);
        if (lane < NPW * 4) S.pacc[wv][lane >> 2][lane & 3] = 0.f;
        if (lane < 32) {
            const int n = lane >> 2, w = lane & 3;
            reinterpret_cast<u32*>(S.lut)[lane] = (n < 5 && (n >> 1) == w) ? (0x3F80u << (16 * (n & 1))) : 0u;
        }
    };
    const unsigned long long ltmask = (1ull << lane) - 1ull;
    const int CHB4 = ((CH * RW + 3) >> 2) << 2;
    uint4* rec = S.rec[wv];
    u32* key = reinterpret_cast<u32*>(S.rec[wv]);

    __syncthreads();
    load_tab();
    __syncthreads();
    f32x4 acc2[8];
    int dHead = 0, hflags = 0;
    float hm[8], hp0, hp1, hp2;
    auto reduce = [&]() __attribute__((always_inline)) {
        float wxc[8];
#pragma unroll
        for (int tt = 0; tt < 8; ++tt) wxc[tt] = S.wxs[tt * 16 + e16];
        float pv0 = 0.f, pv1 = 0.f, pv2 = 0.f;
#pragma unroll
        for (int tt = 0; tt < 8; ++tt) {
            hm[tt] = acc2[tt][0] * NLN2;
            pv0 += acc2[tt][1] * wxc[tt];
            pv1 += acc2[tt][2] * wxc[tt];
            pv2 += acc2[tt][3] * wxc[tt];
            acc2[tt] = (f32x4){0.f, 0.f, 0.f, 0.f};
        }
        hp0 = rsum16(pv0) * NLN2; hp1 = rsum16(pv1) * NLN2; hp2 = rsum16(pv2) * NLN2;
    };
    auto commit = [&](int d, auto accc) __attribute__((always_inline)) {
        constexpr bool ACC = decltype(accc)::value;
        const int bk = (d >> 16) & 3;
        const int nl = ((d >> 18) & 1) * 4 + g;
        const int rb = (d >> 20) & 31, nb = (d >> 25) & 7;
        const bool on = nl < nb;
        float* mrow = &S.macc[rb + (on ? nl : 0)][e16];
        float* prow = &S.pacc[bk][on ? nl : 0][0];
        float old[8], po[3];
        if (ACC) {
#pragma unroll
            for (int tt = 0; tt < 8; ++tt) old[tt] = mrow[tt * 16];
            po[0] = prow[0]; po[1] = prow[1]; po[2] = prow[2];
            CBAR();
        }
        if (on) {
#pragma unroll
            for (int tt = 0; tt < 8; ++tt) mrow[tt * 16] = ACC ? old[tt] + hm[tt] : hm[tt];
            if (e16 == 0) {
                prow[0] = (ACC ? po[0] : 0.f) + hp0;
                prow[1] = (ACC ? po[1] : 0.f) + hp1;
                prow[2] = (ACC ? po[2] : 0.f) + hp2;
            }
        }
    };
    auto flush = [&](int d, auto accc) __attribute__((always_inline)) { reduce(); commit(d, accc); };
    auto finish = [&]() __attribute__((always_inline)) {
        const std::integral_constant<bool, true> accc;
        __syncthreads();
        for (int w = 1; w < KB_WAVES; ++w) {
            if ((hflags & 1) && (wv == w || !(hflags & 2))) commit(dHead, accc);
            __syncthreads();
            if (!(hflags & 2)) break;
        }
    };
    auto round = [&](const int base, auto firstc) __attribute__((always_inline)) -> bool {
        constexpr bool FIRST = decltype(firstc)::value;
        int T;
        int fo[GI_MAX];
        {
            int tot = 0;
#pragma unroll
            for (int ci = 0; ci < GI_MAX; ++ci) { sc[ci] -= so[ci]; tot += sc[ci]; }
            int x = tot;
            int ls = lane;
            asm volatile("" : "+v"(ls));
#pragma unroll
            for (int o = 1; o < 64; o <<= 1) {
                int y = __builtin_amdgcn_ds_bpermute((ls - o) << 2, x);
                if (ls >= o) x += y;
            }
            T = __builtin_amdgcn_readlane(x, 63);
            int off = x - tot;
#pragma unroll
            for (int ci = 0; ci < GI_MAX; ++ci) { fo[ci] = off; off += sc[ci]; }
        }
        const int Tr = max(0, min(CAP, T - base));
        int lo2 = lane;
        asm volatile("" : "+v"(lo2));
#pragma unroll
        for (int ci = 0; ci < GI_MAX; ++ci) {
            int gs = (lo2 + 64 * ci) * CHB4 + so[ci] * RW;
            for (int q = 0; q < sc[ci]; ++q) {
                int fp = fo[ci] + q - base;
                if (fp >= 0 && fp < CAP) key[fp] = (u32)(gs + q * RW);
            }
        }
        CBAR();
        uint4 r[RPL];
        uint2 ps[RPL];
#pragma unroll
        for (int i = 0; i < RPL; ++i) {
            int p = lane + 64 * i;
            r[i] = make_uint4(0xffffffffu, 0u, 0u, 0u);
            if (p < Tr) {
                const u32* rp = rec_g + key[p];
                r[i] = make_uint4(rp[0], rp[1], rp[2], 0u);
            }
        }
        if (FIRST) stage();
#pragma unroll
        for (int i = 0; i < RPL; ++i) {
            ps[i] = make_uint2(0u, 0u);
            if (r[i].x != 0xffffffffu) ps[i] = pos8[r[i].x >> 3];
        }
        int posn[RPL];
        int cst = 0, cnt4 = 0, c0 = 0, g1s = 0;
#pragma unroll
        for (int i = 0; i < RPL; ++i) posn[i] = 0;
#pragma unroll
        for (int j = 0; j < NPW; ++j) {
            if (j == 4) { c0 = cst; g1s = (cst + 15) & ~15; cst = g1s; }
            int c = 0;
#pragma unroll
            for (int i = 0; i < RPL; ++i) {
                bool mt = (r[i].x != 0xffffffffu) && ((int)(r[i].x & 7u) == j);
                unsigned long long m = __ballot(mt);
                if (mt) posn[i] = cst + c + __popcll(m & ltmask);
                c += __popcll(m);
            }
            if (j == 4) cnt4 = c;
            cst += c;
        }
        const int g1e = (g1s + cnt4 + 15) & ~15;
        CBAR();
#pragma unroll
        for (int i = 0; i < RPL; ++i)
            if (r[i].x != 0xffffffffu) {
                const float4 pn = *reinterpret_cast<const float4*>(&S.posl[wv][r[i].x & 7u][0]);
                float dx = pn.x - __uint_as_float(ps[i].x << 16), dy = pn.y - __uint_as_float(ps[i].x & 0xffff0000u);
                float dz = pn.z - __uint_as_float(ps[i].y << 16);
                float d2v = dx * dx + dy * dy + dz * dz;
                u16 d2h = f2bf(d2v);
                u16 d2l = f2bf(d2v - bf2f(d2h));
                rec[posn[i]] = make_uint4((u32)d2h | (r[i].y << 16), (r[i].y >> 16) | (r[i].z << 16), (r[i].z >> 16) | ((u32)d2l << 16), r[i].x);
                S.geo[wv][posn[i]] = make_uint2(0x3F80u | ((u32)f2bf(dx) << 16), pack2(dy, dz));
            }
        if (lane < 16) {
            if (c0 + lane < g1s) { rec[c0 + lane] = make_uint4(0u, 0u, 0u, 7u); S.geo[wv][c0 + lane] = make_uint2(0u, 0u); }
            if (g1s + cnt4 + lane < g1e) { rec[g1s + cnt4 + lane] = make_uint4(0u, 0u, 0u, 7u); S.geo[wv][g1s + cnt4 + lane] = make_uint2(0u, 0u); }
        }
        if (lane == 0) {
            S.nt[wv][0] = g1s >> 4;
            S.nt[wv][1] = g1e >> 4;
            S.nt[wv][2] = (base + CAP < T) ? 1 : 0;
        }
        __syncthreads();

        int sb1, sb2, sb3, sb4, sb5, sb6, sb7, TT, anymore;
        {
            const int4 q0 = *reinterpret_cast<const int4*>(&S.nt[0][0]);
            const int4 q1 = *reinterpret_cast<const int4*>(&S.nt[1][0]);
            const int4 q2 = *reinterpret_cast<const int4*>(&S.nt[2][0]);
            const int4 q3 = *reinterpret_cast<const int4*>(&S.nt[3][0]);
            sb1 = __builtin_amdgcn_readfirstlane(q0.x);
            sb2 = __builtin_amdgcn_readfirstlane(q0.y);
            sb3 = sb2 + __builtin_amdgcn_readfirstlane(q1.x);
            sb4 = sb2 + __builtin_amdgcn_readfirstlane(q1.y);
            sb5 = sb4 + __builtin_amdgcn_readfirstlane(q2.x);
            sb6 = sb4 + __builtin_amdgcn_readfirstlane(q2.y);
            sb7 = sb6 + __builtin_amdgcn_readfirstlane(q3.x);
            TT = sb6 + __builtin_amdgcn_readfirstlane(q3.y);
            anymore = __builtin_amdgcn_readfirstlane(q0.z | q1.z | q2.z | q3.z);
        }
        auto seg_of = [&](int x) __attribute__((always_inline)) {
            return (x >= sb1) + (x >= sb2) + (x >= sb3) + (x >= sb4) + (x >= sb5) + (x >= sb6) + (x >= sb7);
        };
        auto is_start = [&](int x) __attribute__((always_inline)) {
            return (x == 0) | (x == sb1) | (x == sb2) | (x == sb3) | (x == sb4) | (x == sb5) | (x == sb6) | (x == sb7);
        };
        auto seg_end = [&](int x) __attribute__((always_inline)) {
            int r = TT;
            r = (x < sb7) ? +sb7 : +r; r = (x < sb6) ? +sb6 : +r; r = (x < sb5) ? +sb5 : +r; r = (x < sb4) ? +sb4 : +r;
            r = (x < sb3) ? +sb3 : +r; r = (x < sb2) ? +sb2 : +r; r = (x < sb1) ? +sb1 : +r;
            return r;
        };
        const int ts = (TT * wv) >> 2, te = (TT * (wv + 1)) >> 2;
        const int nst = te - ts;
        const bool has_head = (nst > 0) && !is_start(ts);
        const int th = has_head ? min(te, seg_end(ts)) : +ts;
        bool serial = false;
        {
            const int s1 = TT >> 2, s2 = (TT * 2) >> 2, s3 = (TT * 3) >> 2;
            serial = (!is_start(s1) && seg_end(s1) > s2) || (!is_start(s2) && seg_end(s2) > s3);
        }
        int lq = lane;
        asm volatile("" : "+v"(lq));
        int descv;
        {
            const int ns1 = node_start(bx * KB_WAVES + 1, N, NBW, mNBW) - N0;
            const int ns2 = node_start(bx * KB_WAVES + 2, N, NBW, mNBW) - N0;
            const int ns3 = node_start(bx * KB_WAVES + 3, N, NBW, mNBW) - N0;
            const int ns4 = N1 - N0;
            const int nh = te - th;
            const int tl = (lq < nh) ? (th + lq) : (ts + lq - nh);
            const int k = seg_of(tl);
            const int b = k >> 1;
            const int cb = (b == 0) ? 0 : (b == 1) ? +sb2 : (b == 2) ? +sb4 : +sb6;
            const int r0 = (b == 0) ? 0 : (b == 1) ? +ns1 : (b == 2) ? +ns2 : +ns3;
            const int r1 = (b == 0) ? +ns1 : (b == 1) ? +ns2 : (b == 2) ? +ns3 : +ns4;
            const int fl = (lq < nh) && ((tl + 1 == te) || is_start(tl + 1));
            descv = ((b * CAPR + (tl - cb) * 16) * 16) | (b << 16) | ((k & 1) << 18) | (fl << 19) | (r0 << 20) | ((r1 - r0) << 25);
        }
        bf16x8 selLo, selHi;
        {
            int dl = (lq & 15) - 8 * (lq >> 4);
            unsigned long long one = 0x3F80ull;
            u32 l0 = 0, l1 = 0, l2 = 0, l3 = 0, h0 = 0, h1 = 0, h2 = 0, h3 = 0;
            if (dl >= 0 && dl < 4) { unsigned long long v = one << (16 * dl); l0 = (u32)v; l1 = (u32)(v >> 32); }
            if (dl >= 4 && dl < 8) { unsigned long long v = one << (16 * (dl - 4)); l2 = (u32)v; l3 = (u32)(v >> 32); }
            int dh = dl + 16;
            if (dh >= 0 && dh < 4) { unsigned long long v = one << (16 * dh); h0 = (u32)v; h1 = (u32)(v >> 32); }
            if (dh >= 4 && dh < 8) { unsigned long long v = one << (16 * (dh - 4)); h2 = (u32)v; h3 = (u32)(v >> 32); }
            selLo = as_bf16x8(make_uint4(l0, l1, l2, l3));
            selHi = as_bf16x8(make_uint4(h0, h1, h2, h3));
        }
        const char* w5b = (g < 2) ? reinterpret_cast<const char*>(&S.w5s[g][e16]) : reinterpret_cast<const char*>(&S.At[0][e16]);
        const int w5m = (g < 2) ? 0 : (int)sizeof(S.At[0]);
        const char* recb = reinterpret_cast<const char*>(&S.rec[0][e16]);
        const char* geob = reinterpret_cast<const char*>(&S.geo[0][e16]);
        const char* Bb = reinterpret_cast<const char*>(Bg);
        const u32 g16 = (u32)g * 16u;
        const u32 ndor = (g == 3) ? 7u : 0u;
        const u32 wmask = (g < 2) ? 0u : 0xffffffffu;
        bf16x8 gA[4], gB[4];
        u32 pA0 = 0, pB0 = 0;
        auto issue = [&](int d, bf16x8 (&gat)[4], u32& q0) __attribute__((always_inline)) {
            q0 = reinterpret_cast<const uint4*>(recb + (d & 0xffff))->w;
            const u32 off = ((q0 >> 3) << 8) + g16;
#pragma unroll
            for (int tp = 0; tp < 4; ++tp)
                gat[tp] = *reinterpret_cast<const bf16x8*>(Bb + off + tp * 64);
        };
#pragma unroll
        for (int tt = 0; tt < 8; ++tt) acc2[tt] = (f32x4){0.f, 0.f, 0.f, 0.f};
        auto compute = [&](int d, bf16x8 (&gat)[4], u32 q0) __attribute__((always_inline)) {
            const int bk = (d >> 16) & 3;
            const u32 nd = q0 & 7u;
            const char* xp = (g < 2) ? (recb + (d & 0xffff)) : (reinterpret_cast<const char*>(S.lut) + ((nd | ndor) << 4));
            uint4 xu = *reinterpret_cast<const uint4*>(xp);
            const uint2 gq = *reinterpret_cast<const uint2*>(geob + ((d & 0xffff) >> 1));
            const uint4* w5p = reinterpret_cast<const uint4*>(w5b + w5m * bk);
            S.M[wv][e16][g] = ((nd & 3u) == (u32)g) ? gq : make_uint2(0u, 0u);
            xu.w &= wmask;
            const bf16x8 x5 = as_bf16x8(xu);
            asm volatile("s_waitcnt lgkmcnt(0)" ::: "memory");
            u32 sw01, sw23;
            {
                const u16* wp = reinterpret_cast<const u16*>(&S.M[wv][g * 4][0]) + e16;
                sw01 = (u32)wp[0] | ((u32)wp[16] << 16);
                sw23 = (u32)wp[32] | ((u32)wp[48] << 16);
            }
            const bf16x4 sop = as_bf16x4(make_uint2(sw01, sw23));
#pragma unroll
            for (int tp = 0; tp < 4; ++tp) {
#pragma unroll
                for (int u = 0; u < 2; ++u) {
                    const int tt = tp * 2 + u;
                    bf16x8 w5 = as_bf16x8(w5p[tt * 16]);
                    f32x4 acc = __builtin_amdgcn_mfma_f32_16x16x32_bf16(x5, w5, (f32x4){0.f, 0.f, 0.f, 0.f}, 0, 0, 0);
                    acc = __builtin_amdgcn_mfma_f32_16x16x32_bf16(gat[tp], u ? selHi : selLo, acc, 0, 0, 0);
                    f32x2 t01 = {acc[0], acc[1]}, t23 = {acc[2], acc[3]};
                    f32x2 e01 = {__builtin_amdgcn_exp2f(acc[0]), __builtin_amdgcn_exp2f(acc[1])};
                    f32x2 e23 = {__builtin_amdgcn_exp2f(acc[2]), __builtin_amdgcn_exp2f(acc[3])};
                    e01 = e01 + (f32x2){1.f, 1.f};
                    e23 = e23 + (f32x2){1.f, 1.f};
                    f32x2 r01 = {__builtin_amdgcn_rcpf(e01[0]), __builtin_amdgcn_rcpf(e01[1])};
                    f32x2 r23 = {__builtin_amdgcn_rcpf(e23[0]), __builtin_amdgcn_rcpf(e23[1])};
                    f32x2 m01 = t01 * r01, m23 = t23 * r23;
                    u32 k01 = cvt_pk(m01), k23 = cvt_pk(m23);
                    bf16x4 mb = as_bf16x4(make_uint2(k01, k23));
                    acc2[tt] = __builtin_amdgcn_mfma_f32_16x16x16bf16_1k(sop, mb, acc2[tt], 0, 0, 0);
                }
            }
        };
        const std::integral_constant<bool, !FIRST> dirc;
        if (nst > 0) {
            issue(__builtin_amdgcn_readlane(descv, 0), gA, pA0);
            int i = 0;
            for (; i + 1 < nst; i += 2) {
                {
                    const int rem = nst - i;
                    if (rem >= 5) __builtin_amdgcn_s_setprio(3);
                    else if (rem >= 4) __builtin_amdgcn_s_setprio(2);
                    else if (rem >= 3) __builtin_amdgcn_s_setprio(1);
                    else __builtin_amdgcn_s_setprio(0);
                }
                const int dA = __builtin_amdgcn_readlane(descv, i);
                const int dB = __builtin_amdgcn_readlane(descv, i + 1);
                const int dC = __builtin_amdgcn_readlane(descv, min(i + 2, nst - 1));
                issue(dB, gB, pB0);
                compute(dA, gA, pA0);
                if (dA & (1 << 19)) flush(dA, dirc);
                issue(dC, gA, pA0);
                compute(dB, gB, pB0);
                if (dB & (1 << 19)) flush(dB, dirc);
            }
            if (i < nst) {
                const int dA = __builtin_amdgcn_readlane(descv, i);
                compute(dA, gA, pA0);
                if (dA & (1 << 19)) flush(dA, dirc);
            }
        }
        dHead = __builtin_amdgcn_readlane(descv, max(nst - 1, 0));
        hflags = (has_head ? 1 : 0) | (serial ? 2 : 0);
        reduce();
        return anymore != 0;
    };
    if (round(0, std::integral_constant<bool, true>())) {
        for (int base = CAP;; base += CAP) {
            finish();
            load_tab_g();
            if (!round(base, std::integral_constant<bool, false>())) break;
        }
    }

    int le = lane;
    asm volatile("" : "+v"(le));
    const int f16 = le & 15, gg = le >> 4;
    bf16x8 bw[2][4];
#pragma unroll
    for (int q = 0; q < 2; ++q)
#pragma unroll
        for (int ks = 0; ks < 4; ++ks)
            bw[q][ks] = *reinterpret_cast<const bf16x8*>(whT + (size_t)(wv * 32 + (f16 >> 2) * 8 + q * 4 + (f16 & 3)) * D + ks * 32 + gg * 8);
    finish();
    if (le < nn * 3) {
        int n = le / 3, cpt = le - n * 3;
        out_pos[(size_t)(n0 + n) * 3 + cpt] = f2bf(S.posl[wv][n][cpt] + S.pacc[wv][n][cpt]);
    }
#pragma unroll
    for (int rt = 0; rt < 2; ++rt) {
        if (rt * 16 < NR) {
            bf16x8 af[4];
            int lr = rt * 16 + f16;
#pragma unroll
            for (int ks = 0; ks < 4; ++ks) {
                const float* mp = &S.macc[lr < NR ? lr : 0][ks * 32 + gg * 8];
                af[ks] = as_bf16x8(make_uint4(pack2(mp[0], mp[1]), pack2(mp[2], mp[3]),
                                              pack2(mp[4], mp[5]), pack2(mp[6], mp[7])));
            }
            u32 ov[4];
#pragma unroll
            for (int q = 0; q < 2; ++q) {
                const u32 u0 = q ? uv4[rt].z : uv4[rt].x, u1 = q ? uv4[rt].w : uv4[rt].y;
                const u32 h0 = q ? hv4[rt].z : hv4[rt].x, h1 = q ? hv4[rt].w : hv4[rt].y;
                f32x4 acc = {__uint_as_float(u0 << 16), __uint_as_float(u0 & 0xffff0000u),
                             __uint_as_float(u1 << 16), __uint_as_float(u1 & 0xffff0000u)};
#pragma unroll
                for (int ks = 0; ks < 4; ++ks)
                    acc = __builtin_amdgcn_mfma_f32_16x16x32_bf16(bw[q][ks], af[ks], acc, 0, 0, 0);
                ov[q * 2] = pack2(__uint_as_float(h0 << 16) + silu_f(acc[0]), __uint_as_float(h0 & 0xffff0000u) + silu_f(acc[1]));
                ov[q * 2 + 1] = pack2(__uint_as_float(h1 << 16) + silu_f(acc[2]), __uint_as_float(h1 & 0xffff0000u) + silu_f(acc[3]));
            }
            if (lr < NR)
                *reinterpret_cast<uint4*>(out_h + (size_t)(N0 + lr) * D + wv * 32 + gg * 8) = make_uint4(ov[0], ov[1], ov[2], ov[3]);
        }
    }
}

extern "C" void kernel_launch(void* const* d_in, const int* in_sizes, int n_in,
                              void* d_out, int out_size, void* d_ws, size_t ws_size,
                              hipStream_t stream) {
    const u16* h   = (const u16*)d_in[0];
    const u16* pos = (const u16*)d_in[1];
    const u16* ea  = (const u16*)d_in[2];
    const int* src = (const int*)d_in[3];
    const int* dst = (const int*)d_in[4];
    const u16* we  = (const u16*)d_in[5];
    const u16* wx  = (const u16*)d_in[6];
    const u16* wh  = (const u16*)d_in[7];
    int N = in_sizes[0] / D;
    int E = in_sizes[3];
    u16* out_h = (u16*)d_out;
    u16* out_pos = out_h + (size_t)N * D;

    int G = 128;
    while ((E + G - 1) / G > KA_THREADS * KA_ITERS && G < 64 * GI_MAX) G += 64;
    int CH = (E + G - 1) / G;
    int NBW = 3072;
    while (NBW * NPW < N && NBW < NBW_MAX) NBW += 4;
    int nGemm = (N + 127) / 128;
    if (nGemm < D * D / KA_THREADS) nGemm = D * D / KA_THREADS;
    unsigned long long mN = (1ull << 40) / (unsigned long long)N + 1ull;
    unsigned long long mNBW = (1ull << 40) / (unsigned long long)NBW + 1ull;

    char* ws = (char*)d_ws;
    size_t off = 0;
    auto take = [&](size_t bytes) { char* p = ws + off; off += (bytes + 255) & ~(size_t)255; return p; };
    u32* rec_g = (u32*)take((size_t)G * ((CH * RW + 3) >> 2) * 16 + 64);
    u16* tab = (u16*)take((size_t)G * (NBW + 4) * 2);
    u16* ABU = (u16*)take((size_t)3 * N * D * 2);
    u16* whT = (u16*)take((size_t)D * D * 2);
    uint4* w5g = (uint4*)take((size_t)256 * 16);
    uint2* pos8 = (uint2*)take((size_t)N * 8);

    k_part<<<G + nGemm, KA_THREADS, 0, stream>>>(h, pos, src, dst, ea, we, wh, rec_g, tab, ABU, whT, w5g, pos8,
                                                      mN, mNBW, N, E, G, CH, NBW, nGemm);
    k_main<<<NBW / KB_WAVES, KB_THREADS, 0, stream>>>(h, pos, wx, rec_g, tab, ABU, whT, w5g, pos8,
                                                     out_h, out_pos, mNBW, N, G, CH, NBW);
}
```

```cpp
#include <hip/hip_runtime.h>
#include <hip/hip_bf16.h>
#include <stdint.h>
#include <type_traits>


typedef unsigned short u16;
typedef unsigned int u32;
typedef __attribute__((ext_vector_type(8))) short bf16x8;
typedef __attribute__((ext_vector_type(4))) float f32x4;
typedef __attribute__((ext_vector_type(4))) short bf16x4;
typedef __attribute__((ext_vector_type(2))) float f32x2;
typedef __attribute__((ext_vector_type(4))) unsigned int u32x4;
__device__ __forceinline__ void st_nt(void* p, uint4 v) { u32x4 x = {v.x, v.y, v.z, v.w}; __builtin_nontemporal_store(x, reinterpret_cast<u32x4*>(p)); }

#define D 128
#define KA_THREADS 1024
#define KA_ITERS 4
#define NBW_MAX 8192
#define KB_THREADS 256
#define KB_WAVES 4
#define NPW 5
#define RPL 4
#define CAP 240
#define CAPS 272
#define CAPR (CAP + 32)
#define RW 3
#define GI_MAX 2
#define AST (D + 4)
#define CBAR() asm volatile("" ::: "memory")
#define NLOG2E (-1.4426950408889634f)
#define NLN2 (-0.6931471805599453f)

__device__ __forceinline__ float bf2f(u16 v) { return __uint_as_float(((u32)v) << 16); }
__device__ __forceinline__ u16 f2bf(float f) {
    __hip_bfloat16 b = __float2bfloat16(f);
    return *reinterpret_cast<u16*>(&b);
}
__device__ __forceinline__ float silu_f(float x) {
    float e = __builtin_amdgcn_exp2f(x * NLOG2E);
    return x * __builtin_amdgcn_rcpf(1.0f + e);
}
__device__ __forceinline__ u32 pack2(float a, float b) {
    return (u32)f2bf(a) | ((u32)f2bf(b) << 16);
}
typedef __attribute__((ext_vector_type(2))) __bf16 bf16x2;
__device__ __forceinline__ u32 cvt_pk(f32x2 v) {
    bf16x2 p = __builtin_convertvector(v, bf16x2);
    return *reinterpret_cast<u32*>(&p);
}
__device__ __forceinline__ bf16x8 as_bf16x8(uint4 v) { return *reinterpret_cast<bf16x8*>(&v); }
__device__ __forceinline__ bf16x4 as_bf16x4(uint2 v) { return *reinterpret_cast<bf16x4*>(&v); }
template <int CTRL>
__device__ __forceinline__ float dpp_f(float x) {
    return __int_as_float(__builtin_amdgcn_update_dpp(0, __float_as_int(x), CTRL, 0xf, 0xf, true));
}
__device__ __forceinline__ float rsum16(float x) {
    x += dpp_f<0x128>(x);
    x += dpp_f<0x124>(x);
    x += dpp_f<0x122>(x);
    x += dpp_f<0x121>(x);
    return x;
}
__device__ __forceinline__ u32 mdiv(u32 x, unsigned long long m) {
    return (u32)(((unsigned long long)x * m) >> 40);
}
__device__ __forceinline__ int node_start(int b, int N, int NBW, unsigned long long mNBW) {
    return (int)mdiv((u32)b * (u32)N + (u32)NBW - 1u, mNBW);
}

__global__ void __launch_bounds__(KA_THREADS)
k_part(const u16* __restrict__ h, const u16* __restrict__ pos, const int* __restrict__ src, const int* __restrict__ dst,
       const u16* __restrict__ ea, const u16* __restrict__ we, const u16* __restrict__ wh,
       u32* __restrict__ rec_g, u16* __restrict__ tab, u16* __restrict__ ABU,
       u16* __restrict__ whT, uint4* __restrict__ w5g, uint2* __restrict__ pos8,
       unsigned long long mN, unsigned long long mNBW,
       int N, int E, int G, int CH, int NBW, int nGemm) {
    __shared__ __align__(16) u16 smem[(NBW_MAX + 8) * 2 + KA_THREADS * KA_ITERS + KA_THREADS * KA_ITERS * 8];
    __shared__ int wsum[KA_THREADS / 64];
    int t = threadIdx.x;
    int bid = blockIdx.x;
    if (bid < G) {
        int* hist = (int*)smem;
        for (int i = t; i < NBW; i += KA_THREADS) hist[i] = 0;
        int e0 = bid * CH;
        int e1 = min(E, e0 + CH);
        int sv[KA_ITERS], bv[KA_ITERS], rk[KA_ITERS], lo[KA_ITERS];
        uint2 eav[KA_ITERS];
#pragma unroll
        for (int i = 0; i < KA_ITERS; ++i) {
            int e = e0 + t + i * KA_THREADS;
            bv[i] = -1;
            if (e < e1) {
                int d = dst[e];
                sv[i] = src[e];
                eav[i] = *reinterpret_cast<const uint2*>(ea + (size_t)e * 4);
                int b = (int)mdiv((u32)d * (u32)NBW, mN);
                bv[i] = b;
                lo[i] = d - node_start(b, N, NBW, mNBW);
            }
        }
        __syncthreads();
#pragma unroll
        for (int i = 0; i < KA_ITERS; ++i)
            if (bv[i] >= 0) rk[i] = atomicAdd(&hist[bv[i]], 1);
        __syncthreads();
        const int per = (NBW + KA_THREADS - 1) / KA_THREADS;
        int b0 = t * per, b1 = min(NBW, b0 + per);
        int cnt = 0;
        for (int b = b0; b < b1; ++b) cnt += hist[b];
        int lane = t & 63, wv = t >> 6;
        int x = cnt;
#pragma unroll
        for (int o = 1; o < 64; o <<= 1) {
            int y = __shfl_up(x, o);
            if (lane >= o) x += y;
        }
        if (lane == 63) wsum[wv] = x;
        __syncthreads();
        int base = 0;
#pragma unroll
        for (int w = 0; w < KA_THREADS / 64; ++w) base += (w < wv) ? wsum[w] : 0;
        int off = base + x - cnt;
        u16* trow = tab + (size_t)bid * (NBW + 4);
        for (int b = b0; b < b1; ++b) {
            int c = hist[b];
            hist[b] = off;
            trow[b] = (u16)off;
            off += c;
        }
        if (t == KA_THREADS - 1) { trow[NBW] = (u16)(base + x); hist[NBW] = base + x; }
        __syncthreads();
        u16* ids = reinterpret_cast<u16*>(hist + NBW_MAX + 8);
        u32* lrec = reinterpret_cast<u32*>(ids + KA_THREADS * KA_ITERS);
#pragma unroll
        for (int i = 0; i < KA_ITERS; ++i)
            if (bv[i] >= 0) ids[hist[bv[i]] + rk[i]] = (u16)(t + i * KA_THREADS);
        __syncthreads();
        int cs0[KA_ITERS], cn[KA_ITERS], rnk[KA_ITERS];
        int mxn = 0;
#pragma unroll
        for (int i = 0; i < KA_ITERS; ++i) {
            cs0[i] = 0; cn[i] = 0; rnk[i] = 0;
            if (bv[i] >= 0) { cs0[i] = hist[bv[i]]; cn[i] = hist[bv[i] + 1] - cs0[i]; }
            mxn = max(mxn, cn[i]);
        }
#pragma clang loop unroll(disable) vectorize(disable) interleave(disable)
        for (int q = 0; q < mxn; ++q) {
#pragma unroll
            for (int i = 0; i < KA_ITERS; ++i) {
                int v = (int)ids[cs0[i] + (q < cn[i] ? q : 0)];
                rnk[i] += (q < cn[i] && v < t + i * KA_THREADS) ? 1 : 0;
            }
        }
#pragma unroll
        for (int i = 0; i < KA_ITERS; ++i) {
            if (bv[i] >= 0) {
                u32* q = lrec + (cs0[i] + rnk[i]) * RW;
                q[0] = ((u32)sv[i] << 3) | (u32)lo[i];
                q[1] = eav[i].x; q[2] = eav[i].y;
            }
        }
        __syncthreads();
        {
            const int CHB = (CH * RW + 3) >> 2;
            const int nq = ((e1 - e0) * RW + 3) >> 2;
            uint4* og = reinterpret_cast<uint4*>(rec_g) + (size_t)bid * CHB;
            const uint4* lq4 = reinterpret_cast<const uint4*>(lrec);
#pragma unroll
            for (int i = 0; i < KA_ITERS * RW / 4; ++i) {
                int p = t + i * KA_THREADS;
                if (p < nq) og[p] = lq4[p];
            }
        }
        return;
    }
    bid -= G;
    if (bid < nGemm) {
        {
            int i = bid * KA_THREADS + t;
            if (i < D * D) {
                int k = i >> 7, c = i & 127;
                whT[(size_t)c * D + k] = wh[D * D + i];
            }
            if (t < 128 && bid * 128 + t < N) {
                const u16* pp = pos + (size_t)(bid * 128 + t) * 3;
                pos8[bid * 128 + t] = make_uint2((u32)pp[0] | ((u32)pp[1] << 16), (u32)pp[2]);
            }
            if (bid == nGemm - 1 && t < 2 * D) {
                int gq = t >> 7, c = t & 127;
                u32 v[6];
#pragma unroll
                for (int j = 0; j < 6; ++j) {
                    int kr = (j == 5) ? 0 : j;
                    float w = bf2f(we[(size_t)(256 + kr) * D + c]) * NLOG2E;
                    u16 hi = f2bf(w);
                    u16 lo = f2bf(w - bf2f(hi));
                    v[j] = (u32)(gq ? lo : hi);
                }
                w5g[t] = make_uint4(v[0] | (v[1] << 16), v[2] | (v[3] << 16), v[4] | (v[5] << 16), 0u);
            }
        }
        {
            const int c2 = t & 63, kb = t >> 6;
            u32 wr[16];
#pragma unroll
            for (int j = 0; j < 16; ++j)
                wr[j] = *reinterpret_cast<const u32*>(we + (size_t)(kb * 16 + j) * D + c2 * 2);
#pragma unroll
            for (int sb = 0; sb < 2; ++sb) {
                int kk = kb * 16 + sb * 8;
                int half = kk >> 7, k = kk & 127;
                const u32* w = &wr[sb * 8];
                uint4 lo4 = make_uint4((w[0] & 0xffffu) | (w[1] << 16), (w[2] & 0xffffu) | (w[3] << 16),
                                       (w[4] & 0xffffu) | (w[5] << 16), (w[6] & 0xffffu) | (w[7] << 16));
                uint4 hi4 = make_uint4((w[0] >> 16) | (w[1] & 0xffff0000u), (w[2] >> 16) | (w[3] & 0xffff0000u),
                                       (w[4] >> 16) | (w[5] & 0xffff0000u), (w[6] >> 16) | (w[7] & 0xffff0000u));
                *reinterpret_cast<uint4*>(&smem[(half * D + c2 * 2) * (D + 8) + k]) = lo4;
                *reinterpret_cast<uint4*>(&smem[(half * D + c2 * 2 + 1) * (D + 8) + k]) = hi4;
            }
        }
        {
            const int c2 = t & 63, kb = t >> 6;
            u32 w[8];
#pragma unroll
            for (int j = 0; j < 8; ++j)
                w[j] = *reinterpret_cast<const u32*>(wh + (size_t)(kb * 8 + j) * D + c2 * 2);
            int k = kb * 8;
            uint4 lo4 = make_uint4((w[0] & 0xffffu) | (w[1] << 16), (w[2] & 0xffffu) | (w[3] << 16),
                                   (w[4] & 0xffffu) | (w[5] << 16), (w[6] & 0xffffu) | (w[7] << 16));
            uint4 hi4 = make_uint4((w[0] >> 16) | (w[1] & 0xffff0000u), (w[2] >> 16) | (w[3] & 0xffff0000u),
                                   (w[4] >> 16) | (w[5] & 0xffff0000u), (w[6] >> 16) | (w[7] & 0xffff0000u));
            *reinterpret_cast<uint4*>(&smem[(2 * D + c2 * 2) * (D + 8) + k]) = lo4;
            *reinterpret_cast<uint4*>(&smem[(2 * D + c2 * 2 + 1) * (D + 8) + k]) = hi4;
        }
        __syncthreads();
        int lane = t & 63, wv = t >> 6;
        int e16 = lane & 15, g = lane >> 4;
        int rt = wv & 7;
        int half = wv >> 3;
        int n = bid * 128 + rt * 16 + e16;
        int nn = n < N ? n : N - 1;
        bf16x8 af[4];
#pragma unroll
        for (int ks = 0; ks < 4; ++ks)
            af[ks] = *reinterpret_cast<const bf16x8*>(h + (size_t)nn * D + ks * 32 + g * 8);
        const int wr0 = (e16 >> 2) * 8 + (e16 & 3);
#pragma unroll
        for (int jj = 0; jj < 6; ++jj) {
            const int jb = half * 6 + jj;
            f32x4 acc0 = {0.f, 0.f, 0.f, 0.f}, acc1 = {0.f, 0.f, 0.f, 0.f};
#pragma unroll
            for (int ks = 0; ks < 4; ++ks) {
                const u16* wp = &smem[(jb * 32 + wr0) * (D + 8) + ks * 32 + g * 8];
                bf16x8 w0 = *reinterpret_cast<const bf16x8*>(wp);
                bf16x8 w1 = *reinterpret_cast<const bf16x8*>(wp + 4 * (D + 8));
                acc0 = __builtin_amdgcn_mfma_f32_16x16x32_bf16(w0, af[ks], acc0, 0, 0, 0);
                acc1 = __builtin_amdgcn_mfma_f32_16x16x32_bf16(w1, af[ks], acc1, 0, 0, 0);
            }
            const float sc = (jb < 8) ? NLOG2E : 1.0f;
            uint4 o = make_uint4(cvt_pk((f32x2){acc0[0] * sc, acc0[1] * sc}), cvt_pk((f32x2){acc0[2] * sc, acc0[3] * sc}),
                                 cvt_pk((f32x2){acc1[0] * sc, acc1[1] * sc}), cvt_pk((f32x2){acc1[2] * sc, acc1[3] * sc}));
            if (n < N) {
                u16* op = ABU + ((size_t)(jb >> 2) * N + n) * D + (jb & 3) * 32 + g * 8;
                if ((jb >> 2) == 1) *reinterpret_cast<uint4*>(op) = o;
                else st_nt(op, o);
            }
        }
        return;
    }
}

struct KbSmem {
    uint4 rec[KB_WAVES][CAPR];
    uint2 geo[KB_WAVES][CAPR];
    uint4 lut[8];
    uint4 At[KB_WAVES][D];
    uint4 w5s[2][D];
    float macc[KB_WAVES * NPW][AST];
    float pacc[KB_WAVES][NPW][4];
    float posl[KB_WAVES][NPW + 3][4];
    float wxs[D];
    uint2 M[KB_WAVES][16][4];
    int nt[KB_WAVES][4];
};

__global__ void __launch_bounds__(KB_THREADS, 3)
k_main(const u16* __restrict__ h, const u16* __restrict__ pos,
       const u16* __restrict__ wx,
       const u32* __restrict__ rec_g, const u16* __restrict__ tab,
       const u16* __restrict__ ABU,
       const u16* __restrict__ whT, const uint4* __restrict__ w5g, const uint2* __restrict__ pos8,
       u16* __restrict__ out_h, u16* __restrict__ out_pos,
       unsigned long long mNBW, int N, int G, int CH, int NBW) {
    __shared__ KbSmem S;
    const u16* __restrict__ Bg = ABU + (size_t)N * D;
    const u16* __restrict__ Ug = ABU + (size_t)2 * N * D;
    const int t = threadIdx.x, lane = t & 63;
    const int wv = __builtin_amdgcn_readfirstlane(t >> 6);
    const int e16 = lane & 15, g = lane >> 4;
    const int nwg = NBW / KB_WAVES, per8 = nwg >> 3;
    const int bx = ((int)blockIdx.x < per8 * 8) ? ((int)(blockIdx.x & 7) * per8 + (int)(blockIdx.x >> 3)) : (int)blockIdx.x;
    const int wb = bx * KB_WAVES + wv;
    const int N0 = node_start(bx * KB_WAVES, N, NBW, mNBW);
    const int N1 = node_start(bx * KB_WAVES + KB_WAVES, N, NBW, mNBW);
    const int n0 = node_start(wb, N, NBW, mNBW);
    const int nn = node_start(wb + 1, N, NBW, mNBW) - n0;
    const int rowb = n0 - N0;

    int so[GI_MAX], sc[GI_MAX];
    u16 (*tabs)[5] = reinterpret_cast<u16 (*)[5]>(&S.M[0][0][0]);
    auto fetch_tab = [&]() {
        if (t < G) {
            const u16* tp = tab + (size_t)t * (NBW + 4) + bx * KB_WAVES;
            uint2 v = *reinterpret_cast<const uint2*>(tp);
            u32 v4 = tp[4];
            tabs[t][0] = (u16)v.x; tabs[t][1] = (u16)(v.x >> 16);
            tabs[t][2] = (u16)v.y; tabs[t][3] = (u16)(v.y >> 16);
            tabs[t][4] = (u16)v4;
        }
    };
    auto load_tab = [&]() {
        int lo_ = lane;
        asm volatile("" : "+v"(lo_));
#pragma unroll
        for (int ci = 0; ci < GI_MAX; ++ci) {
            int c = lo_ + 64 * ci;
            so[ci] = 0; sc[ci] = 0;
            if (c < G) {
                so[ci] = tabs[c][wv];
                sc[ci] = tabs[c][wv + 1];
            }
        }
    };
    auto load_tab_g = [&]() {
        int lo_ = lane;
        asm volatile("" : "+v"(lo_));
#pragma unroll
        for (int ci = 0; ci < GI_MAX; ++ci) {
            int c = lo_ + 64 * ci;
            so[ci] = 0; sc[ci] = 0;
            if (c < G) {
                const u16* tp = tab + (size_t)c * (NBW + 4) + wb;
                so[ci] = tp[0];
                sc[ci] = tp[1];
            }
        }
    };
    fetch_tab();
    u32 a0[NPW];
#pragma unroll
    for (int j = 0; j < NPW; ++j) {
        a0[j] = 0u;
        if (j < nn) a0[j] = *reinterpret_cast<const u32*>(ABU + (size_t)(n0 + j) * D + lane * 2);
    }
    const uint4 w40 = w5g[lane], w41 = w5g[lane + 64], w42 = w5g[lane + 128], w43 = w5g[lane + 192];
    u16 wx0 = wx[lane], wx1 = wx[lane + 64];
    u16 pz16 = 0;
    {
        int n = lane >> 2, cpt = lane & 3;
        if (n < nn && cpt < 3) pz16 = pos[(n0 + n) * 3 + cpt];
    }
    const int NR = N1 - N0;
    uint4 hv4[2], uv4[2];
#pragma unroll
    for (int rt = 0; rt < 2; ++rt) {
        int lr = rt * 16 + e16;
        size_t o = (size_t)(N0 + (lr < NR ? lr : 0)) * D + wv * 32 + g * 8;
        hv4[rt] = *reinterpret_cast<const uint4*>(h + o);
        uv4[rt] = *reinterpret_cast<const uint4*>(Ug + o);
    }
    auto stage = [&]() {
        S.At[wv][lane * 2] = make_uint4((a0[0] & 0xffffu) | (a0[1] << 16), (a0[2] & 0xffffu) | (a0[3] << 16), a0[4] & 0xffffu, 0u);
        S.At[wv][lane * 2 + 1] = make_uint4((a0[0] >> 16) | (a0[1] & 0xffff0000u), (a0[2] >> 16) | (a0[3] & 0xffff0000u), a0[4] >> 16, 0u);
#pragma unroll
        for (int j = 0; j < NPW; ++j)
            if (j < nn) { S.macc[rowb + j][lane] = 0.f; S.macc[rowb + j][64 + lane] = 0.f; }
        uint4* wq = &S.w5s[0][0] + lane;
        wq[0] = w40; wq[64] = w41; wq[128] = w42; wq[192] = w43;
        S.wxs[lane] = bf2f(wx0);
        S.wxs[lane + 64] = bf2f(wx1);
        if (lane < (NPW + 3) * 4) S.posl[wv][lane >> 2][lane & 3] = bf2f(pz16);
        if (lane < NPW * 4) S.pacc[wv][lane >> 2][lane & 3] = 0.f;
        if (lane < 32) {
            const int n = lane >> 2, w = lane & 3;
            reinterpret_cast<u32*>(S.lut)[lane] = (n < 5 && (n >> 1) == w) ? (0x3F80u << (16 * (n & 1))) : 0u;
        }
    };
    const unsigned long long ltmask = (1ull << lane) - 1ull;
    const int CHB4 = ((CH * RW + 3) >> 2) << 2;
    uint4* rec = S.rec[wv];
    u32* key = reinterpret_cast<u32*>(S.rec[wv]);

    __syncthreads();
    load_tab();
    __syncthreads();
    f32x4 acc2[8];
    int dHead = 0, hflags = 0;
    float hm[8], hp0, hp1, hp2;
    auto reduce = [&]() __attribute__((always_inline)) {
        float wxc[8];
#pragma unroll
        for (int tt = 0; tt < 8; ++tt) wxc[tt] = S.wxs[tt * 16 + e16];
        float pv0 = 0.f, pv1 = 0.f, pv2 = 0.f;
#pragma unroll
        for (int tt = 0; tt < 8; ++tt) {
            hm[tt] = acc2[tt][0] * NLN2;
            pv0 += acc2[tt][1] * wxc[tt];
            pv1 += acc2[tt][2] * wxc[tt];
            pv2 += acc2[tt][3] * wxc[tt];
            acc2[tt] = (f32x4){0.f, 0.f, 0.f, 0.f};
        }
        hp0 = rsum16(pv0) * NLN2; hp1 = rsum16(pv1) * NLN2; hp2 = rsum16(pv2) * NLN2;
    };
    auto commit = [&](int d, auto accc) __attribute__((always_inline)) {
        constexpr bool ACC = decltype(accc)::value;
        const int bk = (d >> 16) & 3;
        const int nl = ((d >> 18) & 1) * 4 + g;
        const int rb = (d >> 20) & 31, nb = (d >> 25) & 7;
        const bool on = nl < nb;
        float* mrow = &S.macc[rb + (on ? nl : 0)][e16];
        float* prow = &S.pacc[bk][on ? nl : 0][0];
        float old[8], po[3];
        if (ACC) {
#pragma unroll
            for (int tt = 0; tt < 8; ++tt) old[tt] = mrow[tt * 16];
            po[0] = prow[0]; po[1] = prow[1]; po[2] = prow[2];
            CBAR();
        }
        if (on) {
#pragma unroll
            for (int tt = 0; tt < 8; ++tt) mrow[tt * 16] = ACC ? old[tt] + hm[tt] : hm[tt];
            if (e16 == 0) {
                prow[0] = (ACC ? po[0] : 0.f) + hp0;
                prow[1] = (ACC ? po[1] : 0.f) + hp1;
                prow[2] = (ACC ? po[2] : 0.f) + hp2;
            }
        }
    };
    auto flush = [&](int d, auto accc) __attribute__((always_inline)) { reduce(); commit(d, accc); };
    auto finish = [&]() __attribute__((always_inline)) {
        const std::integral_constant<bool, true> accc;
        __syncthreads();
        for (int w = 1; w < KB_WAVES; ++w) {
            if ((hflags & 1) && (wv == w || !(hflags & 2))) commit(dHead, accc);
            __syncthreads();
            if (!(hflags & 2)) break;
        }
    };
    auto round = [&](const int base, auto firstc) __attribute__((always_inline)) -> bool {
        constexpr bool FIRST = decltype(firstc)::value;
        int T;
        int fo[GI_MAX];
        {
            int tot = 0;
#pragma unroll
            for (int ci = 0; ci < GI_MAX; ++ci) { sc[ci] -= so[ci]; tot += sc[ci]; }
            int x = tot;
            int ls = lane;
            asm volatile("" : "+v"(ls));
#pragma unroll
            for (int o = 1; o < 64; o <<= 1) {
                int y = __builtin_amdgcn_ds_bpermute((ls - o) << 2, x);
                if (ls >= o) x += y;
            }
            T = __builtin_amdgcn_readlane(x, 63);
            int off = x - tot;
#pragma unroll
            for (int ci = 0; ci < GI_MAX; ++ci) { fo[ci] = off; off += sc[ci]; }
        }
        const int Tr = max(0, min(CAP, T - base));
        int lo2 = lane;
        asm volatile("" : "+v"(lo2));
#pragma unroll
        for (int ci = 0; ci < GI_MAX; ++ci) {
            int gs = (lo2 + 64 * ci) * CHB4 + so[ci] * RW;
            for (int q = 0; q < sc[ci]; ++q) {
                int fp = fo[ci] + q - base;
                if (fp >= 0 && fp < CAP) key[fp] = (u32)(gs + q * RW);
            }
        }
        CBAR();
        uint4 r[RPL];
        uint2 ps[RPL];
#pragma unroll
        for (int i = 0; i < RPL; ++i) {
            int p = lane + 64 * i;
            r[i] = make_uint4(0xffffffffu, 0u, 0u, 0u);
            if (p < Tr) {
                const u32* rp = rec_g + key[p];
                r[i] = make_uint4(rp[0], rp[1], rp[2], 0u);
            }
        }
        if (FIRST) stage();
#pragma unroll
        for (int i = 0; i < RPL; ++i) {
            ps[i] = make_uint2(0u, 0u);
            if (r[i].x != 0xffffffffu) ps[i] = pos8[r[i].x >> 3];
        }
        int posn[RPL];
        int cst = 0, cnt4 = 0, c0 = 0, g1s = 0;
#pragma unroll
        for (int i = 0; i < RPL; ++i) posn[i] = 0;
#pragma unroll
        for (int j = 0; j < NPW; ++j) {
            if (j == 4) { c0 = cst; g1s = (cst + 15) & ~15; cst = g1s; }
            int c = 0;
#pragma unroll
            for (int i = 0; i < RPL; ++i) {
                bool mt = (r[i].x != 0xffffffffu) && ((int)(r[i].x & 7u) == j);
                unsigned long long m = __ballot(mt);
                if (mt) posn[i] = cst + c + __popcll(m & ltmask);
                c += __popcll(m);
            }
            if (j == 4) cnt4 = c;
            cst += c;
        }
        const int g1e = (g1s + cnt4 + 15) & ~15;
        CBAR();
#pragma unroll
        for (int i = 0; i < RPL; ++i)
            if (r[i].x != 0xffffffffu) {
                const float4 pn = *reinterpret_cast<const float4*>(&S.posl[wv][r[i].x & 7u][0]);
                float dx = pn.x - __uint_as_float(ps[i].x << 16), dy = pn.y - __uint_as_float(ps[i].x & 0xffff0000u);
                float dz = pn.z - __uint_as_float(ps[i].y << 16);
                float d2v = dx * dx + dy * dy + dz * dz;
                u16 d2h = f2bf(d2v);
                u16 d2l = f2bf(d2v - bf2f(d2h));
                rec[posn[i]] = make_uint4((u32)d2h | (r[i].y << 16), (r[i].y >> 16) | (r[i].z << 16), (r[i].z >> 16) | ((u32)d2l << 16), r[i].x);
                S.geo[wv][posn[i]] = make_uint2(0x3F80u | ((u32)f2bf(dx) << 16), pack2(dy, dz));
            }
        if (lane < 16) {
            if (c0 + lane < g1s) { rec[c0 + lane] = make_uint4(0u, 0u, 0u, 7u); S.geo[wv][c0 + lane] = make_uint2(0u, 0u); }
            if (g1s + cnt4 + lane < g1e) { rec[g1s + cnt4 + lane] = make_uint4(0u, 0u, 0u, 7u); S.geo[wv][g1s + cnt4 + lane] = make_uint2(0u, 0u); }
        }
        if (lane == 0) {
            S.nt[wv][0] = g1s >> 4;
            S.nt[wv][1] = g1e >> 4;
            S.nt[wv][2] = (base + CAP < T) ? 1 : 0;
        }
        __syncthreads();

        int sb1, sb2, sb3, sb4, sb5, sb6, sb7, TT, anymore;
        {
            const int4 q0 = *reinterpret_cast<const int4*>(&S.nt[0][0]);
            const int4 q1 = *reinterpret_cast<const int4*>(&S.nt[1][0]);
            const int4 q2 = *reinterpret_cast<const int4*>(&S.nt[2][0]);
            const int4 q3 = *reinterpret_cast<const int4*>(&S.nt[3][0]);
            sb1 = __builtin_amdgcn_readfirstlane(q0.x);
            sb2 = __builtin_amdgcn_readfirstlane(q0.y);
            sb3 = sb2 + __builtin_amdgcn_readfirstlane(q1.x);
            sb4 = sb2 + __builtin_amdgcn_readfirstlane(q1.y);
            sb5 = sb4 + __builtin_amdgcn_readfirstlane(q2.x);
            sb6 = sb4 + __builtin_amdgcn_readfirstlane(q2.y);
            sb7 = sb6 + __builtin_amdgcn_readfirstlane(q3.x);
            TT = sb6 + __builtin_amdgcn_readfirstlane(q3.y);
            anymore = __builtin_amdgcn_readfirstlane(q0.z | q1.z | q2.z | q3.z);
        }
        auto seg_of = [&](int x) __attribute__((always_inline)) {
            return (x >= sb1) + (x >= sb2) + (x >= sb3) + (x >= sb4) + (x >= sb5) + (x >= sb6) + (x >= sb7);
        };
        auto is_start = [&](int x) __attribute__((always_inline)) {
            return (x == 0) | (x == sb1) | (x == sb2) | (x == sb3) | (x == sb4) | (x == sb5) | (x == sb6) | (x == sb7);
        };
        auto seg_end = [&](int x) __attribute__((always_inline)) {
            int r = TT;
            r = (x < sb7) ? +sb7 : +r; r = (x < sb6) ? +sb6 : +r; r = (x < sb5) ? +sb5 : +r; r = (x < sb4) ? +sb4 : +r;
            r = (x < sb3) ? +sb3 : +r; r = (x < sb2) ? +sb2 : +r; r = (x < sb1) ? +sb1 : +r;
            return r;
        };
        const int ts = (TT * wv) >> 2, te = (TT * (wv + 1)) >> 2;
        const int nst = te - ts;
        const bool has_head = (nst > 0) && !is_start(ts);
        const int th = has_head ? min(te, seg_end(ts)) : +ts;
        bool serial = false;
        {
            const int s1 = TT >> 2, s2 = (TT * 2) >> 2, s3 = (TT * 3) >> 2;
            serial = (!is_start(s1) && seg_end(s1) > s2) || (!is_start(s2) && seg_end(s2) > s3);
        }
        int lq = lane;
        asm volatile("" : "+v"(lq));
        int descv;
        {
            const int ns1 = node_start(bx * KB_WAVES + 1, N, NBW, mNBW) - N0;
            const int ns2 = node_start(bx * KB_WAVES + 2, N, NBW, mNBW) - N0;
            const int ns3 = node_start(bx * KB_WAVES + 3, N, NBW, mNBW) - N0;
            const int ns4 = N1 - N0;
            const int nh = te - th;
            const int tl = (lq < nh) ? (th + lq) : (ts + lq - nh);
            const int k = seg_of(tl);
            const int b = k >> 1;
            const int cb = (b == 0) ? 0 : (b == 1) ? +sb2 : (b == 2) ? +sb4 : +sb6;
            const int r0 = (b == 0) ? 0 : (b == 1) ? +ns1 : (b == 2) ? +ns2 : +ns3;
            const int r1 = (b == 0) ? +ns1 : (b == 1) ? +ns2 : (b == 2) ? +ns3 : +ns4;
            const int fl = (lq < nh) && ((tl + 1 == te) || is_start(tl + 1));
            descv = ((b * CAPR + (tl - cb) * 16) * 16) | (b << 16) | ((k & 1) << 18) | (fl << 19) | (r0 << 20) | ((r1 - r0) << 25);
        }
        bf16x8 selLo, selHi;
        {
            int dl = (lq & 15) - 8 * (lq >> 4);
            unsigned long long one = 0x3F80ull;
            u32 l0 = 0, l1 = 0, l2 = 0, l3 = 0, h0 = 0, h1 = 0, h2 = 0, h3 = 0;
            if (dl >= 0 && dl < 4) { unsigned long long v = one << (16 * dl); l0 = (u32)v; l1 = (u32)(v >> 32); }
            if (dl >= 4 && dl < 8) { unsigned long long v = one << (16 * (dl - 4)); l2 = (u32)v; l3 = (u32)(v >> 32); }
            int dh = dl + 16;
            if (dh >= 0 && dh < 4) { unsigned long long v = one << (16 * dh); h0 = (u32)v; h1 = (u32)(v >> 32); }
            if (dh >= 4 && dh < 8) { unsigned long long v = one << (16 * (dh - 4)); h2 = (u32)v; h3 = (u32)(v >> 32); }
            selLo = as_bf16x8(make_uint4(l0, l1, l2, l3));
            selHi = as_bf16x8(make_uint4(h0, h1, h2, h3));
        }
        const char* w5b = (g < 2) ? reinterpret_cast<const char*>(&S.w5s[g][e16]) : reinterpret_cast<const char*>(&S.At[0][e16]);
        const int w5m = (g < 2) ? 0 : (int)sizeof(S.At[0]);
        const char* recb = reinterpret_cast<const char*>(&S.rec[0][e16]);
        const char* geob = reinterpret_cast<const char*>(&S.geo[0][e16]);
        const char* Bb = reinterpret_cast<const char*>(Bg);
        const u32 g16 = (u32)g * 16u;
        const u32 ndor = (g == 3) ? 7u : 0u;
        const u32 wmask = (g < 2) ? 0u : 0xffffffffu;
        bf16x8 gA[4], gB[4];
        u32 pA0 = 0, pB0 = 0;
        auto issue = [&](int d, bf16x8 (&gat)[4], u32& q0) __attribute__((always_inline)) {
            q0 = reinterpret_cast<const uint4*>(recb + (d & 0xffff))->w;
            const u32 off = ((q0 >> 3) << 8) + g16;
#pragma unroll
            for (int tp = 0; tp < 4; ++tp)
                gat[tp] = *reinterpret_cast<const bf16x8*>(Bb + off + tp * 64);
        };
#pragma unroll
        for (int tt = 0; tt < 8; ++tt) acc2[tt] = (f32x4){0.f, 0.f, 0.f, 0.f};
        auto compute = [&](int d, bf16x8 (&gat)[4], u32 q0) __attribute__((always_inline)) {
            const int bk = (d >> 16) & 3;
            const u32 nd = q0 & 7u;
            const char* xp = (g < 2) ? (recb + (d & 0xffff)) : (reinterpret_cast<const char*>(S.lut) + ((nd | ndor) << 4));
            uint4 xu = *reinterpret_cast<const uint4*>(xp);
            const uint2 gq = *reinterpret_cast<const uint2*>(geob + ((d & 0xffff) >> 1));
            const uint4* w5p = reinterpret_cast<const uint4*>(w5b + w5m * bk);
            S.M[wv][e16][g] = ((nd & 3u) == (u32)g) ? gq : make_uint2(0u, 0u);
            xu.w &= wmask;
            const bf16x8 x5 = as_bf16x8(xu);
            asm volatile("s_waitcnt lgkmcnt(0)" ::: "memory");
            u32 sw01, sw23;
            {
                const u16* wp = reinterpret_cast<const u16*>(&S.M[wv][g * 4][0]) + e16;
                sw01 = (u32)wp[0] | ((u32)wp[16] << 16);
                sw23 = (u32)wp[32] | ((u32)wp[48] << 16);
            }
            const bf16x4 sop = as_bf16x4(make_uint2(sw01, sw23));
#pragma unroll
            for (int tp = 0; tp < 4; ++tp) {
#pragma unroll
                for (int u = 0; u < 2; ++u) {
                    const int tt = tp * 2 + u;
                    bf16x8 w5 = as_bf16x8(w5p[tt * 16]);
                    f32x4 acc = __builtin_amdgcn_mfma_f32_16x16x32_bf16(x5, w5, (f32x4){0.f, 0.f, 0.f, 0.f}, 0, 0, 0);
                    acc = __builtin_amdgcn_mfma_f32_16x16x32_bf16(gat[tp], u ? selHi : selLo, acc, 0, 0, 0);
                    f32x2 t01 = {acc[0], acc[1]}, t23 = {acc[2], acc[3]};
                    float e0 = __builtin_amdgcn_exp2f(acc[0]), e1 = __builtin_amdgcn_exp2f(acc[1]);
                    float e2 = __builtin_amdgcn_exp2f(acc[2]), e3 = __builtin_amdgcn_exp2f(acc[3]);
                    e0 += 1.f; e1 += 1.f; e2 += 1.f; e3 += 1.f;
                    asm("" : "+v"(e0)); asm("" : "+v"(e1)); asm("" : "+v"(e2)); asm("" : "+v"(e3));
                    f32x2 r01 = {__builtin_amdgcn_rcpf(e0), __builtin_amdgcn_rcpf(e1)};
                    f32x2 r23 = {__builtin_amdgcn_rcpf(e2), __builtin_amdgcn_rcpf(e3)};
                    f32x2 m01 = t01 * r01, m23 = t23 * r23;
                    u32 k01 = cvt_pk(m01), k23 = cvt_pk(m23);
                    bf16x4 mb = as_bf16x4(make_uint2(k01, k23));
                    acc2[tt] = __builtin_amdgcn_mfma_f32_16x16x16bf16_1k(sop, mb, acc2[tt], 0, 0, 0);
                }
            }
        };
        const std::integral_constant<bool, !FIRST> dirc;
        if (nst > 0) {
            issue(__builtin_amdgcn_readlane(descv, 0), gA, pA0);
            int i = 0;
            for (; i + 1 < nst; i += 2) {
                {
                    const int rem = nst - i;
                    if (rem >= 5) __builtin_amdgcn_s_setprio(3);
                    else if (rem >= 4) __builtin_amdgcn_s_setprio(2);
                    else if (rem >= 3) __builtin_amdgcn_s_setprio(1);
                    else __builtin_amdgcn_s_setprio(0);
                }
                const int dA = __builtin_amdgcn_readlane(descv, i);
                const int dB = __builtin_amdgcn_readlane(descv, i + 1);
                const int dC = __builtin_amdgcn_readlane(descv, min(i + 2, nst - 1));
                issue(dB, gB, pB0);
                compute(dA, gA, pA0);
                if (dA & (1 << 19)) flush(dA, dirc);
                issue(dC, gA, pA0);
                compute(dB, gB, pB0);
                if (dB & (1 << 19)) flush(dB, dirc);
            }
            if (i < nst) {
                const int dA = __builtin_amdgcn_readlane(descv, i);
                compute(dA, gA, pA0);
                if (dA & (1 << 19)) flush(dA, dirc);
            }
        }
        dHead = __builtin_amdgcn_readlane(descv, max(nst - 1, 0));
        hflags = (has_head ? 1 : 0) | (serial ? 2 : 0);
        reduce();
        return anymore != 0;
    };
    if (round(0, std::integral_constant<bool, true>())) {
        for (int base = CAP;; base += CAP) {
            finish();
            load_tab_g();
            if (!round(base, std::integral_constant<bool, false>())) break;
        }
    }

    int le = lane;
    asm volatile("" : "+v"(le));
    const int f16 = le & 15, gg = le >> 4;
    bf16x8 bw[2][4];
#pragma unroll
    for (int q = 0; q < 2; ++q)
#pragma unroll
        for (int ks = 0; ks < 4; ++ks)
            bw[q][ks] = *reinterpret_cast<const bf16x8*>(whT + (size_t)(wv * 32 + (f16 >> 2) * 8 + q * 4 + (f16 & 3)) * D + ks * 32 + gg * 8);
    finish();
    if (le < nn * 3) {
        int n = le / 3, cpt = le - n * 3;
        out_pos[(size_t)(n0 + n) * 3 + cpt] = f2bf(S.posl[wv][n][cpt] + S.pacc[wv][n][cpt]);
    }
#pragma unroll
    for (int rt = 0; rt < 2; ++rt) {
        if (rt * 16 < NR) {
            bf16x8 af[4];
            int lr = rt * 16 + f16;
#pragma unroll
            for (int ks = 0; ks < 4; ++ks) {
                const float* mp = &S.macc[lr < NR ? lr : 0][ks * 32 + gg * 8];
                af[ks] = as_bf16x8(make_uint4(pack2(mp[0], mp[1]), pack2(mp[2], mp[3]),
                                              pack2(mp[4], mp[5]), pack2(mp[6], mp[7])));
            }
            u32 ov[4];
#pragma unroll
            for (int q = 0; q < 2; ++q) {
                const u32 u0 = q ? uv4[rt].z : uv4[rt].x, u1 = q ? uv4[rt].w : uv4[rt].y;
                const u32 h0 = q ? hv4[rt].z : hv4[rt].x, h1 = q ? hv4[rt].w : hv4[rt].y;
                f32x4 acc = {__uint_as_float(u0 << 16), __uint_as_float(u0 & 0xffff0000u),
                             __uint_as_float(u1 << 16), __uint_as_float(u1 & 0xffff0000u)};
#pragma unroll
                for (int ks = 0; ks < 4; ++ks)
                    acc = __builtin_amdgcn_mfma_f32_16x16x32_bf16(bw[q][ks], af[ks], acc, 0, 0, 0);
                ov[q * 2] = pack2(__uint_as_float(h0 << 16) + silu_f(acc[0]), __uint_as_float(h0 & 0xffff0000u) + silu_f(acc[1]));
                ov[q * 2 + 1] = pack2(__uint_as_float(h1 << 16) + silu_f(acc[2]), __uint_as_float(h1 & 0xffff0000u) + silu_f(acc[3]));
            }
            if (lr < NR)
                *reinterpret_cast<uint4*>(out_h + (size_t)(N0 + lr) * D + wv * 32 + gg * 8) = make_uint4(ov[0], ov[1], ov[2], ov[3]);
        }
    }
}

extern "C" void kernel_launch(void* const* d_in, const int* in_sizes, int n_in,
                              void* d_out, int out_size, void* d_ws, size_t ws_size,
                              hipStream_t stream) {
    const u16* h   = (const u16*)d_in[0];
    const u16* pos = (const u16*)d_in[1];
    const u16* ea  = (const u16*)d_in[2];
    const int* src = (const int*)d_in[3];
    const int* dst = (const int*)d_in[4];
    const u16* we  = (const u16*)d_in[5];
    const u16* wx  = (const u16*)d_in[6];
    const u16* wh  = (const u16*)d_in[7];
    int N = in_sizes[0] / D;
    int E = in_sizes[3];
    u16* out_h = (u16*)d_out;
    u16* out_pos = out_h + (size_t)N * D;

    int G = 128;
    while ((E + G - 1) / G > KA_THREADS * KA_ITERS && G < 64 * GI_MAX) G += 64;
    int CH = (E + G - 1) / G;
    int NBW = 3072;
    while (NBW * NPW < N && NBW < NBW_MAX) NBW += 4;
    int nGemm = (N + 127) / 128;
    if (nGemm < D * D / KA_THREADS) nGemm = D * D / KA_THREADS;
    unsigned long long mN = (1ull << 40) / (unsigned long long)N + 1ull;
    unsigned long long mNBW = (1ull << 40) / (unsigned long long)NBW + 1ull;

    char* ws = (char*)d_ws;
    size_t off = 0;
    auto take = [&](size_t bytes) { char* p = ws + off; off += (bytes + 255) & ~(size_t)255; return p; };
    u32* rec_g = (u32*)take((size_t)G * ((CH * RW + 3) >> 2) * 16 + 64);
    u16* tab = (u16*)take((size_t)G * (NBW + 4) * 2);
    u16* ABU = (u16*)take((size_t)3 * N * D * 2);
    u16* whT = (u16*)take((size_t)D * D * 2);
    uint4* w5g = (uint4*)take((size_t)256 * 16);
    uint2* pos8 = (uint2*)take((size_t)N * 8);

    k_part<<<G + nGemm, KA_THREADS, 0, stream>>>(h, pos, src, dst, ea, we, wh, rec_g, tab, ABU, whT, w5g, pos8,
                                                      mN, mNBW, N, E, G, CH, NBW, nGemm);
    k_main<<<NBW / KB_WAVES, KB_THREADS, 0, stream>>>(h, pos, wx, rec_g, tab, ABU, whT, w5g, pos8,
                                                     out_h, out_pos, mNBW, N, G, CH, NBW);
}
```

```cpp
#include <hip/hip_runtime.h>
#include <hip/hip_bf16.h>
#include <stdint.h>
#include <type_traits>


typedef unsigned short u16;
typedef unsigned int u32;
typedef __attribute__((ext_vector_type(8))) short bf16x8;
typedef __attribute__((ext_vector_type(4))) float f32x4;
typedef __attribute__((ext_vector_type(4))) short bf16x4;
typedef __attribute__((ext_vector_type(2))) float f32x2;
typedef __attribute__((ext_vector_type(4))) unsigned int u32x4;
__device__ __forceinline__ void st_nt(void* p, uint4 v) { u32x4 x = {v.x, v.y, v.z, v.w}; __builtin_nontemporal_store(x, reinterpret_cast<u32x4*>(p)); }

#define D 128
#define KA_THREADS 1024
#define KA_ITERS 4
#define NBW_MAX 8192
#define KB_THREADS 256
#define KB_WAVES 4
#define NPW 5
#define RPL 4
#define CAP 240
#define CAPS 272
#define CAPR (CAP + 32)
#define RW 3
#define GI_MAX 2
#define AST (D + 4)
#define CBAR() asm volatile("" ::: "memory")
#define NLOG2E (-1.4426950408889634f)
#define NLN2 (-0.6931471805599453f)

__device__ __forceinline__ float bf2f(u16 v) { return __uint_as_float(((u32)v) << 16); }
__device__ __forceinline__ u16 f2bf(float f) {
    __hip_bfloat16 b = __float2bfloat16(f);
    return *reinterpret_cast<u16*>(&b);
}
__device__ __forceinline__ float silu_f(float x) {
    float e = __builtin_amdgcn_exp2f(x * NLOG2E);
    return x * __builtin_amdgcn_rcpf(1.0f + e);
}
__device__ __forceinline__ u32 pack2(float a, float b) {
    return (u32)f2bf(a) | ((u32)f2bf(b) << 16);
}
typedef __attribute__((ext_vector_type(2))) __bf16 bf16x2;
__device__ __forceinline__ u32 cvt_pk(f32x2 v) {
    bf16x2 p = __builtin_convertvector(v, bf16x2);
    return *reinterpret_cast<u32*>(&p);
}
__device__ __forceinline__ bf16x8 as_bf16x8(uint4 v) { return *reinterpret_cast<bf16x8*>(&v); }
__device__ __forceinline__ bf16x4 as_bf16x4(uint2 v) { return *reinterpret_cast<bf16x4*>(&v); }
template <int CTRL>
__device__ __forceinline__ float dpp_f(float x) {
    return __int_as_float(__builtin_amdgcn_update_dpp(0, __float_as_int(x), CTRL, 0xf, 0xf, true));
}
__device__ __forceinline__ float rsum16(float x) {
    x += dpp_f<0x128>(x);
    x += dpp_f<0x124>(x);
    x += dpp_f<0x122>(x);
    x += dpp_f<0x121>(x);
    return x;
}
__device__ __forceinline__ u32 mdiv(u32 x, unsigned long long m) {
    return (u32)(((unsigned long long)x * m) >> 40);
}
__device__ __forceinline__ int node_start(int b, int N, int NBW, unsigned long long mNBW) {
    return (int)mdiv((u32)b * (u32)N + (u32)NBW - 1u, mNBW);
}

__global__ void __launch_bounds__(KA_THREADS)
k_part(const u16* __restrict__ h, const u16* __restrict__ pos, const int* __restrict__ src, const int* __restrict__ dst,
       const u16* __restrict__ ea, const u16* __restrict__ we, const u16* __restrict__ wh,
       u32* __restrict__ rec_g, u16* __restrict__ tab, u16* __restrict__ ABU,
       u16* __restrict__ whT, uint4* __restrict__ w5g, uint2* __restrict__ pos8,
       unsigned long long mN, unsigned long long mNBW,
       int N, int E, int G, int CH, int NBW, int nGemm) {
    __shared__ __align__(16) u16 smem[(NBW_MAX + 8) * 2 + KA_THREADS * KA_ITERS + KA_THREADS * KA_ITERS * 8];
    __shared__ int wsum[KA_THREADS / 64];
    int t = threadIdx.x;
    int bid = blockIdx.x;
    if (bid < G) {
        int* hist = (int*)smem;
        for (int i = t; i < NBW; i += KA_THREADS) hist[i] = 0;
        int e0 = bid * CH;
        int e1 = min(E, e0 + CH);
        int sv[KA_ITERS], bv[KA_ITERS], rk[KA_ITERS], lo[KA_ITERS];
        uint2 eav[KA_ITERS];
#pragma unroll
        for (int i = 0; i < KA_ITERS; ++i) {
            int e = e0 + t + i * KA_THREADS;
            bv[i] = -1;
            if (e < e1) {
                int d = dst[e];
                sv[i] = src[e];
                eav[i] = *reinterpret_cast<const uint2*>(ea + (size_t)e * 4);
                int b = (int)mdiv((u32)d * (u32)NBW, mN);
                bv[i] = b;
                lo[i] = d - node_start(b, N, NBW, mNBW);
            }
        }
        __syncthreads();
#pragma unroll
        for (int i = 0; i < KA_ITERS; ++i)
            if (bv[i] >= 0) rk[i] = atomicAdd(&hist[bv[i]], 1);
        __syncthreads();
        const int per = (NBW + KA_THREADS - 1) / KA_THREADS;
        int b0 = t * per, b1 = min(NBW, b0 + per);
        int cnt = 0;
        for (int b = b0; b < b1; ++b) cnt += hist[b];
        int lane = t & 63, wv = t >> 6;
        int x = cnt;
#pragma unroll
        for (int o = 1; o < 64; o <<= 1) {
            int y = __shfl_up(x, o);
            if (lane >= o) x += y;
        }
        if (lane == 63) wsum[wv] = x;
        __syncthreads();
        int base = 0;
#pragma unroll
        for (int w = 0; w < KA_THREADS / 64; ++w) base += (w < wv) ? wsum[w] : 0;
        int off = base + x - cnt;
        u16* trow = tab + (size_t)bid * (NBW + 4);
        for (int b = b0; b < b1; ++b) {
            int c = hist[b];
            hist[b] = off;
            trow[b] = (u16)off;
            off += c;
        }
        if (t == KA_THREADS - 1) { trow[NBW] = (u16)(base + x); hist[NBW] = base + x; }
        __syncthreads();
        u16* ids = reinterpret_cast<u16*>(hist + NBW_MAX + 8);
        u32* lrec = reinterpret_cast<u32*>(ids + KA_THREADS * KA_ITERS);
#pragma unroll
        for (int i = 0; i < KA_ITERS; ++i)
            if (bv[i] >= 0) ids[hist[bv[i]] + rk[i]] = (u16)(t + i * KA_THREADS);
        __syncthreads();
        int cs0[KA_ITERS], cn[KA_ITERS], rnk[KA_ITERS];
        int mxn = 0;
#pragma unroll
        for (int i = 0; i < KA_ITERS; ++i) {
            cs0[i] = 0; cn[i] = 0; rnk[i] = 0;
            if (bv[i] >= 0) { cs0[i] = hist[bv[i]]; cn[i] = hist[bv[i] + 1] - cs0[i]; }
            mxn = max(mxn, cn[i]);
        }
#pragma clang loop unroll(disable) vectorize(disable) interleave(disable)
        for (int q = 0; q < mxn; ++q) {
#pragma unroll
            for (int i = 0; i < KA_ITERS; ++i) {
                int v = (int)ids[cs0[i] + (q < cn[i] ? q : 0)];
                rnk[i] += (q < cn[i] && v < t + i * KA_THREADS) ? 1 : 0;
            }
        }
#pragma unroll
        for (int i = 0; i < KA_ITERS; ++i) {
            if (bv[i] >= 0) {
                u32* q = lrec + (cs0[i] + rnk[i]) * RW;
                q[0] = ((u32)sv[i] << 3) | (u32)lo[i];
                q[1] = eav[i].x; q[2] = eav[i].y;
            }
        }
        __syncthreads();
        {
            const int CHB = (CH * RW + 3) >> 2;
            const int nq = ((e1 - e0) * RW + 3) >> 2;
            uint4* og = reinterpret_cast<uint4*>(rec_g) + (size_t)bid * CHB;
            const uint4* lq4 = reinterpret_cast<const uint4*>(lrec);
#pragma unroll
            for (int i = 0; i < KA_ITERS * RW / 4; ++i) {
                int p = t + i * KA_THREADS;
                if (p < nq) og[p] = lq4[p];
            }
        }
        return;
    }
    bid -= G;
    if (bid < nGemm) {
        {
            int i = bid * KA_THREADS + t;
            if (i < D * D) {
                int k = i >> 7, c = i & 127;
                whT[(size_t)c * D + k] = wh[D * D + i];
            }
            if (t < 128 && bid * 128 + t < N) {
                const u16* pp = pos + (size_t)(bid * 128 + t) * 3;
                pos8[bid * 128 + t] = make_uint2((u32)pp[0] | ((u32)pp[1] << 16), (u32)pp[2]);
            }
            if (bid == nGemm - 1 && t < 192) {
                const int gq = t >> 6, tp = (t >> 4) & 3, c16 = t & 15;
                u32 o[4];
#pragma unroll
                for (int u = 0; u < 2; ++u) {
                    const int c = tp * 32 + (c16 >> 2) * 8 + u * 4 + (c16 & 3);
                    u32 hi[5], lo[5];
#pragma unroll
                    for (int j = 0; j < 5; ++j) {
                        float w = bf2f(we[(size_t)(256 + j) * D + c]) * NLOG2E;
                        u16 hh = f2bf(w);
                        hi[j] = hh; lo[j] = f2bf(w - bf2f(hh));
                    }
                    o[u * 2] = (gq == 0) ? (hi[0] | (hi[1] << 16)) : (gq == 1) ? (hi[4] | (hi[0] << 16)) : (lo[0] | (lo[1] << 16));
                    o[u * 2 + 1] = (gq == 0) ? (hi[2] | (hi[3] << 16)) : (gq == 1) ? lo[4] : (lo[2] | (lo[3] << 16));
                }
                w5g[t] = make_uint4(o[0], o[1], o[2], o[3]);
            }
        }
        {
            const int c2 = t & 63, kb = t >> 6;
            u32 wr[16];
#pragma unroll
            for (int j = 0; j < 16; ++j)
                wr[j] = *reinterpret_cast<const u32*>(we + (size_t)(kb * 16 + j) * D + c2 * 2);
#pragma unroll
            for (int sb = 0; sb < 2; ++sb) {
                int kk = kb * 16 + sb * 8;
                int half = kk >> 7, k = kk & 127;
                const u32* w = &wr[sb * 8];
                uint4 lo4 = make_uint4((w[0] & 0xffffu) | (w[1] << 16), (w[2] & 0xffffu) | (w[3] << 16),
                                       (w[4] & 0xffffu) | (w[5] << 16), (w[6] & 0xffffu) | (w[7] << 16));
                uint4 hi4 = make_uint4((w[0] >> 16) | (w[1] & 0xffff0000u), (w[2] >> 16) | (w[3] & 0xffff0000u),
                                       (w[4] >> 16) | (w[5] & 0xffff0000u), (w[6] >> 16) | (w[7] & 0xffff0000u));
                *reinterpret_cast<uint4*>(&smem[(half * D + c2 * 2) * (D + 8) + k]) = lo4;
                *reinterpret_cast<uint4*>(&smem[(half * D + c2 * 2 + 1) * (D + 8) + k]) = hi4;
            }
        }
        {
            const int c2 = t & 63, kb = t >> 6;
            u32 w[8];
#pragma unroll
            for (int j = 0; j < 8; ++j)
                w[j] = *reinterpret_cast<const u32*>(wh + (size_t)(kb * 8 + j) * D + c2 * 2);
            int k = kb * 8;
            uint4 lo4 = make_uint4((w[0] & 0xffffu) | (w[1] << 16), (w[2] & 0xffffu) | (w[3] << 16),
                                   (w[4] & 0xffffu) | (w[5] << 16), (w[6] & 0xffffu) | (w[7] << 16));
            uint4 hi4 = make_uint4((w[0] >> 16) | (w[1] & 0xffff0000u), (w[2] >> 16) | (w[3] & 0xffff0000u),
                                   (w[4] >> 16) | (w[5] & 0xffff0000u), (w[6] >> 16) | (w[7] & 0xffff0000u));
            *reinterpret_cast<uint4*>(&smem[(2 * D + c2 * 2) * (D + 8) + k]) = lo4;
            *reinterpret_cast<uint4*>(&smem[(2 * D + c2 * 2 + 1) * (D + 8) + k]) = hi4;
        }
        __syncthreads();
        int lane = t & 63, wv = t >> 6;
        int e16 = lane & 15, g = lane >> 4;
        int rt = wv & 7;
        int half = wv >> 3;
        int n = bid * 128 + rt * 16 + e16;
        int nn = n < N ? n : N - 1;
        bf16x8 af[4];
#pragma unroll
        for (int ks = 0; ks < 4; ++ks)
            af[ks] = *reinterpret_cast<const bf16x8*>(h + (size_t)nn * D + ks * 32 + g * 8);
        const int wr0 = (e16 >> 2) * 8 + (e16 & 3);
#pragma unroll
        for (int jj = 0; jj < 6; ++jj) {
            const int jb = half * 6 + jj;
            f32x4 acc0 = {0.f, 0.f, 0.f, 0.f}, acc1 = {0.f, 0.f, 0.f, 0.f};
#pragma unroll
            for (int ks = 0; ks < 4; ++ks) {
                const u16* wp = &smem[(jb * 32 + wr0) * (D + 8) + ks * 32 + g * 8];
                bf16x8 w0 = *reinterpret_cast<const bf16x8*>(wp);
                bf16x8 w1 = *reinterpret_cast<const bf16x8*>(wp + 4 * (D + 8));
                acc0 = __builtin_amdgcn_mfma_f32_16x16x32_bf16(w0, af[ks], acc0, 0, 0, 0);
                acc1 = __builtin_amdgcn_mfma_f32_16x16x32_bf16(w1, af[ks], acc1, 0, 0, 0);
            }
            const float sc = (jb < 8) ? NLOG2E : 1.0f;
            uint4 o = make_uint4(cvt_pk((f32x2){acc0[0] * sc, acc0[1] * sc}), cvt_pk((f32x2){acc0[2] * sc, acc0[3] * sc}),
                                 cvt_pk((f32x2){acc1[0] * sc, acc1[1] * sc}), cvt_pk((f32x2){acc1[2] * sc, acc1[3] * sc}));
            if (n < N) {
                u16* op = ABU + ((size_t)(jb >> 2) * N + n) * D + (jb & 3) * 32 + g * 8;
                if ((jb >> 2) == 1) *reinterpret_cast<uint4*>(op) = o;
                else st_nt(op, o);
            }
        }
        return;
    }
}

struct KbSmem {
    uint4 rec[KB_WAVES][CAPR];
    uint2 geo[KB_WAVES][CAPR];
    uint2 lut[8];
    uint4 At[KB_WAVES][2][64];
    uint4 w5s[3][64];
    float macc[KB_WAVES * NPW][AST];
    float pacc[KB_WAVES][NPW][4];
    float posl[KB_WAVES][NPW + 3][4];
    float wxs[D];
    uint2 M[KB_WAVES][16][4];
    int nt[KB_WAVES][4];
};

__global__ void __launch_bounds__(KB_THREADS, 3)
k_main(const u16* __restrict__ h, const u16* __restrict__ pos,
       const u16* __restrict__ wx,
       const u32* __restrict__ rec_g, const u16* __restrict__ tab,
       const u16* __restrict__ ABU,
       const u16* __restrict__ whT, const uint4* __restrict__ w5g, const uint2* __restrict__ pos8,
       u16* __restrict__ out_h, u16* __restrict__ out_pos,
       unsigned long long mNBW, int N, int G, int CH, int NBW) {
    __shared__ KbSmem S;
    const u16* __restrict__ Bg = ABU + (size_t)N * D;
    const u16* __restrict__ Ug = ABU + (size_t)2 * N * D;
    const int t = threadIdx.x, lane = t & 63;
    const int wv = __builtin_amdgcn_readfirstlane(t >> 6);
    const int e16 = lane & 15, g = lane >> 4;
    const int ec = (e16 >> 2) * 8 + (e16 & 3);
    const int nwg = NBW / KB_WAVES, per8 = nwg >> 3;
    const int bx = ((int)blockIdx.x < per8 * 8) ? ((int)(blockIdx.x & 7) * per8 + (int)(blockIdx.x >> 3)) : (int)blockIdx.x;
    const int wb = bx * KB_WAVES + wv;
    const int N0 = node_start(bx * KB_WAVES, N, NBW, mNBW);
    const int N1 = node_start(bx * KB_WAVES + KB_WAVES, N, NBW, mNBW);
    const int n0 = node_start(wb, N, NBW, mNBW);
    const int nn = node_start(wb + 1, N, NBW, mNBW) - n0;
    const int rowb = n0 - N0;

    int so[GI_MAX], sc[GI_MAX];
    u16 (*tabs)[5] = reinterpret_cast<u16 (*)[5]>(&S.M[0][0][0]);
    auto fetch_tab = [&]() {
        if (t < G) {
            const u16* tp = tab + (size_t)t * (NBW + 4) + bx * KB_WAVES;
            uint2 v = *reinterpret_cast<const uint2*>(tp);
            u32 v4 = tp[4];
            tabs[t][0] = (u16)v.x; tabs[t][1] = (u16)(v.x >> 16);
            tabs[t][2] = (u16)v.y; tabs[t][3] = (u16)(v.y >> 16);
            tabs[t][4] = (u16)v4;
        }
    };
    auto load_tab = [&]() {
        int lo_ = lane;
        asm volatile("" : "+v"(lo_));
#pragma unroll
        for (int ci = 0; ci < GI_MAX; ++ci) {
            int c = lo_ + 64 * ci;
            so[ci] = 0; sc[ci] = 0;
            if (c < G) {
                so[ci] = tabs[c][wv];
                sc[ci] = tabs[c][wv + 1];
            }
        }
    };
    auto load_tab_g = [&]() {
        int lo_ = lane;
        asm volatile("" : "+v"(lo_));
#pragma unroll
        for (int ci = 0; ci < GI_MAX; ++ci) {
            int c = lo_ + 64 * ci;
            so[ci] = 0; sc[ci] = 0;
            if (c < G) {
                const u16* tp = tab + (size_t)c * (NBW + 4) + wb;
                so[ci] = tp[0];
                sc[ci] = tp[1];
            }
        }
    };
    fetch_tab();
    u32 a0[NPW];
#pragma unroll
    for (int j = 0; j < NPW; ++j) {
        a0[j] = 0u;
        if (j < nn) a0[j] = *reinterpret_cast<const u32*>(ABU + (size_t)(n0 + j) * D + lane * 2);
    }
    const uint4 w40 = w5g[lane], w41 = w5g[lane + 64], w42 = w5g[lane + 128];
    u16 wx0 = wx[lane], wx1 = wx[lane + 64];
    u16 pz16 = 0;
    {
        int n = lane >> 2, cpt = lane & 3;
        if (n < nn && cpt < 3) pz16 = pos[(n0 + n) * 3 + cpt];
    }
    const int NR = N1 - N0;
    uint4 hv4[2], uv4[2];
#pragma unroll
    for (int rt = 0; rt < 2; ++rt) {
        int lr = rt * 16 + e16;
        size_t o = (size_t)(N0 + (lr < NR ? lr : 0)) * D + wv * 32 + g * 8;
        hv4[rt] = *reinterpret_cast<const uint4*>(h + o);
        uv4[rt] = *reinterpret_cast<const uint4*>(Ug + o);
    }
    auto stage = [&]() {
        {
            const int r = (lane * 2) & 31;
            uint2* ap = reinterpret_cast<uint2*>(&S.At[wv][0][(lane >> 4) * 16 + (r >> 3) * 4 + (r & 3)]) + ((r >> 2) & 1);
            ap[0] = make_uint2((a0[0] & 0xffffu) | (a0[1] << 16), (a0[2] & 0xffffu) | (a0[3] << 16));
            ap[2] = make_uint2((a0[0] >> 16) | (a0[1] & 0xffff0000u), (a0[2] >> 16) | (a0[3] & 0xffff0000u));
            ap[128] = make_uint2(a0[4] & 0xffffu, 0u);
            ap[130] = make_uint2(a0[4] >> 16, 0u);
        }
#pragma unroll
        for (int j = 0; j < NPW; ++j)
            if (j < nn) { S.macc[rowb + j][lane] = 0.f; S.macc[rowb + j][64 + lane] = 0.f; }
        uint4* wq = &S.w5s[0][0] + lane;
        wq[0] = w40; wq[64] = w41; wq[128] = w42;
        S.wxs[lane] = bf2f(wx0);
        S.wxs[lane + 64] = bf2f(wx1);
        if (lane < (NPW + 3) * 4) S.posl[wv][lane >> 2][lane & 3] = bf2f(pz16);
        if (lane < NPW * 4) S.pacc[wv][lane >> 2][lane & 3] = 0.f;
        if (lane < 16) {
            const int n = lane >> 1, w = lane & 1;
            reinterpret_cast<u32*>(S.lut)[lane] = (n < 5 && ((n & 3) >> 1) == w) ? (0x3F80u << (16 * (n & 1))) : 0u;
        }
    };
    const unsigned long long ltmask = (1ull << lane) - 1ull;
    const int CHB4 = ((CH * RW + 3) >> 2) << 2;
    uint4* rec = S.rec[wv];
    u32* key = reinterpret_cast<u32*>(S.rec[wv]);

    __syncthreads();
    load_tab();
    __syncthreads();
    f32x4 acc2[8];
    int dHead = 0, hflags = 0;
    float hm[8], hp0, hp1, hp2;
    auto reduce = [&]() __attribute__((always_inline)) {
        float wxc[8];
#pragma unroll
        for (int tt = 0; tt < 8; ++tt) wxc[tt] = S.wxs[(tt >> 1) * 32 + (tt & 1) * 4 + ec];
        float pv0 = 0.f, pv1 = 0.f, pv2 = 0.f;
#pragma unroll
        for (int tt = 0; tt < 8; ++tt) {
            hm[tt] = acc2[tt][0] * NLN2;
            pv0 += acc2[tt][1] * wxc[tt];
            pv1 += acc2[tt][2] * wxc[tt];
            pv2 += acc2[tt][3] * wxc[tt];
            acc2[tt] = (f32x4){0.f, 0.f, 0.f, 0.f};
        }
        hp0 = rsum16(pv0) * NLN2; hp1 = rsum16(pv1) * NLN2; hp2 = rsum16(pv2) * NLN2;
    };
    auto commit = [&](int d, auto accc) __attribute__((always_inline)) {
        constexpr bool ACC = decltype(accc)::value;
        const int bk = (d >> 16) & 3;
        const int nl = ((d >> 18) & 1) * 4 + g;
        const int rb = (d >> 20) & 31, nb = (d >> 25) & 7;
        const bool on = nl < nb;
        float* mrow = &S.macc[rb + (on ? nl : 0)][ec];
        float* prow = &S.pacc[bk][on ? nl : 0][0];
        float old[8], po[3];
        if (ACC) {
#pragma unroll
            for (int tt = 0; tt < 8; ++tt) old[tt] = mrow[(tt >> 1) * 32 + (tt & 1) * 4];
            po[0] = prow[0]; po[1] = prow[1]; po[2] = prow[2];
            CBAR();
        }
        if (on) {
#pragma unroll
            for (int tt = 0; tt < 8; ++tt) mrow[(tt >> 1) * 32 + (tt & 1) * 4] = ACC ? old[tt] + hm[tt] : hm[tt];
            if (e16 == 0) {
                prow[0] = (ACC ? po[0] : 0.f) + hp0;
                prow[1] = (ACC ? po[1] : 0.f) + hp1;
                prow[2] = (ACC ? po[2] : 0.f) + hp2;
            }
        }
    };
    auto flush = [&](int d, auto accc) __attribute__((always_inline)) { reduce(); commit(d, accc); };
    auto finish = [&]() __attribute__((always_inline)) {
        const std::integral_constant<bool, true> accc;
        __syncthreads();
        for (int w = 1; w < KB_WAVES; ++w) {
            if ((hflags & 1) && (wv == w || !(hflags & 2))) commit(dHead, accc);
            __syncthreads();
            if (!(hflags & 2)) break;
        }
    };
    auto round = [&](const int base, auto firstc) __attribute__((always_inline)) -> bool {
        constexpr bool FIRST = decltype(firstc)::value;
        int T;
        int fo[GI_MAX];
        {
            int tot = 0;
#pragma unroll
            for (int ci = 0; ci < GI_MAX; ++ci) { sc[ci] -= so[ci]; tot += sc[ci]; }
            int x = tot;
            int ls = lane;
            asm volatile("" : "+v"(ls));
#pragma unroll
            for (int o = 1; o < 64; o <<= 1) {
                int y = __builtin_amdgcn_ds_bpermute((ls - o) << 2, x);
                if (ls >= o) x += y;
            }
            T = __builtin_amdgcn_readlane(x, 63);
            int off = x - tot;
#pragma unroll
            for (int ci = 0; ci < GI_MAX; ++ci) { fo[ci] = off; off += sc[ci]; }
        }
        const int Tr = max(0, min(CAP, T - base));
        int lo2 = lane;
        asm volatile("" : "+v"(lo2));
#pragma unroll
        for (int ci = 0; ci < GI_MAX; ++ci) {
            int gs = (lo2 + 64 * ci) * CHB4 + so[ci] * RW;
            for (int q = 0; q < sc[ci]; ++q) {
                int fp = fo[ci] + q - base;
                if (fp >= 0 && fp < CAP) key[fp] = (u32)(gs + q * RW);
            }
        }
        CBAR();
        uint4 r[RPL];
        uint2 ps[RPL];
#pragma unroll
        for (int i = 0; i < RPL; ++i) {
            int p = lane + 64 * i;
            r[i] = make_uint4(0xffffffffu, 0u, 0u, 0u);
            if (p < Tr) {
                const u32* rp = rec_g + key[p];
                r[i] = make_uint4(rp[0], rp[1], rp[2], 0u);
            }
        }
        if (FIRST) stage();
#pragma unroll
        for (int i = 0; i < RPL; ++i) {
            ps[i] = make_uint2(0u, 0u);
            if (r[i].x != 0xffffffffu) ps[i] = pos8[r[i].x >> 3];
        }
        int posn[RPL];
        int cst = 0, cnt4 = 0, c0 = 0, g1s = 0;
#pragma unroll
        for (int i = 0; i < RPL; ++i) posn[i] = 0;
#pragma unroll
        for (int j = 0; j < NPW; ++j) {
            if (j == 4) { c0 = cst; g1s = (cst + 15) & ~15; cst = g1s; }
            int c = 0;
#pragma unroll
            for (int i = 0; i < RPL; ++i) {
                bool mt = (r[i].x != 0xffffffffu) && ((int)(r[i].x & 7u) == j);
                unsigned long long m = __ballot(mt);
                if (mt) posn[i] = cst + c + __popcll(m & ltmask);
                c += __popcll(m);
            }
            if (j == 4) cnt4 = c;
            cst += c;
        }
        const int g1e = (g1s + cnt4 + 15) & ~15;
        CBAR();
#pragma unroll
        for (int i = 0; i < RPL; ++i)
            if (r[i].x != 0xffffffffu) {
                const float4 pn = *reinterpret_cast<const float4*>(&S.posl[wv][r[i].x & 7u][0]);
                float dx = pn.x - __uint_as_float(ps[i].x << 16), dy = pn.y - __uint_as_float(ps[i].x & 0xffff0000u);
                float dz = pn.z - __uint_as_float(ps[i].y << 16);
                float d2v = dx * dx + dy * dy + dz * dz;
                u16 d2h = f2bf(d2v);
                u16 d2l = f2bf(d2v - bf2f(d2h));
                rec[posn[i]] = make_uint4((u32)d2h | (r[i].y << 16), (r[i].y >> 16) | (r[i].z << 16), (r[i].z >> 16) | ((u32)d2l << 16), r[i].x);
                S.geo[wv][posn[i]] = make_uint2(0x3F80u | ((u32)f2bf(dx) << 16), pack2(dy, dz));
            }
        if (lane < 16) {
            if (c0 + lane < g1s) { rec[c0 + lane] = make_uint4(0u, 0u, 0u, 7u); S.geo[wv][c0 + lane] = make_uint2(0u, 0u); }
            if (g1s + cnt4 + lane < g1e) { rec[g1s + cnt4 + lane] = make_uint4(0u, 0u, 0u, 7u); S.geo[wv][g1s + cnt4 + lane] = make_uint2(0u, 0u); }
        }
        if (lane == 0) {
            S.nt[wv][0] = g1s >> 4;
            S.nt[wv][1] = g1e >> 4;
            S.nt[wv][2] = (base + CAP < T) ? 1 : 0;
        }
        __syncthreads();

        int sb1, sb2, sb3, sb4, sb5, sb6, sb7, TT, anymore;
        {
            const int4 q0 = *reinterpret_cast<const int4*>(&S.nt[0][0]);
            const int4 q1 = *reinterpret_cast<const int4*>(&S.nt[1][0]);
            const int4 q2 = *reinterpret_cast<const int4*>(&S.nt[2][0]);
            const int4 q3 = *reinterpret_cast<const int4*>(&S.nt[3][0]);
            sb1 = __builtin_amdgcn_readfirstlane(q0.x);
            sb2 = __builtin_amdgcn_readfirstlane(q0.y);
            sb3 = sb2 + __builtin_amdgcn_readfirstlane(q1.x);
            sb4 = sb2 + __builtin_amdgcn_readfirstlane(q1.y);
            sb5 = sb4 + __builtin_amdgcn_readfirstlane(q2.x);
            sb6 = sb4 + __builtin_amdgcn_readfirstlane(q2.y);
            sb7 = sb6 + __builtin_amdgcn_readfirstlane(q3.x);
            TT = sb6 + __builtin_amdgcn_readfirstlane(q3.y);
            anymore = __builtin_amdgcn_readfirstlane(q0.z | q1.z | q2.z | q3.z);
        }
        auto seg_of = [&](int x) __attribute__((always_inline)) {
            return (x >= sb1) + (x >= sb2) + (x >= sb3) + (x >= sb4) + (x >= sb5) + (x >= sb6) + (x >= sb7);
        };
        auto is_start = [&](int x) __attribute__((always_inline)) {
            return (x == 0) | (x == sb1) | (x == sb2) | (x == sb3) | (x == sb4) | (x == sb5) | (x == sb6) | (x == sb7);
        };
        auto seg_end = [&](int x) __attribute__((always_inline)) {
            int r = TT;
            r = (x < sb7) ? +sb7 : +r; r = (x < sb6) ? +sb6 : +r; r = (x < sb5) ? +sb5 : +r; r = (x < sb4) ? +sb4 : +r;
            r = (x < sb3) ? +sb3 : +r; r = (x < sb2) ? +sb2 : +r; r = (x < sb1) ? +sb1 : +r;
            return r;
        };
        const int ts = (TT * wv) >> 2, te = (TT * (wv + 1)) >> 2;
        const int nst = te - ts;
        const bool has_head = (nst > 0) && !is_start(ts);
        const int th = has_head ? min(te, seg_end(ts)) : +ts;
        bool serial = false;
        {
            const int s1 = TT >> 2, s2 = (TT * 2) >> 2, s3 = (TT * 3) >> 2;
            serial = (!is_start(s1) && seg_end(s1) > s2) || (!is_start(s2) && seg_end(s2) > s3);
        }
        int lq = lane;
        asm volatile("" : "+v"(lq));
        int descv;
        {
            const int ns1 = node_start(bx * KB_WAVES + 1, N, NBW, mNBW) - N0;
            const int ns2 = node_start(bx * KB_WAVES + 2, N, NBW, mNBW) - N0;
            const int ns3 = node_start(bx * KB_WAVES + 3, N, NBW, mNBW) - N0;
            const int ns4 = N1 - N0;
            const int nh = te - th;
            const int tl = (lq < nh) ? (th + lq) : (ts + lq - nh);
            const int k = seg_of(tl);
            const int b = k >> 1;
            const int cb = (b == 0) ? 0 : (b == 1) ? +sb2 : (b == 2) ? +sb4 : +sb6;
            const int r0 = (b == 0) ? 0 : (b == 1) ? +ns1 : (b == 2) ? +ns2 : +ns3;
            const int r1 = (b == 0) ? +ns1 : (b == 1) ? +ns2 : (b == 2) ? +ns3 : +ns4;
            const int fl = (lq < nh) && ((tl + 1 == te) || is_start(tl + 1));
            descv = ((b * CAPR + (tl - cb) * 16) * 16) | (b << 16) | ((k & 1) << 18) | (fl << 19) | (r0 << 20) | ((r1 - r0) << 25);
        }
        bf16x4 sel;
        {
            const int dl = (lq & 15) - 4 * (lq >> 4);
            unsigned long long v = (dl >= 0 && dl < 4) ? (0x3F80ull << (16 * dl)) : 0ull;
            sel = as_bf16x4(make_uint2((u32)v, (u32)(v >> 32)));
        }
        const char* w5b = (g < 3) ? reinterpret_cast<const char*>(&S.w5s[g][e16]) : reinterpret_cast<const char*>(&S.At[0][0][e16]);
        const int w5m = (g < 3) ? 0 : (int)sizeof(S.At[0][0]);
        const char* recb = reinterpret_cast<const char*>(&S.rec[0][e16]);
        const char* geob = reinterpret_cast<const char*>(&S.geo[0][e16]);
        const char* Bb = reinterpret_cast<const char*>(Bg);
        const u32 g16 = (u32)g * 16u;
        const char* xrb = recb + ((g == 1) ? 8 : 0);
        const u32 psel = (g == 1) ? 0x0c0c0504u : 0x03020100u;
        bf16x8 gA[4], gB[4];
        u32 pA0 = 0, pB0 = 0;
        auto issue = [&](int d, bf16x8 (&gat)[4], u32& q0) __attribute__((always_inline)) {
            q0 = reinterpret_cast<const uint4*>(recb + (d & 0xffff))->w;
            const u32 off = ((q0 >> 3) << 8) + g16;
#pragma unroll
            for (int tp = 0; tp < 4; ++tp)
                gat[tp] = *reinterpret_cast<const bf16x8*>(Bb + off + tp * 64);
        };
#pragma unroll
        for (int tt = 0; tt < 8; ++tt) acc2[tt] = (f32x4){0.f, 0.f, 0.f, 0.f};
        auto sgi = [&](int d) __attribute__((always_inline)) { return ((d >> 15) & 6) | ((d >> 18) & 1); };
        auto compute = [&](int d, bf16x8 (&gat)[4], u32 q0) __attribute__((always_inline)) {
            const int bg = sgi(d);
            const u32 nd = q0 & 7u;
            const char* xp = (g < 3) ? (xrb + (d & 0xffff)) : (reinterpret_cast<const char*>(S.lut) + (nd << 3));
            uint2 xu = *reinterpret_cast<const uint2*>(xp);
            const uint2 gq = *reinterpret_cast<const uint2*>(geob + ((d & 0xffff) >> 1));
            const uint4* w5p = reinterpret_cast<const uint4*>(w5b + w5m * bg);
            S.M[wv][e16][g] = ((nd & 3u) == (u32)g) ? gq : make_uint2(0u, 0u);
            xu.y = __builtin_amdgcn_perm(xu.x, xu.y, psel);
            const bf16x4 x4 = as_bf16x4(xu);
            asm volatile("s_waitcnt lgkmcnt(0)" ::: "memory");
            u32 sw01, sw23;
            {
                const u16* wp = reinterpret_cast<const u16*>(&S.M[wv][g * 4][0]) + e16;
                sw01 = (u32)wp[0] | ((u32)wp[16] << 16);
                sw23 = (u32)wp[32] | ((u32)wp[48] << 16);
            }
            const bf16x4 sop = as_bf16x4(make_uint2(sw01, sw23));
#pragma unroll
            for (int tp = 0; tp < 4; ++tp) {
                const uint4 w5 = w5p[tp * 16];
                const u32x4 gq4 = *reinterpret_cast<const u32x4*>(&gat[tp]);
#pragma unroll
                for (int u = 0; u < 2; ++u) {
                    const int tt = tp * 2 + u;
                    f32x4 acc = __builtin_amdgcn_mfma_f32_16x16x16bf16_1k(x4, as_bf16x4(u ? make_uint2(w5.z, w5.w) : make_uint2(w5.x, w5.y)),
                                                                          (f32x4){0.f, 0.f, 0.f, 0.f}, 0, 0, 0);
                    acc = __builtin_amdgcn_mfma_f32_16x16x16bf16_1k(as_bf16x4(make_uint2(gq4[u * 2], gq4[u * 2 + 1])), sel, acc, 0, 0, 0);
                    float e0 = __builtin_amdgcn_exp2f(acc[0]), e1 = __builtin_amdgcn_exp2f(acc[1]);
                    float e2 = __builtin_amdgcn_exp2f(acc[2]), e3 = __builtin_amdgcn_exp2f(acc[3]);
                    e0 += 1.f; e1 += 1.f; e2 += 1.f; e3 += 1.f;
                    asm("" : "+v"(e0)); asm("" : "+v"(e1)); asm("" : "+v"(e2)); asm("" : "+v"(e3));
                    float q0 = acc[0] * __builtin_amdgcn_rcpf(e0), q1 = acc[1] * __builtin_amdgcn_rcpf(e1);
                    float q2 = acc[2] * __builtin_amdgcn_rcpf(e2), q3 = acc[3] * __builtin_amdgcn_rcpf(e3);
                    asm("" : "+v"(q0)); asm("" : "+v"(q1)); asm("" : "+v"(q2)); asm("" : "+v"(q3));
                    u32 k01 = cvt_pk((f32x2){q0, q1}), k23 = cvt_pk((f32x2){q2, q3});
                    bf16x4 mb = as_bf16x4(make_uint2(k01, k23));
                    acc2[tt] = __builtin_amdgcn_mfma_f32_16x16x16bf16_1k(sop, mb, acc2[tt], 0, 0, 0);
                }
            }
        };
        const std::integral_constant<bool, !FIRST> dirc;
        if (nst > 0) {
            issue(__builtin_amdgcn_readlane(descv, 0), gA, pA0);
            int i = 0;
            for (; i + 1 < nst; i += 2) {
                {
                    const int rem = nst - i;
                    if (rem >= 5) __builtin_amdgcn_s_setprio(3);
                    else if (rem >= 4) __builtin_amdgcn_s_setprio(2);
                    else if (rem >= 3) __builtin_amdgcn_s_setprio(1);
                    else __builtin_amdgcn_s_setprio(0);
                }
                const int dA = __builtin_amdgcn_readlane(descv, i);
                const int dB = __builtin_amdgcn_readlane(descv, i + 1);
                const int dC = __builtin_amdgcn_readlane(descv, min(i + 2, nst - 1));
                issue(dB, gB, pB0);
                compute(dA, gA, pA0);
                if (dA & (1 << 19)) flush(dA, dirc);
                issue(dC, gA, pA0);
                compute(dB, gB, pB0);
                if (dB & (1 << 19)) flush(dB, dirc);
            }
            if (i < nst) {
                const int dA = __builtin_amdgcn_readlane(descv, i);
                compute(dA, gA, pA0);
                if (dA & (1 << 19)) flush(dA, dirc);
            }
        }
        dHead = __builtin_amdgcn_readlane(descv, max(nst - 1, 0));
        hflags = (has_head ? 1 : 0) | (serial ? 2 : 0);
        reduce();
        return anymore != 0;
    };
    if (round(0, std::integral_constant<bool, true>())) {
        for (int base = CAP;; base += CAP) {
            finish();
            load_tab_g();
            if (!round(base, std::integral_constant<bool, false>())) break;
        }
    }

    int le = lane;
    asm volatile("" : "+v"(le));
    const int f16 = le & 15, gg = le >> 4;
    bf16x8 bw[2][4];
#pragma unroll
    for (int q = 0; q < 2; ++q)
#pragma unroll
        for (int ks = 0; ks < 4; ++ks)
            bw[q][ks] = *reinterpret_cast<const bf16x8*>(whT + (size_t)(wv * 32 + (f16 >> 2) * 8 + q * 4 + (f16 & 3)) * D + ks * 32 + gg * 8);
    finish();
    if (le < nn * 3) {
        int n = le / 3, cpt = le - n * 3;
        out_pos[(size_t)(n0 + n) * 3 + cpt] = f2bf(S.posl[wv][n][cpt] + S.pacc[wv][n][cpt]);
    }
#pragma unroll
    for (int rt = 0; rt < 2; ++rt) {
        if (rt * 16 < NR) {
            bf16x8 af[4];
            int lr = rt * 16 + f16;
#pragma unroll
            for (int ks = 0; ks < 4; ++ks) {
                const float* mp = &S.macc[lr < NR ? lr : 0][ks * 32 + gg * 8];
                af[ks] = as_bf16x8(make_uint4(pack2(mp[0], mp[1]), pack2(mp[2], mp[3]),
                                              pack2(mp[4], mp[5]), pack2(mp[6], mp[7])));
            }
            u32 ov[4];
#pragma unroll
            for (int q = 0; q < 2; ++q) {
                const u32 u0 = q ? uv4[rt].z : uv4[rt].x, u1 = q ? uv4[rt].w : uv4[rt].y;
                const u32 h0 = q ? hv4[rt].z : hv4[rt].x, h1 = q ? hv4[rt].w : hv4[rt].y;
                f32x4 acc = {__uint_as_float(u0 << 16), __uint_as_float(u0 & 0xffff0000u),
                             __uint_as_float(u1 << 16), __uint_as_float(u1 & 0xffff0000u)};
#pragma unroll
                for (int ks = 0; ks < 4; ++ks)
                    acc = __builtin_amdgcn_mfma_f32_16x16x32_bf16(bw[q][ks], af[ks], acc, 0, 0, 0);
                ov[q * 2] = pack2(__uint_as_float(h0 << 16) + silu_f(acc[0]), __uint_as_float(h0 & 0xffff0000u) + silu_f(acc[1]));
                ov[q * 2 + 1] = pack2(__uint_as_float(h1 << 16) + silu_f(acc[2]), __uint_as_float(h1 & 0xffff0000u) + silu_f(acc[3]));
            }
            if (lr < NR)
                *reinterpret_cast<uint4*>(out_h + (size_t)(N0 + lr) * D + wv * 32 + gg * 8) = make_uint4(ov[0], ov[1], ov[2], ov[3]);
        }
    }
}

extern "C" void kernel_launch(void* const* d_in, const int* in_sizes, int n_in,
                              void* d_out, int out_size, void* d_ws, size_t ws_size,
                              hipStream_t stream) {
    const u16* h   = (const u16*)d_in[0];
    const u16* pos = (const u16*)d_in[1];
    const u16* ea  = (const u16*)d_in[2];
    const int* src = (const int*)d_in[3];
    const int* dst = (const int*)d_in[4];
    const u16* we  = (const u16*)d_in[5];
    const u16* wx  = (const u16*)d_in[6];
    const u16* wh  = (const u16*)d_in[7];
    int N = in_sizes[0] / D;
    int E = in_sizes[3];
    u16* out_h = (u16*)d_out;
    u16* out_pos = out_h + (size_t)N * D;

    int G = 128;
    while ((E + G - 1) / G > KA_THREADS * KA_ITERS && G < 64 * GI_MAX) G += 64;
    int CH = (E + G - 1) / G;
    int NBW = 3072;
    while (NBW * NPW < N && NBW < NBW_MAX) NBW += 4;
    int nGemm = (N + 127) / 128;
    if (nGemm < D * D / KA_THREADS) nGemm = D * D / KA_THREADS;
    unsigned long long mN = (1ull << 40) / (unsigned long long)N + 1ull;
    unsigned long long mNBW = (1ull << 40) / (unsigned long long)NBW + 1ull;

    char* ws = (char*)d_ws;
    size_t off = 0;
    auto take = [&](size_t bytes) { char* p = ws + off; off += (bytes + 255) & ~(size_t)255; return p; };
    u32* rec_g = (u32*)take((size_t)G * ((CH * RW + 3) >> 2) * 16 + 64);
    u16* tab = (u16*)take((size_t)G * (NBW + 4) * 2);
    u16* ABU = (u16*)take((size_t)3 * N * D * 2);
    u16* whT = (u16*)take((size_t)D * D * 2);
    uint4* w5g = (uint4*)take((size_t)256 * 16);
    uint2* pos8 = (uint2*)take((size_t)N * 8);

    k_part<<<G + nGemm, KA_THREADS, 0, stream>>>(h, pos, src, dst, ea, we, wh, rec_g, tab, ABU, whT, w5g, pos8,
                                                      mN, mNBW, N, E, G, CH, NBW, nGemm);
    k_main<<<NBW / KB_WAVES, KB_THREADS, 0, stream>>>(h, pos, wx, rec_g, tab, ABU, whT, w5g, pos8,
                                                     out_h, out_pos, mNBW, N, G, CH, NBW);
}
```

```cpp
#include <hip/hip_runtime.h>
#include <hip/hip_bf16.h>
#include <stdint.h>
#include <type_traits>


typedef unsigned short u16;
typedef unsigned int u32;
typedef __attribute__((ext_vector_type(8))) short bf16x8;
typedef __attribute__((ext_vector_type(4))) float f32x4;
typedef __attribute__((ext_vector_type(4))) short bf16x4;
typedef __attribute__((ext_vector_type(2))) float f32x2;
typedef __attribute__((ext_vector_type(4))) unsigned int u32x4;
__device__ __forceinline__ void st_nt(void* p, uint4 v) { u32x4 x = {v.x, v.y, v.z, v.w}; __builtin_nontemporal_store(x, reinterpret_cast<u32x4*>(p)); }

#define D 128
#define KA_THREADS 1024
#define KA_ITERS 4
#define NBW_MAX 8192
#define KB_THREADS 256
#define KB_WAVES 4
#define NPW 5
#define RPL 4
#define CAP 240
#define CAPS 272
#define CAPR (CAP + 32)
#define RW 3
#define GI_MAX 2
#define AST (D + 4)
#define CBAR() asm volatile("" ::: "memory")
#define NLOG2E (-1.4426950408889634f)
#define NLN2 (-0.6931471805599453f)

__device__ __forceinline__ float bf2f(u16 v) { return __uint_as_float(((u32)v) << 16); }
__device__ __forceinline__ u16 f2bf(float f) {
    __hip_bfloat16 b = __float2bfloat16(f);
    return *reinterpret_cast<u16*>(&b);
}
__device__ __forceinline__ float silu_f(float x) {
    float e = __builtin_amdgcn_exp2f(x * NLOG2E);
    return x * __builtin_amdgcn_rcpf(1.0f + e);
}
__device__ __forceinline__ u32 pack2(float a, float b) {
    return (u32)f2bf(a) | ((u32)f2bf(b) << 16);
}
typedef __attribute__((ext_vector_type(2))) __bf16 bf16x2;
__device__ __forceinline__ u32 cvt_pk(f32x2 v) {
    bf16x2 p = __builtin_convertvector(v, bf16x2);
    return *reinterpret_cast<u32*>(&p);
}
__device__ __forceinline__ bf16x8 as_bf16x8(uint4 v) { return *reinterpret_cast<bf16x8*>(&v); }
__device__ __forceinline__ bf16x4 as_bf16x4(uint2 v) { return *reinterpret_cast<bf16x4*>(&v); }
template <int CTRL>
__device__ __forceinline__ float dpp_f(float x) {
    return __int_as_float(__builtin_amdgcn_update_dpp(0, __float_as_int(x), CTRL, 0xf, 0xf, true));
}
__device__ __forceinline__ float rsum16(float x) {
    x += dpp_f<0x128>(x);
    x += dpp_f<0x124>(x);
    x += dpp_f<0x122>(x);
    x += dpp_f<0x121>(x);
    return x;
}
template <int CTRL, int RM>
__device__ __forceinline__ int dpp_add(int x) { return x + __builtin_amdgcn_update_dpp(0, x, CTRL, RM, 0xf, false); }
__device__ __forceinline__ int wave_scan(int x) {
    x = dpp_add<0x111, 0xf>(x);
    x = dpp_add<0x112, 0xf>(x);
    x = dpp_add<0x114, 0xf>(x);
    x = dpp_add<0x118, 0xf>(x);
    x = dpp_add<0x142, 0xa>(x);
    x = dpp_add<0x143, 0xc>(x);
    return x;
}
__device__ __forceinline__ u32 mdiv(u32 x, unsigned long long m) {
    return (u32)(((unsigned long long)x * m) >> 40);
}
__device__ __forceinline__ int node_start(int b, int N, int NBW, unsigned long long mNBW) {
    return (int)mdiv((u32)b * (u32)N + (u32)NBW - 1u, mNBW);
}

__global__ void __launch_bounds__(KA_THREADS)
k_part(const u16* __restrict__ h, const u16* __restrict__ pos, const int* __restrict__ src, const int* __restrict__ dst,
       const u16* __restrict__ ea, const u16* __restrict__ we, const u16* __restrict__ wh,
       u32* __restrict__ rec_g, u16* __restrict__ tab, u16* __restrict__ ABU,
       u16* __restrict__ whT, uint4* __restrict__ w5g, uint2* __restrict__ pos8,
       unsigned long long mN, unsigned long long mNBW,
       int N, int E, int G, int CH, int NBW, int nGemm) {
    __shared__ __align__(16) u16 smem[(NBW_MAX + 8) * 2 + KA_THREADS * KA_ITERS + KA_THREADS * KA_ITERS * 8];
    __shared__ int wsum[KA_THREADS / 64];
    int t = threadIdx.x;
    int bid = blockIdx.x;
    if (bid < G) {
        int* hist = (int*)smem;
        for (int i = t; i < NBW; i += KA_THREADS) hist[i] = 0;
        int e0 = bid * CH;
        int e1 = min(E, e0 + CH);
        int sv[KA_ITERS], bv[KA_ITERS], rk[KA_ITERS], lo[KA_ITERS];
        uint2 eav[KA_ITERS];
#pragma unroll
        for (int i = 0; i < KA_ITERS; ++i) {
            int e = e0 + t + i * KA_THREADS;
            bv[i] = -1;
            if (e < e1) {
                int d = dst[e];
                sv[i] = src[e];
                eav[i] = *reinterpret_cast<const uint2*>(ea + (size_t)e * 4);
                int b = (int)mdiv((u32)d * (u32)NBW, mN);
                bv[i] = b;
                lo[i] = d - node_start(b, N, NBW, mNBW);
            }
        }
        __syncthreads();
#pragma unroll
        for (int i = 0; i < KA_ITERS; ++i)
            if (bv[i] >= 0) rk[i] = atomicAdd(&hist[bv[i]], 1);
        __syncthreads();
        const int per = (NBW + KA_THREADS - 1) / KA_THREADS;
        int b0 = t * per, b1 = min(NBW, b0 + per);
        int cnt = 0;
        for (int b = b0; b < b1; ++b) cnt += hist[b];
        int lane = t & 63, wv = t >> 6;
        int x = cnt;
#pragma unroll
        for (int o = 1; o < 64; o <<= 1) {
            int y = __shfl_up(x, o);
            if (lane >= o) x += y;
        }
        if (lane == 63) wsum[wv] = x;
        __syncthreads();
        int base = 0;
#pragma unroll
        for (int w = 0; w < KA_THREADS / 64; ++w) base += (w < wv) ? wsum[w] : 0;
        int off = base + x - cnt;
        u16* trow = tab + (size_t)bid * (NBW + 4);
        for (int b = b0; b < b1; ++b) {
            int c = hist[b];
            hist[b] = off;
            trow[b] = (u16)off;
            off += c;
        }
        if (t == KA_THREADS - 1) { trow[NBW] = (u16)(base + x); hist[NBW] = base + x; }
        __syncthreads();
        u16* ids = reinterpret_cast<u16*>(hist + NBW_MAX + 8);
        u32* lrec = reinterpret_cast<u32*>(ids + KA_THREADS * KA_ITERS);
#pragma unroll
        for (int i = 0; i < KA_ITERS; ++i)
            if (bv[i] >= 0) ids[hist[bv[i]] + rk[i]] = (u16)(t + i * KA_THREADS);
        __syncthreads();
        int cs0[KA_ITERS], cn[KA_ITERS], rnk[KA_ITERS];
        int mxn = 0;
#pragma unroll
        for (int i = 0; i < KA_ITERS; ++i) {
            cs0[i] = 0; cn[i] = 0; rnk[i] = 0;
            if (bv[i] >= 0) { cs0[i] = hist[bv[i]]; cn[i] = hist[bv[i] + 1] - cs0[i]; }
            mxn = max(mxn, cn[i]);
        }
#pragma clang loop unroll(disable) vectorize(disable) interleave(disable)
        for (int q = 0; q < mxn; ++q) {
#pragma unroll
            for (int i = 0; i < KA_ITERS; ++i) {
                int v = (int)ids[cs0[i] + (q < cn[i] ? q : 0)];
                rnk[i] += (q < cn[i] && v < t + i * KA_THREADS) ? 1 : 0;
            }
        }
#pragma unroll
        for (int i = 0; i < KA_ITERS; ++i) {
            if (bv[i] >= 0) {
                u32* q = lrec + (cs0[i] + rnk[i]) * RW;
                q[0] = ((u32)sv[i] << 3) | (u32)lo[i];
                q[1] = eav[i].x; q[2] = eav[i].y;
            }
        }
        __syncthreads();
        {
            const int CHB = (CH * RW + 3) >> 2;
            const int nq = ((e1 - e0) * RW + 3) >> 2;
            uint4* og = reinterpret_cast<uint4*>(rec_g) + (size_t)bid * CHB;
            const uint4* lq4 = reinterpret_cast<const uint4*>(lrec);
#pragma unroll
            for (int i = 0; i < KA_ITERS * RW / 4; ++i) {
                int p = t + i * KA_THREADS;
                if (p < nq) og[p] = lq4[p];
            }
        }
        return;
    }
    bid -= G;
    if (bid < nGemm) {
        {
            int i = bid * KA_THREADS + t;
            if (i < D * D) {
                const int k = i >> 7, c = i & 127;
                const int f16 = ((c >> 3) & 3) * 4 + (c & 3), q = (c >> 2) & 1;
                whT[((((c >> 5) * 2 + q) * 4 + (k >> 5)) * 64 + ((k >> 3) & 3) * 16 + f16) * 8 + (k & 7)] = wh[D * D + i];
            }
            if (t < 128 && bid * 128 + t < N) {
                const u16* pp = pos + (size_t)(bid * 128 + t) * 3;
                pos8[bid * 128 + t] = make_uint2((u32)pp[0] | ((u32)pp[1] << 16), (u32)pp[2]);
            }
            if (bid == nGemm - 1 && t < 192) {
                const int gq = t >> 6, tp = (t >> 4) & 3, c16 = t & 15;
                u32 o[4];
#pragma unroll
                for (int u = 0; u < 2; ++u) {
                    const int c = tp * 32 + (c16 >> 2) * 8 + u * 4 + (c16 & 3);
                    u32 hi[5], lo[5];
#pragma unroll
                    for (int j = 0; j < 5; ++j) {
                        float w = bf2f(we[(size_t)(256 + j) * D + c]) * NLOG2E;
                        u16 hh = f2bf(w);
                        hi[j] = hh; lo[j] = f2bf(w - bf2f(hh));
                    }
                    o[u * 2] = (gq == 0) ? (hi[0] | (hi[1] << 16)) : (gq == 1) ? (hi[4] | (hi[0] << 16)) : (lo[0] | (lo[1] << 16));
                    o[u * 2 + 1] = (gq == 0) ? (hi[2] | (hi[3] << 16)) : (gq == 1) ? lo[4] : (lo[2] | (lo[3] << 16));
                }
                w5g[t] = make_uint4(o[0], o[1], o[2], o[3]);
            }
        }
        {
            const int c2 = t & 63, kb = t >> 6;
            u32 wr[16];
#pragma unroll
            for (int j = 0; j < 16; ++j)
                wr[j] = *reinterpret_cast<const u32*>(we + (size_t)(kb * 16 + j) * D + c2 * 2);
#pragma unroll
            for (int sb = 0; sb < 2; ++sb) {
                int kk = kb * 16 + sb * 8;
                int half = kk >> 7, k = kk & 127;
                const u32* w = &wr[sb * 8];
                uint4 lo4 = make_uint4((w[0] & 0xffffu) | (w[1] << 16), (w[2] & 0xffffu) | (w[3] << 16),
                                       (w[4] & 0xffffu) | (w[5] << 16), (w[6] & 0xffffu) | (w[7] << 16));
                uint4 hi4 = make_uint4((w[0] >> 16) | (w[1] & 0xffff0000u), (w[2] >> 16) | (w[3] & 0xffff0000u),
                                       (w[4] >> 16) | (w[5] & 0xffff0000u), (w[6] >> 16) | (w[7] & 0xffff0000u));
                *reinterpret_cast<uint4*>(&smem[(half * D + c2 * 2) * (D + 8) + k]) = lo4;
                *reinterpret_cast<uint4*>(&smem[(half * D + c2 * 2 + 1) * (D + 8) + k]) = hi4;
            }
        }
        {
            const int c2 = t & 63, kb = t >> 6;
            u32 w[8];
#pragma unroll
            for (int j = 0; j < 8; ++j)
                w[j] = *reinterpret_cast<const u32*>(wh + (size_t)(kb * 8 + j) * D + c2 * 2);
            int k = kb * 8;
            uint4 lo4 = make_uint4((w[0] & 0xffffu) | (w[1] << 16), (w[2] & 0xffffu) | (w[3] << 16),
                                   (w[4] & 0xffffu) | (w[5] << 16), (w[6] & 0xffffu) | (w[7] << 16));
            uint4 hi4 = make_uint4((w[0] >> 16) | (w[1] & 0xffff0000u), (w[2] >> 16) | (w[3] & 0xffff0000u),
                                   (w[4] >> 16) | (w[5] & 0xffff0000u), (w[6] >> 16) | (w[7] & 0xffff0000u));
            *reinterpret_cast<uint4*>(&smem[(2 * D + c2 * 2) * (D + 8) + k]) = lo4;
            *reinterpret_cast<uint4*>(&smem[(2 * D + c2 * 2 + 1) * (D + 8) + k]) = hi4;
        }
        __syncthreads();
        int lane = t & 63, wv = t >> 6;
        int e16 = lane & 15, g = lane >> 4;
        int rt = wv & 7;
        int half = wv >> 3;
        int n = bid * 128 + rt * 16 + e16;
        int nn = n < N ? n : N - 1;
        bf16x8 af[4];
#pragma unroll
        for (int ks = 0; ks < 4; ++ks)
            af[ks] = *reinterpret_cast<const bf16x8*>(h + (size_t)nn * D + ks * 32 + g * 8);
        const int wr0 = (e16 >> 2) * 8 + (e16 & 3);
#pragma unroll
        for (int jj = 0; jj < 6; ++jj) {
            const int jb = half * 6 + jj;
            f32x4 acc0 = {0.f, 0.f, 0.f, 0.f}, acc1 = {0.f, 0.f, 0.f, 0.f};
#pragma unroll
            for (int ks = 0; ks < 4; ++ks) {
                const u16* wp = &smem[(jb * 32 + wr0) * (D + 8) + ks * 32 + g * 8];
                bf16x8 w0 = *reinterpret_cast<const bf16x8*>(wp);
                bf16x8 w1 = *reinterpret_cast<const bf16x8*>(wp + 4 * (D + 8));
                acc0 = __builtin_amdgcn_mfma_f32_16x16x32_bf16(w0, af[ks], acc0, 0, 0, 0);
                acc1 = __builtin_amdgcn_mfma_f32_16x16x32_bf16(w1, af[ks], acc1, 0, 0, 0);
            }
            const float sc = (jb < 8) ? NLOG2E : 1.0f;
            uint4 o = make_uint4(cvt_pk((f32x2){acc0[0] * sc, acc0[1] * sc}), cvt_pk((f32x2){acc0[2] * sc, acc0[3] * sc}),
                                 cvt_pk((f32x2){acc1[0] * sc, acc1[1] * sc}), cvt_pk((f32x2){acc1[2] * sc, acc1[3] * sc}));
            if (n < N) {
                u16* op = ABU + ((size_t)(jb >> 2) * N + n) * D + (jb & 3) * 32 + g * 8;
                if ((jb >> 2) == 1) *reinterpret_cast<uint4*>(op) = o;
                else st_nt(op, o);
            }
        }
        return;
    }
}

struct KbSmem {
    uint4 rec[KB_WAVES][CAPR];
    uint2 geo[KB_WAVES][CAPR];
    uint2 lut[8];
    uint4 At[KB_WAVES][2][64];
    uint4 w5s[3][64];
    float macc[KB_WAVES * NPW][AST];
    float pacc[KB_WAVES][NPW][4];
    float posl[KB_WAVES][NPW + 3][4];
    float wxs[D];
    uint4 T[KB_WAVES][64];
    int dt[KB_WAVES][18];
};

__global__ void __launch_bounds__(KB_THREADS, 3)
k_main(const u16* __restrict__ h, const u16* __restrict__ pos,
       const u16* __restrict__ wx,
       const u32* __restrict__ rec_g, const u16* __restrict__ tab,
       const u16* __restrict__ ABU,
       const u16* __restrict__ whT, const uint4* __restrict__ w5g, const uint2* __restrict__ pos8,
       u16* __restrict__ out_h, u16* __restrict__ out_pos,
       unsigned long long mNBW, int N, int G, int CH, int NBW) {
    __shared__ KbSmem S;
    asm volatile("" :: "s"(h), "s"(pos), "s"(wx), "s"(rec_g), "s"(tab), "s"(ABU), "s"(whT), "s"(w5g), "s"(pos8),
                 "s"(out_h), "s"(out_pos), "s"(mNBW), "s"(N), "s"(G), "s"(CH), "s"(NBW));
    const u16* __restrict__ Bg = ABU + (size_t)N * D;
    const u16* __restrict__ Ug = ABU + (size_t)2 * N * D;
    const int t = threadIdx.x, lane = t & 63;
    const int wv = __builtin_amdgcn_readfirstlane(t >> 6);
    const int e16 = lane & 15, g = lane >> 4;
    const int ec = (e16 >> 2) * 8 + (e16 & 3);
    const int nwg = NBW / KB_WAVES, per8 = nwg >> 3;
    int bx = (int)blockIdx.x;
    if (bx < per8 * 8) {
        int k = bx >> 3;
        if (per8 == 96) k = (k & 31) * 3 + (k >> 5);
        bx = (bx & 7) * per8 + k;
    }
    const int wb = bx * KB_WAVES + wv;
    const int N0 = node_start(bx * KB_WAVES, N, NBW, mNBW);
    const int N1 = node_start(bx * KB_WAVES + KB_WAVES, N, NBW, mNBW);
    const int n0 = node_start(wb, N, NBW, mNBW);
    const int nn = node_start(wb + 1, N, NBW, mNBW) - n0;
    const int rowb = n0 - N0;

    int so[GI_MAX], sc[GI_MAX];
    u16 (*tabs)[5] = reinterpret_cast<u16 (*)[5]>(&S.T[0][0]);
    uint2 tv = make_uint2(0u, 0u);
    u32 tv4 = 0;
    auto fetch_tab = [&]() {
        if (t < G) {
            const u16* tp = tab + (size_t)t * (NBW + 4) + bx * KB_WAVES;
            tv = *reinterpret_cast<const uint2*>(tp);
            tv4 = tp[4];
        }
    };
    auto put_tab = [&]() {
        if (t < G) {
            tabs[t][0] = (u16)tv.x; tabs[t][1] = (u16)(tv.x >> 16);
            tabs[t][2] = (u16)tv.y; tabs[t][3] = (u16)(tv.y >> 16);
            tabs[t][4] = (u16)tv4;
        }
    };
    auto load_tab = [&]() {
        int lo_ = lane;
        asm volatile("" : "+v"(lo_));
#pragma unroll
        for (int ci = 0; ci < GI_MAX; ++ci) {
            int c = lo_ + 64 * ci;
            so[ci] = 0; sc[ci] = 0;
            if (c < G) {
                so[ci] = tabs[c][wv];
                sc[ci] = tabs[c][wv + 1];
            }
        }
    };
    auto load_tab_g = [&]() {
        int lo_ = lane;
        asm volatile("" : "+v"(lo_));
#pragma unroll
        for (int ci = 0; ci < GI_MAX; ++ci) {
            int c = lo_ + 64 * ci;
            so[ci] = 0; sc[ci] = 0;
            if (c < G) {
                const u16* tp = tab + (size_t)c * (NBW + 4) + wb;
                so[ci] = tp[0];
                sc[ci] = tp[1];
            }
        }
    };
    fetch_tab();
    u32 a0[NPW];
#pragma unroll
    for (int j = 0; j < NPW; ++j) {
        a0[j] = *reinterpret_cast<const u32*>(ABU + (size_t)min(n0 + j, N - 1) * D + lane * 2);
    }
    const uint4 w40 = w5g[lane], w41 = w5g[lane + 64], w42 = w5g[lane + 128];
    u16 wx0 = wx[lane], wx1 = wx[lane + 64];
    const u16 pz16 = pos[min((n0 + (lane >> 2)) * 3 + (lane & 3), N * 3 - 1)];
    const int NR = N1 - N0;
    uint4 hv4[2], uv4[2];
    auto load_hu = [&]() __attribute__((always_inline)) {
#pragma unroll
        for (int rt = 0; rt < 2; ++rt) {
            int lr = rt * 16 + e16;
            size_t o = (size_t)(N0 + (lr < NR ? lr : 0)) * D + wv * 32 + g * 8;
            hv4[rt] = *reinterpret_cast<const uint4*>(h + o);
            uv4[rt] = *reinterpret_cast<const uint4*>(Ug + o);
        }
    };
    auto stage = [&]() {
#pragma unroll
        for (int j = 0; j < NPW; ++j) a0[j] = (j < nn) ? a0[j] : 0u;
        {
            const int r = (lane * 2) & 31;
            uint2* ap = reinterpret_cast<uint2*>(&S.At[wv][0][(lane >> 4) * 16 + (r >> 3) * 4 + (r & 3)]) + ((r >> 2) & 1);
            ap[0] = make_uint2((a0[0] & 0xffffu) | (a0[1] << 16), (a0[2] & 0xffffu) | (a0[3] << 16));
            ap[2] = make_uint2((a0[0] >> 16) | (a0[1] & 0xffff0000u), (a0[2] >> 16) | (a0[3] & 0xffff0000u));
            ap[128] = make_uint2(a0[4] & 0xffffu, 0u);
            ap[130] = make_uint2(a0[4] >> 16, 0u);
        }
#pragma unroll
        for (int j = 0; j < NPW; ++j)
            if (j < nn) { S.macc[rowb + j][lane] = 0.f; S.macc[rowb + j][64 + lane] = 0.f; }
        uint4* wq = &S.w5s[0][0] + lane;
        wq[0] = w40; wq[64] = w41; wq[128] = w42;
        S.wxs[lane] = bf2f(wx0);
        S.wxs[lane + 64] = bf2f(wx1);
        if (lane < (NPW + 3) * 4) S.posl[wv][lane >> 2][lane & 3] = ((lane >> 2) < nn && (lane & 3) < 3) ? bf2f(pz16) : 0.f;
        if (lane < NPW * 4) S.pacc[wv][lane >> 2][lane & 3] = 0.f;
        if (lane < 16) {
            const int n = lane >> 1, w = lane & 1;
            reinterpret_cast<u32*>(S.lut)[lane] = (n < 5 && ((n & 3) >> 1) == w) ? (0x3F80u << (16 * (n & 1))) : 0u;
        }
    };
    const int CHB4 = ((CH * RW + 3) >> 2) << 2;
    uint4* rec = S.rec[wv];
    u32* key = reinterpret_cast<u32*>(S.rec[wv]);

    put_tab();
    __syncthreads();
    load_tab();
    f32x4 acc2[8];
    int dHead = 0, hflags = 0;
    float hm[8], hp0, hp1, hp2;
    auto reduce = [&]() __attribute__((always_inline)) {
        float wxc[8];
#pragma unroll
        for (int tt = 0; tt < 8; ++tt) wxc[tt] = S.wxs[(tt >> 1) * 32 + (tt & 1) * 4 + ec];
        float pv0 = 0.f, pv1 = 0.f, pv2 = 0.f;
#pragma unroll
        for (int tt = 0; tt < 8; ++tt) {
            hm[tt] = acc2[tt][0] * NLN2;
            pv0 += acc2[tt][1] * wxc[tt];
            pv1 += acc2[tt][2] * wxc[tt];
            pv2 += acc2[tt][3] * wxc[tt];
            acc2[tt] = (f32x4){0.f, 0.f, 0.f, 0.f};
        }
        hp0 = rsum16(pv0) * NLN2; hp1 = rsum16(pv1) * NLN2; hp2 = rsum16(pv2) * NLN2;
    };
    auto commit = [&](int d, auto accc) __attribute__((always_inline)) {
        constexpr bool ACC = decltype(accc)::value;
        const int bk = (d >> 16) & 3;
        const int nl = ((d >> 18) & 1) * 4 + g;
        const int rb = (d >> 20) & 31, nb = (d >> 25) & 7;
        const bool on = nl < nb;
        float* mrow = &S.macc[rb + (on ? nl : 0)][ec];
        float* prow = &S.pacc[bk][on ? nl : 0][0];
        float old[8], po[3];
        if (ACC) {
#pragma unroll
            for (int tt = 0; tt < 8; ++tt) old[tt] = mrow[(tt >> 1) * 32 + (tt & 1) * 4];
            po[0] = prow[0]; po[1] = prow[1]; po[2] = prow[2];
            CBAR();
        }
        if (on) {
#pragma unroll
            for (int tt = 0; tt < 8; ++tt) mrow[(tt >> 1) * 32 + (tt & 1) * 4] = ACC ? old[tt] + hm[tt] : hm[tt];
            if (e16 == 0) {
                prow[0] = (ACC ? po[0] : 0.f) + hp0;
                prow[1] = (ACC ? po[1] : 0.f) + hp1;
                prow[2] = (ACC ? po[2] : 0.f) + hp2;
            }
        }
    };
    auto flush = [&](int d, auto accc) __attribute__((always_inline)) { reduce(); commit(d, accc); };
    auto finish = [&]() __attribute__((always_inline)) {
        const std::integral_constant<bool, true> accc;
        __syncthreads();
        for (int w = 1; w < KB_WAVES; ++w) {
            if ((hflags & 1) && (wv == w || !(hflags & 2))) commit(dHead, accc);
            __syncthreads();
            if (!(hflags & 2)) break;
        }
    };
    auto round = [&](const int base, auto firstc) __attribute__((always_inline)) -> bool {
        constexpr bool FIRST = decltype(firstc)::value;
        int T;
        int fo[GI_MAX];
        {
            int tot = 0;
#pragma unroll
            for (int ci = 0; ci < GI_MAX; ++ci) { sc[ci] -= so[ci]; tot += sc[ci]; }
            const int x = wave_scan(tot);
            T = __builtin_amdgcn_readlane(x, 63);
            int off = x - tot - base;
#pragma unroll
            for (int ci = 0; ci < GI_MAX; ++ci) { fo[ci] = off; off += sc[ci]; }
        }
        const int Tr = max(0, min(CAP, T - base));
        int lo2 = lane;
        asm volatile("" : "+v"(lo2));
        {
            int kp[GI_MAX], kn[GI_MAX], kv[GI_MAX], mx = 0;
#pragma unroll
            for (int ci = 0; ci < GI_MAX; ++ci) {
                const int qa = max(0, -fo[ci]);
                kn[ci] = min(sc[ci], CAP - fo[ci]) - qa;
                kp[ci] = fo[ci] + qa;
                kv[ci] = (lo2 + 64 * ci) * CHB4 + (so[ci] + qa) * RW;
                mx = max(mx, kn[ci]);
            }
#pragma clang loop unroll(disable) vectorize(disable) interleave(disable)
            for (int q = 0; q < mx; ++q) {
#pragma unroll
                for (int ci = 0; ci < GI_MAX; ++ci)
                    if (q < kn[ci]) key[kp[ci] + q] = (u32)(kv[ci] + q * RW);
            }
        }
        CBAR();
        const int spl = (Tr + 63) >> 6;
        const int pb = lo2 * spl;
        uint4 r[RPL];
        uint2 ps[RPL];
#pragma unroll
        for (int i = 0; i < RPL; ++i) {
            r[i] = make_uint4(0xffffffffu, 0u, 0u, 0u);
            if (i < spl && pb + i < Tr) {
                const u32* rp = rec_g + key[pb + i];
                r[i] = make_uint4(rp[0], rp[1], rp[2], 0u);
            }
        }
        if (FIRST) stage();
#pragma unroll
        for (int i = 0; i < RPL; ++i) {
            ps[i] = make_uint2(0u, 0u);
            if (r[i].x != 0xffffffffu) ps[i] = pos8[r[i].x >> 3];
        }
        int posn[RPL];
        int c0, g1s, cnt4;
        {
            u32 el[RPL], eh[RPL], tl_ = 0, th_ = 0;
#pragma unroll
            for (int i = 0; i < RPL; ++i) {
                const unsigned long long oh = 1ull << ((r[i].x & 7u) * 8u);
                el[i] = tl_; eh[i] = th_;
                tl_ += (u32)oh; th_ += (u32)(oh >> 32);
            }
            const u32 il = (u32)wave_scan((int)tl_), ih = (u32)wave_scan((int)th_);
            const u32 Tl = (u32)__builtin_amdgcn_readlane((int)il, 63);
            cnt4 = __builtin_amdgcn_readlane((int)ih, 63) & 0xff;
            c0 = (int)((Tl * 0x01010101u) >> 24);
            g1s = (c0 + 15) & ~15;
            const u32 bl = il - tl_ + Tl * 0x01010100u, bh = ih - th_;
#pragma unroll
            for (int i = 0; i < RPL; ++i) {
                const u32 nd = r[i].x & 7u;
                const unsigned long long v = ((unsigned long long)(bh + eh[i]) << 32) | (unsigned long long)(bl + el[i]);
                posn[i] = (int)((u32)(v >> (nd * 8u)) & 0xffu) + ((nd == 4u) ? g1s : 0);
            }
        }
        const int g1e = (g1s + cnt4 + 15) & ~15;
        CBAR();
#pragma unroll
        for (int i = 0; i < RPL; ++i)
            if (r[i].x != 0xffffffffu) {
                const float4 pn = *reinterpret_cast<const float4*>(&S.posl[wv][r[i].x & 7u][0]);
                float dx = pn.x - __uint_as_float(ps[i].x << 16), dy = pn.y - __uint_as_float(ps[i].x & 0xffff0000u);
                float dz = pn.z - __uint_as_float(ps[i].y << 16);
                float d2v = dx * dx + dy * dy + dz * dz;
                u16 d2h = f2bf(d2v);
                u16 d2l = f2bf(d2v - bf2f(d2h));
                rec[posn[i]] = make_uint4((u32)d2h | (r[i].y << 16), (r[i].y >> 16) | (r[i].z << 16), (r[i].z >> 16) | ((u32)d2l << 16), r[i].x);
                S.geo[wv][posn[i]] = make_uint2(0x3F80u | ((u32)f2bf(dx) << 16), pack2(dy, dz));
            }
        if (lo2 < 32) {
            const int pp = (lo2 < 16) ? (c0 + lo2) : (g1s + cnt4 + lo2 - 16);
            if (pp < ((lo2 < 16) ? g1s : g1e)) { rec[pp] = make_uint4(0u, 0u, 0u, 7u); S.geo[wv][pp] = make_uint2(0u, 0u); }
        } else if (lo2 < 32 + 18) {
            const int l = lo2 - 32, n0t = g1s >> 4, n1t = g1e >> 4;
            const int grp = (l >= n0t) ? 1 : 0;
            const int dsc = ((grp ? n1t : n0t) - l) | ((l == 0 || l == n0t) ? 32 : 0) | ((wv * (CAPR / 16) + l) << 8) |
                            (wv << 16) | (grp << 18) | (rowb << 20) | (nn << 25);
            S.dt[wv][l] = (l == 17) ? (n1t | ((base + CAP < T) ? 256 : 0)) : dsc;
        }
        if (FIRST) load_hu();
        __syncthreads();

        int lq = lane;
        asm volatile("" : "+v"(lq));
        int cb1, cb2, cb3, TT, anymore;
        {
            const int q = S.dt[lq & 3][17];
            const int q0 = __builtin_amdgcn_readlane(q, 0), q1 = __builtin_amdgcn_readlane(q, 1);
            const int q2 = __builtin_amdgcn_readlane(q, 2), q3 = __builtin_amdgcn_readlane(q, 3);
            cb1 = q0 & 255; cb2 = cb1 + (q1 & 255); cb3 = cb2 + (q2 & 255); TT = cb3 + (q3 & 255);
            anymore = (q0 | q1 | q2 | q3) & 256;
        }
        const int ts = (TT * wv) >> 2, te = (TT * (wv + 1)) >> 2;
        const int nst = te - ts;
        const int s1 = TT >> 2, s2 = (TT * 2) >> 2, s3 = (TT * 3) >> 2;
        int descv;
        {
            int tl = (lq < 32) ? (ts + lq) : ((lq & 1) ? s2 : s1);
            tl = min(tl, max(TT - 1, 0));
            const int o = (tl >= cb3) ? (3 * 18 - cb3) : (tl >= cb2) ? (2 * 18 - cb2) : (tl >= cb1) ? (18 - cb1) : 0;
            descv = (&S.dt[0][0])[tl + o];
        }
        const int dF = __builtin_amdgcn_readlane(descv, 0);
        const bool has_head = (nst > 0) && !(dF & 32);
        const int hl = has_head ? min(nst, dF & 31) : 0;
        const int nh = nst - hl;
        bool serial;
        {
            const int d1 = __builtin_amdgcn_readlane(descv, 32), d2 = __builtin_amdgcn_readlane(descv, 33);
            serial = (!(d1 & 32) && s1 + (d1 & 31) > s2) || (!(d2 & 32) && s2 + (d2 & 31) > s3);
        }
        descv |= (lq >= hl && ((descv & 31) == 1 || lq == nst - 1)) ? (1 << 19) : 0;
        auto dsc_at = [&](int i) __attribute__((always_inline)) {
            return __builtin_amdgcn_readlane(descv, (i < nh) ? (i + hl) : (i - nh));
        };
        bf16x4 sel;
        {
            const int dl = (lq & 15) - 4 * (lq >> 4);
            unsigned long long v = (dl >= 0 && dl < 4) ? (0x3F80ull << (16 * dl)) : 0ull;
            sel = as_bf16x4(make_uint2((u32)v, (u32)(v >> 32)));
        }
        const char* w5b = (g < 3) ? reinterpret_cast<const char*>(&S.w5s[g][e16]) : reinterpret_cast<const char*>(&S.At[0][0][e16]);
        const int w5m = (g < 3) ? 0 : (int)sizeof(S.At[0][0]);
        const char* recb = reinterpret_cast<const char*>(&S.rec[0][e16]);
        const char* geob = reinterpret_cast<const char*>(&S.geo[0][e16]);
        const char* Bb = reinterpret_cast<const char*>(Bg);
        const u32 g16 = (u32)(lq & 3) * 16u;
        const char* recq = reinterpret_cast<const char*>(&S.rec[0][lq >> 2]);
        uint4* twp = &S.T[wv][(lq & 3) * 16 + ((lq >> 2) ^ ((lq & 3) * 2))];
        const uint4* trp = &S.T[wv][g * 16 + (e16 ^ (g * 2))];
        uint2 (*Mw)[4] = reinterpret_cast<uint2 (*)[4]>(&S.T[wv][0]);
        const char* xrb = recb + ((g == 1) ? 8 : 0);
        const u32 psel = (g == 1) ? 0x0c0c0504u : 0x03020100u;
        bf16x8 gA[4], gB[4];
        u32 pA0 = 0, pB0 = 0;
        auto issue = [&](int d, bf16x8 (&gat)[4], u32& q0) __attribute__((always_inline)) {
            const u32 qs = reinterpret_cast<const uint4*>(recq + (d & 0xff00))->w;
            q0 = reinterpret_cast<const uint4*>(recb + (d & 0xff00))->w;
            const u32 off = ((qs >> 3) << 8) + g16;
#pragma unroll
            for (int tp = 0; tp < 4; ++tp)
                gat[tp] = *reinterpret_cast<const bf16x8*>(Bb + off + tp * 64);
        };
#pragma unroll
        for (int tt = 0; tt < 8; ++tt) acc2[tt] = (f32x4){0.f, 0.f, 0.f, 0.f};
        auto sgi = [&](int d) __attribute__((always_inline)) { return ((d >> 15) & 6) | ((d >> 18) & 1); };
        auto compute = [&](int d, bf16x8 (&gat)[4], u32 q0) __attribute__((always_inline)) {
            const int bg = sgi(d);
            const u32 nd = q0 & 7u;
            const char* xp = (g < 3) ? (xrb + (d & 0xff00)) : (reinterpret_cast<const char*>(S.lut) + (nd << 3));
            uint2 xu = *reinterpret_cast<const uint2*>(xp);
            const uint2 gq = *reinterpret_cast<const uint2*>(geob + ((d & 0xff00) >> 1));
            const uint4* w5p = reinterpret_cast<const uint4*>(w5b + w5m * bg);
#pragma unroll
            for (int tp = 0; tp < 4; ++tp) {
                CBAR();
                *twp = *reinterpret_cast<const uint4*>(&gat[tp]);
                CBAR();
                gat[tp] = as_bf16x8(*trp);
            }
            CBAR();
            Mw[e16][g] = ((nd & 3u) == (u32)g) ? gq : make_uint2(0u, 0u);
            xu.y = __builtin_amdgcn_perm(xu.x, xu.y, psel);
            const bf16x4 x4 = as_bf16x4(xu);
            CBAR();
            u32 sw01, sw23;
            {
                const u16* wp = reinterpret_cast<const u16*>(&Mw[g * 4][0]) + e16;
                sw01 = (u32)wp[0] | ((u32)wp[16] << 16);
                sw23 = (u32)wp[32] | ((u32)wp[48] << 16);
            }
            const bf16x4 sop = as_bf16x4(make_uint2(sw01, sw23));
            uint4 w5[4];
#pragma unroll
            for (int tp = 0; tp < 4; ++tp) w5[tp] = w5p[tp * 16];
            auto feat = [&](int tt) __attribute__((always_inline)) {
                const uint4 w = w5[tt >> 1];
                return __builtin_amdgcn_mfma_f32_16x16x16bf16_1k(x4, as_bf16x4((tt & 1) ? make_uint2(w.z, w.w) : make_uint2(w.x, w.y)),
                                                                 (f32x4){0.f, 0.f, 0.f, 0.f}, 0, 0, 0);
            };
            auto gath = [&](int tt, f32x4 c) __attribute__((always_inline)) {
                const u32x4 gq4 = *reinterpret_cast<const u32x4*>(&gat[tt >> 1]);
                return __builtin_amdgcn_mfma_f32_16x16x16bf16_1k(as_bf16x4(make_uint2(gq4[(tt & 1) * 2], gq4[(tt & 1) * 2 + 1])), sel, c, 0, 0, 0);
            };
            f32x4 acc = gath(0, feat(0));
#pragma unroll
            for (int tt = 0; tt < 8; ++tt) {
                f32x4 accN = acc;
                __builtin_amdgcn_sched_barrier(0);
                if (tt < 7) accN = feat(tt + 1);
                __builtin_amdgcn_sched_barrier(0);
                float e0 = __builtin_amdgcn_exp2f(acc[0]), e1 = __builtin_amdgcn_exp2f(acc[1]);
                float e2 = __builtin_amdgcn_exp2f(acc[2]), e3 = __builtin_amdgcn_exp2f(acc[3]);
                __builtin_amdgcn_sched_barrier(0);
                if (tt < 7) accN = gath(tt + 1, accN);
                __builtin_amdgcn_sched_barrier(0);
                e0 += 1.f; e1 += 1.f; e2 += 1.f; e3 += 1.f;
                asm("" : "+v"(e0)); asm("" : "+v"(e1)); asm("" : "+v"(e2)); asm("" : "+v"(e3));
                float y0 = acc[0] * __builtin_amdgcn_rcpf(e0), y1 = acc[1] * __builtin_amdgcn_rcpf(e1);
                float y2 = acc[2] * __builtin_amdgcn_rcpf(e2), y3 = acc[3] * __builtin_amdgcn_rcpf(e3);
                asm("" : "+v"(y0)); asm("" : "+v"(y1)); asm("" : "+v"(y2)); asm("" : "+v"(y3));
                u32 k01 = cvt_pk((f32x2){y0, y1}), k23 = cvt_pk((f32x2){y2, y3});
                bf16x4 mb = as_bf16x4(make_uint2(k01, k23));
                __builtin_amdgcn_sched_barrier(0);
                acc2[tt] = __builtin_amdgcn_mfma_f32_16x16x16bf16_1k(sop, mb, acc2[tt], 0, 0, 0);
                acc = accN;
            }
        };
        const std::integral_constant<bool, !FIRST> dirc;
        if (nst > 0) {
            issue(dsc_at(0), gA, pA0);
            int i = 0;
            for (; i + 1 < nst; i += 2) {
                {
                    const int rem = nst - i;
                    if (rem >= 5) __builtin_amdgcn_s_setprio(3);
                    else if (rem >= 4) __builtin_amdgcn_s_setprio(2);
                    else if (rem >= 3) __builtin_amdgcn_s_setprio(1);
                    else __builtin_amdgcn_s_setprio(0);
                }
                const int dA = dsc_at(i);
                const int dB = dsc_at(i + 1);
                const int dC = dsc_at(min(i + 2, nst - 1));
                issue(dB, gB, pB0);
                compute(dA, gA, pA0);
                if (dA & (1 << 19)) flush(dA, dirc);
                issue(dC, gA, pA0);
                compute(dB, gB, pB0);
                if (dB & (1 << 19)) flush(dB, dirc);
            }
            if (i < nst) {
                const int dA = dsc_at(i);
                compute(dA, gA, pA0);
                if (dA & (1 << 19)) flush(dA, dirc);
            }
        }
        dHead = dF;
        hflags = (has_head ? 1 : 0) | (serial ? 2 : 0);
        reduce();
        return anymore != 0;
    };
    if (round(0, std::integral_constant<bool, true>())) {
        for (int base = CAP;; base += CAP) {
            finish();
            load_tab_g();
            if (!round(base, std::integral_constant<bool, false>())) break;
        }
    }

    int le = lane;
    asm volatile("" : "+v"(le));
    const int f16 = le & 15, gg = le >> 4;
    bf16x8 bw[2][4];
#pragma unroll
    for (int q = 0; q < 2; ++q)
#pragma unroll
        for (int ks = 0; ks < 4; ++ks)
            bw[q][ks] = *reinterpret_cast<const bf16x8*>(whT + (((wv * 2 + q) * 4 + ks) * 64 + le) * 8);
    finish();
    if (le < nn * 3) {
        int n = le / 3, cpt = le - n * 3;
        out_pos[(size_t)(n0 + n) * 3 + cpt] = f2bf(S.posl[wv][n][cpt] + S.pacc[wv][n][cpt]);
    }
#pragma unroll
    for (int rt = 0; rt < 2; ++rt) {
        if (rt * 16 < NR) {
            bf16x8 af[4];
            int lr = rt * 16 + f16;
#pragma unroll
            for (int ks = 0; ks < 4; ++ks) {
                const float* mp = &S.macc[lr < NR ? lr : 0][ks * 32 + gg * 8];
                af[ks] = as_bf16x8(make_uint4(pack2(mp[0], mp[1]), pack2(mp[2], mp[3]),
                                              pack2(mp[4], mp[5]), pack2(mp[6], mp[7])));
            }
            u32 ov[4];
#pragma unroll
            for (int q = 0; q < 2; ++q) {
                const u32 u0 = q ? uv4[rt].z : uv4[rt].x, u1 = q ? uv4[rt].w : uv4[rt].y;
                const u32 h0 = q ? hv4[rt].z : hv4[rt].x, h1 = q ? hv4[rt].w : hv4[rt].y;
                f32x4 acc = {__uint_as_float(u0 << 16), __uint_as_float(u0 & 0xffff0000u),
                             __uint_as_float(u1 << 16), __uint_as_float(u1 & 0xffff0000u)};
#pragma unroll
                for (int ks = 0; ks < 4; ++ks)
                    acc = __builtin_amdgcn_mfma_f32_16x16x32_bf16(bw[q][ks], af[ks], acc, 0, 0, 0);
                ov[q * 2] = pack2(__uint_as_float(h0 << 16) + silu_f(acc[0]), __uint_as_float(h0 & 0xffff0000u) + silu_f(acc[1]));
                ov[q * 2 + 1] = pack2(__uint_as_float(h1 << 16) + silu_f(acc[2]), __uint_as_float(h1 & 0xffff0000u) + silu_f(acc[3]));
            }
            if (lr < NR)
                *reinterpret_cast<uint4*>(out_h + (size_t)(N0 + lr) * D + wv * 32 + gg * 8) = make_uint4(ov[0], ov[1], ov[2], ov[3]);
        }
    }
}

extern "C" void kernel_launch(void* const* d_in, const int* in_sizes, int n_in,
                              void* d_out, int out_size, void* d_ws, size_t ws_size,
                              hipStream_t stream) {
    const u16* h   = (const u16*)d_in[0];
    const u16* pos = (const u16*)d_in[1];
    const u16* ea  = (const u16*)d_in[2];
    const int* src = (const int*)d_in[3];
    const int* dst = (const int*)d_in[4];
    const u16* we  = (const u16*)d_in[5];
    const u16* wx  = (const u16*)d_in[6];
    const u16* wh  = (const u16*)d_in[7];
    int N = in_sizes[0] / D;
    int E = in_sizes[3];
    u16* out_h = (u16*)d_out;
    u16* out_pos = out_h + (size_t)N * D;

    int G = 128;
    while ((E + G - 1) / G > KA_THREADS * KA_ITERS && G < 64 * GI_MAX) G += 64;
    int CH = (E + G - 1) / G;
    int NBW = 3072;
    while (NBW * NPW < N && NBW < NBW_MAX) NBW += 4;
    int nGemm = (N + 127) / 128;
    if (nGemm < D * D / KA_THREADS) nGemm = D * D / KA_THREADS;
    unsigned long long mN = (1ull << 40) / (unsigned long long)N + 1ull;
    unsigned long long mNBW = (1ull << 40) / (unsigned long long)NBW + 1ull;

    char* ws = (char*)d_ws;
    size_t off = 0;
    auto take = [&](size_t bytes) { char* p = ws + off; off += (bytes + 255) & ~(size_t)255; return p; };
    u32* rec_g = (u32*)take((size_t)G * ((CH * RW + 3) >> 2) * 16 + 64);
    u16* tab = (u16*)take((size_t)G * (NBW + 4) * 2);
    u16* ABU = (u16*)take((size_t)3 * N * D * 2);
    u16* whT = (u16*)take((size_t)D * D * 2);
    uint4* w5g = (uint4*)take((size_t)256 * 16);
    uint2* pos8 = (uint2*)take((size_t)N * 8);

    k_part<<<G + nGemm, KA_THREADS, 0, stream>>>(h, pos, src, dst, ea, we, wh, rec_g, tab, ABU, whT, w5g, pos8,
                                                      mN, mNBW, N, E, G, CH, NBW, nGemm);
    k_main<<<NBW / KB_WAVES, KB_THREADS, 0, stream>>>(h, pos, wx, rec_g, tab, ABU, whT, w5g, pos8,
                                                     out_h, out_pos, mNBW, N, G, CH, NBW);
}
```

```cpp
#include <hip/hip_runtime.h>
#include <hip/hip_bf16.h>
#include <stdint.h>
#include <type_traits>

typedef unsigned short u16;
typedef unsigned int u32;
typedef __attribute__((ext_vector_type(8))) short bf16x8;
typedef __attribute__((ext_vector_type(4))) float f32x4;
typedef __attribute__((ext_vector_type(4))) short bf16x4;
typedef __attribute__((ext_vector_type(2))) float f32x2;
typedef __attribute__((ext_vector_type(4))) unsigned int u32x4;
__device__ __forceinline__ void st_nt(void* p, uint4 v) { u32x4 x = {v.x, v.y, v.z, v.w}; __builtin_nontemporal_store(x, reinterpret_cast<u32x4*>(p)); }

typedef __attribute__((ext_vector_type(2))) unsigned int u32x2;
__device__ __forceinline__ __amdgpu_buffer_rsrc_t mk_rsrc(const void* p, u32 bytes) {
    return __builtin_amdgcn_make_buffer_rsrc(const_cast<void*>(p), (short)0, (int)bytes, 0x00020000);
}
template <int AUX>
__device__ __forceinline__ void st_b128(__amdgpu_buffer_rsrc_t r, u32 off, uint4 v) {
    u32x4 x = {v.x, v.y, v.z, v.w};
    __builtin_amdgcn_raw_buffer_store_b128(x, r, (int)off, 0, AUX);
}
template <int AUX>
__device__ __forceinline__ void st_b64(__amdgpu_buffer_rsrc_t r, u32 off, uint2 v) {
    u32x2 x = {v.x, v.y};
    __builtin_amdgcn_raw_buffer_store_b64(x, r, (int)off, 0, AUX);
}
#define AUX_A 16
#define AUX_B 16
#define AUX_R 16
#define AUX_O 16

#define D 128
#define KA_THREADS 1024
#define KA_ITERS 4
#define KA_CH_MAX 3135
#define NBW_MAX 4096
#define KB_THREADS 256
#define KB_WAVES 4
#define NPW 5
#define RPL 4
#define CAP 240
#define CAPS 272
#define CAPR (CAP + 32)
#define RW 3
#define GI_MAX 2
#define AST (D + 4)
#define CBAR() asm volatile("" ::: "memory")
#define NLOG2E (-1.4426950408889634f)
#define NLN2 (-0.6931471805599453f)

__device__ __forceinline__ float bf2f(u16 v) { return __uint_as_float(((u32)v) << 16); }
__device__ __forceinline__ u16 f2bf(float f) {
    __hip_bfloat16 b = __float2bfloat16(f);
    return *reinterpret_cast<u16*>(&b);
}
__device__ __forceinline__ float silu_f(float x) {
    float e = __builtin_amdgcn_exp2f(x * NLOG2E);
    return x * __builtin_amdgcn_rcpf(1.0f + e);
}
__device__ __forceinline__ u32 pack2(float a, float b) {
    return (u32)f2bf(a) | ((u32)f2bf(b) << 16);
}
typedef __attribute__((ext_vector_type(2))) __bf16 bf16x2;
__device__ __forceinline__ u32 cvt_pk(f32x2 v) {
    bf16x2 p = __builtin_convertvector(v, bf16x2);
    return *reinterpret_cast<u32*>(&p);
}
__device__ __forceinline__ bf16x8 as_bf16x8(uint4 v) { return *reinterpret_cast<bf16x8*>(&v); }
__device__ __forceinline__ bf16x4 as_bf16x4(uint2 v) { return *reinterpret_cast<bf16x4*>(&v); }
template <int CTRL>
__device__ __forceinline__ float dpp_f(float x) {
    return __int_as_float(__builtin_amdgcn_update_dpp(0, __float_as_int(x), CTRL, 0xf, 0xf, true));
}
__device__ __forceinline__ float rsum16(float x) {
    x += dpp_f<0x128>(x);
    x += dpp_f<0x124>(x);
    x += dpp_f<0x122>(x);
    x += dpp_f<0x121>(x);
    return x;
}
template <int CTRL, int RM>
__device__ __forceinline__ int dpp_add(int x) { return x + __builtin_amdgcn_update_dpp(0, x, CTRL, RM, 0xf, false); }
__device__ __forceinline__ int wave_scan(int x) {
    x = dpp_add<0x111, 0xf>(x);
    x = dpp_add<0x112, 0xf>(x);
    x = dpp_add<0x114, 0xf>(x);
    x = dpp_add<0x118, 0xf>(x);
    x = dpp_add<0x142, 0xa>(x);
    x = dpp_add<0x143, 0xc>(x);
    return x;
}
__device__ __forceinline__ u32 mdiv(u32 x, unsigned long long m) {
    return (u32)(((unsigned long long)x * m) >> 40);
}
__device__ __forceinline__ int node_start(int b, int N, int NBW, unsigned long long mNBW) {
    return (int)mdiv((u32)b * (u32)N + (u32)NBW - 1u, mNBW);
}

__global__ void __launch_bounds__(KA_THREADS)
k_part(const u16* __restrict__ h, const u16* __restrict__ pos, const int* __restrict__ src, const int* __restrict__ dst,
       const u16* __restrict__ ea, const u16* __restrict__ we, const u16* __restrict__ wh,
       u32* __restrict__ rec_g, u16* __restrict__ tab, u16* __restrict__ ABU,
       u16* __restrict__ whT, uint4* __restrict__ w5g, uint2* __restrict__ pos8,
       unsigned long long mN, unsigned long long mNBW,
       int N, int E, int G, int CH, int NBW, int nGemm) {
    __shared__ __align__(16) u16 smem[4 * D * D];
    static_assert(NBW_MAX * 8 + (NBW_MAX + 8) + KA_CH_MAX * RW * 2 + 8 <= 4 * D * D, "lds");
    __shared__ int wsum[KA_THREADS / 64];
    asm volatile("" :: "s"(h), "s"(pos), "s"(src), "s"(dst), "s"(ea), "s"(we), "s"(wh), "s"(rec_g), "s"(tab), "s"(ABU),
                 "s"(whT), "s"(w5g), "s"(pos8), "s"(mN), "s"(mNBW), "s"(N), "s"(E), "s"(G), "s"(CH), "s"(NBW), "s"(nGemm));
    const int t = threadIdx.x, lane = t & 63;
    const int wv = __builtin_amdgcn_readfirstlane(t >> 6);
    int bid = blockIdx.x;
    if (bid < G) {
        uint4* cnt = reinterpret_cast<uint4*>(smem);
        u16* base16 = smem + NBW_MAX * 8;
        u32* lrec = reinterpret_cast<u32*>(base16 + NBW_MAX + 8);
        const int e0 = bid * CH;
        const int ne = max(0, min(E, e0 + CH) - e0);
        int dv[KA_ITERS], sv[KA_ITERS];
        uint2 eav[KA_ITERS];
#pragma unroll
        for (int i = 0; i < KA_ITERS; ++i) dv[i] = dst[min(e0 + t + i * KA_THREADS, E - 1)];
#pragma unroll
        for (int i = 0; i < KA_ITERS; ++i) {
            const int e = min(e0 + t + i * KA_THREADS, E - 1);
            sv[i] = src[e];
            eav[i] = *reinterpret_cast<const uint2*>(ea + (size_t)e * 4);
        }
#pragma unroll
        for (int i = 0; i < NBW_MAX / KA_THREADS; ++i)
            if (t + i * KA_THREADS < NBW) cnt[t + i * KA_THREADS] = make_uint4(0u, 0u, 0u, 0u);
        __syncthreads();
        const int wq = wv >> 2, wsft = (wv & 3) * 8;
        const u32 inc = 1u << wsft;
        int bv[KA_ITERS], rk[KA_ITERS];
#pragma unroll
        for (int i = 0; i < KA_ITERS; ++i) {
            const int b = (int)mdiv((u32)dv[i] * (u32)NBW, mN);
            bv[i] = b;
            rk[i] = 0;
            if (t + i * KA_THREADS < ne) rk[i] = (int)atomicAdd(reinterpret_cast<u32*>(cnt) + b * 4 + wq, inc);
            dv[i] -= node_start(b, N, NBW, mNBW);
        }
        __syncthreads();
        {
            const int per = (NBW + KA_THREADS - 1) / KA_THREADS;
            const int b0 = t * per;
            int c[NBW_MAX / KA_THREADS], tot = 0;
            u32x4 q[NBW_MAX / KA_THREADS];
#pragma unroll
            for (int j = 0; j < NBW_MAX / KA_THREADS; ++j)
                q[j] = *reinterpret_cast<const u32x4*>(&cnt[min(b0 + j, NBW_MAX - 1)]);
#pragma unroll
            for (int j = 0; j < NBW_MAX / KA_THREADS; ++j) {
                c[j] = (int)__builtin_amdgcn_sad_u8(q[j][0], 0u, __builtin_amdgcn_sad_u8(q[j][1], 0u,
                            __builtin_amdgcn_sad_u8(q[j][2], 0u, __builtin_amdgcn_sad_u8(q[j][3], 0u, 0u))));
                c[j] = (j < per && b0 + j < NBW) ? c[j] : 0;
                tot += c[j];
            }
            const int x = wave_scan(tot);
            if (lane == 63) wsum[wv] = x;
            __syncthreads();
            int y = wsum[lane & 15];
            y = ((lane & 15) < wv) ? y : 0;
            y += __builtin_amdgcn_update_dpp(0, y, 0x128, 0xf, 0xf, true);
            y += __builtin_amdgcn_update_dpp(0, y, 0x124, 0xf, 0xf, true);
            y += __builtin_amdgcn_update_dpp(0, y, 0x122, 0xf, 0xf, true);
            y += __builtin_amdgcn_update_dpp(0, y, 0x121, 0xf, 0xf, true);
            int off = y + x - tot;
#pragma unroll
            for (int j = 0; j < NBW_MAX / KA_THREADS; ++j) {
                if (j < per && b0 + j < NBW) base16[b0 + j] = (u16)off;
                off += c[j];
            }
            if (t < 4) base16[NBW + t] = (u16)ne;
        }
        __syncthreads();
        {
            const u32 m0 = (wq > 0) ? ~0u : inc - 1u;
            const u32 m1 = (wq > 1) ? ~0u : (wq == 1) ? inc - 1u : 0u;
            const u32 m2 = (wq > 2) ? ~0u : (wq == 2) ? inc - 1u : 0u;
            const u32 m3 = (wq == 3) ? inc - 1u : 0u;
            uint4 q[KA_ITERS];
            u32 bs[KA_ITERS];
#pragma unroll
            for (int i = 0; i < KA_ITERS; ++i) {
                if (i * KA_THREADS + wv * 64 < ne) { q[i] = cnt[bv[i]]; bs[i] = base16[bv[i]]; }
            }
#pragma unroll
            for (int i = 0; i < KA_ITERS; ++i) {
                if (t + i * KA_THREADS < ne) {
                    u32 p = bs[i] + (((u32)rk[i] >> wsft) & 255u);
                    p = __builtin_amdgcn_sad_u8(q[i].x & m0, 0u, p);
                    p = __builtin_amdgcn_sad_u8(q[i].y & m1, 0u, p);
                    p = __builtin_amdgcn_sad_u8(q[i].z & m2, 0u, p);
                    p = __builtin_amdgcn_sad_u8(q[i].w & m3, 0u, p);
                    u32* o = lrec + p * RW;
                    o[0] = ((u32)sv[i] << 3) | (u32)dv[i];
                    o[1] = eav[i].x; o[2] = eav[i].y;
                }
            }
        }
        {
            const __amdgpu_buffer_rsrc_t rt = mk_rsrc(tab + (size_t)bid * (NBW + 4), (u32)(NBW + 4) * 2u);
#pragma unroll
            for (int i = 0; i < (NBW_MAX / 4 + KA_THREADS) / KA_THREADS; ++i) {
                const int p = t + i * KA_THREADS;
                if (p <= (NBW >> 2)) st_b64<AUX_R>(rt, (u32)p * 8u, reinterpret_cast<const uint2*>(base16)[p]);
            }
        }
        __syncthreads();
        {
            const int CHB = (CH * RW + 3) >> 2;
            const int nq = (ne * RW + 3) >> 2;
            const __amdgpu_buffer_rsrc_t rr = mk_rsrc(reinterpret_cast<uint4*>(rec_g) + (size_t)bid * CHB, (u32)nq * 16u);
            const uint4* lq4 = reinterpret_cast<const uint4*>(lrec);
#pragma unroll
            for (int i = 0; i < KA_ITERS * RW / 4; ++i) {
                int p = t + i * KA_THREADS;
                if (p < nq) st_b128<AUX_R>(rr, (u32)p * 16u, lq4[p]);
            }
        }
        return;
    }
    bid -= G;
    if (bid < nGemm) {
        const int e16 = lane & 15, g = lane >> 4;
        char* wl = reinterpret_cast<char*>(smem);
        char* hl = wl + 3 * D * 256;
        u32 wr[16], w8[8];
#pragma unroll
        for (int j = 0; j < 16; ++j)
            wr[j] = *reinterpret_cast<const u32*>(we + (size_t)(wv * 16 + j) * D + lane * 2);
#pragma unroll
        for (int j = 0; j < 8; ++j)
            w8[j] = *reinterpret_cast<const u32*>(wh + (size_t)(wv * 8 + j) * D + lane * 2);
        u32x4 hs[2];
#pragma unroll
        for (int r = 0; r < 2; ++r)
            hs[r] = *reinterpret_cast<const u32x4*>(h + (size_t)min(bid * 128 + r * 64 + (t >> 4), N - 1) * D + e16 * 8);
        const u16* pp = pos + (size_t)min(bid * 128 + (t & 127), N - 1) * 3;
        u32 p01 = 0, p2 = 0;
        if (wv >= 14) { p01 = (u32)pp[0] | ((u32)pp[1] << 16); p2 = pp[2]; }
        CBAR();
        {
            const int jj = (lane & 3) * 2, pr = (lane >> 2) ^ jj;
            char* wp = wl + (jj * 16 + pr) * 256;
#pragma unroll
            for (int sb = 0; sb < 3; ++sb) {
                const u32* w = (sb < 2) ? &wr[sb * 8] : w8;
                const int kc = (sb < 2) ? wv * 2 + sb : 32 + wv;
                uint4 lo4 = make_uint4((w[0] & 0xffffu) | (w[1] << 16), (w[2] & 0xffffu) | (w[3] << 16),
                                       (w[4] & 0xffffu) | (w[5] << 16), (w[6] & 0xffffu) | (w[7] << 16));
                uint4 hi4 = make_uint4((w[0] >> 16) | (w[1] & 0xffff0000u), (w[2] >> 16) | (w[3] & 0xffff0000u),
                                       (w[4] >> 16) | (w[5] & 0xffff0000u), (w[6] >> 16) | (w[7] & 0xffff0000u));
                char* q = wp + (kc >> 4) * (D * 256) + (((kc & 15) ^ pr) << 4);
                *reinterpret_cast<uint4*>(q) = lo4;
                *reinterpret_cast<uint4*>(q + 16 * 256) = hi4;
            }
#pragma unroll
            for (int r = 0; r < 2; ++r) {
                const int row = r * 64 + (t >> 4);
                *reinterpret_cast<u32x4*>(hl + row * 256 + ((e16 ^ (row & 15)) << 4)) = hs[r];
            }
        }
        __syncthreads();
        if (wv >= 12) {
            if (wv >= 14 && bid * 128 + (t & 127) < N) pos8[bid * 128 + (t & 127)] = make_uint2(p01, p2);
            return;
        }
        const int rp = wv & 3, m = wv >> 2;
        const u32 lb = (u32)(e16 * 256 + ((g ^ e16) << 4));
        f32x4 acc[2][8];
#pragma unroll
        for (int r = 0; r < 2; ++r)
#pragma unroll
            for (int j = 0; j < 8; ++j) acc[r][j] = (f32x4){0.f, 0.f, 0.f, 0.f};
#pragma unroll
        for (int ks = 0; ks < 4; ++ks) {
            const char* hp = hl + rp * (32 * 256) + (lb ^ (u32)(ks * 64));
            const bf16x8 a0 = *reinterpret_cast<const bf16x8*>(hp);
            const bf16x8 a1 = *reinterpret_cast<const bf16x8*>(hp + 16 * 256);
#pragma unroll
            for (int j = 0; j < 8; ++j) {
                const char* wp = wl + m * (D * 256) + j * (16 * 256) + (lb ^ (u32)((j & 6) * 256 + (((ks * 4) ^ (j & 6)) << 4)));
                const bf16x8 w = *reinterpret_cast<const bf16x8*>(wp);
                acc[0][j] = __builtin_amdgcn_mfma_f32_16x16x32_bf16(a0, w, acc[0][j], 0, 0, 0);
                acc[1][j] = __builtin_amdgcn_mfma_f32_16x16x32_bf16(a1, w, acc[1][j], 0, 0, 0);
            }
        }
        const float sc = (m < 2) ? NLOG2E : 1.0f;
        const __amdgpu_buffer_rsrc_t ro = mk_rsrc(ABU + (size_t)m * N * D, (u32)N * (D * 2));
#pragma unroll
        for (int r = 0; r < 2; ++r)
#pragma unroll
            for (int i = 0; i < 4; ++i) {
                const int n = bid * 128 + rp * 32 + r * 16 + g * 4 + i;
                uint4 o = make_uint4(cvt_pk((f32x2){acc[r][0][i] * sc, acc[r][1][i] * sc}), cvt_pk((f32x2){acc[r][2][i] * sc, acc[r][3][i] * sc}),
                                     cvt_pk((f32x2){acc[r][4][i] * sc, acc[r][5][i] * sc}), cvt_pk((f32x2){acc[r][6][i] * sc, acc[r][7][i] * sc}));
                if (m == 1) st_b128<AUX_B>(ro, (u32)n * (D * 2) + e16 * 16, o);
                else st_b128<AUX_A>(ro, (u32)n * (D * 2) + e16 * 16, o);
            }
        return;
    }
    {
        u32 w[8];
#pragma unroll
        for (int j = 0; j < 8; ++j)
            w[j] = *reinterpret_cast<const u32*>(wh + (size_t)(D + wv * 8 + j) * D + lane * 2);
        if (t < 192) {
            const int gq = t >> 6, tp = (t >> 4) & 3, c16 = t & 15;
            u32 o[4];
#pragma unroll
            for (int u = 0; u < 2; ++u) {
                const int c = tp * 32 + (c16 >> 2) * 8 + u * 4 + (c16 & 3);
                u32 hi[5], lo[5];
#pragma unroll
                for (int j = 0; j < 5; ++j) {
                    float x = bf2f(we[(size_t)(256 + j) * D + c]) * NLOG2E;
                    u16 hh = f2bf(x);
                    hi[j] = hh; lo[j] = f2bf(x - bf2f(hh));
                }
                o[u * 2] = (gq == 0) ? (hi[0] | (hi[1] << 16)) : (gq == 1) ? (hi[4] | (hi[0] << 16)) : (lo[0] | (lo[1] << 16));
                o[u * 2 + 1] = (gq == 0) ? (hi[2] | (hi[3] << 16)) : (gq == 1) ? lo[4] : (lo[2] | (lo[3] << 16));
            }
            w5g[t] = make_uint4(o[0], o[1], o[2], o[3]);
        }
        const int c = lane * 2;
        const int f16 = ((c >> 3) & 3) * 4 + (c & 3), q = (c >> 2) & 1;
        uint4* o = reinterpret_cast<uint4*>(whT) + (((c >> 5) * 2 + q) * 4 + (wv >> 2)) * 64 + (wv & 3) * 16 + f16;
        o[0] = make_uint4((w[0] & 0xffffu) | (w[1] << 16), (w[2] & 0xffffu) | (w[3] << 16),
                          (w[4] & 0xffffu) | (w[5] << 16), (w[6] & 0xffffu) | (w[7] << 16));
        o[1] = make_uint4((w[0] >> 16) | (w[1] & 0xffff0000u), (w[2] >> 16) | (w[3] & 0xffff0000u),
                          (w[4] >> 16) | (w[5] & 0xffff0000u), (w[6] >> 16) | (w[7] & 0xffff0000u));
    }
}

struct KbSmem {
    uint4 rec[KB_WAVES][CAPR];
    uint2 geo[KB_WAVES][CAPR];
    uint2 lut[8];
    uint4 At[KB_WAVES][2][64];
    uint4 w5s[3][64];
    float macc[KB_WAVES * NPW][AST];
    float pacc[KB_WAVES][NPW][4];
    float posl[KB_WAVES][NPW + 3][4];
    float wxs[D];
    uint4 T[KB_WAVES][64];
    int dt[KB_WAVES][18];
};

__global__ void __launch_bounds__(KB_THREADS, 3)
k_main(const u16* __restrict__ h, const u16* __restrict__ pos,
       const u16* __restrict__ wx,
       const u32* __restrict__ rec_g, const u16* __restrict__ tab,
       const u16* __restrict__ ABU,
       const u16* __restrict__ whT, const uint4* __restrict__ w5g, const uint2* __restrict__ pos8,
       u16* __restrict__ out_h, u16* __restrict__ out_pos,
       unsigned long long mNBW, int N, int G, int CH, int NBW) {
    __shared__ KbSmem S;
    asm volatile("" :: "s"(h), "s"(pos), "s"(wx), "s"(rec_g), "s"(tab), "s"(ABU), "s"(whT), "s"(w5g), "s"(pos8),
                 "s"(out_h), "s"(out_pos), "s"(mNBW), "s"(N), "s"(G), "s"(CH), "s"(NBW));
    const u16* __restrict__ Bg = ABU + (size_t)N * D;
    const u16* __restrict__ Ug = ABU + (size_t)2 * N * D;
    const int t = threadIdx.x, lane = t & 63;
    const int wv = __builtin_amdgcn_readfirstlane(t >> 6);
    const int e16 = lane & 15, g = lane >> 4;
    const int ec = (e16 >> 2) * 8 + (e16 & 3);
    const int nwg = NBW / KB_WAVES, per8 = nwg >> 3;
    int bx = (int)blockIdx.x;
    if (bx < per8 * 8) {
        int k = bx >> 3;
        if (per8 == 96) k = (k & 31) * 3 + (k >> 5);
        bx = (bx & 7) * per8 + k;
    }
    const int wb = bx * KB_WAVES + wv;
    const int N0 = node_start(bx * KB_WAVES, N, NBW, mNBW);
    const int N1 = node_start(bx * KB_WAVES + KB_WAVES, N, NBW, mNBW);
    const int n0 = node_start(wb, N, NBW, mNBW);
    const int nn = node_start(wb + 1, N, NBW, mNBW) - n0;
    const int rowb = n0 - N0;

    int so[GI_MAX], sc[GI_MAX];
    u16 (*tabs)[5] = reinterpret_cast<u16 (*)[5]>(&S.T[0][0]);
    uint2 tv = make_uint2(0u, 0u);
    u32 tv4 = 0;
    auto fetch_tab = [&]() {
        if (t < G) {
            const u16* tp = tab + (size_t)t * (NBW + 4) + bx * KB_WAVES;
            tv = *reinterpret_cast<const uint2*>(tp);
            tv4 = tp[4];
        }
    };
    auto put_tab = [&]() {
        if (t < G) {
            tabs[t][0] = (u16)tv.x; tabs[t][1] = (u16)(tv.x >> 16);
            tabs[t][2] = (u16)tv.y; tabs[t][3] = (u16)(tv.y >> 16);
            tabs[t][4] = (u16)tv4;
        }
    };
    auto load_tab = [&]() {
        int lo_ = lane;
        asm volatile("" : "+v"(lo_));
#pragma unroll
        for (int ci = 0; ci < GI_MAX; ++ci) {
            int c = lo_ + 64 * ci;
            so[ci] = 0; sc[ci] = 0;
            if (c < G) {
                so[ci] = tabs[c][wv];
                sc[ci] = tabs[c][wv + 1];
            }
        }
    };
    auto load_tab_g = [&]() {
        int lo_ = lane;
        asm volatile("" : "+v"(lo_));
#pragma unroll
        for (int ci = 0; ci < GI_MAX; ++ci) {
            int c = lo_ + 64 * ci;
            so[ci] = 0; sc[ci] = 0;
            if (c < G) {
                const u16* tp = tab + (size_t)c * (NBW + 4) + wb;
                so[ci] = tp[0];
                sc[ci] = tp[1];
            }
        }
    };
    fetch_tab();
    u32 a0[NPW];
#pragma unroll
    for (int j = 0; j < NPW; ++j) {
        a0[j] = *reinterpret_cast<const u32*>(ABU + (size_t)min(n0 + j, N - 1) * D + lane * 2);
    }
    const uint4 w40 = w5g[lane], w41 = w5g[lane + 64], w42 = w5g[lane + 128];
    u16 wx0 = wx[lane], wx1 = wx[lane + 64];
    const u16 pz16 = pos[min((n0 + (lane >> 2)) * 3 + (lane & 3), N * 3 - 1)];
    const int NR = N1 - N0;
    uint4 hv4[2], uv4[2];
    auto load_hu = [&]() __attribute__((always_inline)) {
#pragma unroll
        for (int rt = 0; rt < 2; ++rt) {
            int lr = rt * 16 + e16;
            size_t o = (size_t)(N0 + (lr < NR ? lr : 0)) * D + wv * 32 + g * 8;
            hv4[rt] = *reinterpret_cast<const uint4*>(h + o);
            uv4[rt] = *reinterpret_cast<const uint4*>(Ug + o);
        }
    };
    auto stage = [&]() {
#pragma unroll
        for (int j = 0; j < NPW; ++j) a0[j] = (j < nn) ? a0[j] : 0u;
        {
            const int r = (lane * 2) & 31;
            uint2* ap = reinterpret_cast<uint2*>(&S.At[wv][0][(lane >> 4) * 16 + (r >> 3) * 4 + (r & 3)]) + ((r >> 2) & 1);
            ap[0] = make_uint2((a0[0] & 0xffffu) | (a0[1] << 16), (a0[2] & 0xffffu) | (a0[3] << 16));
            ap[2] = make_uint2((a0[0] >> 16) | (a0[1] & 0xffff0000u), (a0[2] >> 16) | (a0[3] & 0xffff0000u));
            ap[128] = make_uint2(a0[4] & 0xffffu, 0u);
            ap[130] = make_uint2(a0[4] >> 16, 0u);
        }
#pragma unroll
        for (int j = 0; j < NPW; ++j)
            if (j < nn) { S.macc[rowb + j][lane] = 0.f; S.macc[rowb + j][64 + lane] = 0.f; }
        uint4* wq = &S.w5s[0][0] + lane;
        wq[0] = w40; wq[64] = w41; wq[128] = w42;
        S.wxs[lane] = bf2f(wx0);
        S.wxs[lane + 64] = bf2f(wx1);
        if (lane < (NPW + 3) * 4) S.posl[wv][lane >> 2][lane & 3] = ((lane >> 2) < nn && (lane & 3) < 3) ? bf2f(pz16) : 0.f;
        if (lane < NPW * 4) S.pacc[wv][lane >> 2][lane & 3] = 0.f;
        if (lane < 16) {
            const int n = lane >> 1, w = lane & 1;
            reinterpret_cast<u32*>(S.lut)[lane] = (n < 5 && ((n & 3) >> 1) == w) ? (0x3F80u << (16 * (n & 1))) : 0u;
        }
    };
    const int CHB4 = ((CH * RW + 3) >> 2) << 2;
    uint4* rec = S.rec[wv];
    u32* key = reinterpret_cast<u32*>(S.rec[wv]);

    put_tab();
    __syncthreads();
    load_tab();
    f32x4 acc2[8];
    int dHead = 0, hflags = 0;
    float hm[8], hp0, hp1, hp2;
    auto reduce = [&]() __attribute__((always_inline)) {
        float wxc[8];
#pragma unroll
        for (int tt = 0; tt < 8; ++tt) wxc[tt] = S.wxs[(tt >> 1) * 32 + (tt & 1) * 4 + ec];
        float pv0 = 0.f, pv1 = 0.f, pv2 = 0.f;
#pragma unroll
        for (int tt = 0; tt < 8; ++tt) {
            hm[tt] = acc2[tt][0] * NLN2;
            pv0 += acc2[tt][1] * wxc[tt];
            pv1 += acc2[tt][2] * wxc[tt];
            pv2 += acc2[tt][3] * wxc[tt];
            acc2[tt] = (f32x4){0.f, 0.f, 0.f, 0.f};
        }
        hp0 = rsum16(pv0) * NLN2; hp1 = rsum16(pv1) * NLN2; hp2 = rsum16(pv2) * NLN2;
    };
    auto commit = [&](int d, auto accc) __attribute__((always_inline)) {
        constexpr bool ACC = decltype(accc)::value;
        const int bk = (d >> 16) & 3;
        const int nl = ((d >> 18) & 1) * 4 + g;
        const int rb = (d >> 20) & 31, nb = (d >> 25) & 7;
        const bool on = nl < nb;
        float* mrow = &S.macc[rb + (on ? nl : 0)][ec];
        float* prow = &S.pacc[bk][on ? nl : 0][0];
        float old[8], po[3];
        if (ACC) {
#pragma unroll
            for (int tt = 0; tt < 8; ++tt) old[tt] = mrow[(tt >> 1) * 32 + (tt & 1) * 4];
            po[0] = prow[0]; po[1] = prow[1]; po[2] = prow[2];
            CBAR();
        }
        if (on) {
#pragma unroll
            for (int tt = 0; tt < 8; ++tt) mrow[(tt >> 1) * 32 + (tt & 1) * 4] = ACC ? old[tt] + hm[tt] : hm[tt];
            if (e16 == 0) {
                prow[0] = (ACC ? po[0] : 0.f) + hp0;
                prow[1] = (ACC ? po[1] : 0.f) + hp1;
                prow[2] = (ACC ? po[2] : 0.f) + hp2;
            }
        }
    };
    auto flush = [&](int d, auto accc) __attribute__((always_inline)) { reduce(); commit(d, accc); };
    auto finish = [&]() __attribute__((always_inline)) {
        const std::integral_constant<bool, true> accc;
        __syncthreads();
        for (int w = 1; w < KB_WAVES; ++w) {
            if ((hflags & 1) && (wv == w || !(hflags & 2))) commit(dHead, accc);
            __syncthreads();
            if (!(hflags & 2)) break;
        }
    };
    auto round = [&](const int base, auto firstc) __attribute__((always_inline)) -> bool {
        constexpr bool FIRST = decltype(firstc)::value;
        int T;
        int fo[GI_MAX];
        {
            int tot = 0;
#pragma unroll
            for (int ci = 0; ci < GI_MAX; ++ci) { sc[ci] -= so[ci]; tot += sc[ci]; }
            const int x = wave_scan(tot);
            T = __builtin_amdgcn_readlane(x, 63);
            int off = x - tot - base;
#pragma unroll
            for (int ci = 0; ci < GI_MAX; ++ci) { fo[ci] = off; off += sc[ci]; }
        }
        const int Tr = max(0, min(CAP, T - base));
        int lo2 = lane;
        asm volatile("" : "+v"(lo2));
        {
            int kp[GI_MAX], kn[GI_MAX], kv[GI_MAX], mx = 0;
#pragma unroll
            for (int ci = 0; ci < GI_MAX; ++ci) {
                const int qa = max(0, -fo[ci]);
                kn[ci] = min(sc[ci], CAP - fo[ci]) - qa;
                kp[ci] = fo[ci] + qa;
                kv[ci] = (lo2 + 64 * ci) * CHB4 + (so[ci] + qa) * RW;
                mx = max(mx, kn[ci]);
            }
#pragma clang loop unroll(disable) vectorize(disable) interleave(disable)
            for (int q = 0; q < mx; ++q) {
#pragma unroll
                for (int ci = 0; ci < GI_MAX; ++ci)
                    if (q < kn[ci]) key[kp[ci] + q] = (u32)(kv[ci] + q * RW);
            }
        }
        CBAR();
        const int spl = (Tr + 63) >> 6;
        const int pb = lo2 * spl;
        uint4 r[RPL];
        uint2 ps[RPL];
#pragma unroll
        for (int i = 0; i < RPL; ++i) {
            r[i] = make_uint4(0xffffffffu, 0u, 0u, 0u);
            if (i < spl && pb + i < Tr) {
                const u32* rp = rec_g + key[pb + i];
                r[i] = make_uint4(rp[0], rp[1], rp[2], 0u);
            }
        }
        if (FIRST) stage();
#pragma unroll
        for (int i = 0; i < RPL; ++i) {
            ps[i] = make_uint2(0u, 0u);
            if (r[i].x != 0xffffffffu) ps[i] = pos8[r[i].x >> 3];
        }
        int posn[RPL];
        int c0, g1s, cnt4;
        {
            u32 el[RPL], eh[RPL], tl_ = 0, th_ = 0;
#pragma unroll
            for (int i = 0; i < RPL; ++i) {
                const unsigned long long oh = 1ull << ((r[i].x & 7u) * 8u);
                el[i] = tl_; eh[i] = th_;
                tl_ += (u32)oh; th_ += (u32)(oh >> 32);
            }
            const u32 il = (u32)wave_scan((int)tl_), ih = (u32)wave_scan((int)th_);
            const u32 Tl = (u32)__builtin_amdgcn_readlane((int)il, 63);
            cnt4 = __builtin_amdgcn_readlane((int)ih, 63) & 0xff;
            c0 = (int)((Tl * 0x01010101u) >> 24);
            g1s = (c0 + 15) & ~15;
            const u32 bl = il - tl_ + Tl * 0x01010100u, bh = ih - th_;
#pragma unroll
            for (int i = 0; i < RPL; ++i) {
                const u32 nd = r[i].x & 7u;
                const unsigned long long v = ((unsigned long long)(bh + eh[i]) << 32) | (unsigned long long)(bl + el[i]);
                posn[i] = (int)((u32)(v >> (nd * 8u)) & 0xffu) + ((nd == 4u) ? g1s : 0);
            }
        }
        const int g1e = (g1s + cnt4 + 15) & ~15;
        CBAR();
#pragma unroll
        for (int i = 0; i < RPL; ++i)
            if (r[i].x != 0xffffffffu) {
                const float4 pn = *reinterpret_cast<const float4*>(&S.posl[wv][r[i].x & 7u][0]);
                float dx = pn.x - __uint_as_float(ps[i].x << 16), dy = pn.y - __uint_as_float(ps[i].x & 0xffff0000u);
                float dz = pn.z - __uint_as_float(ps[i].y << 16);
                float d2v = dx * dx + dy * dy + dz * dz;
                u16 d2h = f2bf(d2v);
                u16 d2l = f2bf(d2v - bf2f(d2h));
                rec[posn[i]] = make_uint4((u32)d2h | (r[i].y << 16), (r[i].y >> 16) | (r[i].z << 16), (r[i].z >> 16) | ((u32)d2l << 16), r[i].x);
                S.geo[wv][posn[i]] = make_uint2(0x3F80u | ((u32)f2bf(dx) << 16), pack2(dy, dz));
            }
        if (lo2 < 32) {
            const int pp = (lo2 < 16) ? (c0 + lo2) : (g1s + cnt4 + lo2 - 16);
            if (pp < ((lo2 < 16) ? g1s : g1e)) { rec[pp] = make_uint4(0u, 0u, 0u, 7u); S.geo[wv][pp] = make_uint2(0u, 0u); }
        } else if (lo2 < 32 + 18) {
            const int l = lo2 - 32, n0t = g1s >> 4, n1t = g1e >> 4;
            const int grp = (l >= n0t) ? 1 : 0;
            const int dsc = ((grp ? n1t : n0t) - l) | ((l == 0 || l == n0t) ? 32 : 0) | ((wv * (CAPR / 16) + l) << 8) |
                            (wv << 16) | (grp << 18) | (rowb << 20) | (nn << 25);
            S.dt[wv][l] = (l == 17) ? (n1t | ((base + CAP < T) ? 256 : 0)) : dsc;
        }
        if (FIRST) load_hu();
        __syncthreads();

        int lq = lane;
        asm volatile("" : "+v"(lq));
        int cb1, cb2, cb3, TT, anymore;
        {
            const int q = S.dt[lq & 3][17];
            const int q0 = __builtin_amdgcn_readlane(q, 0), q1 = __builtin_amdgcn_readlane(q, 1);
            const int q2 = __builtin_amdgcn_readlane(q, 2), q3 = __builtin_amdgcn_readlane(q, 3);
            cb1 = q0 & 255; cb2 = cb1 + (q1 & 255); cb3 = cb2 + (q2 & 255); TT = cb3 + (q3 & 255);
            anymore = (q0 | q1 | q2 | q3) & 256;
        }
        const int ts = (TT * wv) >> 2, te = (TT * (wv + 1)) >> 2;
        const int nst = te - ts;
        const int s1 = TT >> 2, s2 = (TT * 2) >> 2, s3 = (TT * 3) >> 2;
        int descv;
        {
            int tl = (lq < 32) ? (ts + lq) : ((lq & 1) ? s2 : s1);
            tl = min(tl, max(TT - 1, 0));
            const int o = (tl >= cb3) ? (3 * 18 - cb3) : (tl >= cb2) ? (2 * 18 - cb2) : (tl >= cb1) ? (18 - cb1) : 0;
            descv = (&S.dt[0][0])[tl + o];
        }
        const int dF = __builtin_amdgcn_readlane(descv, 0);
        const bool has_head = (nst > 0) && !(dF & 32);
        const int hl = has_head ? min(nst, dF & 31) : 0;
        const int nh = nst - hl;
        bool serial;
        {
            const int d1 = __builtin_amdgcn_readlane(descv, 32), d2 = __builtin_amdgcn_readlane(descv, 33);
            serial = (!(d1 & 32) && s1 + (d1 & 31) > s2) || (!(d2 & 32) && s2 + (d2 & 31) > s3);
        }
        descv |= (lq >= hl && ((descv & 31) == 1 || lq == nst - 1)) ? (1 << 19) : 0;
        auto dsc_at = [&](int i) __attribute__((always_inline)) {
            return __builtin_amdgcn_readlane(descv, (i < nh) ? (i + hl) : (i - nh));
        };
        bf16x4 sel;
        {
            const int dl = (lq & 15) - 4 * (lq >> 4);
            unsigned long long v = (dl >= 0 && dl < 4) ? (0x3F80ull << (16 * dl)) : 0ull;
            sel = as_bf16x4(make_uint2((u32)v, (u32)(v >> 32)));
        }
        const char* w5b = (g < 3) ? reinterpret_cast<const char*>(&S.w5s[g][e16]) : reinterpret_cast<const char*>(&S.At[0][0][e16]);
        const int w5m = (g < 3) ? 0 : (int)sizeof(S.At[0][0]);
        const char* recb = reinterpret_cast<const char*>(&S.rec[0][e16]);
        const char* geob = reinterpret_cast<const char*>(&S.geo[0][e16]);
        const char* Bb = reinterpret_cast<const char*>(Bg);
        const u32 g16 = (u32)(lq & 3) * 16u;
        const char* recq = reinterpret_cast<const char*>(&S.rec[0][lq >> 2]);
        uint4* twp = &S.T[wv][(lq & 3) * 16 + ((lq >> 2) ^ ((lq & 3) * 2))];
        const uint4* trp = &S.T[wv][g * 16 + (e16 ^ (g * 2))];
        uint2 (*Mw)[4] = reinterpret_cast<uint2 (*)[4]>(&S.T[wv][0]);
        const char* xrb = recb + ((g == 1) ? 8 : 0);
        const u32 psel = (g == 1) ? 0x0c0c0504u : 0x03020100u;
        bf16x8 gA[4], gB[4];
        u32 pA0 = 0, pB0 = 0;
        auto issue = [&](int d, bf16x8 (&gat)[4], u32& q0) __attribute__((always_inline)) {
            const u32 qs = reinterpret_cast<const uint4*>(recq + (d & 0xff00))->w;
            q0 = reinterpret_cast<const uint4*>(recb + (d & 0xff00))->w;
            const u32 off = ((qs >> 3) << 8) + g16;
#pragma unroll
            for (int tp = 0; tp < 4; ++tp)
                gat[tp] = *reinterpret_cast<const bf16x8*>(Bb + off + tp * 64);
        };
#pragma unroll
        for (int tt = 0; tt < 8; ++tt) acc2[tt] = (f32x4){0.f, 0.f, 0.f, 0.f};
        auto sgi = [&](int d) __attribute__((always_inline)) { return ((d >> 15) & 6) | ((d >> 18) & 1); };
        auto compute = [&](int d, bf16x8 (&gat)[4], u32 q0) __attribute__((always_inline)) {
            const int bg = sgi(d);
            const u32 nd = q0 & 7u;
            const char* xp = (g < 3) ? (xrb + (d & 0xff00)) : (reinterpret_cast<const char*>(S.lut) + (nd << 3));
            uint2 xu = *reinterpret_cast<const uint2*>(xp);
            const uint2 gq = *reinterpret_cast<const uint2*>(geob + ((d & 0xff00) >> 1));
            const uint4* w5p = reinterpret_cast<const uint4*>(w5b + w5m * bg);
#pragma unroll
            for (int tp = 0; tp < 4; ++tp) {
                CBAR();
                *twp = *reinterpret_cast<const uint4*>(&gat[tp]);
                CBAR();
                gat[tp] = as_bf16x8(*trp);
            }
            CBAR();
            Mw[e16][g] = ((nd & 3u) == (u32)g) ? gq : make_uint2(0u, 0u);
            xu.y = __builtin_amdgcn_perm(xu.x, xu.y, psel);
            const bf16x4 x4 = as_bf16x4(xu);
            CBAR();
            u32 sw01, sw23;
            {
                const u16* wp = reinterpret_cast<const u16*>(&Mw[g * 4][0]) + e16;
                sw01 = (u32)wp[0] | ((u32)wp[16] << 16);
                sw23 = (u32)wp[32] | ((u32)wp[48] << 16);
            }
            const bf16x4 sop = as_bf16x4(make_uint2(sw01, sw23));
            uint4 w5[4];
#pragma unroll
            for (int tp = 0; tp < 4; ++tp) w5[tp] = w5p[tp * 16];
            auto feat = [&](int tt) __attribute__((always_inline)) {
                const uint4 w = w5[tt >> 1];
                return __builtin_amdgcn_mfma_f32_16x16x16bf16_1k(x4, as_bf16x4((tt & 1) ? make_uint2(w.z, w.w) : make_uint2(w.x, w.y)),
                                                                 (f32x4){0.f, 0.f, 0.f, 0.f}, 0, 0, 0);
            };
            auto gath = [&](int tt, f32x4 c) __attribute__((always_inline)) {
                const u32x4 gq4 = *reinterpret_cast<const u32x4*>(&gat[tt >> 1]);
                return __builtin_amdgcn_mfma_f32_16x16x16bf16_1k(as_bf16x4(make_uint2(gq4[(tt & 1) * 2], gq4[(tt & 1) * 2 + 1])), sel, c, 0, 0, 0);
            };
            f32x4 acc = gath(0, feat(0));
#pragma unroll
            for (int tt = 0; tt < 8; ++tt) {
                f32x4 accN = acc;
                __builtin_amdgcn_sched_barrier(0);
                if (tt < 7) accN = feat(tt + 1);
                __builtin_amdgcn_sched_barrier(0);
                float e0 = __builtin_amdgcn_exp2f(acc[0]), e1 = __builtin_amdgcn_exp2f(acc[1]);
                float e2 = __builtin_amdgcn_exp2f(acc[2]), e3 = __builtin_amdgcn_exp2f(acc[3]);
                __builtin_amdgcn_sched_barrier(0);
                if (tt < 7) accN = gath(tt + 1, accN);
                __builtin_amdgcn_sched_barrier(0);
                e0 += 1.f; e1 += 1.f; e2 += 1.f; e3 += 1.f;
                asm("" : "+v"(e0)); asm("" : "+v"(e1)); asm("" : "+v"(e2)); asm("" : "+v"(e3));
                float y0 = acc[0] * __builtin_amdgcn_rcpf(e0), y1 = acc[1] * __builtin_amdgcn_rcpf(e1);
                float y2 = acc[2] * __builtin_amdgcn_rcpf(e2), y3 = acc[3] * __builtin_amdgcn_rcpf(e3);
                asm("" : "+v"(y0)); asm("" : "+v"(y1)); asm("" : "+v"(y2)); asm("" : "+v"(y3));
                u32 k01 = cvt_pk((f32x2){y0, y1}), k23 = cvt_pk((f32x2){y2, y3});
                bf16x4 mb = as_bf16x4(make_uint2(k01, k23));
                __builtin_amdgcn_sched_barrier(0);
                acc2[tt] = __builtin_amdgcn_mfma_f32_16x16x16bf16_1k(sop, mb, acc2[tt], 0, 0, 0);
                acc = accN;
            }
        };
        const std::integral_constant<bool, !FIRST> dirc;
        if (nst > 0) {
            issue(dsc_at(0), gA, pA0);
            int i = 0;
            for (; i + 1 < nst; i += 2) {
                {
                    const int rem = nst - i;
                    if (rem >= 5) __builtin_amdgcn_s_setprio(3);
                    else if (rem >= 4) __builtin_amdgcn_s_setprio(2);
                    else if (rem >= 3) __builtin_amdgcn_s_setprio(1);
                    else __builtin_amdgcn_s_setprio(0);
                }
                const int dA = dsc_at(i);
                const int dB = dsc_at(i + 1);
                const int dC = dsc_at(min(i + 2, nst - 1));
                issue(dB, gB, pB0);
                compute(dA, gA, pA0);
                if (dA & (1 << 19)) flush(dA, dirc);
                issue(dC, gA, pA0);
                compute(dB, gB, pB0);
                if (dB & (1 << 19)) flush(dB, dirc);
            }
            if (i < nst) {
                const int dA = dsc_at(i);
                compute(dA, gA, pA0);
                if (dA & (1 << 19)) flush(dA, dirc);
            }
        }
        dHead = dF;
        hflags = (has_head ? 1 : 0) | (serial ? 2 : 0);
        reduce();
        return anymore != 0;
    };
    if (round(0, std::integral_constant<bool, true>())) {
        for (int base = CAP;; base += CAP) {
            finish();
            load_tab_g();
            if (!round(base, std::integral_constant<bool, false>())) break;
        }
    }

    int le = lane;
    asm volatile("" : "+v"(le));
    const int f16 = le & 15, gg = le >> 4;
    bf16x8 bw[2][4];
#pragma unroll
    for (int q = 0; q < 2; ++q)
#pragma unroll
        for (int ks = 0; ks < 4; ++ks)
            bw[q][ks] = *reinterpret_cast<const bf16x8*>(whT + (((wv * 2 + q) * 4 + ks) * 64 + le) * 8);
    finish();
    if (le < nn * 3) {
        int n = le / 3, cpt = le - n * 3;
        out_pos[(size_t)(n0 + n) * 3 + cpt] = f2bf(S.posl[wv][n][cpt] + S.pacc[wv][n][cpt]);
    }
#pragma unroll
    for (int rt = 0; rt < 2; ++rt) {
        if (rt * 16 < NR) {
            bf16x8 af[4];
            int lr = rt * 16 + f16;
#pragma unroll
            for (int ks = 0; ks < 4; ++ks) {
                const float* mp = &S.macc[lr < NR ? lr : 0][ks * 32 + gg * 8];
                af[ks] = as_bf16x8(make_uint4(pack2(mp[0], mp[1]), pack2(mp[2], mp[3]),
                                              pack2(mp[4], mp[5]), pack2(mp[6], mp[7])));
            }
            u32 ov[4];
#pragma unroll
            for (int q = 0; q < 2; ++q) {
                const u32 u0 = q ? uv4[rt].z : uv4[rt].x, u1 = q ? uv4[rt].w : uv4[rt].y;
                const u32 h0 = q ? hv4[rt].z : hv4[rt].x, h1 = q ? hv4[rt].w : hv4[rt].y;
                f32x4 acc = {__uint_as_float(u0 << 16), __uint_as_float(u0 & 0xffff0000u),
                             __uint_as_float(u1 << 16), __uint_as_float(u1 & 0xffff0000u)};
#pragma unroll
                for (int ks = 0; ks < 4; ++ks)
                    acc = __builtin_amdgcn_mfma_f32_16x16x32_bf16(bw[q][ks], af[ks], acc, 0, 0, 0);
                ov[q * 2] = pack2(__uint_as_float(h0 << 16) + silu_f(acc[0]), __uint_as_float(h0 & 0xffff0000u) + silu_f(acc[1]));
                ov[q * 2 + 1] = pack2(__uint_as_float(h1 << 16) + silu_f(acc[2]), __uint_as_float(h1 & 0xffff0000u) + silu_f(acc[3]));
            }
            if (lr < NR)
                st_b128<AUX_O>(mk_rsrc(out_h, (u32)N * (D * 2)), (u32)(N0 + lr) * (D * 2) + wv * 64 + gg * 16, make_uint4(ov[0], ov[1], ov[2], ov[3]));
        }
    }
}

extern "C" void kernel_launch(void* const* d_in, const int* in_sizes, int n_in,
                              void* d_out, int out_size, void* d_ws, size_t ws_size,
                              hipStream_t stream) {
    const u16* h   = (const u16*)d_in[0];
    const u16* pos = (const u16*)d_in[1];
    const u16* ea  = (const u16*)d_in[2];
    const int* src = (const int*)d_in[3];
    const int* dst = (const int*)d_in[4];
    const u16* we  = (const u16*)d_in[5];
    const u16* wx  = (const u16*)d_in[6];
    const u16* wh  = (const u16*)d_in[7];
    int N = in_sizes[0] / D;
    int E = in_sizes[3];
    u16* out_h = (u16*)d_out;
    u16* out_pos = out_h + (size_t)N * D;

    int G = 128;
    while ((E + G - 1) / G > KA_CH_MAX && G < 64 * GI_MAX) G += 64;
    int CH = (E + G - 1) / G;
    int NBW = 3072;
    while (NBW * NPW < N && NBW < NBW_MAX) NBW += 4;
    int nGemm = (N + 127) / 128;
    unsigned long long mN = (1ull << 40) / (unsigned long long)N + 1ull;
    unsigned long long mNBW = (1ull << 40) / (unsigned long long)NBW + 1ull;

    char* ws = (char*)d_ws;
    size_t off = 0;
    auto take = [&](size_t bytes) { char* p = ws + off; off += (bytes + 255) & ~(size_t)255; return p; };
    u32* rec_g = (u32*)take((size_t)G * ((CH * RW + 3) >> 2) * 16 + 64);
    u16* tab = (u16*)take((size_t)G * (NBW + 4) * 2);
    u16* ABU = (u16*)take((size_t)3 * N * D * 2);
    u16* whT = (u16*)take((size_t)D * D * 2);
    uint4* w5g = (uint4*)take((size_t)256 * 16);
    uint2* pos8 = (uint2*)take((size_t)N * 8);

    k_part<<<G + nGemm + 1, KA_THREADS, 0, stream>>>(h, pos, src, dst, ea, we, wh, rec_g, tab, ABU, whT, w5g, pos8,
                                                      mN, mNBW, N, E, G, CH, NBW, nGemm);
    k_main<<<NBW / KB_WAVES, KB_THREADS, 0, stream>>>(h, pos, wx, rec_g, tab, ABU, whT, w5g, pos8,
                                                     out_h, out_pos, mNBW, N, G, CH, NBW);
}
```

```cpp
#include <hip/hip_runtime.h>
#include <hip/hip_bf16.h>
#include <stdint.h>
#include <type_traits>

typedef unsigned short u16;
typedef unsigned int u32;
typedef __attribute__((ext_vector_type(8))) short bf16x8;
typedef __attribute__((ext_vector_type(4))) float f32x4;
typedef __attribute__((ext_vector_type(4))) short bf16x4;
typedef __attribute__((ext_vector_type(2))) float f32x2;
typedef __attribute__((ext_vector_type(4))) unsigned int u32x4;
__device__ __forceinline__ void st_nt(void* p, uint4 v) { u32x4 x = {v.x, v.y, v.z, v.w}; __builtin_nontemporal_store(x, reinterpret_cast<u32x4*>(p)); }

typedef __attribute__((ext_vector_type(2))) unsigned int u32x2;
typedef __attribute__((ext_vector_type(4), aligned(4))) unsigned int u32x4u;
__device__ __forceinline__ __amdgpu_buffer_rsrc_t mk_rsrc(const void* p, u32 bytes) {
    return __builtin_amdgcn_make_buffer_rsrc(const_cast<void*>(p), (short)0, (int)bytes, 0x00020000);
}
template <int AUX>
__device__ __forceinline__ void st_b128(__amdgpu_buffer_rsrc_t r, u32 off, uint4 v) {
    u32x4 x = {v.x, v.y, v.z, v.w};
    __builtin_amdgcn_raw_buffer_store_b128(x, r, (int)off, 0, AUX);
}
template <int AUX>
__device__ __forceinline__ void st_b64(__amdgpu_buffer_rsrc_t r, u32 off, uint2 v) {
    u32x2 x = {v.x, v.y};
    __builtin_amdgcn_raw_buffer_store_b64(x, r, (int)off, 0, AUX);
}
#define AUX_A 16
#define AUX_B 16
#define AUX_U 16
#define AUX_R 16
#define AUX_O 0

#define D 128
#define KA_THREADS 1024
#define KA_ITERS 4
#define KA_CH_MAX 4032
#define NBW_MAX 4096
#define KB_THREADS 256
#define KB_WAVES 4
#define NPW 5
#define RPL 4
#define CAP 240
#define CAPS 272
#define CAPR (CAP + 32)
#define RW 3
#define GI_MAX 2
#define AST (D + 4)
#define CBAR() asm volatile("" ::: "memory")
#define NLOG2E (-1.4426950408889634f)
#define NLN2 (-0.6931471805599453f)

__device__ __forceinline__ float bf2f(u16 v) { return __uint_as_float(((u32)v) << 16); }
__device__ __forceinline__ u16 f2bf(float f) {
    __hip_bfloat16 b = __float2bfloat16(f);
    return *reinterpret_cast<u16*>(&b);
}
__device__ __forceinline__ float silu_f(float x) {
    float e = __builtin_amdgcn_exp2f(x * NLOG2E);
    return x * __builtin_amdgcn_rcpf(1.0f + e);
}
__device__ __forceinline__ u32 pack2(float a, float b) {
    return (u32)f2bf(a) | ((u32)f2bf(b) << 16);
}
typedef __attribute__((ext_vector_type(2))) __bf16 bf16x2;
__device__ __forceinline__ u32 cvt_pk(f32x2 v) {
    bf16x2 p = __builtin_convertvector(v, bf16x2);
    return *reinterpret_cast<u32*>(&p);
}
__device__ __forceinline__ bf16x8 as_bf16x8(uint4 v) { return *reinterpret_cast<bf16x8*>(&v); }
__device__ __forceinline__ bf16x4 as_bf16x4(uint2 v) { return *reinterpret_cast<bf16x4*>(&v); }
template <int CTRL>
__device__ __forceinline__ float dpp_f(float x) {
    return __int_as_float(__builtin_amdgcn_update_dpp(0, __float_as_int(x), CTRL, 0xf, 0xf, true));
}
__device__ __forceinline__ float rsum16(float x) {
    x += dpp_f<0x128>(x);
    x += dpp_f<0x124>(x);
    x += dpp_f<0x122>(x);
    x += dpp_f<0x121>(x);
    return x;
}
template <int CTRL, int RM>
__device__ __forceinline__ int dpp_add(int x) { return x + __builtin_amdgcn_update_dpp(0, x, CTRL, RM, 0xf, false); }
__device__ __forceinline__ int wave_scan(int x) {
    x = dpp_add<0x111, 0xf>(x);
    x = dpp_add<0x112, 0xf>(x);
    x = dpp_add<0x114, 0xf>(x);
    x = dpp_add<0x118, 0xf>(x);
    x = dpp_add<0x142, 0xa>(x);
    x = dpp_add<0x143, 0xc>(x);
    return x;
}
__device__ __forceinline__ u32 mdiv(u32 x, unsigned long long m) {
    return (u32)(((unsigned long long)x * m) >> 40);
}
__device__ __forceinline__ int node_start(int b, int N, int NBW, unsigned long long mNBW) {
    return (int)mdiv((u32)b * (u32)N + (u32)NBW - 1u, mNBW);
}

__global__ void __launch_bounds__(KA_THREADS)
k_part(const u16* __restrict__ h, const u16* __restrict__ pos, const int* __restrict__ src, const int* __restrict__ dst,
       const u16* __restrict__ ea, const u16* __restrict__ we, const u16* __restrict__ wh,
       u32* __restrict__ rec_g, u16* __restrict__ tab, u16* __restrict__ ABU,
       u16* __restrict__ whT, uint4* __restrict__ w5g, uint2* __restrict__ pos8,
       unsigned long long mN, unsigned long long mNBW,
       int N, int E, int G, int CH, int NBW, int nGemm) {
    __shared__ __align__(16) u16 smem[4 * D * D];
    static_assert(NBW_MAX * 8 + (NBW_MAX + 8) + KA_CH_MAX * RW * 2 + 8 <= 4 * D * D, "lds");
    __shared__ int wsum[KA_THREADS / 64];
    asm volatile("" :: "s"(h), "s"(pos), "s"(src), "s"(dst), "s"(ea), "s"(we), "s"(wh), "s"(rec_g), "s"(tab), "s"(ABU),
                 "s"(whT), "s"(w5g), "s"(pos8), "s"(mN), "s"(mNBW), "s"(N), "s"(E), "s"(G), "s"(CH), "s"(NBW), "s"(nGemm));
    const int t = threadIdx.x, lane = t & 63;
    const int wv = __builtin_amdgcn_readfirstlane(t >> 6);
    int bid = blockIdx.x;
    if (bid < G) {
        uint4* cnt = reinterpret_cast<uint4*>(smem);
        u16* base16 = smem + NBW_MAX * 8;
        u32* lrec = reinterpret_cast<u32*>(base16 + NBW_MAX + 8);
        const int e0 = bid * CH;
        const int ne = max(0, min(E, e0 + CH) - e0);
        const int QPW = ((CH >> 2) + 15) >> 4;
        const int el = (lane < QPW) ? (wv * QPW + lane) * 4 : CH;
        const int eb = min(e0 + el, E - 4);
        const int i0 = e0 + el - eb, i1 = e0 + ne - eb;
        const u32x4 dq = *reinterpret_cast<const u32x4u*>(dst + eb);
        int dv[KA_ITERS];
#pragma unroll
        for (int i = 0; i < NBW_MAX / KA_THREADS; ++i)
            if (t + i * KA_THREADS < NBW) cnt[t + i * KA_THREADS] = make_uint4(0u, 0u, 0u, 0u);
        __syncthreads();
        int eb2 = eb;
        asm volatile("" : "+v"(eb2) : "v"(dq[0]));
        const u32x4 sq = *reinterpret_cast<const u32x4u*>(src + eb2);
        const u32x4 ea0 = *reinterpret_cast<const u32x4u*>(ea + (size_t)eb2 * 4);
        const u32x4 ea1 = *reinterpret_cast<const u32x4u*>(ea + (size_t)eb2 * 4 + 8);
        const int wq = wv >> 2, wsft = (wv & 3) * 8;
        const u32 inc = 1u << wsft;
        int bv[KA_ITERS], rk[KA_ITERS];
#pragma unroll
        for (int i = 0; i < KA_ITERS; ++i) {
            const int b = (int)mdiv(dq[i] * (u32)NBW, mN);
            bv[i] = b;
            rk[i] = 0;
            if (i >= i0 && i < i1) rk[i] = (int)atomicAdd(reinterpret_cast<u32*>(cnt) + b * 4 + wq, inc);
            dv[i] = (int)dq[i] - node_start(b, N, NBW, mNBW);
        }
        __syncthreads();
        {
            const int per = (NBW + KA_THREADS - 1) / KA_THREADS;
            const int b0 = t * per;
            int c[NBW_MAX / KA_THREADS], tot = 0;
            u32x4 q[NBW_MAX / KA_THREADS];
#pragma unroll
            for (int j = 0; j < NBW_MAX / KA_THREADS; ++j)
                q[j] = *reinterpret_cast<const u32x4*>(&cnt[min(b0 + j, NBW_MAX - 1)]);
#pragma unroll
            for (int j = 0; j < NBW_MAX / KA_THREADS; ++j) {
                c[j] = (int)__builtin_amdgcn_sad_u8(q[j][0], 0u, __builtin_amdgcn_sad_u8(q[j][1], 0u,
                            __builtin_amdgcn_sad_u8(q[j][2], 0u, __builtin_amdgcn_sad_u8(q[j][3], 0u, 0u))));
                c[j] = (j < per && b0 + j < NBW) ? c[j] : 0;
                tot += c[j];
            }
            const int x = wave_scan(tot);
            if (lane == 63) wsum[wv] = x;
            __syncthreads();
            int y = wsum[lane & 15];
            y = ((lane & 15) < wv) ? y : 0;
            y += __builtin_amdgcn_update_dpp(0, y, 0x128, 0xf, 0xf, true);
            y += __builtin_amdgcn_update_dpp(0, y, 0x124, 0xf, 0xf, true);
            y += __builtin_amdgcn_update_dpp(0, y, 0x122, 0xf, 0xf, true);
            y += __builtin_amdgcn_update_dpp(0, y, 0x121, 0xf, 0xf, true);
            int off = y + x - tot;
#pragma unroll
            for (int j = 0; j < NBW_MAX / KA_THREADS; ++j) {
                if (j < per && b0 + j < NBW) base16[b0 + j] = (u16)off;
                off += c[j];
            }
            if (t < 4) base16[NBW + t] = (u16)ne;
        }
        __syncthreads();
        {
            const u32 m0 = (wq > 0) ? ~0u : inc - 1u;
            const u32 m1 = (wq > 1) ? ~0u : (wq == 1) ? inc - 1u : 0u;
            const u32 m2 = (wq > 2) ? ~0u : (wq == 2) ? inc - 1u : 0u;
            const u32 m3 = (wq == 3) ? inc - 1u : 0u;
            uint4 q[KA_ITERS];
            u32 bs[KA_ITERS];
#pragma unroll
            for (int i = 0; i < KA_ITERS; ++i) {
                q[i] = cnt[bv[i]]; bs[i] = base16[bv[i]];
            }
#pragma unroll
            for (int i = 0; i < KA_ITERS; ++i) {
                if (i >= i0 && i < i1) {
                    u32 p = bs[i] + (((u32)rk[i] >> wsft) & 255u);
                    p = __builtin_amdgcn_sad_u8(q[i].x & m0, 0u, p);
                    p = __builtin_amdgcn_sad_u8(q[i].y & m1, 0u, p);
                    p = __builtin_amdgcn_sad_u8(q[i].z & m2, 0u, p);
                    p = __builtin_amdgcn_sad_u8(q[i].w & m3, 0u, p);
                    u32* o = lrec + p * RW;
                    o[0] = (sq[i] << 3) | (u32)dv[i];
                    o[1] = (i < 2) ? ea0[(i & 1) * 2] : ea1[(i & 1) * 2]; o[2] = (i < 2) ? ea0[(i & 1) * 2 + 1] : ea1[(i & 1) * 2 + 1];
                }
            }
        }
        {
            const __amdgpu_buffer_rsrc_t rt = mk_rsrc(tab + (size_t)bid * (NBW + 4), (u32)(NBW + 4) * 2u);
#pragma unroll
            for (int i = 0; i < (NBW_MAX / 4 + KA_THREADS) / KA_THREADS; ++i) {
                const int p = t + i * KA_THREADS;
                if (p <= (NBW >> 2)) st_b64<AUX_R>(rt, (u32)p * 8u, reinterpret_cast<const uint2*>(base16)[p]);
            }
        }
        __syncthreads();
        {
            const int CHB = (CH * RW + 3) >> 2;
            const int nq = (ne * RW + 3) >> 2;
            const __amdgpu_buffer_rsrc_t rr = mk_rsrc(reinterpret_cast<uint4*>(rec_g) + (size_t)bid * CHB, (u32)nq * 16u);
            const uint4* lq4 = reinterpret_cast<const uint4*>(lrec);
#pragma unroll
            for (int i = 0; i < KA_ITERS * RW / 4; ++i) {
                int p = t + i * KA_THREADS;
                if (p < nq) st_b128<AUX_R>(rr, (u32)p * 16u, lq4[p]);
            }
        }
        return;
    }
    bid -= G;
    if (bid < nGemm) {
        const int e16 = lane & 15, g = lane >> 4;
        char* wl = reinterpret_cast<char*>(smem);
        char* hl = wl + 3 * D * 256;
        u32x4 r[8];
        u32 p01 = 0, p2 = 0;
        if (wv < 12) {
            const int kb = (wv & 3) * 4 + g;
            const u16* wsrc = ((wv < 8) ? we + (size_t)(wv >> 2) * D * D : wh) + (size_t)kb * 8 * D + e16 * 8;
#pragma unroll
            for (int i = 0; i < 8; ++i) r[i] = *reinterpret_cast<const u32x4*>(wsrc + i * D);
            CBAR();
            char* q = wl + (wv >> 2) * (D * 256) + e16 * 256 + ((kb ^ e16) << 4);
#pragma unroll
            for (int j = 0; j < 8; ++j) {
                const u32 sel = (j & 1) ? 0x07060302u : 0x05040100u;
                u32x4 o;
#pragma unroll
                for (int p = 0; p < 4; ++p) o[p] = __builtin_amdgcn_perm(r[2 * p + 1][j >> 1], r[2 * p][j >> 1], sel);
                *reinterpret_cast<u32x4*>(q + j * (16 * 256)) = o;
            }
        } else {
            const int row0 = (wv - 12) * 32 + g;
#pragma unroll
            for (int i = 0; i < 8; ++i)
                r[i] = *reinterpret_cast<const u32x4*>(h + (size_t)min(bid * 128 + row0 + i * 4, N - 1) * D + e16 * 8);
            const u16* pp = pos + (size_t)min(bid * 128 + (t & 127), N - 1) * 3;
            if (wv >= 14) { p01 = (u32)pp[0] | ((u32)pp[1] << 16); p2 = pp[2]; }
            CBAR();
#pragma unroll
            for (int i = 0; i < 8; ++i) {
                const int row = row0 + i * 4;
                *reinterpret_cast<u32x4*>(hl + row * 256 + ((e16 ^ (row & 15)) << 4)) = r[i];
            }
        }
        __syncthreads();
        if (wv >= 12) {
            if (wv >= 14 && bid * 128 + (t & 127) < N) pos8[bid * 128 + (t & 127)] = make_uint2(p01, p2);
            return;
        }
        const int rp = wv & 3, m = wv >> 2;
        const u32 lb = (u32)(e16 * 256 + ((g ^ e16) << 4));
        f32x4 acc[2][8];
#pragma unroll
        for (int r = 0; r < 2; ++r)
#pragma unroll
            for (int j = 0; j < 8; ++j) acc[r][j] = (f32x4){0.f, 0.f, 0.f, 0.f};
#pragma unroll
        for (int ks = 0; ks < 4; ++ks) {
            const char* hp = hl + rp * (32 * 256) + (lb ^ (u32)(ks * 64));
            const bf16x8 a0 = *reinterpret_cast<const bf16x8*>(hp);
            const bf16x8 a1 = *reinterpret_cast<const bf16x8*>(hp + 16 * 256);
#pragma unroll
            for (int j = 0; j < 8; ++j) {
                const char* wp = wl + m * (D * 256) + j * (16 * 256) + (lb ^ (u32)(ks * 64));
                const bf16x8 w = *reinterpret_cast<const bf16x8*>(wp);
                acc[0][j] = __builtin_amdgcn_mfma_f32_16x16x32_bf16(a0, w, acc[0][j], 0, 0, 0);
                acc[1][j] = __builtin_amdgcn_mfma_f32_16x16x32_bf16(a1, w, acc[1][j], 0, 0, 0);
            }
        }
        const float sc = (m < 2) ? NLOG2E : 1.0f;
        const __amdgpu_buffer_rsrc_t ro = mk_rsrc(ABU + (size_t)m * N * D, (u32)N * (D * 2));
#pragma unroll
        for (int r = 0; r < 2; ++r)
#pragma unroll
            for (int i = 0; i < 4; ++i) {
                const int n = bid * 128 + rp * 32 + r * 16 + g * 4 + i;
                uint4 o = make_uint4(cvt_pk((f32x2){acc[r][0][i] * sc, acc[r][1][i] * sc}), cvt_pk((f32x2){acc[r][2][i] * sc, acc[r][3][i] * sc}),
                                     cvt_pk((f32x2){acc[r][4][i] * sc, acc[r][5][i] * sc}), cvt_pk((f32x2){acc[r][6][i] * sc, acc[r][7][i] * sc}));
                if (m == 1) st_b128<AUX_B>(ro, (u32)n * (D * 2) + e16 * 16, o);
                else if (m == 2) st_b128<AUX_U>(ro, (u32)n * (D * 2) + e16 * 16, o);
                else st_b128<AUX_A>(ro, (u32)n * (D * 2) + e16 * 16, o);
            }
        return;
    }
    {
        u32 w[8];
#pragma unroll
        for (int j = 0; j < 8; ++j)
            w[j] = *reinterpret_cast<const u32*>(wh + (size_t)(D + wv * 8 + j) * D + lane * 2);
        if (t < 192) {
            const int gq = t >> 6, tp = (t >> 4) & 3, c16 = t & 15;
            u32 o[4];
#pragma unroll
            for (int u = 0; u < 2; ++u) {
                const int c = tp * 32 + (c16 >> 2) * 8 + u * 4 + (c16 & 3);
                u32 hi[5], lo[5];
#pragma unroll
                for (int j = 0; j < 5; ++j) {
                    float x = bf2f(we[(size_t)(256 + j) * D + c]) * NLOG2E;
                    u16 hh = f2bf(x);
                    hi[j] = hh; lo[j] = f2bf(x - bf2f(hh));
                }
                o[u * 2] = (gq == 0) ? (hi[0] | (hi[1] << 16)) : (gq == 1) ? (hi[4] | (hi[0] << 16)) : (lo[0] | (lo[1] << 16));
                o[u * 2 + 1] = (gq == 0) ? (hi[2] | (hi[3] << 16)) : (gq == 1) ? lo[4] : (lo[2] | (lo[3] << 16));
            }
            w5g[t] = make_uint4(o[0], o[1], o[2], o[3]);
        }
        const int c = lane * 2;
        const int f16 = ((c >> 3) & 3) * 4 + (c & 3), q = (c >> 2) & 1;
        uint4* o = reinterpret_cast<uint4*>(whT) + (((c >> 5) * 2 + q) * 4 + (wv >> 2)) * 64 + (wv & 3) * 16 + f16;
        o[0] = make_uint4((w[0] & 0xffffu) | (w[1] << 16), (w[2] & 0xffffu) | (w[3] << 16),
                          (w[4] & 0xffffu) | (w[5] << 16), (w[6] & 0xffffu) | (w[7] << 16));
        o[1] = make_uint4((w[0] >> 16) | (w[1] & 0xffff0000u), (w[2] >> 16) | (w[3] & 0xffff0000u),
                          (w[4] >> 16) | (w[5] & 0xffff0000u), (w[6] >> 16) | (w[7] & 0xffff0000u));
    }
}

struct KbSmem {
    uint4 rec[KB_WAVES][CAPR];
    uint2 geo[KB_WAVES][CAPR];
    uint2 lut[8];
    uint4 At[KB_WAVES][2][64];
    uint4 w5s[3][64];
    float macc[KB_WAVES * NPW][AST];
    float pacc[KB_WAVES][NPW][4];
    float posl[KB_WAVES][NPW + 3][4];
    float wxs[D];
    uint4 T[KB_WAVES][64];
    int dt[KB_WAVES][18];
};

__global__ void __launch_bounds__(KB_THREADS, 3)
k_main(const u16* __restrict__ h, const u16* __restrict__ pos,
       const u16* __restrict__ wx,
       const u32* __restrict__ rec_g, const u16* __restrict__ tab,
       const u16* __restrict__ ABU,
       const u16* __restrict__ whT, const uint4* __restrict__ w5g, const uint2* __restrict__ pos8,
       u16* __restrict__ out_h, u16* __restrict__ out_pos,
       unsigned long long mNBW, int N, int G, int CH, int NBW) {
    __shared__ KbSmem S;
    asm volatile("" :: "s"(h), "s"(pos), "s"(wx), "s"(rec_g), "s"(tab), "s"(ABU), "s"(whT), "s"(w5g), "s"(pos8),
                 "s"(out_h), "s"(out_pos), "s"(mNBW), "s"(N), "s"(G), "s"(CH), "s"(NBW));
    const u16* __restrict__ Bg = ABU + (size_t)N * D;
    const u16* __restrict__ Ug = ABU + (size_t)2 * N * D;
    const int t = threadIdx.x, lane = t & 63;
    const int wv = __builtin_amdgcn_readfirstlane(t >> 6);
    const int e16 = lane & 15, g = lane >> 4;
    const int ec = (e16 >> 2) * 8 + (e16 & 3);
    const int nwg = NBW / KB_WAVES, per8 = nwg >> 3;
    int bx = (int)blockIdx.x;
    if (bx < per8 * 8) {
        int k = bx >> 3;
        if (per8 == 96) k = (k & 31) * 3 + (k >> 5);
        bx = (bx & 7) * per8 + k;
    }
    const int wb = bx * KB_WAVES + wv;
    const int N0 = node_start(bx * KB_WAVES, N, NBW, mNBW);
    const int N1 = node_start(bx * KB_WAVES + KB_WAVES, N, NBW, mNBW);
    const int n0 = node_start(wb, N, NBW, mNBW);
    const int nn = node_start(wb + 1, N, NBW, mNBW) - n0;
    const int rowb = n0 - N0;

    int so[GI_MAX], sc[GI_MAX];
    u16 (*tabs)[5] = reinterpret_cast<u16 (*)[5]>(&S.T[0][0]);
    uint2 tv = make_uint2(0u, 0u);
    u32 tv4 = 0;
    auto fetch_tab = [&]() {
        if (t < G) {
            const u16* tp = tab + (size_t)t * (NBW + 4) + bx * KB_WAVES;
            tv = *reinterpret_cast<const uint2*>(tp);
            tv4 = tp[4];
        }
    };
    auto put_tab = [&]() {
        if (t < G) {
            tabs[t][0] = (u16)tv.x; tabs[t][1] = (u16)(tv.x >> 16);
            tabs[t][2] = (u16)tv.y; tabs[t][3] = (u16)(tv.y >> 16);
            tabs[t][4] = (u16)tv4;
        }
    };
    auto load_tab = [&]() {
        int lo_ = lane;
        asm volatile("" : "+v"(lo_));
#pragma unroll
        for (int ci = 0; ci < GI_MAX; ++ci) {
            int c = lo_ + 64 * ci;
            so[ci] = 0; sc[ci] = 0;
            if (c < G) {
                so[ci] = tabs[c][wv];
                sc[ci] = tabs[c][wv + 1];
            }
        }
    };
    auto load_tab_g = [&]() {
        int lo_ = lane;
        asm volatile("" : "+v"(lo_));
#pragma unroll
        for (int ci = 0; ci < GI_MAX; ++ci) {
            int c = lo_ + 64 * ci;
            so[ci] = 0; sc[ci] = 0;
            if (c < G) {
                const u16* tp = tab + (size_t)c * (NBW + 4) + wb;
                so[ci] = tp[0];
                sc[ci] = tp[1];
            }
        }
    };
    fetch_tab();
    u32 a0[NPW];
#pragma unroll
    for (int j = 0; j < NPW; ++j) {
        a0[j] = *reinterpret_cast<const u32*>(ABU + (size_t)min(n0 + j, N - 1) * D + lane * 2);
    }
    const uint4 w40 = w5g[lane], w41 = w5g[lane + 64], w42 = w5g[lane + 128];
    u16 wx0 = wx[lane], wx1 = wx[lane + 64];
    const u16 pz16 = pos[min((n0 + (lane >> 2)) * 3 + (lane & 3), N * 3 - 1)];
    const int NR = N1 - N0;
    uint4 hv4[2], uv4[2];
    auto load_hu = [&]() __attribute__((always_inline)) {
#pragma unroll
        for (int rt = 0; rt < 2; ++rt) {
            int lr = rt * 16 + e16;
            size_t o = (size_t)(N0 + (lr < NR ? lr : 0)) * D + wv * 32 + g * 8;
            hv4[rt] = *reinterpret_cast<const uint4*>(h + o);
            uv4[rt] = *reinterpret_cast<const uint4*>(Ug + o);
        }
    };
    auto stage = [&]() {
#pragma unroll
        for (int j = 0; j < NPW; ++j) a0[j] = (j < nn) ? a0[j] : 0u;
        {
            const int r = (lane * 2) & 31;
            uint2* ap = reinterpret_cast<uint2*>(&S.At[wv][0][(lane >> 4) * 16 + (r >> 3) * 4 + (r & 3)]) + ((r >> 2) & 1);
            ap[0] = make_uint2((a0[0] & 0xffffu) | (a0[1] << 16), (a0[2] & 0xffffu) | (a0[3] << 16));
            ap[2] = make_uint2((a0[0] >> 16) | (a0[1] & 0xffff0000u), (a0[2] >> 16) | (a0[3] & 0xffff0000u));
            ap[128] = make_uint2(a0[4] & 0xffffu, 0u);
            ap[130] = make_uint2(a0[4] >> 16, 0u);
        }
#pragma unroll
        for (int j = 0; j < NPW; ++j)
            if (j < nn) { S.macc[rowb + j][lane] = 0.f; S.macc[rowb + j][64 + lane] = 0.f; }
        uint4* wq = &S.w5s[0][0] + lane;
        wq[0] = w40; wq[64] = w41; wq[128] = w42;
        S.wxs[lane] = bf2f(wx0);
        S.wxs[lane + 64] = bf2f(wx1);
        if (lane < (NPW + 3) * 4) S.posl[wv][lane >> 2][lane & 3] = ((lane >> 2) < nn && (lane & 3) < 3) ? bf2f(pz16) : 0.f;
        if (lane < NPW * 4) S.pacc[wv][lane >> 2][lane & 3] = 0.f;
        if (lane < 16) {
            const int n = lane >> 1, w = lane & 1;
            reinterpret_cast<u32*>(S.lut)[lane] = (n < 5 && ((n & 3) >> 1) == w) ? (0x3F80u << (16 * (n & 1))) : 0u;
        }
    };
    const int CHB4 = ((CH * RW + 3) >> 2) << 2;
    uint4* rec = S.rec[wv];
    u32* key = reinterpret_cast<u32*>(S.rec[wv]);

    put_tab();
    __syncthreads();
    load_tab();
    f32x4 acc2[8];
    int dHead = 0, hflags = 0;
    float hm[8], hp0, hp1, hp2;
    auto reduce = [&]() __attribute__((always_inline)) {
        float wxc[8];
#pragma unroll
        for (int tt = 0; tt < 8; ++tt) wxc[tt] = S.wxs[(tt >> 1) * 32 + (tt & 1) * 4 + ec];
        float pv0 = 0.f, pv1 = 0.f, pv2 = 0.f;
#pragma unroll
        for (int tt = 0; tt < 8; ++tt) {
            hm[tt] = acc2[tt][0] * NLN2;
            pv0 += acc2[tt][1] * wxc[tt];
            pv1 += acc2[tt][2] * wxc[tt];
            pv2 += acc2[tt][3] * wxc[tt];
            acc2[tt] = (f32x4){0.f, 0.f, 0.f, 0.f};
        }
        hp0 = rsum16(pv0) * NLN2; hp1 = rsum16(pv1) * NLN2; hp2 = rsum16(pv2) * NLN2;
    };
    auto commit = [&](int d, auto accc) __attribute__((always_inline)) {
        constexpr bool ACC = decltype(accc)::value;
        const int bk = (d >> 16) & 3;
        const int nl = ((d >> 18) & 1) * 4 + g;
        const int rb = (d >> 20) & 31, nb = (d >> 25) & 7;
        const bool on = nl < nb;
        float* mrow = &S.macc[rb + (on ? nl : 0)][ec];
        float* prow = &S.pacc[bk][on ? nl : 0][0];
        float old[8], po[3];
        if (ACC) {
#pragma unroll
            for (int tt = 0; tt < 8; ++tt) old[tt] = mrow[(tt >> 1) * 32 + (tt & 1) * 4];
            po[0] = prow[0]; po[1] = prow[1]; po[2] = prow[2];
            CBAR();
        }
        if (on) {
#pragma unroll
            for (int tt = 0; tt < 8; ++tt) mrow[(tt >> 1) * 32 + (tt & 1) * 4] = ACC ? old[tt] + hm[tt] : hm[tt];
            if (e16 == 0) {
                prow[0] = (ACC ? po[0] : 0.f) + hp0;
                prow[1] = (ACC ? po[1] : 0.f) + hp1;
                prow[2] = (ACC ? po[2] : 0.f) + hp2;
            }
        }
    };
    auto flush = [&](int d, auto accc) __attribute__((always_inline)) { reduce(); commit(d, accc); };
    auto finish = [&]() __attribute__((always_inline)) {
        const std::integral_constant<bool, true> accc;
        __syncthreads();
        for (int w = 1; w < KB_WAVES; ++w) {
            if ((hflags & 1) && (wv == w || !(hflags & 2))) commit(dHead, accc);
            __syncthreads();
            if (!(hflags & 2)) break;
        }
    };
    auto round = [&](const int base, auto firstc) __attribute__((always_inline)) -> bool {
        constexpr bool FIRST = decltype(firstc)::value;
        int T;
        int fo[GI_MAX];
        {
            int tot = 0;
#pragma unroll
            for (int ci = 0; ci < GI_MAX; ++ci) { sc[ci] -= so[ci]; tot += sc[ci]; }
            const int x = wave_scan(tot);
            T = __builtin_amdgcn_readlane(x, 63);
            int off = x - tot - base;
#pragma unroll
            for (int ci = 0; ci < GI_MAX; ++ci) { fo[ci] = off; off += sc[ci]; }
        }
        const int Tr = max(0, min(CAP, T - base));
        int lo2 = lane;
        asm volatile("" : "+v"(lo2));
        {
            int kp[GI_MAX], kn[GI_MAX], kv[GI_MAX], mx = 0;
#pragma unroll
            for (int ci = 0; ci < GI_MAX; ++ci) {
                const int qa = max(0, -fo[ci]);
                kn[ci] = min(sc[ci], CAP - fo[ci]) - qa;
                kp[ci] = fo[ci] + qa;
                kv[ci] = (lo2 + 64 * ci) * CHB4 + (so[ci] + qa) * RW;
                mx = max(mx, kn[ci]);
            }
#pragma clang loop unroll(disable) vectorize(disable) interleave(disable)
            for (int q = 0; q < mx; ++q) {
#pragma unroll
                for (int ci = 0; ci < GI_MAX; ++ci)
                    if (q < kn[ci]) key[kp[ci] + q] = (u32)(kv[ci] + q * RW);
            }
        }
        CBAR();
        const int spl = (Tr + 63) >> 6;
        const int pb = lo2 * spl;
        uint4 r[RPL];
        uint2 ps[RPL];
#pragma unroll
        for (int i = 0; i < RPL; ++i) {
            r[i] = make_uint4(0xffffffffu, 0u, 0u, 0u);
            if (i < spl && pb + i < Tr) {
                const u32* rp = rec_g + key[pb + i];
                r[i] = make_uint4(rp[0], rp[1], rp[2], 0u);
            }
        }
        if (FIRST) stage();
#pragma unroll
        for (int i = 0; i < RPL; ++i) {
            ps[i] = make_uint2(0u, 0u);
            if (r[i].x != 0xffffffffu) ps[i] = pos8[r[i].x >> 3];
        }
        int posn[RPL];
        int c0, g1s, cnt4;
        {
            u32 el[RPL], eh[RPL], tl_ = 0, th_ = 0;
#pragma unroll
            for (int i = 0; i < RPL; ++i) {
                const unsigned long long oh = 1ull << ((r[i].x & 7u) * 8u);
                el[i] = tl_; eh[i] = th_;
                tl_ += (u32)oh; th_ += (u32)(oh >> 32);
            }
            const u32 il = (u32)wave_scan((int)tl_), ih = (u32)wave_scan((int)th_);
            const u32 Tl = (u32)__builtin_amdgcn_readlane((int)il, 63);
            cnt4 = __builtin_amdgcn_readlane((int)ih, 63) & 0xff;
            c0 = (int)((Tl * 0x01010101u) >> 24);
            g1s = (c0 + 15) & ~15;
            const u32 bl = il - tl_ + Tl * 0x01010100u, bh = ih - th_;
#pragma unroll
            for (int i = 0; i < RPL; ++i) {
                const u32 nd = r[i].x & 7u;
                const unsigned long long v = ((unsigned long long)(bh + eh[i]) << 32) | (unsigned long long)(bl + el[i]);
                posn[i] = (int)((u32)(v >> (nd * 8u)) & 0xffu) + ((nd == 4u) ? g1s : 0);
            }
        }
        const int g1e = (g1s + cnt4 + 15) & ~15;
        CBAR();
#pragma unroll
        for (int i = 0; i < RPL; ++i)
            if (r[i].x != 0xffffffffu) {
                const float4 pn = *reinterpret_cast<const float4*>(&S.posl[wv][r[i].x & 7u][0]);
                float dx = pn.x - __uint_as_float(ps[i].x << 16), dy = pn.y - __uint_as_float(ps[i].x & 0xffff0000u);
                float dz = pn.z - __uint_as_float(ps[i].y << 16);
                float d2v = dx * dx + dy * dy + dz * dz;
                u16 d2h = f2bf(d2v);
                u16 d2l = f2bf(d2v - bf2f(d2h));
                rec[posn[i]] = make_uint4((u32)d2h | (r[i].y << 16), (r[i].y >> 16) | (r[i].z << 16), (r[i].z >> 16) | ((u32)d2l << 16), r[i].x);
                S.geo[wv][posn[i]] = make_uint2(0x3F80u | ((u32)f2bf(dx) << 16), pack2(dy, dz));
            }
        if (lo2 < 32) {
            const int pp = (lo2 < 16) ? (c0 + lo2) : (g1s + cnt4 + lo2 - 16);
            if (pp < ((lo2 < 16) ? g1s : g1e)) { rec[pp] = make_uint4(0u, 0u, 0u, 7u); S.geo[wv][pp] = make_uint2(0u, 0u); }
        } else if (lo2 < 32 + 18) {
            const int l = lo2 - 32, n0t = g1s >> 4, n1t = g1e >> 4;
            const int grp = (l >= n0t) ? 1 : 0;
            const int dsc = ((grp ? n1t : n0t) - l) | ((l == 0 || l == n0t) ? 32 : 0) | ((wv * (CAPR / 16) + l) << 8) |
                            (wv << 16) | (grp << 18) | (rowb << 20) | (nn << 25);
            S.dt[wv][l] = (l == 17) ? (n1t | ((base + CAP < T) ? 256 : 0)) : dsc;
        }
        if (FIRST) load_hu();
        __syncthreads();

        int lq = lane;
        asm volatile("" : "+v"(lq));
        int cb1, cb2, cb3, TT, anymore;
        {
            const int q = S.dt[lq & 3][17];
            const int q0 = __builtin_amdgcn_readlane(q, 0), q1 = __builtin_amdgcn_readlane(q, 1);
            const int q2 = __builtin_amdgcn_readlane(q, 2), q3 = __builtin_amdgcn_readlane(q, 3);
            cb1 = q0 & 255; cb2 = cb1 + (q1 & 255); cb3 = cb2 + (q2 & 255); TT = cb3 + (q3 & 255);
            anymore = (q0 | q1 | q2 | q3) & 256;
        }
        const int ts = (TT * wv) >> 2, te = (TT * (wv + 1)) >> 2;
        const int nst = te - ts;
        const int s1 = TT >> 2, s2 = (TT * 2) >> 2, s3 = (TT * 3) >> 2;
        int descv;
        {
            int tl = (lq < 32) ? (ts + lq) : ((lq & 1) ? s2 : s1);
            tl = min(tl, max(TT - 1, 0));
            const int o = (tl >= cb3) ? (3 * 18 - cb3) : (tl >= cb2) ? (2 * 18 - cb2) : (tl >= cb1) ? (18 - cb1) : 0;
            descv = (&S.dt[0][0])[tl + o];
        }
        const int dF = __builtin_amdgcn_readlane(descv, 0);
        const bool has_head = (nst > 0) && !(dF & 32);
        const int hl = has_head ? min(nst, dF & 31) : 0;
        const int nh = nst - hl;
        bool serial;
        {
            const int d1 = __builtin_amdgcn_readlane(descv, 32), d2 = __builtin_amdgcn_readlane(descv, 33);
            serial = (!(d1 & 32) && s1 + (d1 & 31) > s2) || (!(d2 & 32) && s2 + (d2 & 31) > s3);
        }
        descv |= (lq >= hl && ((descv & 31) == 1 || lq == nst - 1)) ? (1 << 19) : 0;
        auto dsc_at = [&](int i) __attribute__((always_inline)) {
            return __builtin_amdgcn_readlane(descv, (i < nh) ? (i + hl) : (i - nh));
        };
        bf16x4 sel;
        {
            const int dl = (lq & 15) - 4 * (lq >> 4);
            unsigned long long v = (dl >= 0 && dl < 4) ? (0x3F80ull << (16 * dl)) : 0ull;
            sel = as_bf16x4(make_uint2((u32)v, (u32)(v >> 32)));
        }
        const char* w5b = (g < 3) ? reinterpret_cast<const char*>(&S.w5s[g][e16]) : reinterpret_cast<const char*>(&S.At[0][0][e16]);
        const int w5m = (g < 3) ? 0 : (int)sizeof(S.At[0][0]);
        const char* recb = reinterpret_cast<const char*>(&S.rec[0][e16]);
        const char* geob = reinterpret_cast<const char*>(&S.geo[0][e16]);
        const char* Bb = reinterpret_cast<const char*>(Bg);
        const u32 g16 = (u32)(lq & 3) * 16u;
        const char* recq = reinterpret_cast<const char*>(&S.rec[0][lq >> 2]);
        uint4* twp = &S.T[wv][(lq & 3) * 16 + ((lq >> 2) ^ ((lq & 3) * 2))];
        const uint4* trp = &S.T[wv][g * 16 + (e16 ^ (g * 2))];
        uint2 (*Mw)[4] = reinterpret_cast<uint2 (*)[4]>(&S.T[wv][0]);
        const char* xrb = recb + ((g == 1) ? 8 : 0);
        const u32 psel = (g == 1) ? 0x0c0c0504u : 0x03020100u;
        bf16x8 gA[4], gB[4];
        u32 pA0 = 0, pB0 = 0;
        auto issue = [&](int d, bf16x8 (&gat)[4], u32& q0) __attribute__((always_inline)) {
            const u32 qs = reinterpret_cast<const uint4*>(recq + (d & 0xff00))->w;
            q0 = reinterpret_cast<const uint4*>(recb + (d & 0xff00))->w;
            const u32 off = ((qs >> 3) << 8) + g16;
#pragma unroll
            for (int tp = 0; tp < 4; ++tp)
                gat[tp] = *reinterpret_cast<const bf16x8*>(Bb + off + tp * 64);
        };
#pragma unroll
        for (int tt = 0; tt < 8; ++tt) acc2[tt] = (f32x4){0.f, 0.f, 0.f, 0.f};
        auto sgi = [&](int d) __attribute__((always_inline)) { return ((d >> 15) & 6) | ((d >> 18) & 1); };
        auto compute = [&](int d, bf16x8 (&gat)[4], u32 q0) __attribute__((always_inline)) {
            const int bg = sgi(d);
            const u32 nd = q0 & 7u;
            const char* xp = (g < 3) ? (xrb + (d & 0xff00)) : (reinterpret_cast<const char*>(S.lut) + (nd << 3));
            uint2 xu = *reinterpret_cast<const uint2*>(xp);
            const uint2 gq = *reinterpret_cast<const uint2*>(geob + ((d & 0xff00) >> 1));
            const uint4* w5p = reinterpret_cast<const uint4*>(w5b + w5m * bg);
#pragma unroll
            for (int tp = 0; tp < 4; ++tp) {
                CBAR();
                *twp = *reinterpret_cast<const uint4*>(&gat[tp]);
                CBAR();
                gat[tp] = as_bf16x8(*trp);
            }
            CBAR();
            Mw[e16][g] = ((nd & 3u) == (u32)g) ? gq : make_uint2(0u, 0u);
            xu.y = __builtin_amdgcn_perm(xu.x, xu.y, psel);
            const bf16x4 x4 = as_bf16x4(xu);
            CBAR();
            u32 sw01, sw23;
            {
                const u16* wp = reinterpret_cast<const u16*>(&Mw[g * 4][0]) + e16;
                sw01 = (u32)wp[0] | ((u32)wp[16] << 16);
                sw23 = (u32)wp[32] | ((u32)wp[48] << 16);
            }
            const bf16x4 sop = as_bf16x4(make_uint2(sw01, sw23));
            uint4 w5[4];
#pragma unroll
            for (int tp = 0; tp < 4; ++tp) w5[tp] = w5p[tp * 16];
            auto feat = [&](int tt) __attribute__((always_inline)) {
                const uint4 w = w5[tt >> 1];
                return __builtin_amdgcn_mfma_f32_16x16x16bf16_1k(x4, as_bf16x4((tt & 1) ? make_uint2(w.z, w.w) : make_uint2(w.x, w.y)),
                                                                 (f32x4){0.f, 0.f, 0.f, 0.f}, 0, 0, 0);
            };
            auto gath = [&](int tt, f32x4 c) __attribute__((always_inline)) {
                const u32x4 gq4 = *reinterpret_cast<const u32x4*>(&gat[tt >> 1]);
                return __builtin_amdgcn_mfma_f32_16x16x16bf16_1k(as_bf16x4(make_uint2(gq4[(tt & 1) * 2], gq4[(tt & 1) * 2 + 1])), sel, c, 0, 0, 0);
            };
            f32x4 acc = gath(0, feat(0));
#pragma unroll
            for (int tt = 0; tt < 8; ++tt) {
                f32x4 accN = acc;
                __builtin_amdgcn_sched_barrier(0);
                if (tt < 7) accN = feat(tt + 1);
                __builtin_amdgcn_sched_barrier(0);
                float e0 = __builtin_amdgcn_exp2f(acc[0]), e1 = __builtin_amdgcn_exp2f(acc[1]);
                float e2 = __builtin_amdgcn_exp2f(acc[2]), e3 = __builtin_amdgcn_exp2f(acc[3]);
                __builtin_amdgcn_sched_barrier(0);
                if (tt < 7) accN = gath(tt + 1, accN);
                __builtin_amdgcn_sched_barrier(0);
                e0 += 1.f; e1 += 1.f; e2 += 1.f; e3 += 1.f;
                asm("" : "+v"(e0)); asm("" : "+v"(e1)); asm("" : "+v"(e2)); asm("" : "+v"(e3));
                float y0 = acc[0] * __builtin_amdgcn_rcpf(e0), y1 = acc[1] * __builtin_amdgcn_rcpf(e1);
                float y2 = acc[2] * __builtin_amdgcn_rcpf(e2), y3 = acc[3] * __builtin_amdgcn_rcpf(e3);
                asm("" : "+v"(y0)); asm("" : "+v"(y1)); asm("" : "+v"(y2)); asm("" : "+v"(y3));
                u32 k01 = cvt_pk((f32x2){y0, y1}), k23 = cvt_pk((f32x2){y2, y3});
                bf16x4 mb = as_bf16x4(make_uint2(k01, k23));
                __builtin_amdgcn_sched_barrier(0);
                acc2[tt] = __builtin_amdgcn_mfma_f32_16x16x16bf16_1k(sop, mb, acc2[tt], 0, 0, 0);
                acc = accN;
            }
        };
        const std::integral_constant<bool, !FIRST> dirc;
        if (nst > 0) {
            issue(dsc_at(0), gA, pA0);
            int i = 0;
            for (; i + 1 < nst; i += 2) {
                {
                    const int rem = nst - i;
                    if (rem >= 5) __builtin_amdgcn_s_setprio(3);
                    else if (rem >= 4) __builtin_amdgcn_s_setprio(2);
                    else if (rem >= 3) __builtin_amdgcn_s_setprio(1);
                    else __builtin_amdgcn_s_setprio(0);
                }
                const int dA = dsc_at(i);
                const int dB = dsc_at(i + 1);
                const int dC = dsc_at(min(i + 2, nst - 1));
                issue(dB, gB, pB0);
                compute(dA, gA, pA0);
                if (dA & (1 << 19)) flush(dA, dirc);
                issue(dC, gA, pA0);
                compute(dB, gB, pB0);
                if (dB & (1 << 19)) flush(dB, dirc);
            }
            if (i < nst) {
                const int dA = dsc_at(i);
                compute(dA, gA, pA0);
                if (dA & (1 << 19)) flush(dA, dirc);
            }
        }
        dHead = dF;
        hflags = (has_head ? 1 : 0) | (serial ? 2 : 0);
        reduce();
        return anymore != 0;
    };
    if (round(0, std::integral_constant<bool, true>())) {
        for (int base = CAP;; base += CAP) {
            finish();
            load_tab_g();
            if (!round(base, std::integral_constant<bool, false>())) break;
        }
    }

    int le = lane;
    asm volatile("" : "+v"(le));
    const int f16 = le & 15, gg = le >> 4;
    bf16x8 bw[2][4];
#pragma unroll
    for (int q = 0; q < 2; ++q)
#pragma unroll
        for (int ks = 0; ks < 4; ++ks)
            bw[q][ks] = *reinterpret_cast<const bf16x8*>(whT + (((wv * 2 + q) * 4 + ks) * 64 + le) * 8);
    finish();
    if (le < nn * 3) {
        int n = le / 3, cpt = le - n * 3;
        out_pos[(size_t)(n0 + n) * 3 + cpt] = f2bf(S.posl[wv][n][cpt] + S.pacc[wv][n][cpt]);
    }
#pragma unroll
    for (int rt = 0; rt < 2; ++rt) {
        if (rt * 16 < NR) {
            bf16x8 af[4];
            int lr = rt * 16 + f16;
#pragma unroll
            for (int ks = 0; ks < 4; ++ks) {
                const float* mp = &S.macc[lr < NR ? lr : 0][ks * 32 + gg * 8];
                af[ks] = as_bf16x8(make_uint4(pack2(mp[0], mp[1]), pack2(mp[2], mp[3]),
                                              pack2(mp[4], mp[5]), pack2(mp[6], mp[7])));
            }
            u32 ov[4];
#pragma unroll
            for (int q = 0; q < 2; ++q) {
                const u32 u0 = q ? uv4[rt].z : uv4[rt].x, u1 = q ? uv4[rt].w : uv4[rt].y;
                const u32 h0 = q ? hv4[rt].z : hv4[rt].x, h1 = q ? hv4[rt].w : hv4[rt].y;
                f32x4 acc = {__uint_as_float(u0 << 16), __uint_as_float(u0 & 0xffff0000u),
                             __uint_as_float(u1 << 16), __uint_as_float(u1 & 0xffff0000u)};
#pragma unroll
                for (int ks = 0; ks < 4; ++ks)
                    acc = __builtin_amdgcn_mfma_f32_16x16x32_bf16(bw[q][ks], af[ks], acc, 0, 0, 0);
                ov[q * 2] = pack2(__uint_as_float(h0 << 16) + silu_f(acc[0]), __uint_as_float(h0 & 0xffff0000u) + silu_f(acc[1]));
                ov[q * 2 + 1] = pack2(__uint_as_float(h1 << 16) + silu_f(acc[2]), __uint_as_float(h1 & 0xffff0000u) + silu_f(acc[3]));
            }
            if (lr < NR)
                st_b128<AUX_O>(mk_rsrc(out_h, (u32)N * (D * 2)), (u32)(N0 + lr) * (D * 2) + wv * 64 + gg * 16, make_uint4(ov[0], ov[1], ov[2], ov[3]));
        }
    }
}

extern "C" void kernel_launch(void* const* d_in, const int* in_sizes, int n_in,
                              void* d_out, int out_size, void* d_ws, size_t ws_size,
                              hipStream_t stream) {
    const u16* h   = (const u16*)d_in[0];
    const u16* pos = (const u16*)d_in[1];
    const u16* ea  = (const u16*)d_in[2];
    const int* src = (const int*)d_in[3];
    const int* dst = (const int*)d_in[4];
    const u16* we  = (const u16*)d_in[5];
    const u16* wx  = (const u16*)d_in[6];
    const u16* wh  = (const u16*)d_in[7];
    int N = in_sizes[0] / D;
    int E = in_sizes[3];
    u16* out_h = (u16*)d_out;
    u16* out_pos = out_h + (size_t)N * D;

    int G = 128;
    while ((E + G - 1) / G > KA_CH_MAX && G < 64 * GI_MAX) G += 64;
    int CH = ((E + G - 1) / G + 3) & ~3;
    int NBW = 3072;
    while (NBW * NPW < N && NBW < NBW_MAX) NBW += 4;
    int nGemm = (N + 127) / 128;
    unsigned long long mN = (1ull << 40) / (unsigned long long)N + 1ull;
    unsigned long long mNBW = (1ull << 40) / (unsigned long long)NBW + 1ull;

    char* ws = (char*)d_ws;
    size_t off = 0;
    auto take = [&](size_t bytes) { char* p = ws + off; off += (bytes + 255) & ~(size_t)255; return p; };
    u32* rec_g = (u32*)take((size_t)G * ((CH * RW + 3) >> 2) * 16 + 64);
    u16* tab = (u16*)take((size_t)G * (NBW + 4) * 2);
    u16* ABU = (u16*)take((size_t)3 * N * D * 2);
    u16* whT = (u16*)take((size_t)D * D * 2);
    uint4* w5g = (uint4*)take((size_t)256 * 16);
    uint2* pos8 = (uint2*)take((size_t)N * 8);

    k_part<<<G + nGemm + 1, KA_THREADS, 0, stream>>>(h, pos, src, dst, ea, we, wh, rec_g, tab, ABU, whT, w5g, pos8,
                                                      mN, mNBW, N, E, G, CH, NBW, nGemm);
    k_main<<<NBW / KB_WAVES, KB_THREADS, 0, stream>>>(h, pos, wx, rec_g, tab, ABU, whT, w5g, pos8,
                                                     out_h, out_pos, mNBW, N, G, CH, NBW);
}
```

```cpp
#include <hip/hip_runtime.h>
#include <hip/hip_bf16.h>
#include <stdint.h>
#include <type_traits>

typedef unsigned short u16;
typedef unsigned int u32;
typedef __attribute__((ext_vector_type(8))) short bf16x8;
typedef __attribute__((ext_vector_type(4))) float f32x4;
typedef __attribute__((ext_vector_type(4))) short bf16x4;
typedef __attribute__((ext_vector_type(2))) float f32x2;
typedef __attribute__((ext_vector_type(4))) unsigned int u32x4;
__device__ __forceinline__ void st_nt(void* p, uint4 v) { u32x4 x = {v.x, v.y, v.z, v.w}; __builtin_nontemporal_store(x, reinterpret_cast<u32x4*>(p)); }

typedef __attribute__((ext_vector_type(2))) unsigned int u32x2;
typedef __attribute__((ext_vector_type(3))) unsigned int u32x3;
typedef __attribute__((ext_vector_type(3), aligned(4))) unsigned int u32x3u;
typedef __attribute__((ext_vector_type(4), aligned(4))) unsigned int u32x4u;
__device__ __forceinline__ __amdgpu_buffer_rsrc_t mk_rsrc(const void* p, u32 bytes) {
    return __builtin_amdgcn_make_buffer_rsrc(const_cast<void*>(p), (short)0, (int)bytes, 0x00020000);
}
template <int AUX>
__device__ __forceinline__ void st_b128(__amdgpu_buffer_rsrc_t r, u32 off, uint4 v) {
    u32x4 x = {v.x, v.y, v.z, v.w};
    __builtin_amdgcn_raw_buffer_store_b128(x, r, (int)off, 0, AUX);
}
template <int AUX>
__device__ __forceinline__ void st_b64(__amdgpu_buffer_rsrc_t r, u32 off, uint2 v) {
    u32x2 x = {v.x, v.y};
    __builtin_amdgcn_raw_buffer_store_b64(x, r, (int)off, 0, AUX);
}
#define AUX_A 16
#define AUX_B 16
#define AUX_U 16
#define AUX_R 16
#define AUX_O 0

#define D 128
#define KA_THREADS 1024
#define KA_ITERS 4
#define KA_CH_MAX 4032
#define NCH 128
#define NWG_MAX 1024
#define GPW 5
#define TS 6
#define KB_THREADS 256
#define KB_WAVES 4
#define WSL 256
#define RW 3
#define MBIAS 2048
#define KA_SMEM (NWG_MAX * GPW * 16 + NWG_MAX * TS * 2 + KA_CH_MAX * RW * 4 + 16)
#define AST (D + 4)
#define CBAR() asm volatile("" ::: "memory")
#define NLOG2E (-1.4426950408889634f)
#define NLN2 (-0.6931471805599453f)

__device__ __forceinline__ float bf2f(u16 v) { return __uint_as_float(((u32)v) << 16); }
__device__ __forceinline__ u16 f2bf(float f) {
    __hip_bfloat16 b = __float2bfloat16(f);
    return *reinterpret_cast<u16*>(&b);
}
__device__ __forceinline__ float silu_f(float x) {
    float e = __builtin_amdgcn_exp2f(x * NLOG2E);
    return x * __builtin_amdgcn_rcpf(1.0f + e);
}
__device__ __forceinline__ u32 pack2(float a, float b) {
    return (u32)f2bf(a) | ((u32)f2bf(b) << 16);
}
typedef __attribute__((ext_vector_type(2))) __bf16 bf16x2;
__device__ __forceinline__ u32 cvt_pk(f32x2 v) {
    bf16x2 p = __builtin_convertvector(v, bf16x2);
    return *reinterpret_cast<u32*>(&p);
}
__device__ __forceinline__ bf16x8 as_bf16x8(uint4 v) { return *reinterpret_cast<bf16x8*>(&v); }
__device__ __forceinline__ bf16x4 as_bf16x4(uint2 v) { return *reinterpret_cast<bf16x4*>(&v); }
template <int CTRL>
__device__ __forceinline__ float dpp_f(float x) {
    return __int_as_float(__builtin_amdgcn_update_dpp(0, __float_as_int(x), CTRL, 0xf, 0xf, true));
}
__device__ __forceinline__ float rsum16(float x) {
    x += dpp_f<0x128>(x);
    asm("" : "+v"(x));
    x += dpp_f<0x124>(x);
    asm("" : "+v"(x));
    x += dpp_f<0x122>(x);
    asm("" : "+v"(x));
    x += dpp_f<0x121>(x);
    asm("" : "+v"(x));
    return x;
}
template <int CTRL, int RM>
__device__ __forceinline__ int dpp_add(int x) { return x + __builtin_amdgcn_update_dpp(0, x, CTRL, RM, 0xf, false); }
__device__ __forceinline__ int wave_scan(int x) {
    x = dpp_add<0x111, 0xf>(x);
    x = dpp_add<0x112, 0xf>(x);
    x = dpp_add<0x114, 0xf>(x);
    x = dpp_add<0x118, 0xf>(x);
    x = dpp_add<0x142, 0xa>(x);
    x = dpp_add<0x143, 0xc>(x);
    return x;
}
template <int CTRL, int RM>
__device__ __forceinline__ u32 dpp_max(u32 x) { return max(x, (u32)__builtin_amdgcn_update_dpp(0, (int)x, CTRL, RM, 0xf, false)); }
__device__ __forceinline__ u32 wave_scan_max(u32 x) {
    x = dpp_max<0x111, 0xf>(x);
    x = dpp_max<0x112, 0xf>(x);
    x = dpp_max<0x114, 0xf>(x);
    x = dpp_max<0x118, 0xf>(x);
    x = dpp_max<0x142, 0xa>(x);
    x = dpp_max<0x143, 0xc>(x);
    return x;
}
__device__ __forceinline__ u32 mdiv(u32 x, unsigned long long m) {
    return (u32)(((unsigned long long)x * m) >> 40);
}
__device__ __forceinline__ int node_start(int b, int N, int NBW, unsigned long long mNBW) {
    return (int)mdiv((u32)b * (u32)N + (u32)NBW - 1u, mNBW);
}

__global__ void __launch_bounds__(KA_THREADS)
k_part(const u16* __restrict__ h, const u16* __restrict__ pos, const int* __restrict__ src, const int* __restrict__ dst,
       const u16* __restrict__ ea, const u16* __restrict__ we, const u16* __restrict__ wx, const u16* __restrict__ wh,
       u32* __restrict__ rec_g, u16* __restrict__ tab, u16* __restrict__ ABU,
       u16* __restrict__ whT, uint4* __restrict__ w5g, uint2* __restrict__ pos8,
       unsigned long long mN, unsigned long long mNWG,
       int N, int E, int CH, int NWG, int nGemm, int unused) {
    __shared__ __align__(16) u16 smem[KA_SMEM / 2];
    __shared__ int wsum[KA_THREADS / 64];
    asm volatile("" :: "s"(h), "s"(pos), "s"(src), "s"(dst), "s"(ea), "s"(we), "s"(wx), "s"(wh), "s"(rec_g), "s"(tab), "s"(ABU),
                 "s"(whT), "s"(w5g), "s"(pos8), "s"(mN), "s"(mNWG), "s"(N), "s"(E), "s"(CH), "s"(NWG), "s"(nGemm));
    const int t = threadIdx.x, lane = t & 63;
    const int wv = __builtin_amdgcn_readfirstlane(t >> 6);
    int bid = blockIdx.x;
    if (bid < NCH) {
        uint4* cnt = reinterpret_cast<uint4*>(smem);
        u16* base16 = smem + NWG_MAX * GPW * 8;
        u32* lrec = reinterpret_cast<u32*>(base16 + NWG_MAX * TS);
        const int e0 = bid * CH;
        const int ne = max(0, min(E, e0 + CH) - e0);
        const int QPW = ((CH >> 2) + 15) >> 4;
        const int el = (lane < QPW) ? (wv * QPW + lane) * 4 : CH;
        const int eb = min(e0 + el, E - 4);
        const int i0 = e0 + el - eb, i1 = e0 + ne - eb;
        const u32x4 dq = *reinterpret_cast<const u32x4u*>(dst + eb);
        int dv[KA_ITERS];
#pragma unroll
        for (int i = 0; i < NWG_MAX * GPW / KA_THREADS; ++i)
            if (t + i * KA_THREADS < NWG * GPW) cnt[t + i * KA_THREADS] = make_uint4(0u, 0u, 0u, 0u);
        __syncthreads();
        int eb2 = eb;
        asm volatile("" : "+v"(eb2) : "v"(dq[0]));
        const u32x4 sq = *reinterpret_cast<const u32x4u*>(src + eb2);
        const u32x4 ea0 = *reinterpret_cast<const u32x4u*>(ea + (size_t)eb2 * 4);
        const u32x4 ea1 = *reinterpret_cast<const u32x4u*>(ea + (size_t)eb2 * 4 + 8);
        const int wq = wv >> 2, wsft = (wv & 3) * 8;
        const u32 inc = 1u << wsft;
        int bv[KA_ITERS], b6[KA_ITERS], rk[KA_ITERS];
#pragma unroll
        for (int i = 0; i < KA_ITERS; ++i) {
            const int w = (int)mdiv(dq[i] * (u32)NWG, mN);
            dv[i] = (int)dq[i] - node_start(w, N, NWG, mNWG);
            const int b = w * GPW + (dv[i] >> 2);
            bv[i] = b;
            b6[i] = b + w;
            rk[i] = 0;
            if (i >= i0 && i < i1) rk[i] = (int)atomicAdd(reinterpret_cast<u32*>(cnt) + b * 4 + wq, inc);
        }
        __syncthreads();
        {
            int c[GPW], tot = 0;
            u32x4 q[GPW];
#pragma unroll
            for (int j = 0; j < GPW; ++j)
                q[j] = *reinterpret_cast<const u32x4*>(&cnt[min(t, NWG - 1) * GPW + j]);
#pragma unroll
            for (int j = 0; j < GPW; ++j) {
                c[j] = (int)__builtin_amdgcn_sad_u8(q[j][0], 0u, __builtin_amdgcn_sad_u8(q[j][1], 0u,
                            __builtin_amdgcn_sad_u8(q[j][2], 0u, __builtin_amdgcn_sad_u8(q[j][3], 0u, 0u))));
                tot += c[j];
            }
            tot = (t < NWG) ? tot : 0;
            const int x = wave_scan(tot);
            if (lane == 63) wsum[wv] = x;
            __syncthreads();
            int y = wsum[lane & 15];
            y = ((lane & 15) < wv) ? y : 0;
            y += __builtin_amdgcn_update_dpp(0, y, 0x128, 0xf, 0xf, true);
            y += __builtin_amdgcn_update_dpp(0, y, 0x124, 0xf, 0xf, true);
            y += __builtin_amdgcn_update_dpp(0, y, 0x122, 0xf, 0xf, true);
            y += __builtin_amdgcn_update_dpp(0, y, 0x121, 0xf, 0xf, true);
            const u32 o0 = (u32)(y + x - tot), o1 = o0 + c[0], o2 = o1 + c[1], o3 = o2 + c[2], o4 = o3 + c[3], o5 = o4 + c[4];
            const u32x3 ov = {o0 | (o1 << 16), o2 | (o3 << 16), o4 | (o5 << 16)};
            if (t < NWG) *reinterpret_cast<u32x3u*>(base16 + t * TS) = ov;
            const __amdgpu_buffer_rsrc_t rt = mk_rsrc(tab + (size_t)bid * (NWG * TS), (u32)NWG * (TS * 2u));
            __builtin_amdgcn_raw_buffer_store_b96(ov, rt, t * (TS * 2), 0, AUX_R);
        }
        __syncthreads();
        {
            const u32 m0 = (wq > 0) ? ~0u : inc - 1u;
            const u32 m1 = (wq > 1) ? ~0u : (wq == 1) ? inc - 1u : 0u;
            const u32 m2 = (wq > 2) ? ~0u : (wq == 2) ? inc - 1u : 0u;
            const u32 m3 = (wq == 3) ? inc - 1u : 0u;
            uint4 q[KA_ITERS];
            u32 bs[KA_ITERS];
#pragma unroll
            for (int i = 0; i < KA_ITERS; ++i) {
                q[i] = cnt[bv[i]]; bs[i] = base16[b6[i]];
            }
#pragma unroll
            for (int i = 0; i < KA_ITERS; ++i) {
                if (i >= i0 && i < i1) {
                    u32 p = bs[i] + (((u32)rk[i] >> wsft) & 255u);
                    p = __builtin_amdgcn_sad_u8(q[i].x & m0, 0u, p);
                    p = __builtin_amdgcn_sad_u8(q[i].y & m1, 0u, p);
                    p = __builtin_amdgcn_sad_u8(q[i].z & m2, 0u, p);
                    p = __builtin_amdgcn_sad_u8(q[i].w & m3, 0u, p);
                    u32* o = lrec + p * RW;
                    o[0] = (i < 2) ? ea0[(i & 1) * 2] : ea1[(i & 1) * 2]; o[1] = (i < 2) ? ea0[(i & 1) * 2 + 1] : ea1[(i & 1) * 2 + 1];
                    o[2] = ((sq[i] + 1u) << 8) | (u32)dv[i];
                }
            }
        }
        __syncthreads();
        {
            const int CHB = (CH * RW + 3) >> 2;
            const int nq = (ne * RW + 3) >> 2;
            const __amdgpu_buffer_rsrc_t rr = mk_rsrc(reinterpret_cast<uint4*>(rec_g) + (size_t)bid * CHB, (u32)nq * 16u);
            const uint4* lq4 = reinterpret_cast<const uint4*>(lrec);
#pragma unroll
            for (int i = 0; i < KA_ITERS * RW / 4; ++i) {
                int p = t + i * KA_THREADS;
                if (p < nq) st_b128<AUX_R>(rr, (u32)p * 16u, lq4[p]);
            }
        }
        return;
    }
    bid -= NCH;
    if (bid < nGemm) {
        const int e16 = lane & 15, g = lane >> 4;
        char* wl = reinterpret_cast<char*>(smem);
        char* hl = wl + 3 * D * 256;
        u32x4 r[8];
        u32 p01 = 0, p2 = 0;
        if (wv < 12) {
            const int kb = (wv & 3) * 4 + g;
            const u16* wsrc = ((wv < 8) ? we + (size_t)(wv >> 2) * D * D : wh) + (size_t)kb * 8 * D + e16 * 8;
#pragma unroll
            for (int i = 0; i < 8; ++i) r[i] = *reinterpret_cast<const u32x4*>(wsrc + i * D);
            CBAR();
            char* q = wl + (wv >> 2) * (D * 256) + e16 * 256 + ((kb ^ e16) << 4);
#pragma unroll
            for (int j = 0; j < 8; ++j) {
                const u32 sel = (j & 1) ? 0x07060302u : 0x05040100u;
                u32x4 o;
#pragma unroll
                for (int p = 0; p < 4; ++p) o[p] = __builtin_amdgcn_perm(r[2 * p + 1][j >> 1], r[2 * p][j >> 1], sel);
                *reinterpret_cast<u32x4*>(q + j * (16 * 256)) = o;
            }
        } else {
            const int row0 = (wv - 12) * 32 + g;
#pragma unroll
            for (int i = 0; i < 8; ++i)
                r[i] = *reinterpret_cast<const u32x4*>(h + (size_t)min(bid * 128 + row0 + i * 4, N - 1) * D + e16 * 8);
            const u16* pp = pos + (size_t)min(bid * 128 + (t & 127), N - 1) * 3;
            if (wv >= 14) { p01 = 0x3F80u | ((u32)pp[0] << 16); p2 = (u32)pp[1] | ((u32)pp[2] << 16); }
            CBAR();
#pragma unroll
            for (int i = 0; i < 8; ++i) {
                const int row = row0 + i * 4;
                *reinterpret_cast<u32x4*>(hl + row * 256 + ((e16 ^ (row & 15)) << 4)) = r[i];
            }
        }
        __syncthreads();
        if (wv >= 12) {
            if (wv >= 14 && bid * 128 + (t & 127) < N) pos8[1 + bid * 128 + (t & 127)] = make_uint2(p01, p2);
            return;
        }
        const int rp = wv & 3, m = wv >> 2;
        const u32 lb = (u32)(e16 * 256 + ((g ^ e16) << 4));
        f32x4 acc[2][8];
#pragma unroll
        for (int r = 0; r < 2; ++r)
#pragma unroll
            for (int j = 0; j < 8; ++j) acc[r][j] = (f32x4){0.f, 0.f, 0.f, 0.f};
#pragma unroll
        for (int ks = 0; ks < 4; ++ks) {
            const char* hp = hl + rp * (32 * 256) + (lb ^ (u32)(ks * 64));
            const bf16x8 a0 = *reinterpret_cast<const bf16x8*>(hp);
            const bf16x8 a1 = *reinterpret_cast<const bf16x8*>(hp + 16 * 256);
#pragma unroll
            for (int j = 0; j < 8; ++j) {
                const char* wp = wl + m * (D * 256) + j * (16 * 256) + (lb ^ (u32)(ks * 64));
                const bf16x8 w = *reinterpret_cast<const bf16x8*>(wp);
                acc[0][j] = __builtin_amdgcn_mfma_f32_16x16x32_bf16(a0, w, acc[0][j], 0, 0, 0);
                acc[1][j] = __builtin_amdgcn_mfma_f32_16x16x32_bf16(a1, w, acc[1][j], 0, 0, 0);
            }
        }
        const float sc = (m < 2) ? NLOG2E : 1.0f;
        const __amdgpu_buffer_rsrc_t ro = mk_rsrc(ABU + (size_t)m * N * D, (u32)N * (D * 2));
#pragma unroll
        for (int r = 0; r < 2; ++r)
#pragma unroll
            for (int i = 0; i < 4; ++i) {
                const int n = bid * 128 + rp * 32 + r * 16 + g * 4 + i;
                uint4 o = make_uint4(cvt_pk((f32x2){acc[r][0][i] * sc, acc[r][1][i] * sc}), cvt_pk((f32x2){acc[r][2][i] * sc, acc[r][3][i] * sc}),
                                     cvt_pk((f32x2){acc[r][4][i] * sc, acc[r][5][i] * sc}), cvt_pk((f32x2){acc[r][6][i] * sc, acc[r][7][i] * sc}));
                if (m == 1) st_b128<AUX_B>(ro, (u32)n * (D * 2) + e16 * 16, o);
                else if (m == 2) st_b128<AUX_U>(ro, (u32)n * (D * 2) + e16 * 16, o);
                else st_b128<AUX_A>(ro, (u32)n * (D * 2) + e16 * 16, o);
            }
        return;
    }
    {
        u32 w[8];
#pragma unroll
        for (int j = 0; j < 8; ++j)
            w[j] = *reinterpret_cast<const u32*>(wh + (size_t)(D + wv * 8 + j) * D + lane * 2);
        if (t < 192) {
            const int gq = t >> 6, tp = (t >> 4) & 3, c16 = t & 15;
            u32 o[4];
#pragma unroll
            for (int u = 0; u < 2; ++u) {
                const int c = tp * 32 + (c16 >> 2) * 8 + u * 4 + (c16 & 3);
                u32 hi[5], lo[5];
#pragma unroll
                for (int j = 0; j < 5; ++j) {
                    float x = bf2f(we[(size_t)(256 + j) * D + c]) * NLOG2E;
                    u16 hh = f2bf(x);
                    hi[j] = hh; lo[j] = f2bf(x - bf2f(hh));
                }
                o[u * 2] = (gq == 0) ? (hi[1] | (hi[2] << 16)) : (gq == 1) ? lo[0] : (lo[1] | (lo[2] << 16));
                o[u * 2 + 1] = (gq == 0) ? (hi[3] | (hi[4] << 16)) : (gq == 1) ? (hi[0] | (hi[0] << 16)) : (lo[3] | (lo[4] << 16));
            }
            w5g[t] = make_uint4(o[0], o[1], o[2], o[3]);
        } else if (t < 192 + D) {
            reinterpret_cast<float*>(w5g + 192)[t - 192] = bf2f(wx[t - 192]) * NLN2;
        } else if (t < 192 + D + 8) {
            const int q = t - 192 - D, n = q >> 1;
            reinterpret_cast<u32*>(w5g + 224)[q] = ((n >> 1) == (q & 1)) ? (0x3F80u << (16 * (n & 1))) : 0u;
        } else if (t == 192 + D + 8) {
            pos8[0] = make_uint2(0u, 0u);
        }
        const int c = lane * 2;
        const int f16 = ((c >> 3) & 3) * 4 + (c & 3), q = (c >> 2) & 1;
        uint4* o = reinterpret_cast<uint4*>(whT) + (((c >> 5) * 2 + q) * 4 + (wv >> 2)) * 64 + (wv & 3) * 16 + f16;
        o[0] = make_uint4((w[0] & 0xffffu) | (w[1] << 16), (w[2] & 0xffffu) | (w[3] << 16),
                          (w[4] & 0xffffu) | (w[5] << 16), (w[6] & 0xffffu) | (w[7] << 16));
        o[1] = make_uint4((w[0] >> 16) | (w[1] & 0xffff0000u), (w[2] >> 16) | (w[3] & 0xffff0000u),
                          (w[4] >> 16) | (w[5] & 0xffff0000u), (w[6] >> 16) | (w[7] & 0xffff0000u));
    }
}

#define PRO_PRIO 0
#define HUI 2
struct KbSmem {
    uint4 rec[KB_WAVES][WSL];
    uint2 geo[KB_WAVES][WSL];
    uint4 cst[256];
    uint4 At[GPW][64];
    float macc[GPW * 4][AST];
    float4 pacc[GPW * 4];
    float4 posl[KB_WAVES][32];
    uint4 T[KB_WAVES][64];
};

__global__ void __launch_bounds__(KB_THREADS, 3)
k_main(const u16* __restrict__ h, const u32* __restrict__ rec_g, const u16* __restrict__ tab,
       const u16* __restrict__ ABU,
       const u16* __restrict__ whT, const uint4* __restrict__ w5g, const uint2* __restrict__ pos8,
       u16* __restrict__ out_h, u16* __restrict__ out_pos,
       unsigned long long mNWG, int N, int CH, int NWG, int unused) {
    __shared__ KbSmem S;
    asm volatile("" :: "s"(h), "s"(rec_g), "s"(tab), "s"(ABU), "s"(whT), "s"(w5g), "s"(pos8),
                 "s"(out_h), "s"(out_pos), "s"(mNWG), "s"(N), "s"(CH), "s"(NWG));
    const int t = threadIdx.x, lane = t & 63;
    const int wv = __builtin_amdgcn_readfirstlane(t >> 6);
    const int e16 = lane & 15, g = lane >> 4;
    const int ec = (e16 >> 2) * 8 + (e16 & 3);
    const int per8 = NWG >> 3;
    int bx = (int)blockIdx.x;
    if (bx < per8 * 8) {
        int k = bx >> 3;
        if (per8 == 96) k = (k & 31) * 3 + (k >> 5);
        bx = (bx & 7) * per8 + k;
    }
    u32* tabs = reinterpret_cast<u32*>(&S.T[0][0]);
    u32* marks = reinterpret_cast<u32*>(&S.macc[0][0]);
    u32x3 tv = {0u, 0u, 0u};
    if (wv < 2) tv = *reinterpret_cast<const u32x3u*>(reinterpret_cast<const char*>(tab) + (u32)(t * NWG + bx) * (TS * 2u));
    const int N0 = node_start(bx, N, NWG, mNWG);
    const int NR = node_start(bx + 1, N, NWG, mNWG) - N0;
    u32 a0[5];
    {
        const char* ap = reinterpret_cast<const char*>(ABU) + (size_t)N0 * (D * 2);
        const u32 lo = (u32)lane * 4u + (u32)wv * (4 * D * 2);
#pragma unroll
        for (int j = 0; j < 4; ++j) a0[j] = *reinterpret_cast<const u32*>(ap + lo + j * (D * 2));
        a0[4] = *reinterpret_cast<const u32*>(ap + (u32)lane * 4u + (u32)(16 + wv) * (D * 2));
    }
    const uint4 cv = w5g[t];
    const uint2 pz = pos8[1 + N0 + (lane & 31)];
    reinterpret_cast<uint4*>(marks)[t] = make_uint4(0u, 0u, 0u, 0u);
    reinterpret_cast<uint4*>(marks)[t + 256] = make_uint4(0u, 0u, 0u, 0u);

    const char* Bb = reinterpret_cast<const char*>(ABU) + (size_t)(N - 1) * (D * 2);
    const __amdgpu_buffer_rsrc_t rrec = mk_rsrc(rec_g, (u32)CH * (NCH * RW * 4u));
    uint4 hv4[2], uv4[2];
    auto load_hu = [&]() __attribute__((always_inline)) {
        const char* hp = reinterpret_cast<const char*>(h) + (size_t)N0 * (D * 2);
        const char* up = reinterpret_cast<const char*>(ABU) + ((size_t)2 * N + N0) * (D * 2);
#pragma unroll
        for (int rt = 0; rt < 2; ++rt) {
            const int lr = rt * 16 + e16;
            const u32 o = (u32)(lr < NR ? lr : 0) * (D * 2) + (u32)wv * 64u + (u32)g * 16u;
            hv4[rt] = *reinterpret_cast<const uint4*>(hp + o);
            uv4[rt] = *reinterpret_cast<const uint4*>(up + o);
        }
    };
    auto stage = [&]() __attribute__((always_inline)) {
        const int r = (lane * 2) & 31;
        const int ao = ((lane >> 4) * 16 + (r >> 3) * 4 + (r & 3)) * 2 + ((r >> 2) & 1);
        uint2* ap = reinterpret_cast<uint2*>(&S.At[wv][0]) + ao;
        ap[0] = make_uint2(__builtin_amdgcn_perm(a0[1], a0[0], 0x05040100u), __builtin_amdgcn_perm(a0[3], a0[2], 0x05040100u));
        ap[2] = make_uint2(__builtin_amdgcn_perm(a0[1], a0[0], 0x07060302u), __builtin_amdgcn_perm(a0[3], a0[2], 0x07060302u));
        u16* aq = reinterpret_cast<u16*>(reinterpret_cast<uint2*>(&S.At[4][0]) + ao) + wv;
        aq[0] = (u16)a0[4];
        aq[8] = (u16)(a0[4] >> 16);
        S.cst[t] = cv;
        S.posl[wv][lane & 31] = make_float4(__uint_as_float(pz.x & 0xffff0000u), __uint_as_float(pz.y << 16),
                                           __uint_as_float(pz.y & 0xffff0000u), 0.f);
    };
    u32x3 r[4];
    uint2 ps[4];
    auto ld_rec = [&](const u32 (&off)[4], auto kc) __attribute__((always_inline)) {
#pragma unroll
        for (int i = 0; i < decltype(kc)::value; ++i)
            r[i] = __builtin_amdgcn_raw_buffer_load_b96(rrec, (int)off[i], 0, 0);
    };
    auto put_rec = [&](int s0, auto kc) __attribute__((always_inline)) {
#pragma unroll
        for (int i = 0; i < decltype(kc)::value; ++i) {
            ps[i] = *reinterpret_cast<const uint2*>(reinterpret_cast<const char*>(pos8) + ((r[i][2] >> 8) << 3));
            *reinterpret_cast<u32x3*>(&S.rec[wv][s0 + i]) = r[i];
        }
    };
    auto geom = [&](int s0, auto kc) __attribute__((always_inline)) {
        float4 pn[4];
#pragma unroll
        for (int i = 0; i < decltype(kc)::value; ++i) pn[i] = S.posl[wv][r[i][2] & 31u];
#pragma unroll
        for (int i = 0; i < decltype(kc)::value; ++i) {
            const float dx = pn[i].x - __uint_as_float(ps[i].x & 0xffff0000u), dy = pn[i].y - __uint_as_float(ps[i].y << 16);
            const float dz = pn[i].z - __uint_as_float(ps[i].y & 0xffff0000u);
            const float d2v = dx * dx + dy * dy + dz * dz;
            const float d2h = __uint_as_float(__float_as_uint(d2v) & 0xffff0000u);
            S.rec[wv][s0 + i].w = cvt_pk((f32x2){d2h, d2v - d2h});
            S.geo[wv][s0 + i] = ps[i];
        }
    };

    f32x4 acc2[8];
    float hm[8];
    f32x4 hp;
    auto reduce = [&]() __attribute__((always_inline)) {
        const float* wxs = reinterpret_cast<const float*>(&S.cst[192]);
        float wxc[8];
#pragma unroll
        for (int tt = 0; tt < 8; ++tt) wxc[tt] = wxs[(tt >> 1) * 32 + (tt & 1) * 4 + ec];
        f32x4 pv = {0.f, 0.f, 0.f, 0.f};
#pragma unroll
        for (int tt = 0; tt < 8; ++tt) {
            hm[tt] = acc2[tt][0] * NLN2;
            pv += acc2[tt] * wxc[tt];
            acc2[tt] = (f32x4){0.f, 0.f, 0.f, 0.f};
        }
        hp = (f32x4){rsum16(pv[0]), rsum16(pv[1]), rsum16(pv[2]), rsum16(pv[3])};
    };
    auto commit = [&](int gr, auto accc) __attribute__((always_inline)) {
        constexpr bool ACC = decltype(accc)::value;
        float* mrow = &S.macc[gr * 4 + g][ec];
        f32x4* prow = reinterpret_cast<f32x4*>(&S.pacc[gr * 4 + g]);
        float old[8];
        f32x4 po = {0.f, 0.f, 0.f, 0.f};
        if (ACC) {
#pragma unroll
            for (int tt = 0; tt < 8; ++tt) old[tt] = mrow[(tt >> 1) * 32 + (tt & 1) * 4];
            po = *prow;
            CBAR();
        }
#pragma unroll
        for (int tt = 0; tt < 8; ++tt) mrow[(tt >> 1) * 32 + (tt & 1) * 4] = ACC ? old[tt] + hm[tt] : hm[tt];
        if (e16 == 0) *prow = ACC ? po + hp : hp;
    };
    const std::integral_constant<bool, true> c_acc;
    const std::integral_constant<bool, false> c_set;

    int lq = lane;
    asm volatile("" : "+v"(lq));
    bf16x4 sel;
    {
        const int dl = (lq & 15) - 4 * (lq >> 4);
        unsigned long long v = (dl >= 0 && dl < 4) ? (0x3F80ull << (16 * dl)) : 0ull;
        sel = as_bf16x4(make_uint2((u32)v, (u32)(v >> 32)));
    }
    const char* w5b = (g < 3) ? reinterpret_cast<const char*>(&S.cst[g * 64 + e16]) : reinterpret_cast<const char*>(&S.At[0][e16]);
    const int w5m = (g < 3) ? 0 : (int)sizeof(S.At[0]);
    const char* recb = reinterpret_cast<const char*>(&S.rec[wv][e16]);
    const char* geob = reinterpret_cast<const char*>(&S.geo[wv][e16]);
    const u32 g16 = (u32)(lq & 3) * 16u;
    const char* recq = reinterpret_cast<const char*>(&S.rec[wv][lq >> 2]);
    uint4* twp = &S.T[wv][(lq & 3) * 16 + ((lq >> 2) ^ ((lq & 3) * 2))];
    const uint4* trp = &S.T[wv][g * 16 + (e16 ^ (g * 2))];
    uint2 (*Mw)[4] = reinterpret_cast<uint2 (*)[4]>(&S.T[wv][0]);
    const char* xrb = recb + ((g == 1) ? 8 : 0);
    const char* lutb = reinterpret_cast<const char*>(&S.cst[224]);
    const u32 psel = (g == 1) ? 0x0c0c0504u : 0x03020100u;
    bf16x8 gA[4], gB[4];
    u32 pA0 = 0, pB0 = 0;
    auto issue = [&](int ti, bf16x8 (&gat)[4], u32& q0) __attribute__((always_inline)) {
        const u32 qs = reinterpret_cast<const uint4*>(recq + ti * 256)->z;
        q0 = reinterpret_cast<const uint4*>(recb + ti * 256)->z;
        const u32 off = (qs & 0xffffff00u) | g16;
#pragma unroll
        for (int tp = 0; tp < 4; ++tp)
            gat[tp] = *reinterpret_cast<const bf16x8*>(Bb + off + tp * 64);
    };
    auto compute = [&](int ti, int gr, bf16x8 (&gat)[4], u32 q0, bool hu) __attribute__((always_inline)) {
        const u32 nd = q0 & 3u;
        const char* xp = (g < 3) ? (xrb + ti * 256) : (lutb + (nd << 3));
        uint2 xu = *reinterpret_cast<const uint2*>(xp);
        const uint2 gq = *reinterpret_cast<const uint2*>(geob + ti * 128);
        const uint4* w5p = reinterpret_cast<const uint4*>(w5b + w5m * gr);
#pragma unroll
        for (int tp = 0; tp < 4; ++tp) {
            CBAR();
            *twp = *reinterpret_cast<const uint4*>(&gat[tp]);
            CBAR();
            gat[tp] = as_bf16x8(*trp);
        }
        CBAR();
        if (hu) load_hu();
        CBAR();
        Mw[e16][g] = (nd == (u32)g) ? gq : make_uint2(0u, 0u);
        xu.x = __builtin_amdgcn_perm(xu.y, xu.x, psel);
        const bf16x4 x4 = as_bf16x4(xu);
        CBAR();
        u32 sw01, sw23;
        {
            const u16* wp = reinterpret_cast<const u16*>(&Mw[g * 4][0]) + e16;
            sw01 = (u32)wp[0] | ((u32)wp[16] << 16);
            sw23 = (u32)wp[32] | ((u32)wp[48] << 16);
        }
        const bf16x4 sop = as_bf16x4(make_uint2(sw01, sw23));
        uint4 w5[4];
#pragma unroll
        for (int tp = 0; tp < 4; ++tp) w5[tp] = w5p[tp * 16];
        auto feat = [&](int tt) __attribute__((always_inline)) {
            const uint4 w = w5[tt >> 1];
            return __builtin_amdgcn_mfma_f32_16x16x16bf16_1k(x4, as_bf16x4((tt & 1) ? make_uint2(w.z, w.w) : make_uint2(w.x, w.y)),
                                                             (f32x4){0.f, 0.f, 0.f, 0.f}, 0, 0, 0);
        };
        auto gath = [&](int tt, f32x4 c) __attribute__((always_inline)) {
            const u32x4 gq4 = *reinterpret_cast<const u32x4*>(&gat[tt >> 1]);
            return __builtin_amdgcn_mfma_f32_16x16x16bf16_1k(as_bf16x4(make_uint2(gq4[(tt & 1) * 2], gq4[(tt & 1) * 2 + 1])), sel, c, 0, 0, 0);
        };
        f32x4 acc = gath(0, feat(0));
#pragma unroll
        for (int tt = 0; tt < 8; ++tt) {
            f32x4 accN = acc;
            __builtin_amdgcn_sched_barrier(0);
            if (tt < 7) accN = feat(tt + 1);
            __builtin_amdgcn_sched_barrier(0);
            float e0 = __builtin_amdgcn_exp2f(acc[0]), e1 = __builtin_amdgcn_exp2f(acc[1]);
            float e2 = __builtin_amdgcn_exp2f(acc[2]), e3 = __builtin_amdgcn_exp2f(acc[3]);
            __builtin_amdgcn_sched_barrier(0);
            if (tt < 7) accN = gath(tt + 1, accN);
            __builtin_amdgcn_sched_barrier(0);
            e0 += 1.f; e1 += 1.f; e2 += 1.f; e3 += 1.f;
            asm("" : "+v"(e0)); asm("" : "+v"(e1)); asm("" : "+v"(e2)); asm("" : "+v"(e3));
            float y0 = acc[0] * __builtin_amdgcn_rcpf(e0), y1 = acc[1] * __builtin_amdgcn_rcpf(e1);
            float y2 = acc[2] * __builtin_amdgcn_rcpf(e2), y3 = acc[3] * __builtin_amdgcn_rcpf(e3);
            asm("" : "+v"(y0)); asm("" : "+v"(y1)); asm("" : "+v"(y2)); asm("" : "+v"(y3));
            u32 k01 = cvt_pk((f32x2){y0, y1}), k23 = cvt_pk((f32x2){y2, y3});
            bf16x4 mb = as_bf16x4(make_uint2(k01, k23));
            __builtin_amdgcn_sched_barrier(0);
            acc2[tt] = __builtin_amdgcn_mfma_f32_16x16x16bf16_1k(sop, mb, acc2[tt], 0, 0, 0);
            acc = accN;
        }
    };

    asm volatile("" :: "v"(sel), "v"(w5b), "v"(w5m), "v"(recb), "v"(geob), "v"(g16), "v"(recq), "v"(twp), "v"(trp), "v"(xrb), "v"(psel), "v"(ec));
    if (wv < 2) *reinterpret_cast<u32x3u*>(tabs + t * 3) = tv;
    __syncthreads();
    int lo2 = lane;
    asm volatile("" : "+v"(lo2));
    const uint2* tp2 = reinterpret_cast<const uint2*>(tabs + lo2 * 6);
    const uint2 q0 = tp2[0], q1 = tp2[1], q2 = tp2[2];
    const u16* tq = reinterpret_cast<const u16*>(tp2) + wv;
    const u32 oA = tq[0], eA = tq[1], oB = tq[TS], eB = tq[TS + 1];
    const u32 S0 = q0.x + q1.y, S1 = q0.y + q2.x, S2 = q1.x + q2.y;
    const u32 x01 = (u32)wave_scan((int)(__builtin_amdgcn_alignbit(S1, S0, 16) - S0));
    const u32 x23 = (u32)wave_scan((int)(__builtin_amdgcn_alignbit(S2, S1, 16) - S1));
    const u32 x4 = (u32)wave_scan((int)((S2 >> 16) - (S2 & 0xffffu)));
    const bool big = __builtin_amdgcn_ballot_w64((S2 >> 16) - (S0 & 0xffffu) > 64u) != 0ull;
    const u32 T01 = (u32)__builtin_amdgcn_readlane((int)x01, 63), T23 = (u32)__builtin_amdgcn_readlane((int)x23, 63);
    const u32 T4 = (u32)__builtin_amdgcn_readlane((int)x4, 63);
    const int cb1 = (int)(((T01 & 0xffffu) + 15u) >> 4), cb2 = cb1 + (int)(((T01 >> 16) + 15u) >> 4);
    const int cb3 = cb2 + (int)(((T23 & 0xffffu) + 15u) >> 4), cb4 = cb3 + (int)(((T23 >> 16) + 15u) >> 4);
    const int TT = cb4 + (int)((T4 + 15u) >> 4);
    asm volatile("" :: "v"(oA), "v"(eA), "v"(oB), "v"(eB));
    int dgr = -1;
    bool serial = false;
    if (__builtin_expect(!big && TT <= KB_WAVES * WSL / 16, 1)) {
        const unsigned long long cbp = ((unsigned long long)(u32)(cb4 | (TT << 8) | (TT << 16)) << 32) | (u32)((cb1 << 8) | (cb2 << 16) | (cb3 << 24));
        auto mark = [&](int gr, u32 inc, u32 oa, u32 ea_, u32 ob, u32 eb_) __attribute__((always_inline)) {
            const u32 gb = (u32)((cbp >> (8 * gr)) & 0xffu) * 16u;
            const u32 pE = gb + inc, pB = pE - (eb_ - ob), pA = pB - (ea_ - oa);
            const u32 lb = (u32)lo2 * (2u * (u32)CH) + (((u32)gr + 1u) << 20) + MBIAS;
            atomicMax(&marks[64 + pA], lb + oa - pA);
            atomicMax(&marks[64 + pB], lb + (u32)CH + ob - pB);
            if (lo2 == 63) atomicMax(&marks[64 + pE], (((u32)gr + 2u) << 20) - 1u);
        };
        mark(wv, __builtin_amdgcn_ubfe((wv < 2) ? x01 : x23, (u32)(wv & 1) * 16u, 16u), oA, eA, oB, eB);
        if (wv == 3 && T4 != 0u) mark(4, x4, q1.x & 0xffffu, q1.x >> 16, q2.y & 0xffffu, q2.y >> 16);
        const int ts = (TT * wv) >> 2, te = (TT * (wv + 1)) >> 2;
        const int nst = te - ts;
        int gc, rem;
        auto seek = [&](int tn) __attribute__((always_inline)) {
            gc = (tn >= cb1 ? 1 : 0) + (tn >= cb2 ? 1 : 0) + (tn >= cb3 ? 1 : 0) + (tn >= cb4 ? 1 : 0);
            rem = (int)((cbp >> (8 * gc + 8)) & 0xffu) - tn;
        };
        auto fill = [&](auto kc) __attribute__((always_inline)) {
            constexpr int K = decltype(kc)::value;
            const int s0 = K * lo2;
            const u32* mp = marks + 64 + ts * 16;
            const u32 lbk = mp[lo2 - 64];
            u32 m[4];
#pragma unroll
            for (int i = 0; i < K; ++i) m[i] = mp[s0 + i];
#pragma unroll
            for (int i = 1; i < K; ++i) m[i] = max(m[i], m[i - 1]);
            u32 ex = wave_scan_max((u32)__builtin_amdgcn_update_dpp(0, (int)m[K - 1], 0x138, 0xf, 0xf, false));
            const u32 cr = (u32)__builtin_amdgcn_readlane((int)wave_scan_max(lbk), 63);
            ex = (s0 < nst * 16) ? max(ex, cr) : 0xffffffffu;
            const u32 po = (u32)(ts * 16 + s0 - MBIAS) * (RW * 4u);
            u32 off[4];
#pragma unroll
            for (int i = 0; i < K; ++i) off[i] = (max(m[i], ex) & 0xfffffu) * (RW * 4u) + po + i * RW * 4u;
            ld_rec(off, kc);
            stage();
            put_rec(s0, kc);
            issue(0, gA, pA0);
#pragma unroll
            for (int tt = 0; tt < 8; ++tt) {
                acc2[tt] = (f32x4){0.f, 0.f, 0.f, 0.f};
                asm volatile("" : "+v"(acc2[tt]));
            }
            seek(ts);
            asm volatile("" : "+s"(gc), "+s"(rem));
            geom(s0, kc);
        };
        __syncthreads();
        if (nst <= 8) fill(std::integral_constant<int, 2>());
        else if (nst <= 12) fill(std::integral_constant<int, 3>());
        else fill(std::integral_constant<int, 4>());
        {
            const int mx = max(max(max(cb1, cb2 - cb1), max(cb3 - cb2, cb4 - cb3)), TT - cb4);
            serial = mx >= (TT >> 2) + 2;
        }
        auto step = [&](int tn) __attribute__((always_inline)) {
            dgr = gc;
            if (--rem == 0) {
                reduce();
                commit(gc, c_set);
                dgr = -1;
                seek(tn);
            }
        };
        __syncthreads();
        if (nst > 0) {
            int i = 0;
            for (; i + 1 < nst; i += 2) {
                {
                    const int left = nst - i;
                    if (left >= 5) __builtin_amdgcn_s_setprio(3);
                    else if (left >= 4) __builtin_amdgcn_s_setprio(2);
                    else if (left >= 3) __builtin_amdgcn_s_setprio(1);
                    else __builtin_amdgcn_s_setprio(0);
                }
                issue(i + 1, gB, pB0);
                compute(i, gc, gA, pA0, i == HUI);
                step(ts + i + 1);
                issue(min(i + 2, nst - 1), gA, pA0);
                compute(i + 1, gc, gB, pB0, false);
                step(ts + i + 2);
            }
            if (i < nst) {
                compute(i, gc, gA, pA0, false);
                step(ts + i + 1);
            }
        }
        if (nst < HUI + 2) load_hu();
        {
            const int ng = (int)((cbp >> (8 * wv + 8)) & 0xffu) - (int)((cbp >> (8 * wv)) & 0xffu);
            if (ng == 0 || (wv == 0 && T4 == 0u)) {
#pragma unroll
                for (int tt = 0; tt < 8; ++tt) hm[tt] = 0.f;
                hp = (f32x4){0.f, 0.f, 0.f, 0.f};
                if (ng == 0) commit(wv, c_set);
                if (wv == 0 && T4 == 0u && NR > 16) commit(4, c_set);
            }
        }
        reduce();
    } else {
        __syncthreads();
        stage();
        load_hu();
        for (int i = t; i < GPW * 4 * AST; i += KB_THREADS) (&S.macc[0][0])[i] = 0.f;
        if (t < GPW * 4) S.pacc[t] = make_float4(0.f, 0.f, 0.f, 0.f);
#pragma unroll
        for (int tt = 0; tt < 8; ++tt) acc2[tt] = (f32x4){0.f, 0.f, 0.f, 0.f};
        __syncthreads();
        u32* key = reinterpret_cast<u32*>(S.rec[wv]);
        for (int gr = 0; gr * 4 < NR; ++gr) {
            int so[2], sc[2], fo[2];
#pragma unroll
            for (int ci = 0; ci < 2; ++ci) {
                const u16* tg = tab + (size_t)((lo2 * 2 + ci) * NWG + bx) * TS + gr;
                so[ci] = tg[0];
                sc[ci] = (int)tg[1] - so[ci];
            }
            const int x = wave_scan(sc[0] + sc[1]);
            const int T = __builtin_amdgcn_readlane(x, 63);
            fo[1] = x - sc[1]; fo[0] = fo[1] - sc[0];
            for (int base = 0; base < T; base += KB_WAVES * WSL) {
                const int sh = ((min(KB_WAVES * WSL, T - base) + 63) >> 6) << 4;
                const int wb = base + wv * sh;
                const int Tr = max(0, min(sh, T - wb));
                if (Tr == 0) continue;
                CBAR();
#pragma unroll
                for (int ci = 0; ci < 2; ++ci) {
                    const int qa = max(0, wb - fo[ci]), qb = min(sc[ci], wb + Tr - fo[ci]);
#pragma clang loop unroll(disable) vectorize(disable) interleave(disable)
                    for (int q = qa; q < qb; ++q) key[fo[ci] + q - wb] = (u32)((lo2 * 2 + ci) * CH + so[ci] + q) * (RW * 4u);
                }
                CBAR();
                u32 off[4];
#pragma unroll
                for (int i = 0; i < 4; ++i) off[i] = (lo2 * 4 + i < Tr) ? key[lo2 * 4 + i] : 0xfffffff0u;
                CBAR();
                const std::integral_constant<int, 4> k4;
                ld_rec(off, k4);
                put_rec(lo2 * 4, k4);
                geom(lo2 * 4, k4);
                CBAR();
                for (int i = 0; i * 16 < Tr; ++i) {
                    issue(i, gA, pA0);
                    compute(i, gr, gA, pA0, false);
                }
            }
            reduce();
            for (int w = 0; w < KB_WAVES; ++w) {
                if (wv == w) commit(gr, c_acc);
                __syncthreads();
            }
        }
    }

    int le = lane;
    asm volatile("" : "+v"(le));
    const int f16 = le & 15, gg = le >> 4;
    bf16x8 bw[2][4];
#pragma unroll
    for (int q = 0; q < 2; ++q)
#pragma unroll
        for (int ks = 0; ks < 4; ++ks)
            bw[q][ks] = *reinterpret_cast<const bf16x8*>(whT + (((wv * 2 + q) * 4 + ks) * 64 + le) * 8);
    __syncthreads();
    for (int w = 0; w < KB_WAVES - 1; ++w) {
        if (dgr >= 0 && (wv == w || !serial)) commit(dgr, c_acc);
        __syncthreads();
        if (!serial) break;
    }
    if (wv == 3 && le < NR * 3) {
        const int n = le / 3, cpt = le - n * 3;
        const float p = reinterpret_cast<const float*>(&S.posl[3][n])[cpt];
        const float* pa = reinterpret_cast<const float*>(&S.pacc[n]);
        out_pos[(size_t)N0 * 3 + le] = f2bf(p + (p * pa[0] - pa[1 + cpt]));
    }
#pragma unroll
    for (int rt = 0; rt < 2; ++rt) {
        if (rt * 16 < NR) {
            bf16x8 af[4];
            int lr = rt * 16 + f16;
#pragma unroll
            for (int ks = 0; ks < 4; ++ks) {
                const float* mp = &S.macc[lr < NR ? lr : 0][ks * 32 + gg * 8];
                af[ks] = as_bf16x8(make_uint4(pack2(mp[0], mp[1]), pack2(mp[2], mp[3]),
                                              pack2(mp[4], mp[5]), pack2(mp[6], mp[7])));
            }
            u32 ov[4];
#pragma unroll
            for (int q = 0; q < 2; ++q) {
                const u32 u0 = q ? uv4[rt].z : uv4[rt].x, u1 = q ? uv4[rt].w : uv4[rt].y;
                const u32 h0 = q ? hv4[rt].z : hv4[rt].x, h1 = q ? hv4[rt].w : hv4[rt].y;
                f32x4 acc = {__uint_as_float(u0 << 16), __uint_as_float(u0 & 0xffff0000u),
                             __uint_as_float(u1 << 16), __uint_as_float(u1 & 0xffff0000u)};
#pragma unroll
                for (int ks = 0; ks < 4; ++ks)
                    acc = __builtin_amdgcn_mfma_f32_16x16x32_bf16(bw[q][ks], af[ks], acc, 0, 0, 0);
                ov[q * 2] = pack2(__uint_as_float(h0 << 16) + silu_f(acc[0]), __uint_as_float(h0 & 0xffff0000u) + silu_f(acc[1]));
                ov[q * 2 + 1] = pack2(__uint_as_float(h1 << 16) + silu_f(acc[2]), __uint_as_float(h1 & 0xffff0000u) + silu_f(acc[3]));
            }
            if (lr < NR)
                st_b128<AUX_O>(mk_rsrc(out_h, (u32)N * (D * 2)), (u32)(N0 + lr) * (D * 2) + wv * 64 + gg * 16, make_uint4(ov[0], ov[1], ov[2], ov[3]));
        }
    }
}

extern "C" void kernel_launch(void* const* d_in, const int* in_sizes, int n_in,
                              void* d_out, int out_size, void* d_ws, size_t ws_size,
                              hipStream_t stream) {
    const u16* h   = (const u16*)d_in[0];
    const u16* pos = (const u16*)d_in[1];
    const u16* ea  = (const u16*)d_in[2];
    const int* src = (const int*)d_in[3];
    const int* dst = (const int*)d_in[4];
    const u16* we  = (const u16*)d_in[5];
    const u16* wx  = (const u16*)d_in[6];
    const u16* wh  = (const u16*)d_in[7];
    int N = in_sizes[0] / D;
    int E = in_sizes[3];
    u16* out_h = (u16*)d_out;
    u16* out_pos = out_h + (size_t)N * D;

    int CH = ((E + NCH - 1) / NCH + 3) & ~3;
    int NWG = 768;
    while (NWG * GPW * 4 < N && NWG < NWG_MAX) NWG += 2;
    int nGemm = (N + 127) / 128;
    unsigned long long mN = (1ull << 40) / (unsigned long long)N + 1ull;
    unsigned long long mNWG = (1ull << 40) / (unsigned long long)NWG + 1ull;

    char* ws = (char*)d_ws;
    size_t off = 0;
    auto take = [&](size_t bytes) { char* p = ws + off; off += (bytes + 255) & ~(size_t)255; return p; };
    u32* rec_g = (u32*)take((size_t)NCH * CH * RW * 4 + 64);
    u16* tab = (u16*)take((size_t)NCH * NWG * TS * 2);
    u16* ABU = (u16*)take((size_t)(3 * N + 32) * D * 2);
    u16* whT = (u16*)take((size_t)D * D * 2);
    uint4* w5g = (uint4*)take((size_t)256 * 16);
    uint2* pos8 = (uint2*)take((size_t)(N + 64) * 8);

    k_part<<<NCH + nGemm + 1, KA_THREADS, 0, stream>>>(h, pos, src, dst, ea, we, wx, wh, rec_g, tab, ABU, whT, w5g, pos8,
                                                        mN, mNWG, N, E, CH, NWG, nGemm, 0);
    k_main<<<NWG, KB_THREADS, 0, stream>>>(h, rec_g, tab, ABU, whT, w5g, pos8, out_h, out_pos, mNWG, N, CH, NWG, 0);
}
```

```cpp
#include <hip/hip_runtime.h>
#include <hip/hip_bf16.h>
#include <stdint.h>
#include <type_traits>

typedef unsigned short u16;
typedef unsigned int u32;
typedef __attribute__((ext_vector_type(8))) short bf16x8;
typedef __attribute__((ext_vector_type(4))) float f32x4;
typedef __attribute__((ext_vector_type(4))) short bf16x4;
typedef __attribute__((ext_vector_type(2))) float f32x2;
typedef __attribute__((ext_vector_type(4))) unsigned int u32x4;
__device__ __forceinline__ void st_nt(void* p, uint4 v) { u32x4 x = {v.x, v.y, v.z, v.w}; __builtin_nontemporal_store(x, reinterpret_cast<u32x4*>(p)); }

typedef __attribute__((ext_vector_type(2))) unsigned int u32x2;
typedef __attribute__((ext_vector_type(3))) unsigned int u32x3;
typedef __attribute__((ext_vector_type(3), aligned(4))) unsigned int u32x3u;
typedef __attribute__((ext_vector_type(4), aligned(4))) unsigned int u32x4u;
__device__ __forceinline__ __amdgpu_buffer_rsrc_t mk_rsrc(const void* p, u32 bytes) {
    return __builtin_amdgcn_make_buffer_rsrc(const_cast<void*>(p), (short)0, (int)bytes, 0x00020000);
}
template <int AUX>
__device__ __forceinline__ void st_b128(__amdgpu_buffer_rsrc_t r, u32 off, uint4 v) {
    u32x4 x = {v.x, v.y, v.z, v.w};
    __builtin_amdgcn_raw_buffer_store_b128(x, r, (int)off, 0, AUX);
}
template <int AUX>
__device__ __forceinline__ void st_b64(__amdgpu_buffer_rsrc_t r, u32 off, uint2 v) {
    u32x2 x = {v.x, v.y};
    __builtin_amdgcn_raw_buffer_store_b64(x, r, (int)off, 0, AUX);
}
#define AUX_A 16
#define AUX_B 16
#define AUX_U 16
#define AUX_R 16
#define AUX_O 0

#define D 128
#define KA_THREADS 1024
#define KA_ITERS 4
#define KA_CH_MAX 4032
#define NCH 128
#define NWG_MAX 1024
#define GPW 5
#define TS 6
#define KB_THREADS 256
#define KB_WAVES 4
#define WSL 256
#define RW 3
#define MBIAS 2048
#define KA_SMEM (NWG_MAX * GPW * 16 + NWG_MAX * TS * 2 + KA_CH_MAX * RW * 4 + 16)
#define AST (D + 4)
#define CBAR() asm volatile("" ::: "memory")
#define NLOG2E (-1.4426950408889634f)
#define NLN2 (-0.6931471805599453f)

__device__ __forceinline__ float bf2f(u16 v) { return __uint_as_float(((u32)v) << 16); }
__device__ __forceinline__ u16 f2bf(float f) {
    __hip_bfloat16 b = __float2bfloat16(f);
    return *reinterpret_cast<u16*>(&b);
}
__device__ __forceinline__ float silu_f(float x) {
    float e = __builtin_amdgcn_exp2f(x * NLOG2E);
    return x * __builtin_amdgcn_rcpf(1.0f + e);
}
__device__ __forceinline__ u32 pack2(float a, float b) {
    return (u32)f2bf(a) | ((u32)f2bf(b) << 16);
}
typedef __attribute__((ext_vector_type(2))) __bf16 bf16x2;
__device__ __forceinline__ u32 cvt_pk(f32x2 v) {
    bf16x2 p = __builtin_convertvector(v, bf16x2);
    return *reinterpret_cast<u32*>(&p);
}
__device__ __forceinline__ bf16x8 as_bf16x8(uint4 v) { return *reinterpret_cast<bf16x8*>(&v); }
__device__ __forceinline__ bf16x4 as_bf16x4(uint2 v) { return *reinterpret_cast<bf16x4*>(&v); }
template <int CTRL>
__device__ __forceinline__ float dpp_f(float x) {
    return __int_as_float(__builtin_amdgcn_update_dpp(0, __float_as_int(x), CTRL, 0xf, 0xf, true));
}
__device__ __forceinline__ float rsum16(float x) {
    x += dpp_f<0x128>(x);
    asm("" : "+v"(x));
    x += dpp_f<0x124>(x);
    asm("" : "+v"(x));
    x += dpp_f<0x122>(x);
    asm("" : "+v"(x));
    x += dpp_f<0x121>(x);
    asm("" : "+v"(x));
    return x;
}
template <int CTRL, int RM>
__device__ __forceinline__ int dpp_add(int x) { return x + __builtin_amdgcn_update_dpp(0, x, CTRL, RM, 0xf, false); }
__device__ __forceinline__ int wave_scan(int x) {
    x = dpp_add<0x111, 0xf>(x);
    x = dpp_add<0x112, 0xf>(x);
    x = dpp_add<0x114, 0xf>(x);
    x = dpp_add<0x118, 0xf>(x);
    x = dpp_add<0x142, 0xa>(x);
    x = dpp_add<0x143, 0xc>(x);
    return x;
}
template <int CTRL, int RM>
__device__ __forceinline__ u32 dpp_max(u32 x) { return max(x, (u32)__builtin_amdgcn_update_dpp(0, (int)x, CTRL, RM, 0xf, false)); }
__device__ __forceinline__ u32 wave_scan_max(u32 x) {
    x = dpp_max<0x111, 0xf>(x);
    x = dpp_max<0x112, 0xf>(x);
    x = dpp_max<0x114, 0xf>(x);
    x = dpp_max<0x118, 0xf>(x);
    x = dpp_max<0x142, 0xa>(x);
    x = dpp_max<0x143, 0xc>(x);
    return x;
}
__device__ __forceinline__ u32 mdiv(u32 x, unsigned long long m) {
    return (u32)(((unsigned long long)x * m) >> 40);
}
__device__ __forceinline__ int node_start(int b, int N, int NBW, unsigned long long mNBW) {
    return (int)mdiv((u32)b * (u32)N + (u32)NBW - 1u, mNBW);
}

__global__ void __launch_bounds__(KA_THREADS)
k_part(const u16* __restrict__ h, const u16* __restrict__ pos, const int* __restrict__ src, const int* __restrict__ dst,
       const u16* __restrict__ ea, const u16* __restrict__ we, const u16* __restrict__ wx, const u16* __restrict__ wh,
       u32* __restrict__ rec_g, u16* __restrict__ tab, u16* __restrict__ ABU,
       u16* __restrict__ whT, uint4* __restrict__ w5g, uint2* __restrict__ pos8,
       unsigned long long mN, unsigned long long mNWG,
       int N, int E, int CH, int NWG, int nGemm, int unused) {
    __shared__ __align__(16) u16 smem[KA_SMEM / 2];
    __shared__ int wsum[KA_THREADS / 64];
    asm volatile("" :: "s"(h), "s"(pos), "s"(src), "s"(dst), "s"(ea), "s"(we), "s"(wx), "s"(wh), "s"(rec_g), "s"(tab), "s"(ABU),
                 "s"(whT), "s"(w5g), "s"(pos8), "s"(mN), "s"(mNWG), "s"(N), "s"(E), "s"(CH), "s"(NWG), "s"(nGemm));
    const int t = threadIdx.x, lane = t & 63;
    const int wv = __builtin_amdgcn_readfirstlane(t >> 6);
    int bid = blockIdx.x;
    if (bid < NCH) {
        uint4* cnt = reinterpret_cast<uint4*>(smem);
        u16* base16 = smem + NWG_MAX * GPW * 8;
        u32* lrec = reinterpret_cast<u32*>(base16 + NWG_MAX * TS);
        const int e0 = bid * CH;
        const int ne = max(0, min(E, e0 + CH) - e0);
        const int QPW = ((CH >> 2) + 15) >> 4;
        const int el = (lane < QPW) ? (wv * QPW + lane) * 4 : CH;
        const int eb = min(e0 + el, E - 4);
        const int i0 = e0 + el - eb, i1 = e0 + ne - eb;
        const u32x4 dq = *reinterpret_cast<const u32x4u*>(dst + eb);
        int dv[KA_ITERS];
#pragma unroll
        for (int i = 0; i < NWG_MAX * GPW / KA_THREADS; ++i)
            if (t + i * KA_THREADS < NWG * GPW) cnt[t + i * KA_THREADS] = make_uint4(0u, 0u, 0u, 0u);
        __syncthreads();
        int eb2 = eb;
        asm volatile("" : "+v"(eb2) : "v"(dq[0]));
        const u32x4 sq = *reinterpret_cast<const u32x4u*>(src + eb2);
        const u32x4 ea0 = *reinterpret_cast<const u32x4u*>(ea + (size_t)eb2 * 4);
        const u32x4 ea1 = *reinterpret_cast<const u32x4u*>(ea + (size_t)eb2 * 4 + 8);
        const int wq = wv >> 2, wsft = (wv & 3) * 8;
        const u32 inc = 1u << wsft;
        int bv[KA_ITERS], b6[KA_ITERS], rk[KA_ITERS];
#pragma unroll
        for (int i = 0; i < KA_ITERS; ++i) {
            const int w = (int)mdiv(dq[i] * (u32)NWG, mN);
            dv[i] = (int)dq[i] - node_start(w, N, NWG, mNWG);
            const int b = w * GPW + (dv[i] >> 2);
            bv[i] = b;
            b6[i] = b + w;
            rk[i] = 0;
            if (i >= i0 && i < i1) rk[i] = (int)atomicAdd(reinterpret_cast<u32*>(cnt) + b * 4 + wq, inc);
        }
        __syncthreads();
        {
            int c[GPW], tot = 0;
            u32x4 q[GPW];
#pragma unroll
            for (int j = 0; j < GPW; ++j)
                q[j] = *reinterpret_cast<const u32x4*>(&cnt[min(t, NWG - 1) * GPW + j]);
#pragma unroll
            for (int j = 0; j < GPW; ++j) {
                c[j] = (int)__builtin_amdgcn_sad_u8(q[j][0], 0u, __builtin_amdgcn_sad_u8(q[j][1], 0u,
                            __builtin_amdgcn_sad_u8(q[j][2], 0u, __builtin_amdgcn_sad_u8(q[j][3], 0u, 0u))));
                tot += c[j];
            }
            tot = (t < NWG) ? tot : 0;
            const int x = wave_scan(tot);
            if (lane == 63) wsum[wv] = x;
            __syncthreads();
            int y = wsum[lane & 15];
            y = ((lane & 15) < wv) ? y : 0;
            y += __builtin_amdgcn_update_dpp(0, y, 0x128, 0xf, 0xf, true);
            y += __builtin_amdgcn_update_dpp(0, y, 0x124, 0xf, 0xf, true);
            y += __builtin_amdgcn_update_dpp(0, y, 0x122, 0xf, 0xf, true);
            y += __builtin_amdgcn_update_dpp(0, y, 0x121, 0xf, 0xf, true);
            const u32 o0 = (u32)(y + x - tot), o1 = o0 + c[0], o2 = o1 + c[1], o3 = o2 + c[2], o4 = o3 + c[3], o5 = o4 + c[4];
            const u32x3 ov = {o0 | (o1 << 16), o2 | (o3 << 16), o4 | (o5 << 16)};
            if (t < NWG) *reinterpret_cast<u32x3u*>(base16 + t * TS) = ov;
            const __amdgpu_buffer_rsrc_t rt = mk_rsrc(tab + (size_t)bid * (NWG * TS), (u32)NWG * (TS * 2u));
            __builtin_amdgcn_raw_buffer_store_b96(ov, rt, t * (TS * 2), 0, AUX_R);
        }
        __syncthreads();
        {
            const u32 m0 = (wq > 0) ? ~0u : inc - 1u;
            const u32 m1 = (wq > 1) ? ~0u : (wq == 1) ? inc - 1u : 0u;
            const u32 m2 = (wq > 2) ? ~0u : (wq == 2) ? inc - 1u : 0u;
            const u32 m3 = (wq == 3) ? inc - 1u : 0u;
            uint4 q[KA_ITERS];
            u32 bs[KA_ITERS];
#pragma unroll
            for (int i = 0; i < KA_ITERS; ++i) {
                q[i] = cnt[bv[i]]; bs[i] = base16[b6[i]];
            }
#pragma unroll
            for (int i = 0; i < KA_ITERS; ++i) {
                if (i >= i0 && i < i1) {
                    u32 p = bs[i] + (((u32)rk[i] >> wsft) & 255u);
                    p = __builtin_amdgcn_sad_u8(q[i].x & m0, 0u, p);
                    p = __builtin_amdgcn_sad_u8(q[i].y & m1, 0u, p);
                    p = __builtin_amdgcn_sad_u8(q[i].z & m2, 0u, p);
                    p = __builtin_amdgcn_sad_u8(q[i].w & m3, 0u, p);
                    u32* o = lrec + p * RW;
                    o[0] = (i < 2) ? ea0[(i & 1) * 2] : ea1[(i & 1) * 2]; o[1] = (i < 2) ? ea0[(i & 1) * 2 + 1] : ea1[(i & 1) * 2 + 1];
                    o[2] = ((sq[i] + 1u) << 8) | (u32)dv[i];
                }
            }
        }
        __syncthreads();
        {
            const int CHB = (CH * RW + 3) >> 2;
            const int nq = (ne * RW + 3) >> 2;
            const __amdgpu_buffer_rsrc_t rr = mk_rsrc(reinterpret_cast<uint4*>(rec_g) + (size_t)bid * CHB, (u32)nq * 16u);
            const uint4* lq4 = reinterpret_cast<const uint4*>(lrec);
#pragma unroll
            for (int i = 0; i < KA_ITERS * RW / 4; ++i) {
                int p = t + i * KA_THREADS;
                if (p < nq) st_b128<AUX_R>(rr, (u32)p * 16u, lq4[p]);
            }
        }
        return;
    }
    bid -= NCH;
    if (bid < nGemm) {
        const int e16 = lane & 15, g = lane >> 4;
        char* wl = reinterpret_cast<char*>(smem);
        char* hl = wl + 3 * D * 256;
        u32x4 r[8];
        u32 p01 = 0, p2 = 0;
        if (wv < 12) {
            const int kb = (wv & 3) * 4 + g;
            const u16* wsrc = ((wv < 8) ? we + (size_t)(wv >> 2) * D * D : wh) + (size_t)kb * 8 * D + e16 * 8;
#pragma unroll
            for (int i = 0; i < 8; ++i) r[i] = *reinterpret_cast<const u32x4*>(wsrc + i * D);
            CBAR();
            char* q = wl + (wv >> 2) * (D * 256) + e16 * 256 + ((kb ^ e16) << 4);
#pragma unroll
            for (int j = 0; j < 8; ++j) {
                const u32 sel = (j & 1) ? 0x07060302u : 0x05040100u;
                u32x4 o;
#pragma unroll
                for (int p = 0; p < 4; ++p) o[p] = __builtin_amdgcn_perm(r[2 * p + 1][j >> 1], r[2 * p][j >> 1], sel);
                *reinterpret_cast<u32x4*>(q + j * (16 * 256)) = o;
            }
        } else {
            const int row0 = (wv - 12) * 32 + g;
#pragma unroll
            for (int i = 0; i < 8; ++i)
                r[i] = *reinterpret_cast<const u32x4*>(h + (size_t)min(bid * 128 + row0 + i * 4, N - 1) * D + e16 * 8);
            const u16* pp = pos + (size_t)min(bid * 128 + (t & 127), N - 1) * 3;
            if (wv >= 14) { p01 = 0x3F80u | ((u32)pp[0] << 16); p2 = (u32)pp[1] | ((u32)pp[2] << 16); }
            CBAR();
#pragma unroll
            for (int i = 0; i < 8; ++i) {
                const int row = row0 + i * 4;
                *reinterpret_cast<u32x4*>(hl + row * 256 + ((e16 ^ (row & 15)) << 4)) = r[i];
            }
        }
        __syncthreads();
        if (wv >= 12) {
            if (wv >= 14 && bid * 128 + (t & 127) < N) pos8[1 + bid * 128 + (t & 127)] = make_uint2(p01, p2);
            return;
        }
        const int rp = wv & 3, m = wv >> 2;
        const u32 lb = (u32)(e16 * 256 + ((g ^ e16) << 4));
        f32x4 acc[2][8];
#pragma unroll
        for (int r = 0; r < 2; ++r)
#pragma unroll
            for (int j = 0; j < 8; ++j) acc[r][j] = (f32x4){0.f, 0.f, 0.f, 0.f};
#pragma unroll
        for (int ks = 0; ks < 4; ++ks) {
            const char* hp = hl + rp * (32 * 256) + (lb ^ (u32)(ks * 64));
            const bf16x8 a0 = *reinterpret_cast<const bf16x8*>(hp);
            const bf16x8 a1 = *reinterpret_cast<const bf16x8*>(hp + 16 * 256);
#pragma unroll
            for (int j = 0; j < 8; ++j) {
                const char* wp = wl + m * (D * 256) + j * (16 * 256) + (lb ^ (u32)(ks * 64));
                const bf16x8 w = *reinterpret_cast<const bf16x8*>(wp);
                acc[0][j] = __builtin_amdgcn_mfma_f32_16x16x32_bf16(a0, w, acc[0][j], 0, 0, 0);
                acc[1][j] = __builtin_amdgcn_mfma_f32_16x16x32_bf16(a1, w, acc[1][j], 0, 0, 0);
            }
        }
        const float sc = (m < 2) ? NLOG2E : 1.0f;
        const __amdgpu_buffer_rsrc_t ro = mk_rsrc(ABU + (size_t)m * N * D, (u32)N * (D * 2));
#pragma unroll
        for (int r = 0; r < 2; ++r)
#pragma unroll
            for (int i = 0; i < 4; ++i) {
                const int n = bid * 128 + rp * 32 + r * 16 + g * 4 + i;
                uint4 o = make_uint4(cvt_pk((f32x2){acc[r][0][i] * sc, acc[r][1][i] * sc}), cvt_pk((f32x2){acc[r][2][i] * sc, acc[r][3][i] * sc}),
                                     cvt_pk((f32x2){acc[r][4][i] * sc, acc[r][5][i] * sc}), cvt_pk((f32x2){acc[r][6][i] * sc, acc[r][7][i] * sc}));
                if (m == 1) st_b128<AUX_B>(ro, (u32)n * (D * 2) + e16 * 16, o);
                else if (m == 2) st_b128<AUX_U>(ro, (u32)n * (D * 2) + e16 * 16, o);
                else st_b128<AUX_A>(ro, (u32)n * (D * 2) + e16 * 16, o);
            }
        return;
    }
    {
        u32 w[8];
#pragma unroll
        for (int j = 0; j < 8; ++j)
            w[j] = *reinterpret_cast<const u32*>(wh + (size_t)(D + wv * 8 + j) * D + lane * 2);
        if (t < 192) {
            const int gq = t >> 6, tp = (t >> 4) & 3, c16 = t & 15;
            u32 o[4];
#pragma unroll
            for (int u = 0; u < 2; ++u) {
                const int c = tp * 32 + (c16 >> 2) * 8 + u * 4 + (c16 & 3);
                u32 hi[5], lo[5];
#pragma unroll
                for (int j = 0; j < 5; ++j) {
                    float x = bf2f(we[(size_t)(256 + j) * D + c]) * NLOG2E;
                    u16 hh = f2bf(x);
                    hi[j] = hh; lo[j] = f2bf(x - bf2f(hh));
                }
                o[u * 2] = (gq == 0) ? (hi[1] | (hi[2] << 16)) : (gq == 1) ? lo[0] : (lo[1] | (lo[2] << 16));
                o[u * 2 + 1] = (gq == 0) ? (hi[3] | (hi[4] << 16)) : (gq == 1) ? (hi[0] | (hi[0] << 16)) : (lo[3] | (lo[4] << 16));
            }
            w5g[t] = make_uint4(o[0], o[1], o[2], o[3]);
        } else if (t < 192 + D) {
            reinterpret_cast<float*>(w5g + 192)[t - 192] = bf2f(wx[t - 192]) * NLN2;
        } else if (t < 192 + D + 8) {
            const int q = t - 192 - D, n = q >> 1;
            reinterpret_cast<u32*>(w5g + 224)[q] = ((n >> 1) == (q & 1)) ? (0x3F80u << (16 * (n & 1))) : 0u;
        } else if (t == 192 + D + 8) {
            pos8[0] = make_uint2(0u, 0u);
        }
        const int c = lane * 2;
        const int f16 = ((c >> 3) & 3) * 4 + (c & 3), q = (c >> 2) & 1;
        uint4* o = reinterpret_cast<uint4*>(whT) + (((c >> 5) * 2 + q) * 4 + (wv >> 2)) * 64 + (wv & 3) * 16 + f16;
        o[0] = make_uint4((w[0] & 0xffffu) | (w[1] << 16), (w[2] & 0xffffu) | (w[3] << 16),
                          (w[4] & 0xffffu) | (w[5] << 16), (w[6] & 0xffffu) | (w[7] << 16));
        o[1] = make_uint4((w[0] >> 16) | (w[1] & 0xffff0000u), (w[2] >> 16) | (w[3] & 0xffff0000u),
                          (w[4] >> 16) | (w[5] & 0xffff0000u), (w[6] >> 16) | (w[7] & 0xffff0000u));
    }
}

#define PR3 6
#define PR2 4
#define PR1 2
#define PR_EVERY 1
#define PRO_PRIO 0
#define HUI 2
struct KbSmem {
    uint4 rec[KB_WAVES][WSL];
    uint2 geo[KB_WAVES][WSL];
    uint4 cst[256];
    uint4 At[GPW][64];
    float macc[GPW * 4][AST];
    float4 pacc[GPW * 4];
    float4 posl[KB_WAVES][32];
    uint4 T[KB_WAVES][64];
};

__global__ void __launch_bounds__(KB_THREADS, 3)
k_main(const u16* __restrict__ h, const u32* __restrict__ rec_g, const u16* __restrict__ tab,
       const u16* __restrict__ ABU,
       const u16* __restrict__ whT, const uint4* __restrict__ w5g, const uint2* __restrict__ pos8,
       u16* __restrict__ out_h, u16* __restrict__ out_pos,
       unsigned long long mNWG, int N, int CH, int NWG, int unused) {
    __shared__ KbSmem S;
    asm volatile("" :: "s"(h), "s"(rec_g), "s"(tab), "s"(ABU), "s"(whT), "s"(w5g), "s"(pos8),
                 "s"(out_h), "s"(out_pos), "s"(mNWG), "s"(N), "s"(CH), "s"(NWG));
    const int t = threadIdx.x, lane = t & 63;
    const int wv = __builtin_amdgcn_readfirstlane(t >> 6);
    const int e16 = lane & 15, g = lane >> 4;
    const int ec = (e16 >> 2) * 8 + (e16 & 3);
    const int per8 = NWG >> 3;
    int bx = (int)blockIdx.x;
    if (bx < per8 * 8) {
        int k = bx >> 3;
        if (per8 == 96) k = (k & 31) * 3 + (k >> 5);
        bx = (bx & 7) * per8 + k;
    }
    u32* tabs = reinterpret_cast<u32*>(&S.T[0][0]);
    u32* marks = reinterpret_cast<u32*>(&S.macc[0][0]);
    u32x3 tv = {0u, 0u, 0u};
    if (wv < 2) tv = *reinterpret_cast<const u32x3u*>(reinterpret_cast<const char*>(tab) + (u32)(t * NWG + bx) * (TS * 2u));
    const int N0 = node_start(bx, N, NWG, mNWG);
    const int NR = node_start(bx + 1, N, NWG, mNWG) - N0;
    u32 a0[5];
    {
        const char* ap = reinterpret_cast<const char*>(ABU) + (size_t)N0 * (D * 2);
        const u32 lo = (u32)lane * 4u + (u32)wv * (4 * D * 2);
#pragma unroll
        for (int j = 0; j < 4; ++j) a0[j] = *reinterpret_cast<const u32*>(ap + lo + j * (D * 2));
        a0[4] = *reinterpret_cast<const u32*>(ap + (u32)lane * 4u + (u32)(16 + wv) * (D * 2));
    }
    const uint4 cv = w5g[t];
    const uint2 pz = pos8[1 + N0 + (lane & 31)];
    reinterpret_cast<uint4*>(marks)[t] = make_uint4(0u, 0u, 0u, 0u);
    reinterpret_cast<uint4*>(marks)[t + 256] = make_uint4(0u, 0u, 0u, 0u);

    const char* Bb = reinterpret_cast<const char*>(ABU) + (size_t)(N - 1) * (D * 2);
    const __amdgpu_buffer_rsrc_t rrec = mk_rsrc(rec_g, (u32)CH * (NCH * RW * 4u));
    uint4 hv4[2], uv4[2];
    auto load_hu = [&]() __attribute__((always_inline)) {
        const char* hp = reinterpret_cast<const char*>(h) + (size_t)N0 * (D * 2);
        const char* up = reinterpret_cast<const char*>(ABU) + ((size_t)2 * N + N0) * (D * 2);
#pragma unroll
        for (int rt = 0; rt < 2; ++rt) {
            const int lr = rt * 16 + e16;
            const u32 o = (u32)(lr < NR ? lr : 0) * (D * 2) + (u32)wv * 64u + (u32)g * 16u;
            hv4[rt] = *reinterpret_cast<const uint4*>(hp + o);
            uv4[rt] = *reinterpret_cast<const uint4*>(up + o);
        }
    };
    bf16x8 bw[2][4];
    auto load_bw = [&]() __attribute__((always_inline)) {
        int le = lane;
        asm volatile("" : "+v"(le));
        const char* bp = reinterpret_cast<const char*>(whT) + wv * 8192;
#pragma unroll
        for (int q = 0; q < 2; ++q)
#pragma unroll
            for (int ks = 0; ks < 4; ++ks)
                bw[q][ks] = *reinterpret_cast<const bf16x8*>(bp + ((u32)le * 16u + (u32)q * 4096u) + ks * 1024);
    };
    auto stage = [&]() __attribute__((always_inline)) {
        const int r = (lane * 2) & 31;
        const int ao = ((lane >> 4) * 16 + (r >> 3) * 4 + (r & 3)) * 2 + ((r >> 2) & 1);
        uint2* ap = reinterpret_cast<uint2*>(&S.At[wv][0]) + ao;
        ap[0] = make_uint2(__builtin_amdgcn_perm(a0[1], a0[0], 0x05040100u), __builtin_amdgcn_perm(a0[3], a0[2], 0x05040100u));
        ap[2] = make_uint2(__builtin_amdgcn_perm(a0[1], a0[0], 0x07060302u), __builtin_amdgcn_perm(a0[3], a0[2], 0x07060302u));
        u16* aq = reinterpret_cast<u16*>(reinterpret_cast<uint2*>(&S.At[4][0]) + ao) + wv;
        aq[0] = (u16)a0[4];
        aq[8] = (u16)(a0[4] >> 16);
        S.cst[t] = cv;
        S.posl[wv][lane & 31] = make_float4(__uint_as_float(pz.x & 0xffff0000u), __uint_as_float(pz.y << 16),
                                           __uint_as_float(pz.y & 0xffff0000u), 0.f);
    };
    u32x3 r[4];
    uint2 ps[4];
    auto ld_rec = [&](const u32 (&off)[4], auto kc) __attribute__((always_inline)) {
#pragma unroll
        for (int i = 0; i < decltype(kc)::value; ++i)
            r[i] = __builtin_amdgcn_raw_buffer_load_b96(rrec, (int)off[i], 0, 0);
    };
    auto put_rec = [&](int s0, auto kc) __attribute__((always_inline)) {
#pragma unroll
        for (int i = 0; i < decltype(kc)::value; ++i) {
            ps[i] = *reinterpret_cast<const uint2*>(reinterpret_cast<const char*>(pos8) + ((r[i][2] >> 8) << 3));
            *reinterpret_cast<u32x3*>(&S.rec[wv][s0 + i]) = r[i];
        }
    };
    auto geom = [&](int s0, auto kc) __attribute__((always_inline)) {
        float4 pn[4];
#pragma unroll
        for (int i = 0; i < decltype(kc)::value; ++i) pn[i] = S.posl[wv][r[i][2] & 31u];
#pragma unroll
        for (int i = 0; i < decltype(kc)::value; ++i) {
            const float dx = pn[i].x - __uint_as_float(ps[i].x & 0xffff0000u), dy = pn[i].y - __uint_as_float(ps[i].y << 16);
            const float dz = pn[i].z - __uint_as_float(ps[i].y & 0xffff0000u);
            const float d2v = dx * dx + dy * dy + dz * dz;
            const float d2h = __uint_as_float(__float_as_uint(d2v) & 0xffff0000u);
            S.rec[wv][s0 + i].w = cvt_pk((f32x2){d2h, d2v - d2h});
            S.geo[wv][s0 + i] = ps[i];
        }
    };

    f32x4 acc2[8];
    float hm[8];
    f32x4 hp;
    auto reduce = [&]() __attribute__((always_inline)) {
        const float* wxs = reinterpret_cast<const float*>(&S.cst[192]);
        float wxc[8];
#pragma unroll
        for (int tt = 0; tt < 8; ++tt) wxc[tt] = wxs[(tt >> 1) * 32 + (tt & 1) * 4 + ec];
        f32x4 pv = {0.f, 0.f, 0.f, 0.f};
#pragma unroll
        for (int tt = 0; tt < 8; ++tt) {
            hm[tt] = acc2[tt][0] * NLN2;
            pv += acc2[tt] * wxc[tt];
            acc2[tt] = (f32x4){0.f, 0.f, 0.f, 0.f};
        }
        float p0 = pv[0], p1 = pv[1], p2 = pv[2], p3 = pv[3];
#define RS4(C) p0 += dpp_f<C>(p0); p1 += dpp_f<C>(p1); p2 += dpp_f<C>(p2); p3 += dpp_f<C>(p3); \
        asm volatile("" : "+v"(p0), "+v"(p1), "+v"(p2), "+v"(p3));
        RS4(0x128) RS4(0x124) RS4(0x122) RS4(0x121)
        hp = (f32x4){p0, p1, p2, p3};
    };
    auto commit = [&](int gr, auto accc) __attribute__((always_inline)) {
        constexpr bool ACC = decltype(accc)::value;
        float* mrow = &S.macc[gr * 4 + g][ec];
        f32x4* prow = reinterpret_cast<f32x4*>(&S.pacc[gr * 4 + g]);
        float old[8];
        f32x4 po = {0.f, 0.f, 0.f, 0.f};
        if (ACC) {
#pragma unroll
            for (int tt = 0; tt < 8; ++tt) old[tt] = mrow[(tt >> 1) * 32 + (tt & 1) * 4];
            po = *prow;
            CBAR();
        }
#pragma unroll
        for (int tt = 0; tt < 8; ++tt) mrow[(tt >> 1) * 32 + (tt & 1) * 4] = ACC ? old[tt] + hm[tt] : hm[tt];
        if (e16 == 0) *prow = ACC ? po + hp : hp;
    };
    const std::integral_constant<bool, true> c_acc;
    const std::integral_constant<bool, false> c_set;

    int lq = lane;
    asm volatile("" : "+v"(lq));
    bf16x4 sel;
    {
        const int dl = (lq & 15) - 4 * (lq >> 4);
        unsigned long long v = (dl >= 0 && dl < 4) ? (0x3F80ull << (16 * dl)) : 0ull;
        sel = as_bf16x4(make_uint2((u32)v, (u32)(v >> 32)));
    }
    const char* w5b = (g < 3) ? reinterpret_cast<const char*>(&S.cst[g * 64 + e16]) : reinterpret_cast<const char*>(&S.At[0][e16]);
    const int w5m = (g < 3) ? 0 : (int)sizeof(S.At[0]);
    const char* recb = reinterpret_cast<const char*>(&S.rec[wv][e16]);
    const char* geob = reinterpret_cast<const char*>(&S.geo[wv][e16]);
    const u32 g16 = (u32)(lq & 3) * 16u;
    const char* recq = reinterpret_cast<const char*>(&S.rec[wv][lq >> 2]);
    uint4* twp = &S.T[wv][(lq & 3) * 16 + ((lq >> 2) ^ ((lq & 3) * 2))];
    const uint4* trp = &S.T[wv][g * 16 + (e16 ^ (g * 2))];
    uint2 (*Mw)[4] = reinterpret_cast<uint2 (*)[4]>(&S.T[wv][0]);
    const char* xrb = recb + ((g == 1) ? 8 : 0);
    const char* lutb = reinterpret_cast<const char*>(&S.cst[224]);
    const u32 psel = (g == 1) ? 0x0c0c0504u : 0x03020100u;
    bf16x8 gA[4], gB[4];
    u32 pA0 = 0, pB0 = 0;
    auto issue = [&](int ti, bf16x8 (&gat)[4], u32& q0) __attribute__((always_inline)) {
        const u32 qs = reinterpret_cast<const uint4*>(recq + ti * 256)->z;
        q0 = reinterpret_cast<const uint4*>(recb + ti * 256)->z;
        const u32 off = (qs & 0xffffff00u) | g16;
#pragma unroll
        for (int tp = 0; tp < 4; ++tp)
            gat[tp] = *reinterpret_cast<const bf16x8*>(Bb + off + tp * 64);
    };
    auto compute = [&](int ti, int gr, bf16x8 (&gat)[4], u32 q0, bool hu) __attribute__((always_inline)) {
        const u32 nd = q0 & 3u;
        const char* xp = (g < 3) ? (xrb + ti * 256) : (lutb + (nd << 3));
        uint2 xu = *reinterpret_cast<const uint2*>(xp);
        const uint2 gq = *reinterpret_cast<const uint2*>(geob + ti * 128);
        const uint4* w5p = reinterpret_cast<const uint4*>(w5b + w5m * gr);
#pragma unroll
        for (int tp = 0; tp < 4; ++tp) {
            CBAR();
            *twp = *reinterpret_cast<const uint4*>(&gat[tp]);
            CBAR();
            gat[tp] = as_bf16x8(*trp);
        }
        CBAR();
        if (hu) load_hu();
        CBAR();
        Mw[e16][g] = (nd == (u32)g) ? gq : make_uint2(0u, 0u);
        xu.x = __builtin_amdgcn_perm(xu.y, xu.x, psel);
        const bf16x4 x4 = as_bf16x4(xu);
        CBAR();
        u32 sw01, sw23;
        {
            const u16* wp = reinterpret_cast<const u16*>(&Mw[g * 4][0]) + e16;
            sw01 = (u32)wp[0] | ((u32)wp[16] << 16);
            sw23 = (u32)wp[32] | ((u32)wp[48] << 16);
        }
        const bf16x4 sop = as_bf16x4(make_uint2(sw01, sw23));
        uint4 w5[4];
#pragma unroll
        for (int tp = 0; tp < 4; ++tp) w5[tp] = w5p[tp * 16];
        auto feat = [&](int tt) __attribute__((always_inline)) {
            const uint4 w = w5[tt >> 1];
            return __builtin_amdgcn_mfma_f32_16x16x16bf16_1k(x4, as_bf16x4((tt & 1) ? make_uint2(w.z, w.w) : make_uint2(w.x, w.y)),
                                                             (f32x4){0.f, 0.f, 0.f, 0.f}, 0, 0, 0);
        };
        auto gath = [&](int tt, f32x4 c) __attribute__((always_inline)) {
            const u32x4 gq4 = *reinterpret_cast<const u32x4*>(&gat[tt >> 1]);
            return __builtin_amdgcn_mfma_f32_16x16x16bf16_1k(as_bf16x4(make_uint2(gq4[(tt & 1) * 2], gq4[(tt & 1) * 2 + 1])), sel, c, 0, 0, 0);
        };
        f32x4 acc = gath(0, feat(0));
#pragma unroll
        for (int tt = 0; tt < 8; ++tt) {
            f32x4 accN = acc;
            __builtin_amdgcn_sched_barrier(0);
            if (tt < 7) accN = feat(tt + 1);
            __builtin_amdgcn_sched_barrier(0);
            float e0 = __builtin_amdgcn_exp2f(acc[0]), e1 = __builtin_amdgcn_exp2f(acc[1]);
            float e2 = __builtin_amdgcn_exp2f(acc[2]), e3 = __builtin_amdgcn_exp2f(acc[3]);
            __builtin_amdgcn_sched_barrier(0);
            if (tt < 7) accN = gath(tt + 1, accN);
            __builtin_amdgcn_sched_barrier(0);
            e0 += 1.f; e1 += 1.f; e2 += 1.f; e3 += 1.f;
            asm("" : "+v"(e0)); asm("" : "+v"(e1)); asm("" : "+v"(e2)); asm("" : "+v"(e3));
            float y0 = acc[0] * __builtin_amdgcn_rcpf(e0), y1 = acc[1] * __builtin_amdgcn_rcpf(e1);
            float y2 = acc[2] * __builtin_amdgcn_rcpf(e2), y3 = acc[3] * __builtin_amdgcn_rcpf(e3);
            asm("" : "+v"(y0)); asm("" : "+v"(y1)); asm("" : "+v"(y2)); asm("" : "+v"(y3));
            u32 k01 = cvt_pk((f32x2){y0, y1}), k23 = cvt_pk((f32x2){y2, y3});
            bf16x4 mb = as_bf16x4(make_uint2(k01, k23));
            __builtin_amdgcn_sched_barrier(0);
            acc2[tt] = __builtin_amdgcn_mfma_f32_16x16x16bf16_1k(sop, mb, acc2[tt], 0, 0, 0);
            acc = accN;
        }
    };

    asm volatile("" :: "v"(sel), "v"(w5b), "v"(w5m), "v"(recb), "v"(geob), "v"(g16), "v"(recq), "v"(twp), "v"(trp), "v"(xrb), "v"(psel), "v"(ec));
    if (wv < 2) *reinterpret_cast<u32x3u*>(tabs + t * 3) = tv;
    __syncthreads();
    int lo2 = lane;
    asm volatile("" : "+v"(lo2));
    const uint2* tp2 = reinterpret_cast<const uint2*>(tabs + lo2 * 6);
    const uint2 q0 = tp2[0], q1 = tp2[1], q2 = tp2[2];
    const u16* tq = reinterpret_cast<const u16*>(tp2) + wv;
    const u32 oA = tq[0], eA = tq[1], oB = tq[TS], eB = tq[TS + 1];
    const u32 S0 = q0.x + q1.y, S1 = q0.y + q2.x, S2 = q1.x + q2.y;
    const u32 x01 = (u32)wave_scan((int)(__builtin_amdgcn_alignbit(S1, S0, 16) - S0));
    const u32 x23 = (u32)wave_scan((int)(__builtin_amdgcn_alignbit(S2, S1, 16) - S1));
    const u32 x4 = (u32)wave_scan((int)((S2 >> 16) - (S2 & 0xffffu)));
    const bool big = __builtin_amdgcn_ballot_w64((S2 >> 16) - (S0 & 0xffffu) > 64u) != 0ull;
    const u32 T01 = (u32)__builtin_amdgcn_readlane((int)x01, 63), T23 = (u32)__builtin_amdgcn_readlane((int)x23, 63);
    const u32 T4 = (u32)__builtin_amdgcn_readlane((int)x4, 63);
    const int cb1 = (int)(((T01 & 0xffffu) + 15u) >> 4), cb2 = cb1 + (int)(((T01 >> 16) + 15u) >> 4);
    const int cb3 = cb2 + (int)(((T23 & 0xffffu) + 15u) >> 4), cb4 = cb3 + (int)(((T23 >> 16) + 15u) >> 4);
    const int TT = cb4 + (int)((T4 + 15u) >> 4);
    asm volatile("" :: "v"(oA), "v"(eA), "v"(oB), "v"(eB));
    int dgr = -1;
    bool serial = false;
    if (__builtin_expect(!big && TT <= KB_WAVES * WSL / 16, 1)) {
        const unsigned long long cbp = ((unsigned long long)(u32)(cb4 | (TT << 8) | (TT << 16)) << 32) | (u32)((cb1 << 8) | (cb2 << 16) | (cb3 << 24));
        auto mark = [&](int gr, u32 inc, u32 oa, u32 ea_, u32 ob, u32 eb_) __attribute__((always_inline)) {
            const u32 gb = (u32)((cbp >> (8 * gr)) & 0xffu) * 16u;
            const u32 pE = gb + inc, pB = pE - (eb_ - ob), pA = pB - (ea_ - oa);
            const u32 lb = (u32)lo2 * (2u * (u32)CH) + (((u32)gr + 1u) << 20) + MBIAS;
            atomicMax(&marks[64 + pA], lb + oa - pA);
            atomicMax(&marks[64 + pB], lb + (u32)CH + ob - pB);
            if (lo2 == 63) atomicMax(&marks[64 + pE], (((u32)gr + 2u) << 20) - 1u);
        };
        mark(wv, __builtin_amdgcn_ubfe((wv < 2) ? x01 : x23, (u32)(wv & 1) * 16u, 16u), oA, eA, oB, eB);
        if (wv == 3 && T4 != 0u) mark(4, x4, q1.x & 0xffffu, q1.x >> 16, q2.y & 0xffffu, q2.y >> 16);
        const int ts = (TT * wv) >> 2, te = (TT * (wv + 1)) >> 2;
        const int nst = te - ts;
        int gc, rem;
        auto seek = [&](int tn) __attribute__((always_inline)) {
            gc = (tn >= cb1 ? 1 : 0) + (tn >= cb2 ? 1 : 0) + (tn >= cb3 ? 1 : 0) + (tn >= cb4 ? 1 : 0);
            rem = (int)((cbp >> (8 * gc + 8)) & 0xffu) - tn;
        };
        auto fill = [&](auto kc) __attribute__((always_inline)) {
            constexpr int K = decltype(kc)::value;
            const int s0 = K * lo2;
            const u32* mp = marks + 64 + ts * 16;
            const u32 lbk = mp[lo2 - 64];
            u32 m[4];
#pragma unroll
            for (int i = 0; i < K; ++i) m[i] = mp[s0 + i];
#pragma unroll
            for (int i = 1; i < K; ++i) m[i] = max(m[i], m[i - 1]);
            u32 ex = wave_scan_max((u32)__builtin_amdgcn_update_dpp(0, (int)m[K - 1], 0x138, 0xf, 0xf, false));
            const u32 cr = (u32)__builtin_amdgcn_readlane((int)wave_scan_max(lbk), 63);
            ex = (s0 < nst * 16) ? max(ex, cr) : 0xffffffffu;
            const u32 po = (u32)(ts * 16 + s0 - MBIAS) * (RW * 4u);
            u32 off[4];
#pragma unroll
            for (int i = 0; i < K; ++i) off[i] = (max(m[i], ex) & 0xfffffu) * (RW * 4u) + po + i * RW * 4u;
            ld_rec(off, kc);
            stage();
            put_rec(s0, kc);
            issue(0, gA, pA0);
#pragma unroll
            for (int tt = 0; tt < 8; ++tt) {
                acc2[tt] = (f32x4){0.f, 0.f, 0.f, 0.f};
                asm volatile("" : "+v"(acc2[tt]));
            }
            seek(ts);
            asm volatile("" : "+s"(gc), "+s"(rem));
            geom(s0, kc);
        };
        __syncthreads();
        if (nst <= 8) fill(std::integral_constant<int, 2>());
        else if (nst <= 12) fill(std::integral_constant<int, 3>());
        else fill(std::integral_constant<int, 4>());
        {
            const int mx = max(max(max(cb1, cb2 - cb1), max(cb3 - cb2, cb4 - cb3)), TT - cb4);
            serial = mx >= (TT >> 2) + 2;
        }
        auto prio = [&](int left) __attribute__((always_inline)) {
            if (left >= PR3) __builtin_amdgcn_s_setprio(3);
            else if (left >= PR2) __builtin_amdgcn_s_setprio(2);
            else if (left >= PR1) __builtin_amdgcn_s_setprio(1);
            else __builtin_amdgcn_s_setprio(0);
        };
        auto step = [&](int tn) __attribute__((always_inline)) {
            dgr = gc;
            if (--rem == 0) {
                reduce();
                commit(gc, c_set);
                dgr = -1;
                seek(tn);
            }
        };
        __syncthreads();
        if (nst > 0) {
            int i = 0;
            for (; i + 1 < nst; i += 2) {
                prio(nst - i);
                issue(i + 1, gB, pB0);
                compute(i, gc, gA, pA0, i == HUI);
                step(ts + i + 1);
                prio(nst - i - 1);
                issue(min(i + 2, nst - 1), gA, pA0);
                compute(i + 1, gc, gB, pB0, false);
                step(ts + i + 2);
            }
            if (i < nst) {
                prio(1);
                compute(i, gc, gA, pA0, false);
                step(ts + i + 1);
            }
        }
        if (nst < HUI + 2) load_hu();
        load_bw();
        {
            const int ng = (int)((cbp >> (8 * wv + 8)) & 0xffu) - (int)((cbp >> (8 * wv)) & 0xffu);
            if (ng == 0 || (wv == 0 && T4 == 0u)) {
#pragma unroll
                for (int tt = 0; tt < 8; ++tt) hm[tt] = 0.f;
                hp = (f32x4){0.f, 0.f, 0.f, 0.f};
                if (ng == 0) commit(wv, c_set);
                if (wv == 0 && T4 == 0u && NR > 16) commit(4, c_set);
            }
        }
        if (dgr >= 0) reduce();
    } else {
        __syncthreads();
        stage();
        load_hu();
        for (int i = t; i < GPW * 4 * AST; i += KB_THREADS) (&S.macc[0][0])[i] = 0.f;
        if (t < GPW * 4) S.pacc[t] = make_float4(0.f, 0.f, 0.f, 0.f);
#pragma unroll
        for (int tt = 0; tt < 8; ++tt) acc2[tt] = (f32x4){0.f, 0.f, 0.f, 0.f};
        __syncthreads();
        u32* key = reinterpret_cast<u32*>(S.rec[wv]);
        for (int gr = 0; gr * 4 < NR; ++gr) {
            int so[2], sc[2], fo[2];
#pragma unroll
            for (int ci = 0; ci < 2; ++ci) {
                const u16* tg = tab + (size_t)((lo2 * 2 + ci) * NWG + bx) * TS + gr;
                so[ci] = tg[0];
                sc[ci] = (int)tg[1] - so[ci];
            }
            const int x = wave_scan(sc[0] + sc[1]);
            const int T = __builtin_amdgcn_readlane(x, 63);
            fo[1] = x - sc[1]; fo[0] = fo[1] - sc[0];
            for (int base = 0; base < T; base += KB_WAVES * WSL) {
                const int sh = ((min(KB_WAVES * WSL, T - base) + 63) >> 6) << 4;
                const int wb = base + wv * sh;
                const int Tr = max(0, min(sh, T - wb));
                if (Tr == 0) continue;
                CBAR();
#pragma unroll
                for (int ci = 0; ci < 2; ++ci) {
                    const int qa = max(0, wb - fo[ci]), qb = min(sc[ci], wb + Tr - fo[ci]);
#pragma clang loop unroll(disable) vectorize(disable) interleave(disable)
                    for (int q = qa; q < qb; ++q) key[fo[ci] + q - wb] = (u32)((lo2 * 2 + ci) * CH + so[ci] + q) * (RW * 4u);
                }
                CBAR();
                u32 off[4];
#pragma unroll
                for (int i = 0; i < 4; ++i) off[i] = (lo2 * 4 + i < Tr) ? key[lo2 * 4 + i] : 0xfffffff0u;
                CBAR();
                const std::integral_constant<int, 4> k4;
                ld_rec(off, k4);
                put_rec(lo2 * 4, k4);
                geom(lo2 * 4, k4);
                CBAR();
                for (int i = 0; i * 16 < Tr; ++i) {
                    issue(i, gA, pA0);
                    compute(i, gr, gA, pA0, false);
                }
            }
            reduce();
            for (int w = 0; w < KB_WAVES; ++w) {
                if (wv == w) commit(gr, c_acc);
                __syncthreads();
            }
        }
        load_bw();
    }

    int le = lane;
    asm volatile("" : "+v"(le));
    const int f16 = le & 15, gg = le >> 4;
    __syncthreads();
    for (int w = 0; w < KB_WAVES - 1; ++w) {
        if (dgr >= 0 && (wv == w || !serial)) commit(dgr, c_acc);
        __syncthreads();
        if (!serial) break;
    }
    auto node = [&](auto nc) __attribute__((always_inline)) {
        constexpr int NRT = decltype(nc)::value;
        f32x4 mv[NRT][8];
#pragma unroll
        for (int rt = 0; rt < NRT; ++rt) {
            const int lr = rt * 16 + f16;
            const float* mp = &S.macc[lr < NR ? lr : 0][gg * 8];
#pragma unroll
            for (int ks = 0; ks < 4; ++ks) {
                mv[rt][ks * 2] = *reinterpret_cast<const f32x4*>(mp + ks * 32);
                mv[rt][ks * 2 + 1] = *reinterpret_cast<const f32x4*>(mp + ks * 32 + 4);
            }
        }
        f32x4 acc[NRT][2];
#pragma unroll
        for (int rt = 0; rt < NRT; ++rt)
#pragma unroll
            for (int q = 0; q < 2; ++q) {
                const u32 u0 = q ? uv4[rt].z : uv4[rt].x, u1 = q ? uv4[rt].w : uv4[rt].y;
                acc[rt][q] = (f32x4){__uint_as_float(u0 << 16), __uint_as_float(u0 & 0xffff0000u),
                                     __uint_as_float(u1 << 16), __uint_as_float(u1 & 0xffff0000u)};
            }
        __builtin_amdgcn_sched_barrier(0);
#pragma unroll
        for (int ks = 0; ks < 4; ++ks)
#pragma unroll
            for (int rt = 0; rt < NRT; ++rt) {
                const f32x4 a = mv[rt][ks * 2], b = mv[rt][ks * 2 + 1];
                const bf16x8 af = as_bf16x8(make_uint4(cvt_pk((f32x2){a[0], a[1]}), cvt_pk((f32x2){a[2], a[3]}),
                                                       cvt_pk((f32x2){b[0], b[1]}), cvt_pk((f32x2){b[2], b[3]})));
#pragma unroll
                for (int q = 0; q < 2; ++q)
                    acc[rt][q] = __builtin_amdgcn_mfma_f32_16x16x32_bf16(bw[q][ks], af, acc[rt][q], 0, 0, 0);
            }
        const __amdgpu_buffer_rsrc_t ro = mk_rsrc(out_h, (u32)N * (D * 2));
#pragma unroll
        for (int rt = 0; rt < NRT; ++rt) {
            const int lr = rt * 16 + f16;
            u32 ov[4];
#pragma unroll
            for (int q = 0; q < 2; ++q) {
                const u32 h0 = q ? hv4[rt].z : hv4[rt].x, h1 = q ? hv4[rt].w : hv4[rt].y;
                ov[q * 2] = cvt_pk((f32x2){__uint_as_float(h0 << 16) + silu_f(acc[rt][q][0]), __uint_as_float(h0 & 0xffff0000u) + silu_f(acc[rt][q][1])});
                ov[q * 2 + 1] = cvt_pk((f32x2){__uint_as_float(h1 << 16) + silu_f(acc[rt][q][2]), __uint_as_float(h1 & 0xffff0000u) + silu_f(acc[rt][q][3])});
            }
            st_b128<AUX_O>(ro, (lr < NR) ? (u32)(N0 + lr) * (D * 2) + wv * 64 + gg * 16 : 0xfffffff0u, make_uint4(ov[0], ov[1], ov[2], ov[3]));
        }
    };
    if (NR > 16) node(std::integral_constant<int, 2>());
    else node(std::integral_constant<int, 1>());
    if (wv == 3 && le < NR * 3) {
        const int n = le / 3, cpt = le - n * 3;
        const float p = reinterpret_cast<const float*>(&S.posl[3][n])[cpt];
        const float* pa = reinterpret_cast<const float*>(&S.pacc[n]);
        out_pos[(size_t)N0 * 3 + le] = f2bf(p + (p * pa[0] - pa[1 + cpt]));
    }
}

extern "C" void kernel_launch(void* const* d_in, const int* in_sizes, int n_in,
                              void* d_out, int out_size, void* d_ws, size_t ws_size,
                              hipStream_t stream) {
    const u16* h   = (const u16*)d_in[0];
    const u16* pos = (const u16*)d_in[1];
    const u16* ea  = (const u16*)d_in[2];
    const int* src = (const int*)d_in[3];
    const int* dst = (const int*)d_in[4];
    const u16* we  = (const u16*)d_in[5];
    const u16* wx  = (const u16*)d_in[6];
    const u16* wh  = (const u16*)d_in[7];
    int N = in_sizes[0] / D;
    int E = in_sizes[3];
    u16* out_h = (u16*)d_out;
    u16* out_pos = out_h + (size_t)N * D;

    int CH = ((E + NCH - 1) / NCH + 3) & ~3;
    int NWG = 768;
    while (NWG * GPW * 4 < N && NWG < NWG_MAX) NWG += 2;
    int nGemm = (N + 127) / 128;
    unsigned long long mN = (1ull << 40) / (unsigned long long)N + 1ull;
    unsigned long long mNWG = (1ull << 40) / (unsigned long long)NWG + 1ull;

    char* ws = (char*)d_ws;
    size_t off = 0;
    auto take = [&](size_t bytes) { char* p = ws + off; off += (bytes + 255) & ~(size_t)255; return p; };
    u32* rec_g = (u32*)take((size_t)NCH * CH * RW * 4 + 64);
    u16* tab = (u16*)take((size_t)NCH * NWG * TS * 2);
    u16* ABU = (u16*)take((size_t)(3 * N + 32) * D * 2);
    u16* whT = (u16*)take((size_t)D * D * 2);
    uint4* w5g = (uint4*)take((size_t)256 * 16);
    uint2* pos8 = (uint2*)take((size_t)(N + 64) * 8);

    k_part<<<NCH + nGemm + 1, KA_THREADS, 0, stream>>>(h, pos, src, dst, ea, we, wx, wh, rec_g, tab, ABU, whT, w5g, pos8,
                                                        mN, mNWG, N, E, CH, NWG, nGemm, 0);
    k_main<<<NWG, KB_THREADS, 0, stream>>>(h, rec_g, tab, ABU, whT, w5g, pos8, out_h, out_pos, mNWG, N, CH, NWG, 0);
}
```

```cpp
#include <hip/hip_runtime.h>
#include <hip/hip_bf16.h>
#include <stdint.h>
#include <type_traits>

typedef unsigned short u16;
typedef unsigned int u32;
typedef __attribute__((ext_vector_type(8))) short bf16x8;
typedef __attribute__((ext_vector_type(4))) float f32x4;
typedef __attribute__((ext_vector_type(4))) short bf16x4;
typedef __attribute__((ext_vector_type(2))) float f32x2;
typedef __attribute__((ext_vector_type(4))) unsigned int u32x4;
__device__ __forceinline__ void st_nt(void* p, uint4 v) { u32x4 x = {v.x, v.y, v.z, v.w}; __builtin_nontemporal_store(x, reinterpret_cast<u32x4*>(p)); }

typedef __attribute__((ext_vector_type(2))) unsigned int u32x2;
typedef __attribute__((ext_vector_type(3))) unsigned int u32x3;
typedef __attribute__((ext_vector_type(3), aligned(4))) unsigned int u32x3u;
typedef __attribute__((ext_vector_type(4), aligned(4))) unsigned int u32x4u;
__device__ __forceinline__ __amdgpu_buffer_rsrc_t mk_rsrc(const void* p, u32 bytes) {
    return __builtin_amdgcn_make_buffer_rsrc(const_cast<void*>(p), (short)0, (int)bytes, 0x00020000);
}
template <int AUX>
__device__ __forceinline__ void st_b128(__amdgpu_buffer_rsrc_t r, u32 off, uint4 v) {
    u32x4 x = {v.x, v.y, v.z, v.w};
    __builtin_amdgcn_raw_buffer_store_b128(x, r, (int)off, 0, AUX);
}
template <int AUX>
__device__ __forceinline__ void st_b64(__amdgpu_buffer_rsrc_t r, u32 off, uint2 v) {
    u32x2 x = {v.x, v.y};
    __builtin_amdgcn_raw_buffer_store_b64(x, r, (int)off, 0, AUX);
}
#define AUX_A 16
#define AUX_B 16
#define AUX_U 16
#define AUX_R 16
#define AUX_O 0

#define D 128
#define KA_THREADS 1024
#define KA_ITERS 4
#define KA_CH_MAX 4032
#define NCH 128
#define NWG_MAX 1024
#define GPW 5
#define TS 6
#define KB_THREADS 256
#define KB_WAVES 4
#define WSL 256
#define RW 3
#define MBIAS 2048
#define KA_SMEM (NWG_MAX * GPW * 16 + NWG_MAX * TS * 2 + KA_CH_MAX * RW * 4 + 16)
#define AST (D + 4)
#define CBAR() asm volatile("" ::: "memory")
#define NLOG2E (-1.4426950408889634f)
#define NLN2 (-0.6931471805599453f)

__device__ __forceinline__ float bf2f(u16 v) { return __uint_as_float(((u32)v) << 16); }
__device__ __forceinline__ u16 f2bf(float f) {
    __hip_bfloat16 b = __float2bfloat16(f);
    return *reinterpret_cast<u16*>(&b);
}
__device__ __forceinline__ float silu_f(float x) {
    float e = __builtin_amdgcn_exp2f(x * NLOG2E);
    return x * __builtin_amdgcn_rcpf(1.0f + e);
}
__device__ __forceinline__ float silu_t(float t, float a) {
    return __builtin_fmaf(t * __builtin_amdgcn_rcpf(1.0f + __builtin_amdgcn_exp2f(t)), NLN2, a);
}
__device__ __forceinline__ u32 pack2(float a, float b) {
    return (u32)f2bf(a) | ((u32)f2bf(b) << 16);
}
typedef __attribute__((ext_vector_type(2))) __bf16 bf16x2;
__device__ __forceinline__ u32 cvt_pk(f32x2 v) {
    bf16x2 p = __builtin_convertvector(v, bf16x2);
    return *reinterpret_cast<u32*>(&p);
}
typedef __attribute__((ext_vector_type(2))) _Float16 f16x2;
typedef __attribute__((ext_vector_type(4))) _Float16 f16x4;
__device__ __forceinline__ u32 cvt_pkh(f32x2 v) {
    f16x2 p = __builtin_convertvector(v, f16x2);
    return *reinterpret_cast<u32*>(&p);
}
__device__ __forceinline__ bf16x8 as_bf16x8(uint4 v) { return *reinterpret_cast<bf16x8*>(&v); }
__device__ __forceinline__ bf16x4 as_bf16x4(uint2 v) { return *reinterpret_cast<bf16x4*>(&v); }
template <int CTRL>
__device__ __forceinline__ float dpp_f(float x) {
    return __int_as_float(__builtin_amdgcn_update_dpp(0, __float_as_int(x), CTRL, 0xf, 0xf, true));
}
__device__ __forceinline__ float rsum16(float x) {
    x += dpp_f<0x128>(x);
    asm("" : "+v"(x));
    x += dpp_f<0x124>(x);
    asm("" : "+v"(x));
    x += dpp_f<0x122>(x);
    asm("" : "+v"(x));
    x += dpp_f<0x121>(x);
    asm("" : "+v"(x));
    return x;
}
template <int CTRL, int RM>
__device__ __forceinline__ int dpp_add(int x) { return x + __builtin_amdgcn_update_dpp(0, x, CTRL, RM, 0xf, false); }
__device__ __forceinline__ int wave_scan(int x) {
    x = dpp_add<0x111, 0xf>(x);
    x = dpp_add<0x112, 0xf>(x);
    x = dpp_add<0x114, 0xf>(x);
    x = dpp_add<0x118, 0xf>(x);
    x = dpp_add<0x142, 0xa>(x);
    x = dpp_add<0x143, 0xc>(x);
    return x;
}
template <int CTRL, int RM>
__device__ __forceinline__ u32 dpp_max(u32 x) { return max(x, (u32)__builtin_amdgcn_update_dpp(0, (int)x, CTRL, RM, 0xf, false)); }
__device__ __forceinline__ u32 wave_scan_max(u32 x) {
    x = dpp_max<0x111, 0xf>(x);
    x = dpp_max<0x112, 0xf>(x);
    x = dpp_max<0x114, 0xf>(x);
    x = dpp_max<0x118, 0xf>(x);
    x = dpp_max<0x142, 0xa>(x);
    x = dpp_max<0x143, 0xc>(x);
    return x;
}
__device__ __forceinline__ u32 mdiv(u32 x, unsigned long long m) {
    return (u32)(((unsigned long long)x * m) >> 40);
}
__device__ __forceinline__ int node_start(int b, int N, int NBW, unsigned long long mNBW) {
    return (int)mdiv((u32)b * (u32)N + (u32)NBW - 1u, mNBW);
}

__global__ void __launch_bounds__(KA_THREADS)
k_part(const u16* __restrict__ h, const u16* __restrict__ pos, const int* __restrict__ src, const int* __restrict__ dst,
       const u16* __restrict__ ea, const u16* __restrict__ we, const u16* __restrict__ wx, const u16* __restrict__ wh,
       u32* __restrict__ rec_g, u16* __restrict__ tab, u16* __restrict__ ABU,
       u16* __restrict__ whT, uint4* __restrict__ w5g, uint2* __restrict__ pos8,
       unsigned long long mN, unsigned long long mNWG,
       int N, int E, int CH, int NWG, int nGemm, int unused) {
    __shared__ __align__(16) u16 smem[KA_SMEM / 2];
    __shared__ int wsum[KA_THREADS / 64];
    asm volatile("" :: "s"(h), "s"(pos), "s"(src), "s"(dst), "s"(ea), "s"(we), "s"(wx), "s"(wh), "s"(rec_g), "s"(tab), "s"(ABU),
                 "s"(whT), "s"(w5g), "s"(pos8), "s"(mN), "s"(mNWG), "s"(N), "s"(E), "s"(CH), "s"(NWG), "s"(nGemm));
    const int t = threadIdx.x, lane = t & 63;
    const int wv = __builtin_amdgcn_readfirstlane(t >> 6);
    int bid = blockIdx.x;
    if (bid < NCH) {
        uint4* cnt = reinterpret_cast<uint4*>(smem);
        u16* base16 = smem + NWG_MAX * GPW * 8;
        u32* lrec = reinterpret_cast<u32*>(base16 + NWG_MAX * TS);
        const int e0 = bid * CH;
        const int ne = max(0, min(E, e0 + CH) - e0);
        const int QPW = ((CH >> 2) + 15) >> 4;
        const int el = (lane < QPW) ? (wv * QPW + lane) * 4 : CH;
        const int eb = min(e0 + el, E - 4);
        const int i0 = e0 + el - eb, i1 = e0 + ne - eb;
        const u32x4 dq = *reinterpret_cast<const u32x4u*>(dst + eb);
        int dv[KA_ITERS];
#pragma unroll
        for (int i = 0; i < NWG_MAX * GPW / KA_THREADS; ++i)
            if (t + i * KA_THREADS < NWG * GPW) cnt[t + i * KA_THREADS] = make_uint4(0u, 0u, 0u, 0u);
        __syncthreads();
        int eb2 = eb;
        asm volatile("" : "+v"(eb2) : "v"(dq[0]));
        const u32x4 sq = *reinterpret_cast<const u32x4u*>(src + eb2);
        const u32x4 ea0 = *reinterpret_cast<const u32x4u*>(ea + (size_t)eb2 * 4);
        const u32x4 ea1 = *reinterpret_cast<const u32x4u*>(ea + (size_t)eb2 * 4 + 8);
        const int wq = wv >> 2, wsft = (wv & 3) * 8;
        const u32 inc = 1u << wsft;
        int bv[KA_ITERS], b6[KA_ITERS], rk[KA_ITERS];
#pragma unroll
        for (int i = 0; i < KA_ITERS; ++i) {
            const int w = (int)mdiv(dq[i] * (u32)NWG, mN);
            dv[i] = (int)dq[i] - node_start(w, N, NWG, mNWG);
            const int b = w * GPW + (dv[i] >> 2);
            bv[i] = b;
            b6[i] = b + w;
            rk[i] = 0;
            if (i >= i0 && i < i1) rk[i] = (int)atomicAdd(reinterpret_cast<u32*>(cnt) + b * 4 + wq, inc);
        }
        __syncthreads();
        {
            int c[GPW], tot = 0;
            u32x4 q[GPW];
#pragma unroll
            for (int j = 0; j < GPW; ++j)
                q[j] = *reinterpret_cast<const u32x4*>(&cnt[min(t, NWG - 1) * GPW + j]);
#pragma unroll
            for (int j = 0; j < GPW; ++j) {
                c[j] = (int)__builtin_amdgcn_sad_u8(q[j][0], 0u, __builtin_amdgcn_sad_u8(q[j][1], 0u,
                            __builtin_amdgcn_sad_u8(q[j][2], 0u, __builtin_amdgcn_sad_u8(q[j][3], 0u, 0u))));
                tot += c[j];
            }
            tot = (t < NWG) ? tot : 0;
            const int x = wave_scan(tot);
            if (lane == 63) wsum[wv] = x;
            __syncthreads();
            int y = wsum[lane & 15];
            y = ((lane & 15) < wv) ? y : 0;
            y += __builtin_amdgcn_update_dpp(0, y, 0x128, 0xf, 0xf, true);
            y += __builtin_amdgcn_update_dpp(0, y, 0x124, 0xf, 0xf, true);
            y += __builtin_amdgcn_update_dpp(0, y, 0x122, 0xf, 0xf, true);
            y += __builtin_amdgcn_update_dpp(0, y, 0x121, 0xf, 0xf, true);
            const u32 o0 = (u32)(y + x - tot), o1 = o0 + c[0], o2 = o1 + c[1], o3 = o2 + c[2], o4 = o3 + c[3], o5 = o4 + c[4];
            const u32x3 ov = {o0 | (o1 << 16), o2 | (o3 << 16), o4 | (o5 << 16)};
            if (t < NWG) *reinterpret_cast<u32x3u*>(base16 + t * TS) = ov;
            const __amdgpu_buffer_rsrc_t rt = mk_rsrc(tab + (size_t)bid * (NWG * TS), (u32)NWG * (TS * 2u));
            __builtin_amdgcn_raw_buffer_store_b96(ov, rt, t * (TS * 2), 0, AUX_R);
        }
        __syncthreads();
        {
            const u32 m0 = (wq > 0) ? ~0u : inc - 1u;
            const u32 m1 = (wq > 1) ? ~0u : (wq == 1) ? inc - 1u : 0u;
            const u32 m2 = (wq > 2) ? ~0u : (wq == 2) ? inc - 1u : 0u;
            const u32 m3 = (wq == 3) ? inc - 1u : 0u;
            uint4 q[KA_ITERS];
            u32 bs[KA_ITERS];
#pragma unroll
            for (int i = 0; i < KA_ITERS; ++i) {
                q[i] = cnt[bv[i]]; bs[i] = base16[b6[i]];
            }
#pragma unroll
            for (int i = 0; i < KA_ITERS; ++i) {
                if (i >= i0 && i < i1) {
                    u32 p = bs[i] + (((u32)rk[i] >> wsft) & 255u);
                    p = __builtin_amdgcn_sad_u8(q[i].x & m0, 0u, p);
                    p = __builtin_amdgcn_sad_u8(q[i].y & m1, 0u, p);
                    p = __builtin_amdgcn_sad_u8(q[i].z & m2, 0u, p);
                    p = __builtin_amdgcn_sad_u8(q[i].w & m3, 0u, p);
                    u32* o = lrec + p * RW;
                    o[0] = (i < 2) ? ea0[(i & 1) * 2] : ea1[(i & 1) * 2]; o[1] = (i < 2) ? ea0[(i & 1) * 2 + 1] : ea1[(i & 1) * 2 + 1];
                    o[2] = ((sq[i] + 1u) << 8) | (((u32)dv[i] & 3u) << 3) | ((u32)dv[i] >> 2);
                }
            }
        }
        __syncthreads();
        {
            const int CHB = (CH * RW + 3) >> 2;
            const int nq = (ne * RW + 3) >> 2;
            const __amdgpu_buffer_rsrc_t rr = mk_rsrc(reinterpret_cast<uint4*>(rec_g) + (size_t)bid * CHB, (u32)nq * 16u);
            const uint4* lq4 = reinterpret_cast<const uint4*>(lrec);
#pragma unroll
            for (int i = 0; i < KA_ITERS * RW / 4; ++i) {
                int p = t + i * KA_THREADS;
                if (p < nq) st_b128<AUX_R>(rr, (u32)p * 16u, lq4[p]);
            }
        }
        return;
    }
    bid -= NCH;
    if (bid < nGemm) {
        const int e16 = lane & 15, g = lane >> 4;
        char* wl = reinterpret_cast<char*>(smem);
        char* hl = wl + 3 * D * 256;
        u32x4 r[8];
        u32 p01 = 0, p2 = 0;
        if (wv < 12) {
            const int kb = (wv & 3) * 4 + g;
            const u16* wsrc = ((wv < 8) ? we + (size_t)(wv >> 2) * D * D : wh) + (size_t)kb * 8 * D + e16 * 8;
#pragma unroll
            for (int i = 0; i < 8; ++i) r[i] = *reinterpret_cast<const u32x4*>(wsrc + i * D);
            CBAR();
            char* q = wl + (wv >> 2) * (D * 256) + e16 * 256 + ((kb ^ e16) << 4);
#pragma unroll
            for (int j = 0; j < 8; ++j) {
                const u32 sel = (j & 1) ? 0x07060302u : 0x05040100u;
                u32x4 o;
#pragma unroll
                for (int p = 0; p < 4; ++p) o[p] = __builtin_amdgcn_perm(r[2 * p + 1][j >> 1], r[2 * p][j >> 1], sel);
                *reinterpret_cast<u32x4*>(q + j * (16 * 256)) = o;
            }
        } else {
            const int row0 = (wv - 12) * 32 + g;
#pragma unroll
            for (int i = 0; i < 8; ++i)
                r[i] = *reinterpret_cast<const u32x4*>(h + (size_t)min(bid * 128 + row0 + i * 4, N - 1) * D + e16 * 8);
            const u16* pp = pos + (size_t)min(bid * 128 + (t & 127), N - 1) * 3;
            if (wv >= 14) { p01 = 0x3F80u | ((u32)pp[0] << 16); p2 = (u32)pp[1] | ((u32)pp[2] << 16); }
            CBAR();
#pragma unroll
            for (int i = 0; i < 8; ++i) {
                const int row = row0 + i * 4;
                *reinterpret_cast<u32x4*>(hl + row * 256 + ((e16 ^ (row & 15)) << 4)) = r[i];
            }
        }
        __syncthreads();
        if (wv >= 12) {
            if (wv >= 14 && bid * 128 + (t & 127) < N) pos8[1 + bid * 128 + (t & 127)] = make_uint2(p01, p2);
            return;
        }
        const int rp = wv & 3, m = wv >> 2;
        const u32 lb = (u32)(e16 * 256 + ((g ^ e16) << 4));
        f32x4 acc[2][8];
#pragma unroll
        for (int r = 0; r < 2; ++r)
#pragma unroll
            for (int j = 0; j < 8; ++j) acc[r][j] = (f32x4){0.f, 0.f, 0.f, 0.f};
#pragma unroll
        for (int ks = 0; ks < 4; ++ks) {
            const char* hp = hl + rp * (32 * 256) + (lb ^ (u32)(ks * 64));
            const bf16x8 a0 = *reinterpret_cast<const bf16x8*>(hp);
            const bf16x8 a1 = *reinterpret_cast<const bf16x8*>(hp + 16 * 256);
#pragma unroll
            for (int j = 0; j < 8; ++j) {
                const char* wp = wl + m * (D * 256) + j * (16 * 256) + (lb ^ (u32)(ks * 64));
                const bf16x8 w = *reinterpret_cast<const bf16x8*>(wp);
                acc[0][j] = __builtin_amdgcn_mfma_f32_16x16x32_bf16(a0, w, acc[0][j], 0, 0, 0);
                acc[1][j] = __builtin_amdgcn_mfma_f32_16x16x32_bf16(a1, w, acc[1][j], 0, 0, 0);
            }
        }
        const float sc = NLOG2E;
        const __amdgpu_buffer_rsrc_t ro = mk_rsrc(ABU + (size_t)m * N * D, (u32)N * (D * 2));
#pragma unroll
        for (int r = 0; r < 2; ++r)
#pragma unroll
            for (int i = 0; i < 4; ++i) {
                const int n = bid * 128 + rp * 32 + r * 16 + g * 4 + i;
                uint4 o = make_uint4(cvt_pk((f32x2){acc[r][0][i] * sc, acc[r][1][i] * sc}), cvt_pk((f32x2){acc[r][2][i] * sc, acc[r][3][i] * sc}),
                                     cvt_pk((f32x2){acc[r][4][i] * sc, acc[r][5][i] * sc}), cvt_pk((f32x2){acc[r][6][i] * sc, acc[r][7][i] * sc}));
                if (m == 1) st_b128<AUX_B>(ro, (u32)n * (D * 2) + e16 * 16, o);
                else if (m == 2) st_b128<AUX_U>(ro, (u32)n * (D * 2) + e16 * 16, o);
                else st_b128<AUX_A>(ro, (u32)n * (D * 2) + e16 * 16, o);
            }
        return;
    }
    {
        u32 w[8];
#pragma unroll
        for (int j = 0; j < 8; ++j)
            w[j] = *reinterpret_cast<const u32*>(wh + (size_t)(D + wv * 8 + j) * D + lane * 2);
        if (t < 192) {
            const int gq = t >> 6, tp = (t >> 4) & 3, c16 = t & 15;
            u32 o[4];
#pragma unroll
            for (int u = 0; u < 2; ++u) {
                const int c = tp * 32 + (c16 >> 2) * 8 + u * 4 + (c16 & 3);
                u32 hi[5], lo[5];
#pragma unroll
                for (int j = 0; j < 5; ++j) {
                    float x = bf2f(we[(size_t)(256 + j) * D + c]) * NLOG2E;
                    u16 hh = f2bf(x);
                    hi[j] = hh; lo[j] = f2bf(x - bf2f(hh));
                }
                const u32 wd = we[(size_t)256 * D + c];
                o[u * 2] = (gq == 0) ? (hi[1] | (hi[2] << 16)) : (gq == 1) ? 0u : (lo[1] | (lo[2] << 16));
                o[u * 2 + 1] = (gq == 0) ? (hi[3] | (hi[4] << 16)) : (gq == 1) ? (wd | (wd << 16)) : (lo[3] | (lo[4] << 16));
            }
            w5g[t] = make_uint4(o[0], o[1], o[2], o[3]);
        } else if (t < 192 + D) {
            reinterpret_cast<float*>(w5g + 192)[t - 192] = bf2f(wx[t - 192]) * NLN2;
        } else if (t < 192 + D + 128) {
            const int q = t - 192 - D, n = (q & 63) >> 1;
            reinterpret_cast<u32*>(w5g + 224)[q] = (n < 4 && (n >> 1) == (q & 1)) ? (0x3F80u << (16 * (n & 1))) : 0u;
        } else if (t == 192 + D + 128) {
            pos8[0] = make_uint2(0u, 0u);
        }
        const int c = lane * 2;
        const int f16 = ((c >> 3) & 3) * 4 + (c & 3), q = (c >> 2) & 1;
        uint4* o = reinterpret_cast<uint4*>(whT) + (((c >> 5) * 2 + q) * 4 + (wv >> 2)) * 64 + (wv & 3) * 16 + f16;
        o[0] = make_uint4((w[0] & 0xffffu) | (w[1] << 16), (w[2] & 0xffffu) | (w[3] << 16),
                          (w[4] & 0xffffu) | (w[5] << 16), (w[6] & 0xffffu) | (w[7] << 16));
        o[1] = make_uint4((w[0] >> 16) | (w[1] & 0xffff0000u), (w[2] >> 16) | (w[3] & 0xffff0000u),
                          (w[4] >> 16) | (w[5] & 0xffff0000u), (w[6] >> 16) | (w[7] & 0xffff0000u));
    }
}

#define PR3 6
#define PR2 4
#define PR1 2
#define PR_EVERY 1
#define PRO_PRIO 0
#define HUI 2
struct KbSmem {
    uint4 rec[KB_WAVES][WSL];
    uint2 geo[KB_WAVES][WSL];
    uint4 cst[256];
    uint4 At[GPW][64];
    float macc[GPW * 4][AST];
    float pacc[GPW][64];
    float4 posl[KB_WAVES][32];
    uint4 T[KB_WAVES][64];
    u32 flag;
};

#define TILE_ASM(C) \
    "s_nop 1\n" \
    "v_mfma_f32_16x16x16_bf16 v[152:155], %8, %11, 0\n" \
    "v_mfma_f32_16x16x16_bf16 v[152:155], %19, %9, v[152:155]\n" \
    "v_mfma_f32_16x16x16_bf16 v[156:159], %8, %12, 0\n" \
    "s_nop 6\n" \
    "v_cvt_pk_f16_f32 v160, v152, v153\n" \
    "v_cvt_pk_f16_f32 v161, v154, v155\n" \
    "v_exp_f16_e32 v162, v160\n" \
    "v_exp_f16_e32 v163, v161\n" \
    "v_exp_f16_sdwa v162, v160 dst_sel:WORD_1 dst_unused:UNUSED_PRESERVE src0_sel:WORD_1\n" \
    "v_exp_f16_sdwa v163, v161 dst_sel:WORD_1 dst_unused:UNUSED_PRESERVE src0_sel:WORD_1\n" \
    "v_mfma_f32_16x16x16_bf16 v[156:159], %20, %9, v[156:159]\n" \
    "v_pk_add_f16 v162, v162, 1.0 op_sel_hi:[1,0]\n" \
    "v_pk_add_f16 v163, v163, 1.0 op_sel_hi:[1,0]\n" \
    "v_rcp_f16_e32 v164, v162\n" \
    "v_rcp_f16_e32 v165, v163\n" \
    "v_rcp_f16_sdwa v164, v162 dst_sel:WORD_1 dst_unused:UNUSED_PRESERVE src0_sel:WORD_1\n" \
    "v_rcp_f16_sdwa v165, v163 dst_sel:WORD_1 dst_unused:UNUSED_PRESERVE src0_sel:WORD_1\n" \
    "v_pk_mul_f16 v164, v160, v164\n" \
    "v_pk_mul_f16 v165, v161, v165\n" \
    "v_mfma_f32_16x16x16_bf16 v[152:155], %8, %13, 0\n" \
    "v_cvt_pk_f16_f32 v160, v156, v157\n" \
    "v_cvt_pk_f16_f32 v161, v158, v159\n" \
    "v_mfma_f32_16x16x16_f16 %0, v[164:165], %10, " C(0) "\n" \
    "v_exp_f16_e32 v162, v160\n" \
    "v_exp_f16_e32 v163, v161\n" \
    "v_exp_f16_sdwa v162, v160 dst_sel:WORD_1 dst_unused:UNUSED_PRESERVE src0_sel:WORD_1\n" \
    "v_exp_f16_sdwa v163, v161 dst_sel:WORD_1 dst_unused:UNUSED_PRESERVE src0_sel:WORD_1\n" \
    "v_mfma_f32_16x16x16_bf16 v[152:155], %21, %9, v[152:155]\n" \
    "v_pk_add_f16 v162, v162, 1.0 op_sel_hi:[1,0]\n" \
    "v_pk_add_f16 v163, v163, 1.0 op_sel_hi:[1,0]\n" \
    "v_rcp_f16_e32 v164, v162\n" \
    "v_rcp_f16_e32 v165, v163\n" \
    "v_rcp_f16_sdwa v164, v162 dst_sel:WORD_1 dst_unused:UNUSED_PRESERVE src0_sel:WORD_1\n" \
    "v_rcp_f16_sdwa v165, v163 dst_sel:WORD_1 dst_unused:UNUSED_PRESERVE src0_sel:WORD_1\n" \
    "v_pk_mul_f16 v164, v160, v164\n" \
    "v_pk_mul_f16 v165, v161, v165\n" \
    "v_mfma_f32_16x16x16_bf16 v[156:159], %8, %14, 0\n" \
    "v_cvt_pk_f16_f32 v160, v152, v153\n" \
    "v_cvt_pk_f16_f32 v161, v154, v155\n" \
    "v_mfma_f32_16x16x16_f16 %1, v[164:165], %10, " C(1) "\n" \
    "v_exp_f16_e32 v162, v160\n" \
    "v_exp_f16_e32 v163, v161\n" \
    "v_exp_f16_sdwa v162, v160 dst_sel:WORD_1 dst_unused:UNUSED_PRESERVE src0_sel:WORD_1\n" \
    "v_exp_f16_sdwa v163, v161 dst_sel:WORD_1 dst_unused:UNUSED_PRESERVE src0_sel:WORD_1\n" \
    "v_mfma_f32_16x16x16_bf16 v[156:159], %22, %9, v[156:159]\n" \
    "v_pk_add_f16 v162, v162, 1.0 op_sel_hi:[1,0]\n" \
    "v_pk_add_f16 v163, v163, 1.0 op_sel_hi:[1,0]\n" \
    "v_rcp_f16_e32 v164, v162\n" \
    "v_rcp_f16_e32 v165, v163\n" \
    "v_rcp_f16_sdwa v164, v162 dst_sel:WORD_1 dst_unused:UNUSED_PRESERVE src0_sel:WORD_1\n" \
    "v_rcp_f16_sdwa v165, v163 dst_sel:WORD_1 dst_unused:UNUSED_PRESERVE src0_sel:WORD_1\n" \
    "v_pk_mul_f16 v164, v160, v164\n" \
    "v_pk_mul_f16 v165, v161, v165\n" \
    "v_mfma_f32_16x16x16_bf16 v[152:155], %8, %15, 0\n" \
    "v_cvt_pk_f16_f32 v160, v156, v157\n" \
    "v_cvt_pk_f16_f32 v161, v158, v159\n" \
    "v_mfma_f32_16x16x16_f16 %2, v[164:165], %10, " C(2) "\n" \
    "v_exp_f16_e32 v162, v160\n" \
    "v_exp_f16_e32 v163, v161\n" \
    "v_exp_f16_sdwa v162, v160 dst_sel:WORD_1 dst_unused:UNUSED_PRESERVE src0_sel:WORD_1\n" \
    "v_exp_f16_sdwa v163, v161 dst_sel:WORD_1 dst_unused:UNUSED_PRESERVE src0_sel:WORD_1\n" \
    "v_mfma_f32_16x16x16_bf16 v[152:155], %23, %9, v[152:155]\n" \
    "v_pk_add_f16 v162, v162, 1.0 op_sel_hi:[1,0]\n" \
    "v_pk_add_f16 v163, v163, 1.0 op_sel_hi:[1,0]\n" \
    "v_rcp_f16_e32 v164, v162\n" \
    "v_rcp_f16_e32 v165, v163\n" \
    "v_rcp_f16_sdwa v164, v162 dst_sel:WORD_1 dst_unused:UNUSED_PRESERVE src0_sel:WORD_1\n" \
    "v_rcp_f16_sdwa v165, v163 dst_sel:WORD_1 dst_unused:UNUSED_PRESERVE src0_sel:WORD_1\n" \
    "v_pk_mul_f16 v164, v160, v164\n" \
    "v_pk_mul_f16 v165, v161, v165\n" \
    "v_mfma_f32_16x16x16_bf16 v[156:159], %8, %16, 0\n" \
    "v_cvt_pk_f16_f32 v160, v152, v153\n" \
    "v_cvt_pk_f16_f32 v161, v154, v155\n" \
    "v_mfma_f32_16x16x16_f16 %3, v[164:165], %10, " C(3) "\n" \
    "v_exp_f16_e32 v162, v160\n" \
    "v_exp_f16_e32 v163, v161\n" \
    "v_exp_f16_sdwa v162, v160 dst_sel:WORD_1 dst_unused:UNUSED_PRESERVE src0_sel:WORD_1\n" \
    "v_exp_f16_sdwa v163, v161 dst_sel:WORD_1 dst_unused:UNUSED_PRESERVE src0_sel:WORD_1\n" \
    "v_mfma_f32_16x16x16_bf16 v[156:159], %24, %9, v[156:159]\n" \
    "v_pk_add_f16 v162, v162, 1.0 op_sel_hi:[1,0]\n" \
    "v_pk_add_f16 v163, v163, 1.0 op_sel_hi:[1,0]\n" \
    "v_rcp_f16_e32 v164, v162\n" \
    "v_rcp_f16_e32 v165, v163\n" \
    "v_rcp_f16_sdwa v164, v162 dst_sel:WORD_1 dst_unused:UNUSED_PRESERVE src0_sel:WORD_1\n" \
    "v_rcp_f16_sdwa v165, v163 dst_sel:WORD_1 dst_unused:UNUSED_PRESERVE src0_sel:WORD_1\n" \
    "v_pk_mul_f16 v164, v160, v164\n" \
    "v_pk_mul_f16 v165, v161, v165\n" \
    "v_mfma_f32_16x16x16_bf16 v[152:155], %8, %17, 0\n" \
    "v_cvt_pk_f16_f32 v160, v156, v157\n" \
    "v_cvt_pk_f16_f32 v161, v158, v159\n" \
    "v_mfma_f32_16x16x16_f16 %4, v[164:165], %10, " C(4) "\n" \
    "v_exp_f16_e32 v162, v160\n" \
    "v_exp_f16_e32 v163, v161\n" \
    "v_exp_f16_sdwa v162, v160 dst_sel:WORD_1 dst_unused:UNUSED_PRESERVE src0_sel:WORD_1\n" \
    "v_exp_f16_sdwa v163, v161 dst_sel:WORD_1 dst_unused:UNUSED_PRESERVE src0_sel:WORD_1\n" \
    "v_mfma_f32_16x16x16_bf16 v[152:155], %25, %9, v[152:155]\n" \
    "v_pk_add_f16 v162, v162, 1.0 op_sel_hi:[1,0]\n" \
    "v_pk_add_f16 v163, v163, 1.0 op_sel_hi:[1,0]\n" \
    "v_rcp_f16_e32 v164, v162\n" \
    "v_rcp_f16_e32 v165, v163\n" \
    "v_rcp_f16_sdwa v164, v162 dst_sel:WORD_1 dst_unused:UNUSED_PRESERVE src0_sel:WORD_1\n" \
    "v_rcp_f16_sdwa v165, v163 dst_sel:WORD_1 dst_unused:UNUSED_PRESERVE src0_sel:WORD_1\n" \
    "v_pk_mul_f16 v164, v160, v164\n" \
    "v_pk_mul_f16 v165, v161, v165\n" \
    "v_mfma_f32_16x16x16_bf16 v[156:159], %8, %18, 0\n" \
    "v_cvt_pk_f16_f32 v160, v152, v153\n" \
    "v_cvt_pk_f16_f32 v161, v154, v155\n" \
    "v_mfma_f32_16x16x16_f16 %5, v[164:165], %10, " C(5) "\n" \
    "v_exp_f16_e32 v162, v160\n" \
    "v_exp_f16_e32 v163, v161\n" \
    "v_exp_f16_sdwa v162, v160 dst_sel:WORD_1 dst_unused:UNUSED_PRESERVE src0_sel:WORD_1\n" \
    "v_exp_f16_sdwa v163, v161 dst_sel:WORD_1 dst_unused:UNUSED_PRESERVE src0_sel:WORD_1\n" \
    "v_mfma_f32_16x16x16_bf16 v[156:159], %26, %9, v[156:159]\n" \
    "v_pk_add_f16 v162, v162, 1.0 op_sel_hi:[1,0]\n" \
    "v_pk_add_f16 v163, v163, 1.0 op_sel_hi:[1,0]\n" \
    "v_rcp_f16_e32 v164, v162\n" \
    "v_rcp_f16_e32 v165, v163\n" \
    "v_rcp_f16_sdwa v164, v162 dst_sel:WORD_1 dst_unused:UNUSED_PRESERVE src0_sel:WORD_1\n" \
    "v_rcp_f16_sdwa v165, v163 dst_sel:WORD_1 dst_unused:UNUSED_PRESERVE src0_sel:WORD_1\n" \
    "v_pk_mul_f16 v164, v160, v164\n" \
    "v_pk_mul_f16 v165, v161, v165\n" \
    "v_cvt_pk_f16_f32 v160, v156, v157\n" \
    "v_cvt_pk_f16_f32 v161, v158, v159\n" \
    "v_mfma_f32_16x16x16_f16 %6, v[164:165], %10, " C(6) "\n" \
    "v_exp_f16_e32 v162, v160\n" \
    "v_exp_f16_e32 v163, v161\n" \
    "v_exp_f16_sdwa v162, v160 dst_sel:WORD_1 dst_unused:UNUSED_PRESERVE src0_sel:WORD_1\n" \
    "v_exp_f16_sdwa v163, v161 dst_sel:WORD_1 dst_unused:UNUSED_PRESERVE src0_sel:WORD_1\n" \
    "v_pk_add_f16 v162, v162, 1.0 op_sel_hi:[1,0]\n" \
    "v_pk_add_f16 v163, v163, 1.0 op_sel_hi:[1,0]\n" \
    "v_rcp_f16_e32 v164, v162\n" \
    "v_rcp_f16_e32 v165, v163\n" \
    "v_rcp_f16_sdwa v164, v162 dst_sel:WORD_1 dst_unused:UNUSED_PRESERVE src0_sel:WORD_1\n" \
    "v_rcp_f16_sdwa v165, v163 dst_sel:WORD_1 dst_unused:UNUSED_PRESERVE src0_sel:WORD_1\n" \
    "v_pk_mul_f16 v164, v160, v164\n" \
    "v_pk_mul_f16 v165, v161, v165\n" \
    "s_nop 1\n" \
    "v_mfma_f32_16x16x16_f16 %7, v[164:165], %10, " C(7) "\n" \
    "s_nop 7\n"
#define TILE_C0(i) "0"
#define TILE_CA(i) "%" #i
#define TILE_IN "v"(x4), "v"(sel), "v"(sop), \
    "v"(wh_[0]), "v"(wh_[1]), "v"(wh_[2]), "v"(wh_[3]), "v"(wh_[4]), "v"(wh_[5]), "v"(wh_[6]), "v"(wh_[7]), \
    "v"(gh_[0]), "v"(gh_[1]), "v"(gh_[2]), "v"(gh_[3]), "v"(gh_[4]), "v"(gh_[5]), "v"(gh_[6]), "v"(gh_[7])
#define TILE_CLOB "v152", "v153", "v154", "v155", "v156", "v157", "v158", "v159", "v160", "v161", "v162", "v163", "v164", "v165"
typedef __attribute__((address_space(3))) const char* lds_cp;
typedef short s16x4 __attribute__((ext_vector_type(4)));
template <typename T>
__device__ __forceinline__ T lds_ld(u32 a) { return *reinterpret_cast<const __attribute__((address_space(3))) T*>(a); }
__device__ __forceinline__ u32 lds_addr(const void* p) {
    return (u32)reinterpret_cast<uintptr_t>((const __attribute__((address_space(3))) void*)p);
}

__global__ void __launch_bounds__(KB_THREADS, 3)
k_main(const u16* __restrict__ h, const u32* __restrict__ rec_g, const u16* __restrict__ tab,
       const u16* __restrict__ ABU,
       const u16* __restrict__ whT, const uint4* __restrict__ w5g, const uint2* __restrict__ pos8,
       u16* __restrict__ out_h, u16* __restrict__ out_pos,
       unsigned long long mNWG, int N, int CH, int NWG, int unused) {
    __shared__ KbSmem S;
    asm volatile("" :: "s"(h), "s"(rec_g), "s"(tab), "s"(ABU), "s"(whT), "s"(w5g), "s"(pos8),
                 "s"(out_h), "s"(out_pos), "s"(mNWG), "s"(N), "s"(CH), "s"(NWG));
    int t = threadIdx.x, lane = t & 63;
    const int wv = __builtin_amdgcn_readfirstlane(t >> 6);
    int e16 = lane & 15, g = lane >> 4;
    const int per8 = NWG >> 3;
    int bx = (int)blockIdx.x;
    if (bx < per8 * 8) {
        int k = bx >> 3;
        if (per8 == 96) k = (k & 31) * 3 + (k >> 5);
        bx = (bx & 7) * per8 + k;
    }
    u32* tabs = reinterpret_cast<u32*>(&S.T[0][0]);
    u32* marks = reinterpret_cast<u32*>(&S.macc[0][0]);
    u32x3 tv = {0u, 0u, 0u};
    if (wv < 2) tv = *reinterpret_cast<const u32x3u*>(reinterpret_cast<const char*>(tab) + (u32)(t * NWG + bx) * (TS * 2u));
    const int N0 = node_start(bx, N, NWG, mNWG);
    const int NR = node_start(bx + 1, N, NWG, mNWG) - N0;
    u32 a0[5];
    uint4 cv;
    uint2 pz;
    auto load_stage = [&]() __attribute__((always_inline)) {
        const char* ap = reinterpret_cast<const char*>(ABU) + (size_t)N0 * (D * 2);
        const u32 lo = (u32)lane * 4u + (u32)wv * (4 * D * 2);
#pragma unroll
        for (int j = 0; j < 4; ++j) a0[j] = *reinterpret_cast<const u32*>(ap + lo + j * (D * 2));
        a0[4] = *reinterpret_cast<const u32*>(ap + (u32)lane * 4u + (u32)(16 + wv) * (D * 2));
        cv = w5g[t];
        pz = pos8[1 + N0 + (lane & 7) * 4 + ((lane >> 3) & 3)];
    };
    load_stage();
    reinterpret_cast<uint4*>(marks)[t] = make_uint4(0u, 0u, 0u, 0u);
    reinterpret_cast<uint4*>(marks)[t + 256] = make_uint4(0u, 0u, 0u, 0u);
    S.flag = 0u;

    const __amdgpu_buffer_rsrc_t rB = mk_rsrc(ABU + (size_t)(N - 1) * D, (u32)(N + 1) * (D * 2));
    const __amdgpu_buffer_rsrc_t rrec = mk_rsrc(rec_g, (u32)CH * (NCH * RW * 4u));
    uint4 hv4[2], uv4[2];
    auto load_hu = [&]() __attribute__((always_inline)) {
        const char* hp = reinterpret_cast<const char*>(h) + (size_t)N0 * (D * 2);
        const char* up = reinterpret_cast<const char*>(ABU) + ((size_t)2 * N + N0) * (D * 2);
#pragma unroll
        for (int rt = 0; rt < 2; ++rt) {
            const int lr = rt * 16 + e16;
            const u32 o = (u32)(lr < NR ? lr : 0) * (D * 2) + (u32)wv * 64u + (u32)g * 16u;
            hv4[rt] = *reinterpret_cast<const uint4*>(hp + o);
            uv4[rt] = *reinterpret_cast<const uint4*>(up + o);
        }
    };
    bf16x8 bw[2][4];
    auto load_bw = [&]() __attribute__((always_inline)) {
        int le = lane;
        asm volatile("" : "+v"(le));
        const __amdgpu_buffer_rsrc_t rw = mk_rsrc(whT, D * D * 2);
        const u32 o = (u32)le * 16u;
#pragma unroll
        for (int q = 0; q < 2; ++q)
#pragma unroll
            for (int ks = 0; ks < 4; ++ks) {
                const u32x4 v = __builtin_amdgcn_raw_buffer_load_b128(rw, (int)o + ks * 1024, wv * 8192 + q * 4096, 0);
                bw[q][ks] = *reinterpret_cast<const bf16x8*>(&v);
            }
        asm volatile("" :: "v"(o), "v"(le));
    };
    auto stage = [&]() __attribute__((always_inline)) {
        const int r = (lane * 2) & 31;
        const int ao = ((lane >> 4) * 16 + (r >> 3) * 4 + (r & 3)) * 2 + ((r >> 2) & 1);
        uint2* ap = reinterpret_cast<uint2*>(&S.At[wv][0]) + ao;
        ap[0] = make_uint2(__builtin_amdgcn_perm(a0[1], a0[0], 0x05040100u), __builtin_amdgcn_perm(a0[3], a0[2], 0x05040100u));
        ap[2] = make_uint2(__builtin_amdgcn_perm(a0[1], a0[0], 0x07060302u), __builtin_amdgcn_perm(a0[3], a0[2], 0x07060302u));
        u16* aq = reinterpret_cast<u16*>(reinterpret_cast<uint2*>(&S.At[4][0]) + ao) + wv;
        aq[0] = (u16)a0[4];
        aq[8] = (u16)(a0[4] >> 16);
        S.cst[t] = cv;
        S.posl[wv][lane & 31] = make_float4(__uint_as_float(pz.x & 0xffff0000u), __uint_as_float(pz.y << 16),
                                           __uint_as_float(pz.y & 0xffff0000u), 0.f);
    };
    u32x3 r[4];
    uint2 ps[4];
    auto ld_rec = [&](const u32 (&off)[4], auto kc) __attribute__((always_inline)) {
#pragma unroll
        for (int i = 0; i < decltype(kc)::value; ++i)
            r[i] = __builtin_amdgcn_raw_buffer_load_b96(rrec, (int)off[i], 0, 0);
    };
    auto put_rec = [&](int s0, auto kc) __attribute__((always_inline)) {
#pragma unroll
        for (int i = 0; i < decltype(kc)::value; ++i) {
            ps[i] = *reinterpret_cast<const uint2*>(reinterpret_cast<const char*>(pos8) + ((r[i][2] >> 8) << 3));
            *reinterpret_cast<u32x3*>(&S.rec[wv][s0 + i]) = r[i];
        }
    };
    auto geom = [&](int s0, auto kc, auto hc) __attribute__((always_inline)) {
        float4 pn[4];
#pragma unroll
        for (int i = 0; i < decltype(kc)::value; ++i) pn[i] = S.posl[wv][r[i][2] & 31u];
#pragma unroll
        for (int i = 0; i < decltype(kc)::value; ++i) {
            const float xs = __uint_as_float(ps[i].x & 0xffff0000u), ys = __uint_as_float(ps[i].y << 16);
            const float zs = __uint_as_float(ps[i].y & 0xffff0000u);
            const float dx = pn[i].x - xs, dy = pn[i].y - ys, dz = pn[i].z - zs;
            const float d2v = (dx * dx + dy * dy + dz * dz) * NLOG2E;
            const float d2h = __uint_as_float(__float_as_uint(d2v) & 0xffff0000u);
            S.rec[wv][s0 + i].w = cvt_pk((f32x2){d2h, d2v - d2h});
            S.geo[wv][s0 + i] = decltype(hc)::value ? make_uint2(cvt_pkh((f32x2){__uint_as_float(ps[i].x << 16), xs}), cvt_pkh((f32x2){ys, zs})) : ps[i];
        }
    };

    f32x4 acc2[8];
    float pvs;
    unsigned long long sbad = 0ull;
    auto reduce = [&]() __attribute__((always_inline)) {
        const f32x4* wxs = reinterpret_cast<const f32x4*>(&S.cst[192 + g * 2]);
        f32x4 wxc[8];
#pragma unroll
        for (int tt = 0; tt < 8; ++tt) wxc[tt] = wxs[(tt >> 1) * 8 + (tt & 1)];
        f32x4 pv = acc2[0] * wxc[0];
#pragma unroll
        for (int tt = 1; tt < 8; ++tt) pv += acc2[tt] * wxc[tt];
        pvs = (pv[0] + pv[2]) + (pv[1] + pv[3]);
        sbad |= __builtin_amdgcn_ballot_w64(__builtin_amdgcn_classf(pvs, 0x207));
    };
    auto commit = [&](int gr, auto accc) __attribute__((always_inline)) {
        constexpr bool ACC = decltype(accc)::value;
        float* pp = &S.pacc[gr][g * 16 + e16];
        *pp = ACC ? *pp + pvs : pvs;
        if ((e16 & 3) == 0) {
            f32x4* mrow = reinterpret_cast<f32x4*>(&S.macc[gr * 4 + (e16 >> 2)][g * 8]);
            f32x4 old[8];
            if (ACC) {
#pragma unroll
                for (int tt = 0; tt < 8; ++tt) old[tt] = mrow[(tt >> 1) * 8 + (tt & 1)];
                CBAR();
            }
#pragma unroll
            for (int tt = 0; tt < 8; ++tt) mrow[(tt >> 1) * 8 + (tt & 1)] = ACC ? old[tt] + acc2[tt] : acc2[tt];
        }
    };
    auto commit0 = [&](int gr) __attribute__((always_inline)) {
        S.pacc[gr][g * 16 + e16] = 0.f;
        if ((e16 & 3) == 0) {
            f32x4* mrow = reinterpret_cast<f32x4*>(&S.macc[gr * 4 + (e16 >> 2)][g * 8]);
#pragma unroll
            for (int tt = 0; tt < 8; ++tt) mrow[(tt >> 1) * 8 + (tt & 1)] = (f32x4){0.f, 0.f, 0.f, 0.f};
        }
    };
    const std::integral_constant<bool, true> c_acc;
    const std::integral_constant<bool, false> c_set;

    bf16x4 sel;
    u32 w5b, w5m, rq, rb, xb, xs_, xm, gq, g16, c8;
    uint4* twp;
    const uint4* trp;
    u32 zad;
    auto setup = [&](int lq) __attribute__((always_inline)) {
        const int dl = (lq & 15) - 4 * (lq >> 4);
        unsigned long long v = (dl >= 0 && dl < 4) ? (0x3F80ull << (16 * dl)) : 0ull;
        sel = as_bf16x4(make_uint2((u32)v, (u32)(v >> 32)));
        w5b = (g < 3) ? lds_addr(&S.cst[g * 64 + e16]) : lds_addr(&S.At[0][e16]);
        w5m = (g < 3) ? 0u : (u32)sizeof(S.At[0]);
        rb = lds_addr(&S.rec[wv][e16]);
        rq = lds_addr(&S.rec[wv][lq >> 2]);
        gq = lds_addr(&S.geo[wv][lq >> 2]);
        xb = (g < 3) ? rb + ((g == 1) ? 8u : 0u) : lds_addr(&S.cst[224]);
        xs_ = (g < 3) ? 256u : 0u;
        xm = (g < 3) ? 0u : 0x18u;
        zad = lds_addr(&S.cst[226]);
        asm volatile("" : "+v"(zad));
        g16 = (u32)(lq & 3) * 16u;
        c8 = (u32)(lq & 3) * 8u;
        twp = &S.T[wv][(lq & 3) * 16 + ((lq >> 2) ^ ((lq & 3) * 2))];
        trp = &S.T[wv][g * 16 + (e16 ^ (g * 2))];
    };
    {
        int lq = lane;
        asm volatile("" : "+v"(lq));
        setup(lq);
    }
    bf16x8 gA[4], gB[4];
    u32 pA0 = 0, pB0 = 0, pA1 = 0, pB1 = 0;
    u32 w5p, cg;
    auto group = [&](int gr) __attribute__((always_inline)) {
        w5p = w5b + w5m * (u32)gr;
        cg = c8 | (u32)gr;
    };
    auto issue = [&](auto tc, bf16x8 (&gat)[4], u32& q0, u32& qs) __attribute__((always_inline)) {
        constexpr u32 TI = decltype(tc)::value;
        qs = lds_ld<u32>(rq + (TI * 256u + 8u));
        q0 = lds_ld<u32>(rb + (TI * 256u + 8u));
        const u32 off = (qs & 0xffffff00u) | g16;
#pragma unroll
        for (int tp = 0; tp < 4; ++tp) {
            const u32x4 v = __builtin_amdgcn_raw_buffer_load_b128(rB, (int)off + tp * 64, 0, 0);
            gat[tp] = *reinterpret_cast<const bf16x8*>(&v);
        }
        asm volatile("" :: "v"(off));
    };
    auto compute = [&](auto tc, bf16x8 (&gat)[4], u32 q0, u32 qs, bool hu, int fresh, auto hc) __attribute__((always_inline)) {
        constexpr u32 TI = decltype(tc)::value;
        const bf16x4 x4 = lds_ld<bf16x4>(((q0 & xm) | xb) + TI * 256u);
        const u32 sa = ((qs & 0xffu) == cg) ? gq : zad;
        const s16x4 sop = __builtin_amdgcn_ds_read_tr16_b64_v4i16(
            reinterpret_cast<__attribute__((address_space(3))) s16x4*>(sa + TI * 128u));
        uint4 w5[4];
#pragma unroll
        for (int tp = 0; tp < 4; ++tp) {
            CBAR();
            *twp = *reinterpret_cast<const uint4*>(&gat[tp]);
            CBAR();
            gat[tp] = as_bf16x8(*trp);
        }
        CBAR();
        if (hu) load_hu();
        CBAR();
#pragma unroll
        for (int tp = 0; tp < 4; ++tp) {
            const u32x4 v = lds_ld<u32x4>(w5p + tp * 256u);
            w5[tp] = make_uint4(v[0], v[1], v[2], v[3]);
        }
        auto feat = [&](int tt) __attribute__((always_inline)) {
            const uint4 w = w5[tt >> 1];
            return __builtin_amdgcn_mfma_f32_16x16x16bf16_1k(x4, as_bf16x4((tt & 1) ? make_uint2(w.z, w.w) : make_uint2(w.x, w.y)),
                                                             (f32x4){0.f, 0.f, 0.f, 0.f}, 0, 0, 0);
        };
        auto gath = [&](int tt, f32x4 c) __attribute__((always_inline)) {
            const u32x4 gq4 = *reinterpret_cast<const u32x4*>(&gat[tt >> 1]);
            return __builtin_amdgcn_mfma_f32_16x16x16bf16_1k(as_bf16x4(make_uint2(gq4[(tt & 1) * 2], gq4[(tt & 1) * 2 + 1])), sel, c, 0, 0, 0);
        };
        if constexpr (decltype(hc)::value) {
            u32x2 wh_[8], gh_[8];
#pragma unroll
            for (int tp = 0; tp < 4; ++tp) {
                const u32x4 gq4 = *reinterpret_cast<const u32x4*>(&gat[tp]);
                wh_[2 * tp] = (u32x2){w5[tp].x, w5[tp].y}; wh_[2 * tp + 1] = (u32x2){w5[tp].z, w5[tp].w};
                gh_[2 * tp] = (u32x2){gq4[0], gq4[1]}; gh_[2 * tp + 1] = (u32x2){gq4[2], gq4[3]};
            }
            asm volatile("s_cmp_lt_i32 %27, 0\ns_cbranch_scc1 .Ltz%=\n" TILE_ASM(TILE_CA) "s_branch .Lte%=\n.Ltz%=:\n" TILE_ASM(TILE_C0) ".Lte%=:\n"
                : "+v"(acc2[0]), "+v"(acc2[1]), "+v"(acc2[2]), "+v"(acc2[3]), "+v"(acc2[4]), "+v"(acc2[5]), "+v"(acc2[6]), "+v"(acc2[7])
                : TILE_IN, "s"(fresh) : TILE_CLOB, "scc");
            return;
        }
        f32x4 acc = gath(0, feat(0));
#pragma unroll
        for (int tt = 0; tt < 8; ++tt) {
            f32x4 accN = acc;
            __builtin_amdgcn_sched_barrier(0);
            if (tt < 7) accN = feat(tt + 1);
            __builtin_amdgcn_sched_barrier(0);
            float e0 = __builtin_amdgcn_exp2f(acc[0]), e1 = __builtin_amdgcn_exp2f(acc[1]);
            float e2 = __builtin_amdgcn_exp2f(acc[2]), e3 = __builtin_amdgcn_exp2f(acc[3]);
            __builtin_amdgcn_sched_barrier(0);
            if (tt < 7) accN = gath(tt + 1, accN);
            __builtin_amdgcn_sched_barrier(0);
            e0 += 1.f; e1 += 1.f; e2 += 1.f; e3 += 1.f;
            asm("" : "+v"(e0)); asm("" : "+v"(e1)); asm("" : "+v"(e2)); asm("" : "+v"(e3));
            float y0 = acc[0] * __builtin_amdgcn_rcpf(e0), y1 = acc[1] * __builtin_amdgcn_rcpf(e1);
            float y2 = acc[2] * __builtin_amdgcn_rcpf(e2), y3 = acc[3] * __builtin_amdgcn_rcpf(e3);
            asm("" : "+v"(y0)); asm("" : "+v"(y1)); asm("" : "+v"(y2)); asm("" : "+v"(y3));
            u32 k01 = cvt_pk((f32x2){y0, y1}), k23 = cvt_pk((f32x2){y2, y3});
            bf16x4 mb = as_bf16x4(make_uint2(k01, k23));
            __builtin_amdgcn_sched_barrier(0);
            acc2[tt] = __builtin_amdgcn_mfma_f32_16x16x16bf16_1k(mb, sop, acc2[tt], 0, 0, 0);
            acc = accN;
        }
    };

    asm volatile("" :: "v"(sel), "v"(w5b), "v"(w5m), "v"(rb), "v"(rq), "v"(gq), "v"(xb), "v"(xs_), "v"(xm), "v"(g16), "v"(c8), "v"(twp), "v"(trp));
    const std::integral_constant<u32, 0> t0c;
    const std::integral_constant<u32, 1> t1c;
    const std::integral_constant<u32, 2> t2c;
    if (wv < 2) *reinterpret_cast<u32x3u*>(tabs + t * 3) = tv;
    __syncthreads();
    int lo2 = lane;
    asm volatile("" : "+v"(lo2));
    const uint2* tp2 = reinterpret_cast<const uint2*>(tabs + lo2 * 6);
    const uint2 q0 = tp2[0], q1 = tp2[1], q2 = tp2[2];
    const u16* tq = reinterpret_cast<const u16*>(tp2) + wv;
    const u32 oA = tq[0], eA = tq[1], oB = tq[TS], eB = tq[TS + 1];
    const u32 S0 = q0.x + q1.y, S1 = q0.y + q2.x, S2 = q1.x + q2.y;
    const u32 x01 = (u32)wave_scan((int)(__builtin_amdgcn_alignbit(S1, S0, 16) - S0));
    const u32 x23 = (u32)wave_scan((int)(__builtin_amdgcn_alignbit(S2, S1, 16) - S1));
    const u32 x4 = (u32)wave_scan((int)((S2 >> 16) - (S2 & 0xffffu)));
    const bool big = __builtin_amdgcn_ballot_w64((S2 >> 16) - (S0 & 0xffffu) > 64u) != 0ull;
    const u32 T01 = (u32)__builtin_amdgcn_readlane((int)x01, 63), T23 = (u32)__builtin_amdgcn_readlane((int)x23, 63);
    const u32 T4 = (u32)__builtin_amdgcn_readlane((int)x4, 63);
    const int cb1 = (int)(((T01 & 0xffffu) + 15u) >> 4), cb2 = cb1 + (int)(((T01 >> 16) + 15u) >> 4);
    const int cb3 = cb2 + (int)(((T23 & 0xffffu) + 15u) >> 4), cb4 = cb3 + (int)(((T23 >> 16) + 15u) >> 4);
    const int TT = cb4 + (int)((T4 + 15u) >> 4);
    asm volatile("" :: "v"(oA), "v"(eA), "v"(oB), "v"(eB));
    int dgr = -1;
    bool serial = false;
    bool slow = big || TT > KB_WAVES * WSL / 16;
    if (__builtin_expect(!slow, 1)) {
        const unsigned long long cbp = ((unsigned long long)(u32)(cb4 | (TT << 8) | (TT << 16)) << 32) | (u32)((cb1 << 8) | (cb2 << 16) | (cb3 << 24));
        auto mark = [&](int gr, u32 inc, u32 oa, u32 ea_, u32 ob, u32 eb_) __attribute__((always_inline)) {
            const u32 gb = (u32)((cbp >> (8 * gr)) & 0xffu) * 16u;
            const u32 pE = gb + inc, pB = pE - (eb_ - ob), pA = pB - (ea_ - oa);
            const u32 lb = (u32)lo2 * (2u * (u32)CH) + (((u32)gr + 1u) << 20) + MBIAS;
            atomicMax(&marks[64 + pA], lb + oa - pA);
            atomicMax(&marks[64 + pB], lb + (u32)CH + ob - pB);
            if (lo2 == 63) atomicMax(&marks[64 + pE], (((u32)gr + 2u) << 20) - 1u);
        };
        mark(wv, __builtin_amdgcn_ubfe((wv < 2) ? x01 : x23, (u32)(wv & 1) * 16u, 16u), oA, eA, oB, eB);
        if (wv == 3 && T4 != 0u) mark(4, x4, q1.x & 0xffffu, q1.x >> 16, q2.y & 0xffffu, q2.y >> 16);
        const int ts = (TT * wv) >> 2, te = (TT * (wv + 1)) >> 2;
        const int nst = te - ts;
        int gc, rem;
        auto seek = [&](int tn) __attribute__((always_inline)) {
            gc = (tn >= cb1 ? 1 : 0) + (tn >= cb2 ? 1 : 0) + (tn >= cb3 ? 1 : 0) + (tn >= cb4 ? 1 : 0);
            rem = (int)((cbp >> (8 * gc + 8)) & 0xffu) - tn;
        };
        auto fill = [&](auto kc) __attribute__((always_inline)) {
            constexpr int K = decltype(kc)::value;
            const int s0 = K * lo2;
            const u32* mp = marks + 64 + ts * 16;
            const u32 lbk = mp[lo2 - 64];
            u32 m[4];
#pragma unroll
            for (int i = 0; i < K; ++i) m[i] = mp[s0 + i];
#pragma unroll
            for (int i = 1; i < K; ++i) m[i] = max(m[i], m[i - 1]);
            u32 ex = wave_scan_max((u32)__builtin_amdgcn_update_dpp(0, (int)m[K - 1], 0x138, 0xf, 0xf, false));
            const u32 cr = (u32)__builtin_amdgcn_readlane((int)wave_scan_max(lbk), 63);
            ex = (s0 < nst * 16) ? max(ex, cr) : 0xffffffffu;
            const u32 po = (u32)(ts * 16 + s0 - MBIAS) * (RW * 4u);
            u32 off[4];
#pragma unroll
            for (int i = 0; i < K; ++i) off[i] = (max(m[i], ex) & 0xfffffu) * (RW * 4u) + po + i * RW * 4u;
            ld_rec(off, kc);
            stage();
            put_rec(s0, kc);
            issue(t0c, gA, pA0, pA1);
#pragma unroll
            for (int tt = 0; tt < 8; ++tt) asm volatile("" : "=v"(acc2[tt]));
            seek(ts);
            asm volatile("" : "+s"(gc), "+s"(rem));
            group(gc);
            geom(s0, kc, c_acc);
        };
        __syncthreads();
        if (nst <= 8) fill(std::integral_constant<int, 2>());
        else if (nst <= 12) fill(std::integral_constant<int, 3>());
        else fill(std::integral_constant<int, 4>());
        {
            const int mx = max(max(max(cb1, cb2 - cb1), max(cb3 - cb2, cb4 - cb3)), TT - cb4);
            serial = mx >= (TT >> 2) + 2;
        }
        auto prio = [&](int left) __attribute__((always_inline)) {
            if (left >= PR3) __builtin_amdgcn_s_setprio(3);
            else if (left >= PR2) __builtin_amdgcn_s_setprio(2);
            else if (left >= PR1) __builtin_amdgcn_s_setprio(1);
            else __builtin_amdgcn_s_setprio(0);
        };
        auto step = [&](int tn) __attribute__((always_inline)) {
            dgr = gc;
            if (--rem == 0) {
                reduce();
                commit(gc, c_set);
                dgr = -1;
                seek(tn);
                group(gc);
            }
        };
        __syncthreads();
        if (nst > 0) {
            int i = 0, hui = HUI;
            asm volatile("" : "+s"(hui));
#pragma clang loop unroll(disable)
            for (;;) {
                prio(nst - i);
                issue(t1c, gB, pB0, pB1);
                compute(t0c, gA, pA0, pA1, i == hui, dgr, c_acc);
                step(ts + i + 1);
                if (++i == nst) break;
                prio(nst - i);
                issue(t2c, gA, pA0, pA1);
                compute(t1c, gB, pB0, pB1, false, dgr, c_acc);
                step(ts + i + 1);
                if (++i == nst) break;
                rq += 512u; rb += 512u; gq += 256u; xb += 2u * xs_;
            }
        }
        if (nst < HUI + 1) load_hu();
        load_bw();
        {
            const int ng = (int)((cbp >> (8 * wv + 8)) & 0xffu) - (int)((cbp >> (8 * wv)) & 0xffu);
            if (ng == 0) commit0(wv);
            if (wv == 0 && T4 == 0u && NR > 16) commit0(4);
        }
        if (dgr >= 0) reduce();
        if (sbad != 0ull) S.flag = 1u;
    }
    __syncthreads();
    if (__builtin_expect(!slow, 1)) {
        slow = __builtin_amdgcn_readfirstlane((int)S.flag) != 0;
        for (int w = 0; w < KB_WAVES - 1; ++w) {
            if (dgr >= 0 && (wv == w || !serial)) commit(dgr, c_acc);
            __syncthreads();
            if (!serial) break;
        }
    }
    if (__builtin_expect(slow, 0)) {
        lo2 = (int)__builtin_amdgcn_mbcnt_hi(~0u, __builtin_amdgcn_mbcnt_lo(~0u, 0u));
        asm volatile("" : "+v"(lo2));
        lane = lo2; t = wv * 64 + lo2;
        e16 = lo2 & 15; g = lo2 >> 4;
        load_stage();
        stage();
        load_hu();
        for (int i = wv * 64 + lo2; i < GPW * 4 * AST; i += KB_THREADS) (&S.macc[0][0])[i] = 0.f;
        for (int i = wv * 64 + lo2; i < GPW * 64; i += KB_THREADS) (&S.pacc[0][0])[i] = 0.f;
        __syncthreads();
        u32* key = reinterpret_cast<u32*>(S.rec[wv]);
        for (int gr = 0; gr * 4 < NR; ++gr) {
            int so[2], sc[2], fo[2];
#pragma unroll
            for (int tt = 0; tt < 8; ++tt) acc2[tt] = (f32x4){0.f, 0.f, 0.f, 0.f};
#pragma unroll
            for (int ci = 0; ci < 2; ++ci) {
                const u16* tg = tab + (size_t)((lo2 * 2 + ci) * NWG + bx) * TS + gr;
                so[ci] = tg[0];
                sc[ci] = (int)tg[1] - so[ci];
            }
            const int x = wave_scan(sc[0] + sc[1]);
            const int T = __builtin_amdgcn_readlane(x, 63);
            fo[1] = x - sc[1]; fo[0] = fo[1] - sc[0];
            for (int base = 0; base < T; base += KB_WAVES * WSL) {
                const int sh = ((min(KB_WAVES * WSL, T - base) + 63) >> 6) << 4;
                const int wb = base + wv * sh;
                const int Tr = max(0, min(sh, T - wb));
                if (Tr == 0) continue;
                CBAR();
#pragma unroll
                for (int ci = 0; ci < 2; ++ci) {
                    const int qa = max(0, wb - fo[ci]), qb = min(sc[ci], wb + Tr - fo[ci]);
#pragma clang loop unroll(disable) vectorize(disable) interleave(disable)
                    for (int q = qa; q < qb; ++q) key[fo[ci] + q - wb] = (u32)((lo2 * 2 + ci) * CH + so[ci] + q) * (RW * 4u);
                }
                CBAR();
                u32 off[4];
#pragma unroll
                for (int i = 0; i < 4; ++i) off[i] = (lo2 * 4 + i < Tr) ? key[lo2 * 4 + i] : 0xfffffff0u;
                CBAR();
                const std::integral_constant<int, 4> k4;
                ld_rec(off, k4);
                put_rec(lo2 * 4, k4);
                geom(lo2 * 4, k4, c_set);
                CBAR();
                setup(lo2);
                group(gr);
                for (int i = 0; i * 16 < Tr; ++i) {
                    issue(t0c, gA, pA0, pA1);
                    compute(t0c, gA, pA0, pA1, false, 0, c_set);
                    rq += 256u; rb += 256u; gq += 128u; xb += xs_;
                }
            }
            reduce();
            for (int w = 0; w < KB_WAVES; ++w) {
                if (wv == w) commit(gr, c_acc);
                __syncthreads();
            }
        }
        load_bw();
    }

    int le = lane;
    asm volatile("" : "+v"(le));
    const int f16 = le & 15, gg = le >> 4;
    auto node = [&](auto nc) __attribute__((always_inline)) {
        constexpr int NRT = decltype(nc)::value;
        f32x4 mv[NRT][8];
#pragma unroll
        for (int rt = 0; rt < NRT; ++rt) {
            const int lr = rt * 16 + f16;
            const float* mp = &S.macc[lr < NR ? lr : 0][gg * 8];
#pragma unroll
            for (int ks = 0; ks < 4; ++ks) {
                mv[rt][ks * 2] = *reinterpret_cast<const f32x4*>(mp + ks * 32);
                mv[rt][ks * 2 + 1] = *reinterpret_cast<const f32x4*>(mp + ks * 32 + 4);
            }
        }
        f32x4 acc[NRT][2];
#pragma unroll
        for (int rt = 0; rt < NRT; ++rt)
#pragma unroll
            for (int q = 0; q < 2; ++q) {
                const u32 u0 = q ? uv4[rt].z : uv4[rt].x, u1 = q ? uv4[rt].w : uv4[rt].y;
                acc[rt][q] = (f32x4){__uint_as_float(u0 << 16), __uint_as_float(u0 & 0xffff0000u),
                                     __uint_as_float(u1 << 16), __uint_as_float(u1 & 0xffff0000u)};
            }
        __builtin_amdgcn_sched_barrier(0);
#pragma unroll
        for (int ks = 0; ks < 4; ++ks)
#pragma unroll
            for (int rt = 0; rt < NRT; ++rt) {
                const f32x4 a = mv[rt][ks * 2], b = mv[rt][ks * 2 + 1];
                const bf16x8 af = as_bf16x8(make_uint4(cvt_pk((f32x2){a[0], a[1]}), cvt_pk((f32x2){a[2], a[3]}),
                                                       cvt_pk((f32x2){b[0], b[1]}), cvt_pk((f32x2){b[2], b[3]})));
#pragma unroll
                for (int q = 0; q < 2; ++q)
                    acc[rt][q] = __builtin_amdgcn_mfma_f32_16x16x32_bf16(bw[q][ks], af, acc[rt][q], 0, 0, 0);
            }
        const __amdgpu_buffer_rsrc_t ro = mk_rsrc(out_h, (u32)N * (D * 2));
#pragma unroll
        for (int rt = 0; rt < NRT; ++rt) {
            const int lr = rt * 16 + f16;
            u32 ov[4];
#pragma unroll
            for (int q = 0; q < 2; ++q) {
                const u32 h0 = q ? hv4[rt].z : hv4[rt].x, h1 = q ? hv4[rt].w : hv4[rt].y;
                ov[q * 2] = cvt_pk((f32x2){silu_t(acc[rt][q][0], __uint_as_float(h0 << 16)), silu_t(acc[rt][q][1], __uint_as_float(h0 & 0xffff0000u))});
                ov[q * 2 + 1] = cvt_pk((f32x2){silu_t(acc[rt][q][2], __uint_as_float(h1 << 16)), silu_t(acc[rt][q][3], __uint_as_float(h1 & 0xffff0000u))});
            }
            st_b128<AUX_O>(ro, (lr < NR) ? (u32)(N0 + lr) * (D * 2) + wv * 64 + gg * 16 : 0xfffffff0u, make_uint4(ov[0], ov[1], ov[2], ov[3]));
        }
    };
    if (NR > 16) node(std::integral_constant<int, 2>());
    else node(std::integral_constant<int, 1>());
    if (wv == 3 && le < NR * 3) {
        const int n = le / 3, cpt = le - n * 3;
        const float p = reinterpret_cast<const float*>(&S.posl[3][(n & 3) * 8 + (n >> 2)])[cpt];
        const float* pa = &S.pacc[n >> 2][(n & 3) * 4];
        const float s0 = (pa[0] + pa[16]) + (pa[32] + pa[48]);
        const float s1 = (pa[1 + cpt] + pa[17 + cpt]) + (pa[33 + cpt] + pa[49 + cpt]);
        out_pos[(size_t)N0 * 3 + le] = f2bf(p + (p * s0 - s1));
    }
}

extern "C" void kernel_launch(void* const* d_in, const int* in_sizes, int n_in,
                              void* d_out, int out_size, void* d_ws, size_t ws_size,
                              hipStream_t stream) {
    const u16* h   = (const u16*)d_in[0];
    const u16* pos = (const u16*)d_in[1];
    const u16* ea  = (const u16*)d_in[2];
    const int* src = (const int*)d_in[3];
    const int* dst = (const int*)d_in[4];
    const u16* we  = (const u16*)d_in[5];
    const u16* wx  = (const u16*)d_in[6];
    const u16* wh  = (const u16*)d_in[7];
    int N = in_sizes[0] / D;
    int E = in_sizes[3];
    u16* out_h = (u16*)d_out;
    u16* out_pos = out_h + (size_t)N * D;

    int CH = ((E + NCH - 1) / NCH + 3) & ~3;
    int NWG = 768;
    while (NWG * GPW * 4 < N && NWG < NWG_MAX) NWG += 2;
    int nGemm = (N + 127) / 128;
    unsigned long long mN = (1ull << 40) / (unsigned long long)N + 1ull;
    unsigned long long mNWG = (1ull << 40) / (unsigned long long)NWG + 1ull;

    char* ws = (char*)d_ws;
    size_t off = 0;
    auto take = [&](size_t bytes) { char* p = ws + off; off += (bytes + 255) & ~(size_t)255; return p; };
    u32* rec_g = (u32*)take((size_t)NCH * CH * RW * 4 + 64);
    u16* tab = (u16*)take((size_t)NCH * NWG * TS * 2);
    u16* ABU = (u16*)take((size_t)(3 * N + 32) * D * 2);
    u16* whT = (u16*)take((size_t)D * D * 2);
    uint4* w5g = (uint4*)take((size_t)256 * 16);
    uint2* pos8 = (uint2*)take((size_t)(N + 64) * 8);

    k_part<<<NCH + nGemm + 1, KA_THREADS, 0, stream>>>(h, pos, src, dst, ea, we, wx, wh, rec_g, tab, ABU, whT, w5g, pos8,
                                                        mN, mNWG, N, E, CH, NWG, nGemm, 0);
    k_main<<<NWG, KB_THREADS, 0, stream>>>(h, rec_g, tab, ABU, whT, w5g, pos8, out_h, out_pos, mNWG, N, CH, NWG, 0);
}
```

```cpp
#include <hip/hip_runtime.h>
#include <hip/hip_bf16.h>
#include <stdint.h>
#include <type_traits>

typedef unsigned short u16;
typedef unsigned int u32;
typedef __attribute__((ext_vector_type(8))) short bf16x8;
typedef __attribute__((ext_vector_type(4))) float f32x4;
typedef __attribute__((ext_vector_type(4))) short bf16x4;
typedef __attribute__((ext_vector_type(2))) float f32x2;
typedef __attribute__((ext_vector_type(4))) unsigned int u32x4;
__device__ __forceinline__ void st_nt(void* p, uint4 v) { u32x4 x = {v.x, v.y, v.z, v.w}; __builtin_nontemporal_store(x, reinterpret_cast<u32x4*>(p)); }

typedef __attribute__((ext_vector_type(2))) unsigned int u32x2;
typedef __attribute__((ext_vector_type(3))) unsigned int u32x3;
typedef __attribute__((ext_vector_type(3), aligned(4))) unsigned int u32x3u;
typedef __attribute__((ext_vector_type(4), aligned(4))) unsigned int u32x4u;
__device__ __forceinline__ __amdgpu_buffer_rsrc_t mk_rsrc(const void* p, u32 bytes) {
    return __builtin_amdgcn_make_buffer_rsrc(const_cast<void*>(p), (short)0, (int)bytes, 0x00020000);
}
template <int AUX>
__device__ __forceinline__ void st_b128(__amdgpu_buffer_rsrc_t r, u32 off, uint4 v) {
    u32x4 x = {v.x, v.y, v.z, v.w};
    __builtin_amdgcn_raw_buffer_store_b128(x, r, (int)off, 0, AUX);
}
template <int AUX>
__device__ __forceinline__ void st_b64(__amdgpu_buffer_rsrc_t r, u32 off, uint2 v) {
    u32x2 x = {v.x, v.y};
    __builtin_amdgcn_raw_buffer_store_b64(x, r, (int)off, 0, AUX);
}
#define AUX_A 16
#define AUX_B 16
#define AUX_U 16
#define AUX_R 16
#define AUX_O 0

#define D 128
#define KA_THREADS 1024
#define KA_ITERS 4
#define KA_EW 13
#define KA_CH_MAX (KA_EW * 252)
#define NCH 128
#define NWG_MAX 768
#define GPW 5
#define TS 6
#define KB_THREADS 256
#define KB_WAVES 4
#define WSL 256
#define RW 4
#define NPASS 6
#define KA_SMEM (NWG_MAX * GPW * 16 + NWG_MAX * TS * 2 + NWG_MAX * GPW * 4 * 6 + 16)
#define AST (D + 4)
#define CBAR() asm volatile("" ::: "memory")
#define NLOG2E (-1.4426950408889634f)
#define NLN2 (-0.6931471805599453f)

__device__ __forceinline__ float bf2f(u16 v) { return __uint_as_float(((u32)v) << 16); }
__device__ __forceinline__ u16 f2bf(float f) {
    __hip_bfloat16 b = __float2bfloat16(f);
    return *reinterpret_cast<u16*>(&b);
}
__device__ __forceinline__ float silu_f(float x) {
    float e = __builtin_amdgcn_exp2f(x * NLOG2E);
    return x * __builtin_amdgcn_rcpf(1.0f + e);
}
__device__ __forceinline__ float silu_t(float t, float a) {
    return __builtin_fmaf(t * __builtin_amdgcn_rcpf(1.0f + __builtin_amdgcn_exp2f(t)), NLN2, a);
}
__device__ __forceinline__ u32 pack2(float a, float b) {
    return (u32)f2bf(a) | ((u32)f2bf(b) << 16);
}
typedef __attribute__((ext_vector_type(2))) __bf16 bf16x2;
__device__ __forceinline__ u32 cvt_pk(f32x2 v) {
    bf16x2 p = __builtin_convertvector(v, bf16x2);
    return *reinterpret_cast<u32*>(&p);
}
typedef __attribute__((ext_vector_type(2))) _Float16 f16x2;
typedef __attribute__((ext_vector_type(4))) _Float16 f16x4;
__device__ __forceinline__ u32 cvt_pkh(f32x2 v) {
    f16x2 p = __builtin_convertvector(v, f16x2);
    return *reinterpret_cast<u32*>(&p);
}
__device__ __forceinline__ bf16x8 as_bf16x8(uint4 v) { return *reinterpret_cast<bf16x8*>(&v); }
__device__ __forceinline__ bf16x4 as_bf16x4(uint2 v) { return *reinterpret_cast<bf16x4*>(&v); }
template <int CTRL>
__device__ __forceinline__ float dpp_f(float x) {
    return __int_as_float(__builtin_amdgcn_update_dpp(0, __float_as_int(x), CTRL, 0xf, 0xf, true));
}
__device__ __forceinline__ float rsum16(float x) {
    x += dpp_f<0x128>(x);
    asm("" : "+v"(x));
    x += dpp_f<0x124>(x);
    asm("" : "+v"(x));
    x += dpp_f<0x122>(x);
    asm("" : "+v"(x));
    x += dpp_f<0x121>(x);
    asm("" : "+v"(x));
    return x;
}
template <int CTRL, int RM>
__device__ __forceinline__ int dpp_add(int x) { return x + __builtin_amdgcn_update_dpp(0, x, CTRL, RM, 0xf, false); }
__device__ __forceinline__ int wave_scan(int x) {
    x = dpp_add<0x111, 0xf>(x);
    x = dpp_add<0x112, 0xf>(x);
    x = dpp_add<0x114, 0xf>(x);
    x = dpp_add<0x118, 0xf>(x);
    x = dpp_add<0x142, 0xa>(x);
    x = dpp_add<0x143, 0xc>(x);
    return x;
}
template <int CTRL, int RM>
__device__ __forceinline__ u32 dpp_max(u32 x) { return max(x, (u32)__builtin_amdgcn_update_dpp(0, (int)x, CTRL, RM, 0xf, false)); }
__device__ __forceinline__ u32 wave_scan_max(u32 x) {
    x = dpp_max<0x111, 0xf>(x);
    x = dpp_max<0x112, 0xf>(x);
    x = dpp_max<0x114, 0xf>(x);
    x = dpp_max<0x118, 0xf>(x);
    x = dpp_max<0x142, 0xa>(x);
    x = dpp_max<0x143, 0xc>(x);
    return x;
}
__device__ __forceinline__ u32 mdiv(u32 x, unsigned long long m) {
    return (u32)(((unsigned long long)x * m) >> 40);
}
__device__ __forceinline__ int node_start(int b, int N, int NBW, unsigned long long mNBW) {
    return (int)mdiv((u32)b * (u32)N + (u32)NBW - 1u, mNBW);
}

__device__ __forceinline__ u32 brow(u32 n) { return n + ((n * 33027u) >> 22); }
__global__ void __launch_bounds__(KA_THREADS)
k_part(const u16* __restrict__ h, const u16* __restrict__ pos, const int* __restrict__ src, const int* __restrict__ dst,
       const u16* __restrict__ ea, const u16* __restrict__ we, const u16* __restrict__ wx, const u16* __restrict__ wh,
       u32* __restrict__ rec_g, u16* __restrict__ tab, u16* __restrict__ ABU,
       u16* __restrict__ whT, uint4* __restrict__ w5g, uint2* __restrict__ pos8,
       unsigned long long mN, unsigned long long mNWG,
       int N, int E, int CH, int NWG, int nGemm, int unused) {
    __shared__ __align__(16) u16 smem[KA_SMEM / 2];
    __shared__ int wsum[KA_THREADS / 64];
#define KA_ARGS asm volatile("" :: "s"(h), "s"(pos), "s"(src), "s"(dst), "s"(ea), "s"(we), "s"(wx), "s"(wh), "s"(rec_g), "s"(tab), "s"(ABU), \
                 "s"(whT), "s"(w5g), "s"(pos8), "s"(mN), "s"(mNWG), "s"(N), "s"(E), "s"(CH), "s"(NWG), "s"(nGemm))
    const int t = threadIdx.x, lane = t & 63;
    const int wv = __builtin_amdgcn_readfirstlane(t >> 6);
    int bid = blockIdx.x;
    if (__builtin_expect(bid < NCH, 1)) {
        KA_ARGS;
        uint4* cnt = reinterpret_cast<uint4*>(smem);
        u16* base16 = smem + NWG_MAX * GPW * 8;
        u32* lrec = reinterpret_cast<u32*>(base16 + NWG_MAX * TS);
        const int e0 = bid * CH;
        const int ne = max(0, min(E, e0 + CH) - e0);
        if (wv >= KA_EW) {
#pragma unroll
        for (int i = 0; i < (NWG_MAX * GPW + KA_THREADS - 1) / KA_THREADS; ++i)
            if (t + i * KA_THREADS < NWG * GPW) cnt[t + i * KA_THREADS] = make_uint4(0u, 0u, 0u, 0u);
        __syncthreads();
            const u32 pb = ((u32)N * 6u + 3u) & ~3u;
            const u32 np = (pb + 3071u) / 3072u * 3u;
            const unsigned long long pa = reinterpret_cast<unsigned long long>(pos);
            const u32x4 rp = {(u32)pa, (u32)(pa >> 32) & 0xffffu, pb, 0x00020000u};
            const u32 lb = (u32)reinterpret_cast<uintptr_t>((const __attribute__((address_space(3))) void*)lrec);
            u32 p = (((u32)bid >> 3) * np >> 4) + (u32)(wv - KA_EW);
            const u32 vo = (u32)lane * 16u;
            u32 k = 0;
            auto pieces = [&](u32 ke) __attribute__((always_inline)) {
#pragma clang loop unroll(disable)
                for (; k < ke; k += 3u) {
                    p = (p >= np) ? p - np : p;
                    asm volatile("s_mov_b32 m0, %2\n\ts_nop 0\n\tbuffer_load_dwordx4 %0, %1, %3 offen lds"
                                 :: "v"(vo), "s"(rp), "s"(lb + p * 1024u), "s"(p * 1024u) : "memory", "m0");
                    p += 3u;
                }
            };
            pieces(min(np, 9u));
            __builtin_amdgcn_s_barrier();
            pieces(np);
            CBAR();
            __builtin_amdgcn_s_waitcnt(0x0F70);
            CBAR();
            __builtin_amdgcn_s_barrier();
            __builtin_amdgcn_s_barrier();
        } else {
        const int QPW = ((CH >> 2) + KA_EW - 1) / KA_EW;
        const int el = (lane < QPW) ? (wv * QPW + lane) * 4 : CH;
        const int eb = min(e0 + el, E - 4);
        const int i0 = e0 + el - eb, i1 = e0 + ne - eb;
        const u32x4 dq = *reinterpret_cast<const u32x4u*>(dst + eb);
        const u32x4 sq = *reinterpret_cast<const u32x4u*>(src + eb);
        int dv[KA_ITERS];
#pragma unroll
        for (int i = 0; i < (NWG_MAX * GPW + KA_THREADS - 1) / KA_THREADS; ++i)
            if (t + i * KA_THREADS < NWG * GPW) cnt[t + i * KA_THREADS] = make_uint4(0u, 0u, 0u, 0u);
        __syncthreads();
        int eb2 = eb;
        asm volatile("" : "+v"(eb2) : "v"(dq[0]));
        const u32x4 ea0 = *reinterpret_cast<const u32x4u*>(ea + (size_t)eb2 * 4);
        const u32x4 ea1 = *reinterpret_cast<const u32x4u*>(ea + (size_t)eb2 * 4 + 8);
        const int wq = wv >> 2, wsft = (wv & 3) * 8;
        const u32 inc = 1u << wsft;
        int bv[KA_ITERS], b6[KA_ITERS], rk[KA_ITERS];
#pragma unroll
        for (int i = 0; i < KA_ITERS; ++i) {
            const int w = (int)mdiv(dq[i] * (u32)NWG, mN);
            dv[i] = (int)dq[i] - node_start(w, N, NWG, mNWG);
            const int b = w * GPW + (dv[i] >> 2);
            bv[i] = b;
            b6[i] = b + w;
            rk[i] = 0;
            if (i >= i0 && i < i1) rk[i] = (int)atomicAdd(reinterpret_cast<u32*>(cnt) + b * 4 + wq, inc);
        }
        __syncthreads();
        u32x2 pq[KA_ITERS];
        {
            int c[GPW], tot = 0;
            u32x4 q[GPW];
#pragma unroll
            for (int j = 0; j < GPW; ++j)
                q[j] = *reinterpret_cast<const u32x4*>(&cnt[min(t, NWG - 1) * GPW + j]);
#pragma unroll
            for (int j = 0; j < GPW; ++j) {
                c[j] = (int)__builtin_amdgcn_sad_u8(q[j][0], 0u, __builtin_amdgcn_sad_u8(q[j][1], 0u,
                            __builtin_amdgcn_sad_u8(q[j][2], 0u, __builtin_amdgcn_sad_u8(q[j][3], 0u, 0u))));
                tot += c[j];
            }
            tot = (t < NWG) ? tot : 0;
            const int x = wave_scan(tot);
            if (lane == 63) wsum[wv] = x;
            __syncthreads();
#pragma unroll
        for (int i = 0; i < KA_ITERS; ++i) {
            const u32* pp = lrec + ((sq[i] * 3u) >> 1);
            pq[i] = (u32x2){pp[0], pp[1]};
        }
            int y = wsum[lane & 15];
            y = ((lane & 15) < wv) ? y : 0;
            y += __builtin_amdgcn_update_dpp(0, y, 0x128, 0xf, 0xf, true);
            y += __builtin_amdgcn_update_dpp(0, y, 0x124, 0xf, 0xf, true);
            y += __builtin_amdgcn_update_dpp(0, y, 0x122, 0xf, 0xf, true);
            y += __builtin_amdgcn_update_dpp(0, y, 0x121, 0xf, 0xf, true);
            const u32 o0 = (u32)(y + x - tot), o1 = o0 + c[0], o2 = o1 + c[1], o3 = o2 + c[2], o4 = o3 + c[3], o5 = o4 + c[4];
            const u32x3 ov = {o0 | (o1 << 16), o2 | (o3 << 16), o4 | (o5 << 16)};
            if (t < NWG) *reinterpret_cast<u32x3u*>(base16 + t * TS) = ov;
            const __amdgpu_buffer_rsrc_t rt = mk_rsrc(tab + (size_t)bid * (NWG * TS), (u32)NWG * (TS * 2u));
            __builtin_amdgcn_raw_buffer_store_b96(ov, rt, t * (TS * 2), 0, AUX_R);
        }
        __syncthreads();
        {
            const u32 m0 = (wq > 0) ? ~0u : inc - 1u;
            const u32 m1 = (wq > 1) ? ~0u : (wq == 1) ? inc - 1u : 0u;
            const u32 m2 = (wq > 2) ? ~0u : (wq == 2) ? inc - 1u : 0u;
            const u32 m3 = (wq == 3) ? inc - 1u : 0u;
            uint4 q[KA_ITERS];
            u32 bs[KA_ITERS];
#pragma unroll
            for (int i = 0; i < KA_ITERS; ++i) {
                q[i] = cnt[bv[i]]; bs[i] = base16[b6[i]];
            }
#pragma unroll
            for (int i = 0; i < KA_ITERS; ++i) {
                if (i >= i0 && i < i1) {
                    u32 p = bs[i] + (((u32)rk[i] >> wsft) & 255u);
                    p = __builtin_amdgcn_sad_u8(q[i].x & m0, 0u, p);
                    p = __builtin_amdgcn_sad_u8(q[i].y & m1, 0u, p);
                    p = __builtin_amdgcn_sad_u8(q[i].z & m2, 0u, p);
                    p = __builtin_amdgcn_sad_u8(q[i].w & m3, 0u, p);
                    const u32 sh = (sq[i] & 1u) << 4;
                    const u32 f = (brow(sq[i]) << 2) | ((u32)dv[i] & 3u);
                    const u32x4 o = {(i < 2) ? ea0[(i & 1) * 2] : ea1[(i & 1) * 2], (i < 2) ? ea0[(i & 1) * 2 + 1] : ea1[(i & 1) * 2 + 1],
                                     __builtin_amdgcn_alignbit(pq[i][1], pq[i][0], sh), __builtin_amdgcn_perm(f, pq[i][1] >> sh, 0x05040100u)};
                    *reinterpret_cast<u32x4*>(lrec + p * RW) = o;
                }
            }
        }
        }
        __syncthreads();
        {
            const int CHB = (CH * RW + 3) >> 2;
            const int nq = (ne * RW + 3) >> 2;
            const __amdgpu_buffer_rsrc_t rr = mk_rsrc(reinterpret_cast<uint4*>(rec_g) + (size_t)bid * CHB, (u32)nq * 16u);
            const uint4* lq4 = reinterpret_cast<const uint4*>(lrec);
#pragma unroll
            for (int i = 0; i < KA_ITERS * RW / 4; ++i) {
                int p = t + i * KA_THREADS;
                if (p < nq) st_b128<AUX_R>(rr, (u32)p * 16u, lq4[p]);
            }
        }
        return;
    }
    bid -= NCH + 1;
    if (__builtin_expect(bid >= 0, 1)) {
        KA_ARGS;
        const int e16 = lane & 15, g = lane >> 4;
        char* wl = reinterpret_cast<char*>(smem);
        char* hl = wl + 3 * D * 256;
        u32x4 r[8];
        u32 p01 = 0, p2 = 0;
        if (wv < 12) {
            const int kb = (wv & 3) * 4 + g;
            const u16* wsrc = ((wv < 8) ? we + (size_t)(wv >> 2) * D * D : wh) + (size_t)kb * 8 * D + e16 * 8;
#pragma unroll
            for (int i = 0; i < 8; ++i) r[i] = *reinterpret_cast<const u32x4*>(wsrc + i * D);
            CBAR();
            char* q = wl + (wv >> 2) * (D * 256) + e16 * 256 + ((kb ^ e16) << 4);
#pragma unroll
            for (int j = 0; j < 8; ++j) {
                const u32 sel = (j & 1) ? 0x07060302u : 0x05040100u;
                u32x4 o;
#pragma unroll
                for (int p = 0; p < 4; ++p) o[p] = __builtin_amdgcn_perm(r[2 * p + 1][j >> 1], r[2 * p][j >> 1], sel);
                *reinterpret_cast<u32x4*>(q + j * (16 * 256)) = o;
            }
        } else {
            const int row0 = (wv - 12) * 32 + g;
#pragma unroll
            for (int i = 0; i < 8; ++i)
                r[i] = *reinterpret_cast<const u32x4*>(h + (size_t)min(bid * 128 + row0 + i * 4, N - 1) * D + e16 * 8);
            const u16* pp = pos + (size_t)min(bid * 128 + (t & 127), N - 1) * 3;
            if (wv >= 14) { p01 = 0x3F80u | ((u32)pp[0] << 16); p2 = (u32)pp[1] | ((u32)pp[2] << 16); }
            CBAR();
#pragma unroll
            for (int i = 0; i < 8; ++i) {
                const int row = row0 + i * 4;
                *reinterpret_cast<u32x4*>(hl + row * 256 + ((e16 ^ (row & 15)) << 4)) = r[i];
            }
        }
        __syncthreads();
        if (wv >= 12) {
            if (wv == 12) {
                u32 c0, c1, cn;
                asm volatile("s_getpc_b64 s[60:61]\n.Lkb_pc:\n\ts_add_u32 %0, s60, (.Lkb_begin-.Lkb_pc)&4294967295\n\t"
                             "s_addc_u32 %1, s61, (.Lkb_begin-.Lkb_pc)>>32\n\ts_mov_b32 %2, .Lkb_end-.Lkb_begin"
                             : "=s"(c0), "=s"(c1), "=s"(cn) :: "s60", "s61", "scc");
                const u32x4 rc = {c0, c1 & 0xffffu, cn, 0x00020000u};
                u32 x0, x1;
                asm volatile("buffer_load_dword %0, %2, %3, 0 offen\n\tbuffer_load_dword %1, %2, %3, %4 offen\n\ts_waitcnt vmcnt(0)"
                             : "=&v"(x0), "=&v"(x1) : "v"((u32)(((bid >> 3) & 3) * 64 + lane) * 64u), "s"(rc), "s"(16384) : "memory");
            }
            if (wv >= 14 && bid * 128 + (t & 127) < N) pos8[1 + bid * 128 + (t & 127)] = make_uint2(p01, p2);
            return;
        }
        const int rp = wv & 3, m = wv >> 2;
        const u32 lb = (u32)(e16 * 256 + ((g ^ e16) << 4));
        f32x4 acc[2][8];
#pragma unroll
        for (int r = 0; r < 2; ++r)
#pragma unroll
            for (int j = 0; j < 8; ++j) acc[r][j] = (f32x4){0.f, 0.f, 0.f, 0.f};
#pragma unroll
        for (int ks = 0; ks < 4; ++ks) {
            const char* hp = hl + rp * (32 * 256) + (lb ^ (u32)(ks * 64));
            const bf16x8 a0 = *reinterpret_cast<const bf16x8*>(hp);
            const bf16x8 a1 = *reinterpret_cast<const bf16x8*>(hp + 16 * 256);
#pragma unroll
            for (int j = 0; j < 8; ++j) {
                const char* wp = wl + m * (D * 256) + j * (16 * 256) + (lb ^ (u32)(ks * 64));
                const bf16x8 w = *reinterpret_cast<const bf16x8*>(wp);
                acc[0][j] = __builtin_amdgcn_mfma_f32_16x16x32_bf16(a0, w, acc[0][j], 0, 0, 0);
                acc[1][j] = __builtin_amdgcn_mfma_f32_16x16x32_bf16(a1, w, acc[1][j], 0, 0, 0);
            }
        }
        const float sc = NLOG2E;
        const __amdgpu_buffer_rsrc_t ro = mk_rsrc(ABU + (size_t)((m == 0) ? 0 : 3 - m) * N * D, ((m == 1) ? brow((u32)N) : (u32)N) * (D * 2));
#pragma unroll
        for (int r = 0; r < 2; ++r)
#pragma unroll
            for (int i = 0; i < 4; ++i) {
                const int n = bid * 128 + rp * 32 + r * 16 + g * 4 + i;
                uint4 o = make_uint4(cvt_pk((f32x2){acc[r][0][i] * sc, acc[r][1][i] * sc}), cvt_pk((f32x2){acc[r][2][i] * sc, acc[r][3][i] * sc}),
                                     cvt_pk((f32x2){acc[r][4][i] * sc, acc[r][5][i] * sc}), cvt_pk((f32x2){acc[r][6][i] * sc, acc[r][7][i] * sc}));
                if (m == 1) st_b128<AUX_B>(ro, brow((u32)n) * (D * 2) + e16 * 16, o);
                else if (m == 2) st_b128<AUX_U>(ro, (u32)n * (D * 2) + e16 * 16, o);
                else st_b128<AUX_A>(ro, (u32)n * (D * 2) + e16 * 16, o);
            }
        return;
    }
    {
        KA_ARGS;
        u32 w[8];
#pragma unroll
        for (int j = 0; j < 8; ++j)
            w[j] = *reinterpret_cast<const u32*>(wh + (size_t)(D + wv * 8 + j) * D + lane * 2);
        if (t < 192) {
            const int gq = t >> 6, tp = (t >> 4) & 3, c16 = t & 15;
            u32 o[4];
#pragma unroll
            for (int u = 0; u < 2; ++u) {
                const int c = tp * 32 + (c16 >> 2) * 8 + u * 4 + (c16 & 3);
                u32 hi[5], lo[5];
#pragma unroll
                for (int j = 0; j < 5; ++j) {
                    float x = bf2f(we[(size_t)(256 + j) * D + c]) * NLOG2E;
                    u16 hh = f2bf(x);
                    hi[j] = hh; lo[j] = f2bf(x - bf2f(hh));
                }
                const u32 wd = we[(size_t)256 * D + c];
                o[u * 2] = (gq == 0) ? (hi[1] | (hi[2] << 16)) : (gq == 1) ? 0u : (lo[1] | (lo[2] << 16));
                o[u * 2 + 1] = (gq == 0) ? (hi[3] | (hi[4] << 16)) : (gq == 1) ? (wd | (wd << 16)) : (lo[3] | (lo[4] << 16));
            }
            w5g[t] = make_uint4(o[0], o[1], o[2], o[3]);
        } else if (t < 192 + D) {
            reinterpret_cast<float*>(w5g + 192)[t - 192] = bf2f(wx[t - 192]) * NLN2;
        } else if (t < 192 + D + 128) {
            const int q = t - 192 - D, n = (q & 63) >> 4;
            reinterpret_cast<u32*>(w5g + 224)[q] = ((n >> 1) == (q & 15)) ? (0x3F80u << (16 * (n & 1))) : 0u;
        }
        const int c = lane * 2;
        const int f16 = ((c >> 3) & 3) * 4 + (c & 3), q = (c >> 2) & 1;
        uint4* o = reinterpret_cast<uint4*>(whT) + (((c >> 5) * 2 + q) * 4 + (wv >> 2)) * 64 + (wv & 3) * 16 + f16;
        o[0] = make_uint4((w[0] & 0xffffu) | (w[1] << 16), (w[2] & 0xffffu) | (w[3] << 16),
                          (w[4] & 0xffffu) | (w[5] << 16), (w[6] & 0xffffu) | (w[7] << 16));
        o[1] = make_uint4((w[0] >> 16) | (w[1] & 0xffff0000u), (w[2] >> 16) | (w[3] & 0xffff0000u),
                          (w[4] >> 16) | (w[5] & 0xffff0000u), (w[6] >> 16) | (w[7] & 0xffff0000u));
    }
}

#define PR3 5
#define PR2 3
#define PR1 2
#define PR_EVERY 1
#define PRO_PRIO 1
#define HUI 2
struct KbSmem {
    uint4 rec[KB_WAVES * WSL];
    uint2 geo[KB_WAVES * WSL];
    uint4 cst[256];
    uint4 At[GPW][64];
    float macc[GPW * 4][AST];
    float pacc[GPW][64];
    float4 posl[32];
    uint4 T[KB_WAVES][64];
    uint4 wtot[KB_WAVES];
    u32 flag[16];
};

#define TILE_ASM(C) \
    "s_waitcnt lgkmcnt(7)\n" \
    "v_mfma_f32_16x16x16_bf16 v[152:155], %8, %11, 0\n" \
    "v_mfma_f32_16x16x16_bf16 v[152:155], %19, %9, v[152:155]\n" \
    "v_mfma_f32_16x16x16_bf16 v[156:159], %8, %12, 0\n" \
    "s_nop 6\n" \
    "v_cvt_pk_f16_f32 v160, v152, v153\n" \
    "v_cvt_pk_f16_f32 v161, v154, v155\n" \
    "v_exp_f16_e32 v162, v160\n" \
    "v_exp_f16_e32 v163, v161\n" \
    "v_exp_f16_sdwa v162, v160 dst_sel:WORD_1 dst_unused:UNUSED_PRESERVE src0_sel:WORD_1\n" \
    "v_exp_f16_sdwa v163, v161 dst_sel:WORD_1 dst_unused:UNUSED_PRESERVE src0_sel:WORD_1\n" \
    "v_mfma_f32_16x16x16_bf16 v[156:159], %20, %9, v[156:159]\n" \
    "v_pk_add_f16 v162, v162, 1.0 op_sel_hi:[1,0]\n" \
    "v_pk_add_f16 v163, v163, 1.0 op_sel_hi:[1,0]\n" \
    "v_rcp_f16_e32 v164, v162\n" \
    "v_rcp_f16_e32 v165, v163\n" \
    "v_rcp_f16_sdwa v164, v162 dst_sel:WORD_1 dst_unused:UNUSED_PRESERVE src0_sel:WORD_1\n" \
    "v_rcp_f16_sdwa v165, v163 dst_sel:WORD_1 dst_unused:UNUSED_PRESERVE src0_sel:WORD_1\n" \
    "v_pk_mul_f16 v164, v160, v164\n" \
    "v_pk_mul_f16 v165, v161, v165\n" \
    "v_mfma_f32_16x16x16_bf16 v[152:155], %8, %13, 0\n" \
    "v_cvt_pk_f16_f32 v160, v156, v157\n" \
    "v_cvt_pk_f16_f32 v161, v158, v159\n" \
    "s_waitcnt lgkmcnt(4)\n" \
    "v_mfma_f32_16x16x16_f16 %0, v[164:165], %10, " C(0) "\n" \
    "v_exp_f16_e32 v162, v160\n" \
    "v_exp_f16_e32 v163, v161\n" \
    "v_exp_f16_sdwa v162, v160 dst_sel:WORD_1 dst_unused:UNUSED_PRESERVE src0_sel:WORD_1\n" \
    "v_exp_f16_sdwa v163, v161 dst_sel:WORD_1 dst_unused:UNUSED_PRESERVE src0_sel:WORD_1\n" \
    "v_mfma_f32_16x16x16_bf16 v[152:155], %21, %9, v[152:155]\n" \
    "v_pk_add_f16 v162, v162, 1.0 op_sel_hi:[1,0]\n" \
    "v_pk_add_f16 v163, v163, 1.0 op_sel_hi:[1,0]\n" \
    "v_rcp_f16_e32 v164, v162\n" \
    "v_rcp_f16_e32 v165, v163\n" \
    "v_rcp_f16_sdwa v164, v162 dst_sel:WORD_1 dst_unused:UNUSED_PRESERVE src0_sel:WORD_1\n" \
    "v_rcp_f16_sdwa v165, v163 dst_sel:WORD_1 dst_unused:UNUSED_PRESERVE src0_sel:WORD_1\n" \
    "v_pk_mul_f16 v164, v160, v164\n" \
    "v_pk_mul_f16 v165, v161, v165\n" \
    "v_mfma_f32_16x16x16_bf16 v[156:159], %8, %14, 0\n" \
    "v_cvt_pk_f16_f32 v160, v152, v153\n" \
    "v_cvt_pk_f16_f32 v161, v154, v155\n" \
    "v_mfma_f32_16x16x16_f16 %1, v[164:165], %10, " C(1) "\n" \
    "v_exp_f16_e32 v162, v160\n" \
    "v_exp_f16_e32 v163, v161\n" \
    "v_exp_f16_sdwa v162, v160 dst_sel:WORD_1 dst_unused:UNUSED_PRESERVE src0_sel:WORD_1\n" \
    "v_exp_f16_sdwa v163, v161 dst_sel:WORD_1 dst_unused:UNUSED_PRESERVE src0_sel:WORD_1\n" \
    "v_mfma_f32_16x16x16_bf16 v[156:159], %22, %9, v[156:159]\n" \
    "v_pk_add_f16 v162, v162, 1.0 op_sel_hi:[1,0]\n" \
    "v_pk_add_f16 v163, v163, 1.0 op_sel_hi:[1,0]\n" \
    "v_rcp_f16_e32 v164, v162\n" \
    "v_rcp_f16_e32 v165, v163\n" \
    "v_rcp_f16_sdwa v164, v162 dst_sel:WORD_1 dst_unused:UNUSED_PRESERVE src0_sel:WORD_1\n" \
    "v_rcp_f16_sdwa v165, v163 dst_sel:WORD_1 dst_unused:UNUSED_PRESERVE src0_sel:WORD_1\n" \
    "v_pk_mul_f16 v164, v160, v164\n" \
    "v_pk_mul_f16 v165, v161, v165\n" \
    "v_mfma_f32_16x16x16_bf16 v[152:155], %8, %15, 0\n" \
    "v_cvt_pk_f16_f32 v160, v156, v157\n" \
    "v_cvt_pk_f16_f32 v161, v158, v159\n" \
    "s_waitcnt lgkmcnt(0)\n" \
    "v_mfma_f32_16x16x16_f16 %2, v[164:165], %10, " C(2) "\n" \
    "v_exp_f16_e32 v162, v160\n" \
    "v_exp_f16_e32 v163, v161\n" \
    "v_exp_f16_sdwa v162, v160 dst_sel:WORD_1 dst_unused:UNUSED_PRESERVE src0_sel:WORD_1\n" \
    "v_exp_f16_sdwa v163, v161 dst_sel:WORD_1 dst_unused:UNUSED_PRESERVE src0_sel:WORD_1\n" \
    "v_mfma_f32_16x16x16_bf16 v[152:155], %23, %9, v[152:155]\n" \
    "v_pk_add_f16 v162, v162, 1.0 op_sel_hi:[1,0]\n" \
    "v_pk_add_f16 v163, v163, 1.0 op_sel_hi:[1,0]\n" \
    "v_rcp_f16_e32 v164, v162\n" \
    "v_rcp_f16_e32 v165, v163\n" \
    "v_rcp_f16_sdwa v164, v162 dst_sel:WORD_1 dst_unused:UNUSED_PRESERVE src0_sel:WORD_1\n" \
    "v_rcp_f16_sdwa v165, v163 dst_sel:WORD_1 dst_unused:UNUSED_PRESERVE src0_sel:WORD_1\n" \
    "v_pk_mul_f16 v164, v160, v164\n" \
    "v_pk_mul_f16 v165, v161, v165\n" \
    "v_mfma_f32_16x16x16_bf16 v[156:159], %8, %16, 0\n" \
    "v_cvt_pk_f16_f32 v160, v152, v153\n" \
    "v_cvt_pk_f16_f32 v161, v154, v155\n" \
    "v_mfma_f32_16x16x16_f16 %3, v[164:165], %10, " C(3) "\n" \
    "v_exp_f16_e32 v162, v160\n" \
    "v_exp_f16_e32 v163, v161\n" \
    "v_exp_f16_sdwa v162, v160 dst_sel:WORD_1 dst_unused:UNUSED_PRESERVE src0_sel:WORD_1\n" \
    "v_exp_f16_sdwa v163, v161 dst_sel:WORD_1 dst_unused:UNUSED_PRESERVE src0_sel:WORD_1\n" \
    "v_mfma_f32_16x16x16_bf16 v[156:159], %24, %9, v[156:159]\n" \
    "v_pk_add_f16 v162, v162, 1.0 op_sel_hi:[1,0]\n" \
    "v_pk_add_f16 v163, v163, 1.0 op_sel_hi:[1,0]\n" \
    "v_rcp_f16_e32 v164, v162\n" \
    "v_rcp_f16_e32 v165, v163\n" \
    "v_rcp_f16_sdwa v164, v162 dst_sel:WORD_1 dst_unused:UNUSED_PRESERVE src0_sel:WORD_1\n" \
    "v_rcp_f16_sdwa v165, v163 dst_sel:WORD_1 dst_unused:UNUSED_PRESERVE src0_sel:WORD_1\n" \
    "v_pk_mul_f16 v164, v160, v164\n" \
    "v_pk_mul_f16 v165, v161, v165\n" \
    "v_mfma_f32_16x16x16_bf16 v[152:155], %8, %17, 0\n" \
    "v_cvt_pk_f16_f32 v160, v156, v157\n" \
    "v_cvt_pk_f16_f32 v161, v158, v159\n" \
    "v_mfma_f32_16x16x16_f16 %4, v[164:165], %10, " C(4) "\n" \
    "v_exp_f16_e32 v162, v160\n" \
    "v_exp_f16_e32 v163, v161\n" \
    "v_exp_f16_sdwa v162, v160 dst_sel:WORD_1 dst_unused:UNUSED_PRESERVE src0_sel:WORD_1\n" \
    "v_exp_f16_sdwa v163, v161 dst_sel:WORD_1 dst_unused:UNUSED_PRESERVE src0_sel:WORD_1\n" \
    "v_mfma_f32_16x16x16_bf16 v[152:155], %25, %9, v[152:155]\n" \
    "v_pk_add_f16 v162, v162, 1.0 op_sel_hi:[1,0]\n" \
    "v_pk_add_f16 v163, v163, 1.0 op_sel_hi:[1,0]\n" \
    "v_rcp_f16_e32 v164, v162\n" \
    "v_rcp_f16_e32 v165, v163\n" \
    "v_rcp_f16_sdwa v164, v162 dst_sel:WORD_1 dst_unused:UNUSED_PRESERVE src0_sel:WORD_1\n" \
    "v_rcp_f16_sdwa v165, v163 dst_sel:WORD_1 dst_unused:UNUSED_PRESERVE src0_sel:WORD_1\n" \
    "v_pk_mul_f16 v164, v160, v164\n" \
    "v_pk_mul_f16 v165, v161, v165\n" \
    "v_mfma_f32_16x16x16_bf16 v[156:159], %8, %18, 0\n" \
    "v_cvt_pk_f16_f32 v160, v152, v153\n" \
    "v_cvt_pk_f16_f32 v161, v154, v155\n" \
    "v_mfma_f32_16x16x16_f16 %5, v[164:165], %10, " C(5) "\n" \
    "v_exp_f16_e32 v162, v160\n" \
    "v_exp_f16_e32 v163, v161\n" \
    "v_exp_f16_sdwa v162, v160 dst_sel:WORD_1 dst_unused:UNUSED_PRESERVE src0_sel:WORD_1\n" \
    "v_exp_f16_sdwa v163, v161 dst_sel:WORD_1 dst_unused:UNUSED_PRESERVE src0_sel:WORD_1\n" \
    "v_mfma_f32_16x16x16_bf16 v[156:159], %26, %9, v[156:159]\n" \
    "v_pk_add_f16 v162, v162, 1.0 op_sel_hi:[1,0]\n" \
    "v_pk_add_f16 v163, v163, 1.0 op_sel_hi:[1,0]\n" \
    "v_rcp_f16_e32 v164, v162\n" \
    "v_rcp_f16_e32 v165, v163\n" \
    "v_rcp_f16_sdwa v164, v162 dst_sel:WORD_1 dst_unused:UNUSED_PRESERVE src0_sel:WORD_1\n" \
    "v_rcp_f16_sdwa v165, v163 dst_sel:WORD_1 dst_unused:UNUSED_PRESERVE src0_sel:WORD_1\n" \
    "v_pk_mul_f16 v164, v160, v164\n" \
    "v_pk_mul_f16 v165, v161, v165\n" \
    "v_cvt_pk_f16_f32 v160, v156, v157\n" \
    "v_cvt_pk_f16_f32 v161, v158, v159\n" \
    "v_mfma_f32_16x16x16_f16 %6, v[164:165], %10, " C(6) "\n" \
    "v_exp_f16_e32 v162, v160\n" \
    "v_exp_f16_e32 v163, v161\n" \
    "v_exp_f16_sdwa v162, v160 dst_sel:WORD_1 dst_unused:UNUSED_PRESERVE src0_sel:WORD_1\n" \
    "v_exp_f16_sdwa v163, v161 dst_sel:WORD_1 dst_unused:UNUSED_PRESERVE src0_sel:WORD_1\n" \
    "v_pk_add_f16 v162, v162, 1.0 op_sel_hi:[1,0]\n" \
    "v_pk_add_f16 v163, v163, 1.0 op_sel_hi:[1,0]\n" \
    "v_rcp_f16_e32 v164, v162\n" \
    "v_rcp_f16_e32 v165, v163\n" \
    "v_rcp_f16_sdwa v164, v162 dst_sel:WORD_1 dst_unused:UNUSED_PRESERVE src0_sel:WORD_1\n" \
    "v_rcp_f16_sdwa v165, v163 dst_sel:WORD_1 dst_unused:UNUSED_PRESERVE src0_sel:WORD_1\n" \
    "v_pk_mul_f16 v164, v160, v164\n" \
    "v_pk_mul_f16 v165, v161, v165\n" \
    "s_nop 1\n" \
    "v_mfma_f32_16x16x16_f16 %7, v[164:165], %10, " C(7) "\n"
#define TILE_C0(i) "0"
#define TILE_CA(i) "%" #i
#define TILE_IN "v"(x4), "v"(sel), "v"(sop), \
    "v"(wh_[0]), "v"(wh_[1]), "v"(wh_[2]), "v"(wh_[3]), "v"(wh_[4]), "v"(wh_[5]), "v"(wh_[6]), "v"(wh_[7]), \
    "v"(gh_[0]), "v"(gh_[1]), "v"(gh_[2]), "v"(gh_[3]), "v"(gh_[4]), "v"(gh_[5]), "v"(gh_[6]), "v"(gh_[7])
#define TILE_CLOB "v152", "v153", "v154", "v155", "v156", "v157", "v158", "v159", "v160", "v161", "v162", "v163", "v164", "v165"
typedef __attribute__((address_space(3))) const char* lds_cp;
typedef short s16x4 __attribute__((ext_vector_type(4)));
template <typename T>
__device__ __forceinline__ T lds_ld(u32 a) { return *reinterpret_cast<const __attribute__((address_space(3))) T*>(a); }
__device__ __forceinline__ u32 lds_addr(const void* p) {
    return (u32)reinterpret_cast<uintptr_t>((const __attribute__((address_space(3))) void*)p);
}

__global__ void __launch_bounds__(KB_THREADS, 3)
k_main(const u16* __restrict__ h, const u32* __restrict__ rec_g, u16* __restrict__ out_h,
       unsigned long long mNWG, int N, int CH, int NWG, u32 o_tab, u32 o_abu, u32 o_wht) {
    const u16* __restrict__ tab = reinterpret_cast<const u16*>(reinterpret_cast<const char*>(rec_g) + o_tab);
    const u16* __restrict__ ABU = reinterpret_cast<const u16*>(reinterpret_cast<const char*>(rec_g) + o_abu);
    const u16* __restrict__ whT = reinterpret_cast<const u16*>(reinterpret_cast<const char*>(rec_g) + o_wht);
    const uint4* __restrict__ w5g = reinterpret_cast<const uint4*>(whT + D * D);
    const uint2* __restrict__ pos8 = reinterpret_cast<const uint2*>(w5g + 256);
    u16* __restrict__ out_pos = out_h + (size_t)N * D;
    __shared__ KbSmem S;
    asm volatile(".Lkb_begin:" :: "s"(h), "s"(rec_g), "s"(out_h), "s"(mNWG), "s"(N), "s"(CH), "s"(NWG), "s"(o_tab), "s"(o_abu), "s"(o_wht));
    __builtin_amdgcn_s_setprio(3);
    int t = threadIdx.x, lane = t & 63;
    const int wv = __builtin_amdgcn_readfirstlane(t >> 6);
    int e16 = lane & 15, g = lane >> 4;
    const int per8 = NWG >> 3;
    int bx = (int)blockIdx.x;
    if (bx < per8 * 8) {
        int k = bx >> 3;
        if (per8 == 96) k = (k & 31) * 3 + (k >> 5);
        bx = (bx & 7) * per8 + k;
    }
    const u32x3 tv = *reinterpret_cast<const u32x3u*>(reinterpret_cast<const char*>(tab) + (u32)((t >> 1) * NWG + bx) * (TS * 2u));
    const int N0 = node_start(bx, N, NWG, mNWG);
    const int NR = node_start(bx + 1, N, NWG, mNWG) - N0;
    u32 a0[5];
    uint4 cv;
    uint2 pz;
    auto load_stage = [&]() __attribute__((always_inline)) {
        const char* ap = reinterpret_cast<const char*>(ABU) + (size_t)N0 * (D * 2);
        const u32 lo = (u32)lane * 4u + (u32)wv * (4 * D * 2);
#pragma unroll
        for (int j = 0; j < 4; ++j) a0[j] = *reinterpret_cast<const u32*>(ap + lo + j * (D * 2));
        a0[4] = *reinterpret_cast<const u32*>(ap + (u32)lane * 4u + (u32)(16 + wv) * (D * 2));
        cv = w5g[t];
        pz = pos8[1 + N0 + (lane & 7) * 4 + ((lane >> 3) & 3)];
    };
    load_stage();
    S.flag[t & 15] = 0u;
    {
        uint4* z = reinterpret_cast<uint4*>(&S.macc[0][0]);
        constexpr int NZ = (GPW * 4 * AST + GPW * 64) / 4;
        static_assert(NZ > 2 * KB_THREADS && NZ <= 3 * KB_THREADS, "");
        z[t] = make_uint4(0u, 0u, 0u, 0u);
        z[t + KB_THREADS] = make_uint4(0u, 0u, 0u, 0u);
        z[min(t + 2 * KB_THREADS, NZ - 1)] = make_uint4(0u, 0u, 0u, 0u);
    }

    const __amdgpu_buffer_rsrc_t rB = mk_rsrc(ABU + (size_t)2 * N * D, brow((u32)N) * (D * 2));
    const __amdgpu_buffer_rsrc_t rrec = mk_rsrc(rec_g, (u32)CH * (NCH * RW * 4u));
    uint4 hv4[2], uv4[2];
    auto load_hu = [&]() __attribute__((always_inline)) {
        const char* hp = reinterpret_cast<const char*>(h) + (size_t)N0 * (D * 2);
        const char* up = reinterpret_cast<const char*>(ABU) + ((size_t)N + N0) * (D * 2);
#pragma unroll
        for (int rt = 0; rt < 2; ++rt) {
            const int lr = rt * 16 + e16;
            const u32 o = (u32)(lr < NR ? lr : 0) * (D * 2) + (u32)wv * 64u + (u32)g * 16u;
            hv4[rt] = *reinterpret_cast<const uint4*>(hp + o);
            uv4[rt] = *reinterpret_cast<const uint4*>(up + o);
        }
    };
    bf16x8 bw[2][4];
    auto load_bw = [&]() __attribute__((always_inline)) {
        int le = lane;
        asm volatile("" : "+v"(le));
        const __amdgpu_buffer_rsrc_t rw = mk_rsrc(whT, D * D * 2);
        const u32 o = (u32)le * 16u;
#pragma unroll
        for (int q = 0; q < 2; ++q)
#pragma unroll
            for (int ks = 0; ks < 4; ++ks) {
                const u32x4 v = __builtin_amdgcn_raw_buffer_load_b128(rw, (int)o + ks * 1024, wv * 8192 + q * 4096, 0);
                bw[q][ks] = *reinterpret_cast<const bf16x8*>(&v);
            }
        asm volatile("" :: "v"(o), "v"(le));
    };
    auto stage = [&]() __attribute__((always_inline)) {
        const int r = (lane * 2) & 31;
        const int ao = ((lane >> 4) * 16 + (r >> 3) * 4 + (r & 3)) * 2 + ((r >> 2) & 1);
        uint2* ap = reinterpret_cast<uint2*>(&S.At[wv][0]) + ao;
        ap[0] = make_uint2(__builtin_amdgcn_perm(a0[1], a0[0], 0x05040100u), __builtin_amdgcn_perm(a0[3], a0[2], 0x05040100u));
        ap[2] = make_uint2(__builtin_amdgcn_perm(a0[1], a0[0], 0x07060302u), __builtin_amdgcn_perm(a0[3], a0[2], 0x07060302u));
        u16* aq = reinterpret_cast<u16*>(reinterpret_cast<uint2*>(&S.At[4][0]) + ao) + wv;
        aq[0] = (u16)a0[4];
        aq[8] = (u16)(a0[4] >> 16);
        S.cst[t] = cv;
        S.posl[lane & 31] = make_float4(__uint_as_float(pz.x & 0xffff0000u), __uint_as_float(pz.y << 16),
                                           __uint_as_float(pz.y & 0xffff0000u), 0.f);
    };
    auto put_rec = [&](u32 slot, u32x4 r, u32 g8) __attribute__((always_inline)) {
        S.geo[slot].x = r[2];
        r[2] = (r[3] >> 16) * 64u + g8;
        *reinterpret_cast<u32x4*>(&S.rec[slot]) = r;
    };
    auto geom = [&](int s0, int ns, auto kc, auto hc) __attribute__((always_inline)) {
        constexpr int K = decltype(kc)::value;
        u32x2 zw[4];
        u32 xy[4];
        float4 pn[4];
#pragma unroll
        for (int i = 0; i < K; ++i) {
            zw[i] = *reinterpret_cast<const u32x2*>(&S.rec[s0 + i * 64].z);
            xy[i] = S.geo[s0 + i * 64].x;
        }
#pragma unroll
        for (int i = 0; i < K; ++i) pn[i] = *reinterpret_cast<const float4*>(reinterpret_cast<const char*>(S.posl) + (zw[i][0] & 0xf8u) * 2u);
#pragma unroll
        for (int i = 0; i < K; ++i) {
            const float xs = __uint_as_float(xy[i] << 16), ys = __uint_as_float(xy[i] & 0xffff0000u);
            const float zs = __uint_as_float(zw[i][1] << 16);
            const float dx = pn[i].x - xs, dy = pn[i].y - ys, dz = pn[i].z - zs;
            const float d2v = (dx * dx + dy * dy + dz * dz) * NLOG2E;
            const float d2h = __uint_as_float(__float_as_uint(d2v) & 0xffff0000u);
            if (i < K - 1 || i * 64 < ns) {
                S.rec[s0 + i * 64].w = cvt_pk((f32x2){d2h, d2v - d2h});
                S.geo[s0 + i * 64] = decltype(hc)::value ? make_uint2(cvt_pkh((f32x2){1.0f, xs}), cvt_pkh((f32x2){ys, zs}))
                                                         : make_uint2(0x3F80u | (xy[i] << 16), __builtin_amdgcn_alignbit(zw[i][1], xy[i], 16));
            }
        }
    };

    f32x4 acc2[8];
    float pvs;
    unsigned long long sbad = 0ull;
    auto reduce = [&]() __attribute__((always_inline)) {
        asm volatile("s_nop 7" : "+v"(acc2[6]), "+v"(acc2[7]));
        const f32x4* wxs = reinterpret_cast<const f32x4*>(&S.cst[192 + g * 2]);
        f32x4 wxc[8];
#pragma unroll
        for (int tt = 0; tt < 8; ++tt) wxc[tt] = wxs[(tt >> 1) * 8 + (tt & 1)];
        f32x4 pv = acc2[0] * wxc[0];
#pragma unroll
        for (int tt = 1; tt < 8; ++tt) pv += acc2[tt] * wxc[tt];
        pvs = (pv[0] + pv[2]) + (pv[1] + pv[3]);
        sbad |= __builtin_amdgcn_ballot_w64(__builtin_amdgcn_classf(pvs, 0x207));
    };
    auto commit = [&](int gr, auto accc) __attribute__((always_inline)) {
        constexpr bool ACC = decltype(accc)::value;
        float* pp = &S.pacc[gr][g * 16 + e16];
        *pp = ACC ? *pp + pvs : pvs;
        if ((e16 & 3) == 0) {
            f32x4* mrow = reinterpret_cast<f32x4*>(&S.macc[gr * 4 + (e16 >> 2)][g * 8]);
            f32x4 old[8];
            if (ACC) {
#pragma unroll
                for (int tt = 0; tt < 8; ++tt) old[tt] = mrow[(tt >> 1) * 8 + (tt & 1)];
                CBAR();
            }
#pragma unroll
            for (int tt = 0; tt < 8; ++tt) mrow[(tt >> 1) * 8 + (tt & 1)] = ACC ? old[tt] + acc2[tt] : acc2[tt];
        }
    };
    const std::integral_constant<bool, true> c_acc;
    const std::integral_constant<bool, false> c_set;
    auto ticket = [&](int gr) __attribute__((always_inline)) {
        u32 tk;
        asm volatile("s_mov_b64 exec, 1\n\tds_add_rtn_u32 %0, %1, %2\n\ts_mov_b64 exec, -1"
                     : "=&v"(tk) : "v"(lds_addr(&S.flag[8 + gr])), "v"(1u) : "memory");
        return tk;
    };
    auto first = [&](u32 tk) __attribute__((always_inline)) {
        asm volatile("s_waitcnt lgkmcnt(0)" : "+v"(tk));
        return __builtin_amdgcn_readfirstlane((int)tk) == 0;
    };
    auto commit_split = [&](int gr, bool fst) __attribute__((always_inline)) {
        if (fst) {
            commit(gr, c_set);
            CBAR();
            S.flag[2 + gr] = 1u;
        } else {
            while (__builtin_amdgcn_readfirstlane((int)lds_ld<volatile u32>(lds_addr(&S.flag[2 + gr]))) == 0) __builtin_amdgcn_s_sleep(1);
            CBAR();
            commit(gr, c_acc);
        }
    };

    bf16x4 sel;
    u32 w5b, w5m, rq, rb, xb, xs_, xm, gq, g16, c8;
    uint4* twp;
    const uint4* trp;
    u32 zad;
    auto setup = [&](int lq) __attribute__((always_inline)) {
        const int dl = (lq & 15) - 4 * (lq >> 4);
        unsigned long long v = (dl >= 0 && dl < 4) ? (0x3F80ull << (16 * dl)) : 0ull;
        sel = as_bf16x4(make_uint2((u32)v, (u32)(v >> 32)));
        w5b = (g < 3) ? lds_addr(&S.cst[g * 64 + e16]) : lds_addr(&S.At[0][e16]);
        w5m = (g < 3) ? 0u : (u32)sizeof(S.At[0]);
        rb = lds_addr(&S.rec[e16]);
        rq = lds_addr(&S.rec[lq >> 2]);
        gq = lds_addr(&S.geo[lq >> 2]);
        xb = (g < 3) ? rb + ((g == 1) ? 8u : 0u) : lds_addr(&S.cst[224]);
        xs_ = (g < 3) ? 256u : 0u;
        xm = (g < 3) ? 0u : 0xc0u;
        zad = lds_addr(&S.cst[225]);
        asm volatile("" : "+v"(zad));
        g16 = (u32)(lq & 3) * 16u;
        c8 = (u32)(lq & 3) * 64u;
        twp = &S.T[wv][(lq & 3) * 16 + ((lq >> 2) ^ ((lq & 3) * 2))];
        trp = &S.T[wv][g * 16 + (e16 ^ (g * 2))];
    };
    {
        int lq = lane;
        asm volatile("" : "+v"(lq));
        setup(lq);
    }
    bf16x8 gA[4];
    u32 pA0 = 0, pA1 = 0;
    int lcap = 0;
    auto prio = [&](int left) __attribute__((always_inline)) {
        if (left >= PR3 && left <= lcap) __builtin_amdgcn_s_setprio(3);
        else if (left >= PR2) __builtin_amdgcn_s_setprio(2);
        else if (left >= PR1) __builtin_amdgcn_s_setprio(1);
        else __builtin_amdgcn_s_setprio(0);
    };
    u32 w5p, cg;
    u32x4 w5[4];
    auto group = [&](int gr) __attribute__((always_inline)) {
        w5p = w5b + w5m * (u32)gr;
#pragma unroll
        for (int tp = 0; tp < 4; ++tp) {
            w5[tp] = lds_ld<u32x4>(w5p + tp * 256u);
        }
        cg = c8 | ((u32)gr << 3);
    };
    auto origin = [&](int tile) __attribute__((always_inline)) {
        rq += (u32)tile * 256u; rb += (u32)tile * 256u; gq += (u32)tile * 128u; xb += (u32)tile * xs_;
    };
    auto issue_a = [&](auto tc, u32& q0, u32& qs) __attribute__((always_inline)) {
        constexpr u32 TI = decltype(tc)::value;
        qs = lds_ld<u32>(rq + (TI * 256u + 8u));
        q0 = lds_ld<u32>(rb + (TI * 256u + 8u));
        CBAR();
    };
    auto issue_b = [&](bf16x8 (&gat)[4], u32 qs) __attribute__((always_inline)) {
        const u32 off = (qs & 0xffffff00u) | g16;
#pragma unroll
        for (int tp = 0; tp < 4; ++tp) {
            const u32x4 v = __builtin_amdgcn_raw_buffer_load_b128(rB, (int)off + tp * 64, 0, 0);
            gat[tp] = *reinterpret_cast<const bf16x8*>(&v);
        }
        asm volatile("" :: "v"(off));
    };
    auto issue = [&](auto tc, bf16x8 (&gat)[4], u32& q0, u32& qs) __attribute__((always_inline)) {
        issue_a(tc, q0, qs);
        issue_b(gat, qs);
    };
    auto fast = [&](auto tc, u32& q0, u32& qs, bool hu, int fresh, int left) __attribute__((always_inline)) {
        constexpr u32 TI = decltype(tc)::value;
        (void)&acc2; (void)&sel; (void)&rq; (void)&rb;
        const u32 xa = (q0 & xm) | xb;
        const u32 sa = ((qs & 0xffu) == cg) ? gq : zad;
        u32x4 tr[4];
        u32x2 x4, sop;
        asm volatile("ds_read_b32 %6, %16 offset:%c20\n\tds_read_b32 %7, %17 offset:%c20\n\tds_read_b64 %4, %14 offset:%c18\n\t"
                     "ds_write_b128 %12, %8\n\tds_read_b128 %0, %13\n\tds_read_b64_tr_b16 %5, %15 offset:%c19\n\t"
                     "ds_write_b128 %12, %9\n\tds_read_b128 %1, %13\n\tds_write_b128 %12, %10\n\tds_read_b128 %2, %13\n\t"
                     "ds_write_b128 %12, %11\n\tds_read_b128 %3, %13\n\ts_waitcnt lgkmcnt(10)"
            : "=&v"(tr[0]), "=&v"(tr[1]), "=&v"(tr[2]), "=&v"(tr[3]), "=&v"(x4), "=&v"(sop), "=&v"(qs), "=&v"(q0)
            : "v"(gA[0]), "v"(gA[1]), "v"(gA[2]), "v"(gA[3]), "v"(lds_addr(twp)), "v"(lds_addr(trp)), "v"(xa), "v"(sa), "v"(rq), "v"(rb),
              "n"(TI * 256u), "n"(TI * 128u), "n"(TI * 256u + 264u), "v"(w5[0]), "v"(w5[1]), "v"(w5[2]), "v"(w5[3]) : "memory");
        if (hu) load_hu();
        CBAR();
        issue_b(gA, qs);
        prio(left);
        u32x2 wh_[8], gh_[8];
#pragma unroll
        for (int tp = 0; tp < 4; ++tp) {
            wh_[2 * tp] = (u32x2){w5[tp][0], w5[tp][1]}; wh_[2 * tp + 1] = (u32x2){w5[tp][2], w5[tp][3]};
            gh_[2 * tp] = (u32x2){tr[tp][0], tr[tp][1]}; gh_[2 * tp + 1] = (u32x2){tr[tp][2], tr[tp][3]};
        }
        asm volatile("s_cmp_lt_i32 %27, 0\ns_cbranch_scc1 .Ltz%=\n" TILE_ASM(TILE_CA) "s_branch .Lte%=\n.Ltz%=:\n" TILE_ASM(TILE_C0) ".Lte%=:\n"
            : "+v"(acc2[0]), "+v"(acc2[1]), "+v"(acc2[2]), "+v"(acc2[3]), "+v"(acc2[4]), "+v"(acc2[5]), "+v"(acc2[6]), "+v"(acc2[7])
            : TILE_IN, "s"(fresh) : TILE_CLOB, "scc", "memory");
    };
    auto compute = [&](auto tc, bf16x8 (&gat)[4], u32 q0, u32 qs) __attribute__((always_inline)) {
        constexpr u32 TI = decltype(tc)::value;
        const bf16x4 x4 = lds_ld<bf16x4>(((q0 & xm) | xb) + TI * 256u);
        const u32 sa = ((qs & 0xffu) == cg) ? gq : zad;
        const s16x4 sop = __builtin_amdgcn_ds_read_tr16_b64_v4i16(
            reinterpret_cast<__attribute__((address_space(3))) s16x4*>(sa + TI * 128u));
#pragma unroll
        for (int tp = 0; tp < 4; ++tp) {
            CBAR();
            *twp = *reinterpret_cast<const uint4*>(&gat[tp]);
            CBAR();
            gat[tp] = as_bf16x8(*trp);
        }
        CBAR();
        auto feat = [&](int tt) __attribute__((always_inline)) {
            const u32x4 w = w5[tt >> 1];
            return __builtin_amdgcn_mfma_f32_16x16x16bf16_1k(x4, as_bf16x4((tt & 1) ? make_uint2(w[2], w[3]) : make_uint2(w[0], w[1])),
                                                             (f32x4){0.f, 0.f, 0.f, 0.f}, 0, 0, 0);
        };
        auto gath = [&](int tt, f32x4 c) __attribute__((always_inline)) {
            const u32x4 gq4 = *reinterpret_cast<const u32x4*>(&gat[tt >> 1]);
            return __builtin_amdgcn_mfma_f32_16x16x16bf16_1k(as_bf16x4(make_uint2(gq4[(tt & 1) * 2], gq4[(tt & 1) * 2 + 1])), sel, c, 0, 0, 0);
        };
        f32x4 acc = gath(0, feat(0));
#pragma unroll
        for (int tt = 0; tt < 8; ++tt) {
            f32x4 accN = acc;
            __builtin_amdgcn_sched_barrier(0);
            if (tt < 7) accN = feat(tt + 1);
            __builtin_amdgcn_sched_barrier(0);
            float e0 = __builtin_amdgcn_exp2f(acc[0]), e1 = __builtin_amdgcn_exp2f(acc[1]);
            float e2 = __builtin_amdgcn_exp2f(acc[2]), e3 = __builtin_amdgcn_exp2f(acc[3]);
            __builtin_amdgcn_sched_barrier(0);
            if (tt < 7) accN = gath(tt + 1, accN);
            __builtin_amdgcn_sched_barrier(0);
            e0 += 1.f; e1 += 1.f; e2 += 1.f; e3 += 1.f;
            asm("" : "+v"(e0)); asm("" : "+v"(e1)); asm("" : "+v"(e2)); asm("" : "+v"(e3));
            float y0 = acc[0] * __builtin_amdgcn_rcpf(e0), y1 = acc[1] * __builtin_amdgcn_rcpf(e1);
            float y2 = acc[2] * __builtin_amdgcn_rcpf(e2), y3 = acc[3] * __builtin_amdgcn_rcpf(e3);
            asm("" : "+v"(y0)); asm("" : "+v"(y1)); asm("" : "+v"(y2)); asm("" : "+v"(y3));
            u32 k01 = cvt_pk((f32x2){y0, y1}), k23 = cvt_pk((f32x2){y2, y3});
            bf16x4 mb = as_bf16x4(make_uint2(k01, k23));
            __builtin_amdgcn_sched_barrier(0);
            acc2[tt] = __builtin_amdgcn_mfma_f32_16x16x16bf16_1k(mb, sop, acc2[tt], 0, 0, 0);
            acc = accN;
        }
    };

    asm volatile("" :: "v"(sel), "v"(w5b), "v"(w5m), "v"(rb), "v"(rq), "v"(gq), "v"(xb), "v"(xs_), "v"(xm), "v"(g16), "v"(c8), "v"(twp), "v"(trp));
    const std::integral_constant<u32, 0> t0c;
    const std::integral_constant<u32, 1> t1c;
    int lo2 = lane;
    asm volatile("" : "+v"(lo2));
    const u32 i0 = (tv[0] & 0xffffu) + (u32)(lo2 & 1);
    u32 o5 = tv[2] >> 16;
    const u32 roff = ((u32)(t >> 1) * (u32)CH + i0) * (RW * 4u);
    u32x4 r[NPASS];
#pragma unroll
    for (int p = 0; p < NPASS; ++p)
        r[p] = __builtin_amdgcn_raw_buffer_load_b128(rrec, (int)((i0 + 2 * p < o5) ? roff : 0xfffff000u) + p * (2 * RW * 4), 0, 0);
    const u32 e01 = __builtin_amdgcn_alignbit(tv[1], tv[0], 16), e23 = __builtin_amdgcn_alignbit(tv[2], tv[1], 16);
    const bool ev = (lo2 & 1) == 0;
    u32 x01 = (u32)wave_scan((int)(ev ? e01 - tv[0] : 0u));
    u32 x23 = (u32)wave_scan((int)(ev ? e23 - tv[1] : 0u));
    u32 x4 = (u32)wave_scan((int)(ev ? o5 - (tv[2] & 0xffffu) : 0u));
    {
        const u32 bg = (__builtin_amdgcn_ballot_w64(o5 - (tv[0] & 0xffffu) > 64u) != 0ull) ? 0x10000u : 0u;
        if (lo2 == 63) S.wtot[wv] = make_uint4(x01, x23, x4 | bg, 0u);
    }
    stage();
    __syncthreads();
    u32 T01, T23, T4;
    {
        const uint4 w = S.wtot[lo2 & 3];
        const bool lt = (lo2 & 3) < wv;
        auto qsum = [&](u32 v) __attribute__((always_inline)) {
            v += (u32)__builtin_amdgcn_update_dpp(0, (int)v, 0xb1, 0xf, 0xf, true);
            v += (u32)__builtin_amdgcn_update_dpp(0, (int)v, 0x4e, 0xf, 0xf, true);
            return v;
        };
        T01 = (u32)__builtin_amdgcn_readfirstlane((int)qsum(w.x));
        T23 = (u32)__builtin_amdgcn_readfirstlane((int)qsum(w.y));
        T4 = (u32)__builtin_amdgcn_readfirstlane((int)qsum(w.z));
        x01 += qsum(lt ? w.x : 0u);
        x23 += qsum(lt ? w.y : 0u);
        x4 += qsum(lt ? w.z : 0u);
    }
    int cb1 = (int)(((T01 & 0xffffu) + 15u) >> 4), cb2 = cb1 + (int)(((T01 >> 16) + 15u) >> 4);
    int cb3 = cb2 + (int)(((T23 & 0xffffu) + 15u) >> 4), cb4 = cb3 + (int)(((T23 >> 16) + 15u) >> 4);
    int TT = cb4 + (int)((T4 + 15u) >> 4);
    if (__builtin_expect(TT > KB_WAVES * WSL / 16, 0)) {
        T01 = 0u; T23 = 0u; T4 = 0u; o5 = 0u; cb1 = 0; cb2 = 0; cb3 = 0; cb4 = 0; TT = 0;
        sbad = 1ull;
    }
    int dgr = -1;
    bool serial = false;
    {
        const unsigned long long cbp = ((unsigned long long)(u32)(cb4 | (TT << 8) | (TT << 16)) << 32) | (u32)((cb1 << 8) | (cb2 << 16) | (cb3 << 24));
        const int ts = (TT * wv + 3) >> 2, te = (TT * (wv + 1) + 3) >> 2;
        const int nst = te - ts;
        {
            const u32 y01 = x01 + ((u32)cb1 << 20), y23 = x23 + (((u32)cb2 << 4) | ((u32)cb3 << 20)), y4 = x4 + ((u32)cb4 << 4);
            const u32 w0 = (y01 - e01) & 0xffffu;
            const u32 w1 = (u32)(u16)((y01 >> 16) - (e01 >> 16)) | (1u << 19);
            const u32 w2 = ((y23 - e23) & 0xffffu) | (2u << 19);
            const u32 w3 = (u32)(u16)((y23 >> 16) - (e23 >> 16)) | (3u << 19);
            const u32 w4 = ((y4 - o5) & 0xffffu) | (4u << 19);
#define PUT(i, rr) { u32 w = ((i) >= (e01 & 0xffffu)) ? w1 : w0; w = ((i) >= (e01 >> 16)) ? w2 : w;                      w = ((i) >= (e23 & 0xffffu)) ? w3 : w; w = ((i) >= (e23 >> 16)) ? w4 : w; put_rec((w + (i)) & 0xffffu, rr, w >> 16); }
            {
                const u32 tg = __builtin_amdgcn_ubfe((wv < 2) ? T01 : T23, (u32)(wv & 1) * 16u, 16u);
                const u32 sp = (u32)((cbp >> (8 * wv)) & 0xffu) * 16u + tg;
                const bool hi = lo2 >= 16;
                const u32 lp = hi ? (u32)lo2 - 16u : (u32)lo2;
                if (lp < ((0u - (hi ? T4 : tg)) & 15u) && lo2 < ((wv == 3) ? 32 : 16)) {
                    const u32 s = (hi ? (u32)cb4 * 16u + T4 : sp) + lp;
                    S.rec[s] = make_uint4(0u, 0u, 7u, 0u);
                    S.geo[s].x = 0u;
                }
            }
#define REST _Pragma("clang loop unroll(disable)") for (u32 i = i0 + 2 * NPASS; i < o5; i += 2)                 PUT(i, __builtin_amdgcn_raw_buffer_load_b128(rrec, (int)(roff + (i - i0) * (RW * 4u)), 0, 0))
#define STP(P, NEXT) if (i0 + 2 * P < o5) { PUT(i0 + 2 * P, r[P]) NEXT }
            STP(0, STP(1, STP(2, STP(3, STP(4, STP(5, REST))))))
        }
        int gc, rem;
        auto seek = [&](int tn) __attribute__((always_inline)) {
            gc = (tn >= cb1 ? 1 : 0) + (tn >= cb2 ? 1 : 0) + (tn >= cb3 ? 1 : 0) + (tn >= cb4 ? 1 : 0);
            rem = (int)((cbp >> (8 * gc + 8)) & 0xffu) - tn;
        };
        origin(ts);
        seek(ts);
        asm volatile("" : "+s"(gc), "+s"(rem));
        group(gc);
        __syncthreads();
        issue(t0c, gA, pA0, pA1);
#pragma unroll
        for (int tt = 0; tt < 8; ++tt) asm volatile("" : "=v"(acc2[tt]));
        if (nst <= 4) geom(ts * 16 + lo2, nst * 16 - lo2, std::integral_constant<int, 1>(), c_acc);
        else if (nst <= 8) geom(ts * 16 + lo2, nst * 16 - lo2, std::integral_constant<int, 2>(), c_acc);
        else if (nst <= 12) geom(ts * 16 + lo2, nst * 16 - lo2, std::integral_constant<int, 3>(), c_acc);
        else geom(ts * 16 + lo2, nst * 16 - lo2, std::integral_constant<int, 4>(), c_acc);
        CBAR();
        {
            const unsigned long long gs = (1ull << (cb1 & 63)) | (1ull << (cb2 & 63)) | (1ull << (cb3 & 63)) | (1ull << (cb4 & 63));
            const int b1 = (TT + 3) >> 2, b2 = (TT * 2 + 3) >> 2, b3 = (TT * 3 + 3) >> 2;
            serial = (((u32)(gs >> b1) & ((2u << (b2 - b1)) - 1u)) == 0u) || (((u32)(gs >> b2) & ((2u << (b3 - b2)) - 1u)) == 0u);
        }
        int cont = serial ? 0 : (int)((cbp >> (8 * gc)) & 0xffu) - ts;
        lcap = nst - ((blockIdx.x < 256) ? 2 : (blockIdx.x < 512) ? 1 : 0);
        auto step = [&](int tn) __attribute__((always_inline)) {
            dgr = gc;
            if (--rem == 0) {
                u32 tk = 0u;
                if (cont < 0) tk = ticket(gc);
                reduce();
                commit_split(gc, first(tk));
                cont = 0;
                dgr = -1;
                seek(tn);
                group(gc);
            }
        };
        if (nst > 0) {
            int i = 0, hui = HUI;
            asm volatile("" : "+s"(hui));
#pragma clang loop unroll(disable)
            for (;;) {
                fast(t0c, pA0, pA1, i == hui, dgr, nst - i);
                step(ts + i + 1);
                if (++i == nst) break;
                fast(t1c, pA0, pA1, false, dgr, nst - i);
                step(ts + i + 1);
                if (++i == nst) break;
                rq += 512u; rb += 512u; gq += 256u; xb += 2u * xs_;
            }
        } else {
            __builtin_amdgcn_s_waitcnt(0x0F70);
        }
        if (nst < HUI + 1) load_hu();
        load_bw();
        if (dgr >= 0) {
            const u32 tk = ticket(dgr);
            const u32 dn = S.flag[2 + dgr];
            CBAR();
            float* pp = &S.pacc[dgr][g * 16 + e16];
            f32x4* mrow = reinterpret_cast<f32x4*>(&S.macc[dgr * 4 + (e16 >> 2)][g * 8]);
            const float po = *pp;
            f32x4 old[8];
            if ((e16 & 3) == 0) {
#pragma unroll
                for (int tt = 0; tt < 8; ++tt) old[tt] = mrow[(tt >> 1) * 8 + (tt & 1)];
            } else {
#pragma unroll
                for (int tt = 0; tt < 8; ++tt) asm volatile("" : "=v"(old[tt]));
            }
            CBAR();
            reduce();
            if (__builtin_expect(!serial, 1)) {
                if (__builtin_amdgcn_readfirstlane((int)dn) != 0) {
                    *pp = po + pvs;
                    if ((e16 & 3) == 0) {
#pragma unroll
                        for (int tt = 0; tt < 8; ++tt) mrow[(tt >> 1) * 8 + (tt & 1)] = old[tt] + acc2[tt];
                    }
                } else {
                    commit_split(dgr, first(tk));
                }
            }
        }
        if (sbad != 0ull) S.flag[0] = 1u;
    }
    __syncthreads();
    const u32 flg = S.flag[0];
    f32x4 mv[2][8];
    float pres = 0.f;
    auto load_mv = [&]() __attribute__((always_inline)) {
        int le = lane;
        asm volatile("" : "+v"(le));
#pragma unroll
        for (int rt = 0; rt < 2; ++rt) {
            if (rt == 0 || NR > 16) {
                const int lr = rt * 16 + (le & 15);
                const float* mp = &S.macc[lr < NR ? lr : 0][(le >> 4) * 8];
#pragma unroll
                for (int ks = 0; ks < 4; ++ks) {
                    mv[rt][ks * 2] = *reinterpret_cast<const f32x4*>(mp + ks * 32);
                    mv[rt][ks * 2 + 1] = *reinterpret_cast<const f32x4*>(mp + ks * 32 + 4);
                }
            } else {
#pragma unroll
                for (int ks = 0; ks < 8; ++ks) asm volatile("" : "=v"(mv[rt][ks]));
            }
        }
        if (wv == 3) {
            const int n = min(le / 3, GPW * 4 - 1), cpt = le - (le / 3) * 3;
            const float p = reinterpret_cast<const float*>(&S.posl[(n & 3) * 8 + (n >> 2)])[cpt];
            const float* pa = &S.pacc[n >> 2][(n & 3) * 4];
            const float s0 = (pa[0] + pa[16]) + (pa[32] + pa[48]);
            const float s1 = (pa[1 + cpt] + pa[17 + cpt]) + (pa[33 + cpt] + pa[49 + cpt]);
            pres = p + (p * s0 - s1);
        }
        CBAR();
    };
    load_mv();
    if (__builtin_expect((__builtin_amdgcn_readfirstlane((int)flg) | (int)serial) != 0, 0)) {
      for (int w = 0; w < KB_WAVES - 1; ++w) {
          if (serial && dgr >= 0 && wv == w) commit(dgr, c_acc);
          __syncthreads();
      }
      if (__builtin_amdgcn_readfirstlane((int)flg) != 0) {
        lo2 = (int)__builtin_amdgcn_mbcnt_hi(~0u, __builtin_amdgcn_mbcnt_lo(~0u, 0u));
        asm volatile("" : "+v"(lo2));
        lane = lo2; t = wv * 64 + lo2;
        e16 = lo2 & 15; g = lo2 >> 4;
        load_stage();
        stage();
        load_hu();
        for (int i = wv * 64 + lo2; i < GPW * 4 * AST; i += KB_THREADS) (&S.macc[0][0])[i] = 0.f;
        for (int i = wv * 64 + lo2; i < GPW * 64; i += KB_THREADS) (&S.pacc[0][0])[i] = 0.f;
        __syncthreads();
        u32* key = reinterpret_cast<u32*>(&S.rec[wv * WSL]);
        for (int gr = 0; gr * 4 < NR; ++gr) {
            int so[2], sc[2], fo[2];
#pragma unroll
            for (int tt = 0; tt < 8; ++tt) acc2[tt] = (f32x4){0.f, 0.f, 0.f, 0.f};
#pragma unroll
            for (int ci = 0; ci < 2; ++ci) {
                const u16* tg = tab + (size_t)((lo2 * 2 + ci) * NWG + bx) * TS + gr;
                so[ci] = tg[0];
                sc[ci] = (int)tg[1] - so[ci];
            }
            const int x = wave_scan(sc[0] + sc[1]);
            const int T = __builtin_amdgcn_readlane(x, 63);
            fo[1] = x - sc[1]; fo[0] = fo[1] - sc[0];
            for (int base = 0; base < T; base += KB_WAVES * WSL) {
                const int sh = ((min(KB_WAVES * WSL, T - base) + 63) >> 6) << 4;
                const int wb = base + wv * sh;
                const int Tr = max(0, min(sh, T - wb));
                if (Tr == 0) continue;
                CBAR();
#pragma unroll
                for (int ci = 0; ci < 2; ++ci) {
                    const int qa = max(0, wb - fo[ci]), qb = min(sc[ci], wb + Tr - fo[ci]);
#pragma clang loop unroll(disable) vectorize(disable) interleave(disable)
                    for (int q = qa; q < qb; ++q) key[fo[ci] + q - wb] = (u32)((lo2 * 2 + ci) * CH + so[ci] + q) * (RW * 4u);
                }
                CBAR();
                u32 off[4];
#pragma unroll
                for (int i = 0; i < 4; ++i) off[i] = (lo2 + i * 64 < Tr) ? key[lo2 + i * 64] : 0xfffffff0u;
                CBAR();
#pragma unroll
                for (int i = 0; i < 4; ++i) r[i] = __builtin_amdgcn_raw_buffer_load_b128(rrec, (int)off[i], 0, 0);
#pragma unroll
                for (int i = 0; i < 4; ++i) put_rec((u32)(wv * WSL + lo2 + i * 64), r[i], (lo2 + i * 64 < Tr) ? (u32)gr << 3 : 7u);
                CBAR();
                geom(wv * WSL + lo2, 256, std::integral_constant<int, 4>(), c_set);
                CBAR();
                setup(lo2);
                origin(wv * (WSL / 16));
                group(gr);
                for (int i = 0; i * 16 < Tr; ++i) {
                    issue(t0c, gA, pA0, pA1);
                    compute(t0c, gA, pA0, pA1);
                    rq += 256u; rb += 256u; gq += 128u; xb += xs_;
                }
            }
            reduce();
            for (int w = 0; w < KB_WAVES; ++w) {
                if (wv == w) commit(gr, c_acc);
                __syncthreads();
            }
        }
        load_bw();
      }
      load_mv();
    }

    int le = lane;
    asm volatile("" : "+v"(le));
    const int f16 = le & 15, gg = le >> 4;
    auto node = [&](auto nc) __attribute__((always_inline)) {
        constexpr int NRT = decltype(nc)::value;
        f32x4 acc[NRT][2];
#pragma unroll
        for (int rt = 0; rt < NRT; ++rt)
#pragma unroll
            for (int q = 0; q < 2; ++q) {
                const u32 u0 = q ? uv4[rt].z : uv4[rt].x, u1 = q ? uv4[rt].w : uv4[rt].y;
                acc[rt][q] = (f32x4){__uint_as_float(u0 << 16), __uint_as_float(u0 & 0xffff0000u),
                                     __uint_as_float(u1 << 16), __uint_as_float(u1 & 0xffff0000u)};
            }
        __builtin_amdgcn_sched_barrier(0);
#pragma unroll
        for (int ks = 0; ks < 4; ++ks)
#pragma unroll
            for (int rt = 0; rt < NRT; ++rt) {
                const f32x4 a = mv[rt][ks * 2], b = mv[rt][ks * 2 + 1];
                const bf16x8 af = as_bf16x8(make_uint4(cvt_pk((f32x2){a[0], a[1]}), cvt_pk((f32x2){a[2], a[3]}),
                                                       cvt_pk((f32x2){b[0], b[1]}), cvt_pk((f32x2){b[2], b[3]})));
#pragma unroll
                for (int q = 0; q < 2; ++q)
                    acc[rt][q] = __builtin_amdgcn_mfma_f32_16x16x32_bf16(bw[q][ks], af, acc[rt][q], 0, 0, 0);
            }
        const __amdgpu_buffer_rsrc_t ro = mk_rsrc(out_h, (u32)N * (D * 2));
#pragma unroll
        for (int rt = 0; rt < NRT; ++rt) {
            const int lr = rt * 16 + f16;
            u32 ov[4];
#pragma unroll
            for (int q = 0; q < 2; ++q) {
                const u32 h0 = q ? hv4[rt].z : hv4[rt].x, h1 = q ? hv4[rt].w : hv4[rt].y;
                ov[q * 2] = cvt_pk((f32x2){silu_t(acc[rt][q][0], __uint_as_float(h0 << 16)), silu_t(acc[rt][q][1], __uint_as_float(h0 & 0xffff0000u))});
                ov[q * 2 + 1] = cvt_pk((f32x2){silu_t(acc[rt][q][2], __uint_as_float(h1 << 16)), silu_t(acc[rt][q][3], __uint_as_float(h1 & 0xffff0000u))});
            }
            st_b128<AUX_O>(ro, (lr < NR) ? (u32)(N0 + lr) * (D * 2) + wv * 64 + gg * 16 : 0xfffffff0u, make_uint4(ov[0], ov[1], ov[2], ov[3]));
        }
    };
    if (NR > 16) node(std::integral_constant<int, 2>());
    else node(std::integral_constant<int, 1>());
    if (wv == 3 && le < NR * 3) out_pos[(size_t)N0 * 3 + le] = f2bf(pres);
    asm volatile(".Lkb_end:");
}

extern "C" void kernel_launch(void* const* d_in, const int* in_sizes, int n_in,
                              void* d_out, int out_size, void* d_ws, size_t ws_size,
                              hipStream_t stream) {
    const u16* h   = (const u16*)d_in[0];
    const u16* pos = (const u16*)d_in[1];
    const u16* ea  = (const u16*)d_in[2];
    const int* src = (const int*)d_in[3];
    const int* dst = (const int*)d_in[4];
    const u16* we  = (const u16*)d_in[5];
    const u16* wx  = (const u16*)d_in[6];
    const u16* wh  = (const u16*)d_in[7];
    int N = in_sizes[0] / D;
    int E = in_sizes[3];
    u16* out_h = (u16*)d_out;
    u16* out_pos = out_h + (size_t)N * D;

    int CH = ((E + NCH - 1) / NCH + 3) & ~3;
    int NWG = 768;
    while (NWG * GPW * 4 < N && NWG < NWG_MAX) NWG += 2;
    int nGemm = (N + 127) / 128;
    unsigned long long mN = (1ull << 40) / (unsigned long long)N + 1ull;
    unsigned long long mNWG = (1ull << 40) / (unsigned long long)NWG + 1ull;

    char* ws = (char*)d_ws;
    size_t off = 0;
    auto take = [&](size_t bytes) { char* p = ws + off; off += (bytes + 255) & ~(size_t)255; return p; };
    u32* rec_g = (u32*)take((size_t)NCH * CH * RW * 4 + 64);
    u16* tab = (u16*)take((size_t)NCH * NWG * TS * 2);
    u16* ABU = (u16*)take((size_t)(3 * N + N / 64 + 32) * D * 2);
    u16* whT = (u16*)take((size_t)D * D * 2);
    uint4* w5g = (uint4*)take((size_t)256 * 16);
    uint2* pos8 = (uint2*)take((size_t)(N + 64) * 8);

    k_part<<<NCH + nGemm + 1, KA_THREADS, 0, stream>>>(h, pos, src, dst, ea, we, wx, wh, rec_g, tab, ABU, whT, w5g, pos8,
                                                        mN, mNWG, N, E, CH, NWG, nGemm, 0);
    k_main<<<NWG, KB_THREADS, 0, stream>>>(h, rec_g, out_h, mNWG, N, CH, NWG,
        (u32)((char*)tab - ws), (u32)((char*)ABU - ws), (u32)((char*)whT - ws));
}
```
